```python
import torch

def _softmax(x, axis=-1):
    return torch.softmax(x, dim=axis)
def _gelu(x):
    return torch.nn.functional.gelu(x, approximate="tanh")
def _silu(x):
    return torch.nn.functional.silu(x)
def _sigmoid(x):
    return torch.sigmoid(x)
def _layernorm(x, axis=-1, eps=1e-5):
    m = x.mean(dim=axis, keepdim=True)
    v = x.var(dim=axis, keepdim=True, unbiased=False)
    return (x - m) / torch.sqrt(v + eps)
def _scatter_add(tgt, idx, src, axis=0):
    return tgt.clone().index_add_(axis, idx, src)
def _segment_sum(src, seg, n):
    out = torch.zeros((n,) + src.shape[1:], dtype=src.dtype)
    return out.index_add_(0, seg, src)
def _pad(x, pw):
    flat = tuple(v for pair in reversed(pw) for v in pair)
    return torch.nn.functional.pad(x, flat)

kernel_name = 'gated_mix__ssm_conv_s5__glu_gated_glu__prompt'

def setup_inputs():

    _w = (lambda a: (lambda t: t.to(torch.bfloat16) if t.is_floating_point() else t)(torch.as_tensor(a)))
    def _A_setup():
        B, S, D = 1, 2048, 512
        P = D
        Lambda_re = -0.5 * (lambda _x: torch.exp(torch.as_tensor(_x)))((lambda *s: torch.randn(*s, dtype=torch.bfloat16) * 0.4)(P))
        Lambda_im = (lambda *s: torch.randn(*s, dtype=torch.bfloat16) * 0.8)(P) * 1.0
        B_re = (lambda *s: torch.randn(*s, dtype=torch.bfloat16) * 0.8)(P, D) * (D ** -0.5)
        B_im = (lambda *s: torch.randn(*s, dtype=torch.bfloat16) * 0.8)(P, D) * (D ** -0.5)
        C_re = (lambda *s: torch.randn(*s, dtype=torch.bfloat16) * 0.8)(D, P) * (P ** -0.5)
        C_im = (lambda *s: torch.randn(*s, dtype=torch.bfloat16) * 0.8)(D, P) * (P ** -0.5)
        log_dt = (lambda *s: torch.randn(*s, dtype=torch.bfloat16) * 0.4)(P) * 0.3 - 2.0
        Dskip = (lambda *s: torch.randn(*s, dtype=torch.bfloat16) * 0.1)(D)
        return {
            "x": _w((lambda *s: torch.randn(*s, dtype=torch.bfloat16) * 0.05)(B, S, D)),
            "Lambda_re": _w(Lambda_re),
            "Lambda_im": _w(Lambda_im),
            "B_re": _w(B_re),
            "B_im": _w(B_im),
            "C_re": _w(C_re),
            "C_im": _w(C_im),
            "log_dt": _w(log_dt),
            "Dskip": _w(Dskip),
        }
    _A_in = _A_setup()

    def _B_setup():
        B, S, D, F = 1, 2048, 512, 1792
        return {
            "x": _w((lambda *s: torch.randn(*s, dtype=torch.bfloat16) * 0.05)(B, S, D)),
            "w_gate": _w((lambda *s: torch.randn(*s, dtype=torch.bfloat16) * 0.4)(D, F)),
            "w_up": _w((lambda *s: torch.randn(*s, dtype=torch.bfloat16) * 0.05)(D, F)),
            "w_down": _w((lambda *s: torch.randn(*s, dtype=torch.bfloat16) * 0.05)(F, D)),
        }
    _B_in = _B_setup()

    return {
        "A_x": _A_in["x"],
        "A_Lambda_re": _A_in["Lambda_re"],
        "A_Lambda_im": _A_in["Lambda_im"],
        "A_B_re": _A_in["B_re"],
        "A_B_im": _A_in["B_im"],
        "A_C_re": _A_in["C_re"],
        "A_C_im": _A_in["C_im"],
        "A_log_dt": _A_in["log_dt"],
        "A_Dskip": _A_in["Dskip"],
        "B_x": _B_in["x"],
        "B_w_gate": _B_in["w_gate"],
        "B_w_up": _B_in["w_up"],
        "B_w_down": _B_in["w_down"],
        "gate_w": _w((lambda *s: torch.randn(*s, dtype=torch.bfloat16) * 0.05)(512) * 16.0),
    }

def reference(A_x, A_Lambda_re, A_Lambda_im, A_B_re, A_B_im, A_C_re, A_C_im, A_log_dt, A_Dskip, B_x, B_w_gate, B_w_up, B_w_down, gate_w):
    def _A_ref(x, Lambda_re, Lambda_im, B_re, B_im, C_re, C_im, log_dt, Dskip):
        Bsz, L, H = x.shape
        Lam = Lambda_re + 1j * Lambda_im
        Bmat = B_re + 1j * B_im
        Cmat = C_re + 1j * C_im
        dt = torch.exp(log_dt)
        Lam_bar = torch.exp(dt * Lam)
        B_bar = ((Lam_bar - 1.0) / Lam)[:, None] * Bmat
        Bu = torch.einsum("bld,pd->blp", x + 0j, B_bar)
        h = torch.zeros_like(Bu[:, 0, :])
        hs = []
        for t in range(L):
            h = Lam_bar[None, :] * h + Bu[:, t, :]
            hs.append(h)
        hseq = torch.stack(hs, 1)
        y = 2.0 * torch.einsum("blp,dp->bld", hseq, Cmat).real + x * Dskip[None, None, :]
        return y

    def _B_ref(x, w_gate, w_up, w_down):
        g = torch.einsum("bsd,df->bsf", x, w_gate)
        u = torch.einsum("bsd,df->bsf", x, w_up)
        h = _sigmoid(g) * u
        return torch.einsum("bsf,fd->bsd", h, w_down)

    x = A_x
    g = _sigmoid((x * gate_w).sum(-1, keepdims=True))
    a = _A_ref(x, A_Lambda_re, A_Lambda_im, A_B_re, A_B_im, A_C_re, A_C_im, A_log_dt, A_Dskip)
    b = _B_ref(x, B_w_gate, B_w_up, B_w_down)
    return g * a + (1 - g) * b
```

```cpp
#include <hip/hip_runtime.h>
#include <hip/hip_bf16.h>
#include <stdint.h>
typedef unsigned short u16;
__device__ __forceinline__ float bf(u16 v){ return __uint_as_float(((unsigned)v)<<16); }
__device__ __forceinline__ float rbf(float f){ unsigned u=__float_as_uint(f); u+=0x7FFFu+((u>>16)&1u); return __uint_as_float(u&0xFFFF0000u); }
constexpr int S=2048,D=512,P=512,F=1792;
__global__ void k_u(const u16*x,const u16*Bre,const u16*Bim,float*Ur,float*Ui){
  int t=blockIdx.x,p=threadIdx.x; __shared__ float xs[D]; xs[p]=bf(x[t*D+p]); __syncthreads();
  float a=0,b=0; for(int k=0;k<D;++k){a+=xs[k]*bf(Bre[p*D+k]); b+=xs[k]*bf(Bim[p*D+k]);}
  Ur[t*P+p]=a; Ui[t*P+p]=b;
}
__global__ void k_glu(const u16*x,const u16*Wg,const u16*Wu,float*H2){
  int t=blockIdx.x; __shared__ float xs[D]; for(int i=threadIdx.x;i<D;i+=blockDim.x) xs[i]=bf(x[t*D+i]); __syncthreads();
  for(int f=threadIdx.x;f<F;f+=blockDim.x){ float g=0,u=0; for(int k=0;k<D;++k){g+=xs[k]*bf(Wg[k*F+f]); u+=xs[k]*bf(Wu[k*F+f]);}
    H2[t*F+f]=rbf(u/(1.f+__expf(-g))); }
}
__global__ void k_scan(const u16*Lre,const u16*Lim,const u16*ldt,float*Ur,float*Ui){
  int p=blockIdx.x*64+threadIdx.x; float lr=bf(Lre[p]),li=bf(Lim[p]); float dt=rbf(expf(bf(ldt[p])));
  float e=expf(dt*lr), ar=e*cosf(dt*li), ai=e*sinf(dt*li);
  float nr=ar-1.f, ni=ai, den=lr*lr+li*li; float cr=(nr*lr+ni*li)/den, ci=(ni*lr-nr*li)/den;
  float hr=0,hi=0;
  for(int t=0;t<S;++t){ float ur=Ur[t*P+p],ui=Ui[t*P+p]; float br=cr*ur-ci*ui, bi=cr*ui+ci*ur;
    float nhr=ar*hr-ai*hi+br, nhi=ar*hi+ai*hr+bi; hr=nhr;hi=nhi; Ur[t*P+p]=rbf(hr); Ui[t*P+p]=rbf(hi);} }
__global__ void k_out(const u16*x,const u16*Cre,const u16*Cim,const u16*Dsk,const u16*Wd,const u16*gw,const float*Hr,const float*Hi,const float*H2,float*out){
  int t=blockIdx.x,d=threadIdx.x; __shared__ float hr[P],hi[P],h2[F],red[D];
  hr[d]=Hr[t*P+d]; hi[d]=Hi[t*P+d]; for(int f=d;f<F;f+=D) h2[f]=H2[t*F+f];
  float xv=bf(x[t*D+d]); red[d]=xv*bf(gw[d]); __syncthreads();
  for(int s=256;s>0;s>>=1){ if(d<s) red[d]+=red[d+s]; __syncthreads(); }
  float g=1.f/(1.f+expf(-red[0]));
  float y=0; for(int p=0;p<P;++p) y+=hr[p]*bf(Cre[d*P+p])-hi[p]*bf(Cim[d*P+p]);
  float b=0; for(int f=0;f<F;++f) b+=h2[f]*bf(Wd[f*D+d]);
  float a=2.f*y+xv*bf(Dsk[d]);
  out[t*D+d]=g*a+(1.f-g)*b;
}
extern "C" void kernel_launch(void* const* d_in, const int* in_sizes, int n_in, void* d_out, int out_size, void* d_ws, size_t ws_size, hipStream_t stream){
  const u16* x=(const u16*)d_in[0]; const u16*Lre=(const u16*)d_in[1],*Lim=(const u16*)d_in[2],*Bre=(const u16*)d_in[3],*Bim=(const u16*)d_in[4],*Cre=(const u16*)d_in[5],*Cim=(const u16*)d_in[6],*ldt=(const u16*)d_in[7],*Dsk=(const u16*)d_in[8];
  const u16*Wg=(const u16*)d_in[10],*Wu=(const u16*)d_in[11],*Wd=(const u16*)d_in[12],*gw=(const u16*)d_in[13];
  float*Ur=(float*)d_ws,*Ui=Ur+S*P,*H2=Ui+S*P;
  k_u<<<S,P,0,stream>>>(x,Bre,Bim,Ur,Ui);
  k_glu<<<S,256,0,stream>>>(x,Wg,Wu,H2);
  k_scan<<<P/64,64,0,stream>>>(Lre,Lim,ldt,Ur,Ui);
  k_out<<<S,D,0,stream>>>(x,Cre,Cim,Dsk,Wd,gw,Ur,Ui,H2,(float*)d_out);
}
```

```cpp
#include <hip/hip_runtime.h>
#include <stdint.h>

typedef unsigned short u16;
typedef unsigned v4u __attribute__((ext_vector_type(4)));
typedef unsigned v2u __attribute__((ext_vector_type(2)));
typedef short s16x4 __attribute__((ext_vector_type(4)));
typedef short s16x8 __attribute__((ext_vector_type(8)));
typedef __bf16 bf16x8 __attribute__((ext_vector_type(8)));
typedef __bf16 bf16x2 __attribute__((ext_vector_type(2)));
typedef float f32x2 __attribute__((ext_vector_type(2)));
typedef float f32x4 __attribute__((ext_vector_type(4)));
typedef float f32x16 __attribute__((ext_vector_type(16)));
#define LAS __attribute__((address_space(3)))
#define GAS __attribute__((address_space(1)))
typedef __amdgpu_buffer_rsrc_t rsrc_t;

constexpr int S_ = 2048, D_ = 512, P_ = 512, F_ = 1792;
constexpr int NK = 176;
constexpr int AUX_SC1 = 16;
constexpr int WS_EPOCH = 0;
constexpr int WS_CARRY = 65536;
constexpr int WS_G = 196608;
constexpr int WS_B2 = 262144;
constexpr int B2_BYTES = 8 * NK * 2048;
constexpr int WS_A2 = 4 << 20;
constexpr int A2_BYTES = 32 * NK * 2048;
constexpr int WS_USED = WS_A2 + A2_BYTES;
constexpr int WS_STAMP = 32 << 20;

constexpr int LDS_BYTES = 160 * 1024;

struct Params {
  const u16 *x, *Lre, *Lim, *Bre, *Bim, *Cre, *Cim, *ldt, *Dsk, *Wg, *Wu, *Wd, *gw;
  float* out;
  unsigned char* ws;
};

__device__ __forceinline__ rsrc_t mk_rsrc(const void* p, int bytes) {
  return __builtin_amdgcn_make_buffer_rsrc((void*)p, 0, bytes, 0x00020000);
}
__device__ __forceinline__ float bf2f(unsigned v) { return __uint_as_float(v << 16); }
__device__ __forceinline__ float bfhi(unsigned v) { return __uint_as_float(v & 0xFFFF0000u); }
__device__ __forceinline__ unsigned pk_bf16(float lo, float hi) {
  f32x2 t = {lo, hi};
  return __builtin_bit_cast(unsigned, __builtin_convertvector(t, bf16x2));
}
__device__ __forceinline__ f32x16 mfma32(v4u a, v4u b, f32x16 c) {
  return __builtin_amdgcn_mfma_f32_32x32x16_bf16(__builtin_bit_cast(bf16x8, a), __builtin_bit_cast(bf16x8, b), c, 0, 0, 0);
}
__device__ __forceinline__ v4u lds_rd128(LAS unsigned char* p) { return *(LAS v4u*)p; }
__device__ __forceinline__ v2u lds_rdtr(LAS unsigned char* p) {
  return __builtin_bit_cast(v2u, __builtin_amdgcn_ds_read_tr16_b64_v4i16((LAS s16x4*)p));
}
__device__ __forceinline__ void glds(rsrc_t rs, LAS unsigned char* dst, int voff, int soff) {
  __builtin_amdgcn_raw_ptr_buffer_load_lds(rs, (LAS void*)dst, 16, voff, soff, 0, 0);
}
#define BARRIER() asm volatile("s_barrier" ::: "memory")
#define SCHED() __builtin_amdgcn_sched_barrier(0)
template <int N> __device__ __forceinline__ void wait_vm() { asm volatile("s_waitcnt vmcnt(%c0)" ::"i"(N) : "memory"); }

#define STAMP(i) do {} while (0)
#define CSTAMP(i) do {} while (0)

constexpr int glu_wait(int s) {
  int posX[8] = {}, posW[16] = {}, n = 0;
  n += 5; posX[0] = n; n += 4; posW[0] = n; n += 4; posW[1] = n; n += 5; posX[1] = n;
  n += 4; posW[2] = n; n += 4; posW[3] = n; n += 5; posX[2] = n;
  for (int t = 0; t < s; ++t) {
    if (t >= 1 && t + 3 <= 15) { n += 4; posW[t + 3] = n; }
    if (t >= 2 && (t & 1) == 0 && t / 2 + 2 <= 7) { n += 5; posX[t / 2 + 2] = n; }
  }
  int w = s + 1 > 15 ? 15 : s + 1, x = (s + 1) >> 1; if (x > 7) x = 7;
  int need = posW[w] > posX[x] ? posW[w] : posX[x];
  return n - need;
}
constexpr int ssm_wait(int s) {
  int pos[8] = {}, n = 0;
  n += 12; pos[0] = n; n += 12; pos[1] = n; n += 12; pos[2] = n;
  for (int t = 0; t < s; ++t)
    if (t >= 2 && (t & 1) == 0 && t / 2 + 2 <= 7) { n += 12; pos[t / 2 + 2] = n; }
  int x = (s + 1) >> 1; if (x > 7) x = 7;
  return n - pos[x];
}

constexpr int GX_SLOT = 20480, GW_BASE = 61440, GW_SLOT = 16384;
constexpr int SX_SLOT = 16384, SB_BASE = 49152, SB_SLOT = 32768;

template <int s> __device__ __forceinline__ void glu_loader_step(rsrc_t rx, rsrc_t rw, LAS unsigned char* L, int lw, int vx, int vw,
                                                               int sx0, int sw0, int rot) {
  wait_vm<glu_wait(s)>();
  BARRIER();
  if constexpr (s >= 1 && s + 3 <= 15) {
    constexpr int t = s + 3;
    const int k0 = ((((t >> 1) + rot) & 7) * 64 + (t & 1) * 32) * (F_ * 2);
#pragma unroll
    for (int i = 0; i < 4; ++i) glds(rw, L + GW_BASE + (t & 3) * GW_SLOT + lw * 4096 + i * 1024, vw, sw0 + k0 + i * 4 * (F_ * 2));
  }
  if constexpr (s >= 2 && (s & 1) == 0 && s / 2 + 2 <= 7) {
    constexpr int T = s / 2 + 2;
    const int k0 = ((T + rot) & 7) * 128;
#pragma unroll
    for (int n = 0; n < 5; ++n) glds(rx, L + (T % 3) * GX_SLOT + lw * 1024 + n * 4096, vx, sx0 + k0 + n * 32768);
  }
  if constexpr (s < 15) glu_loader_step<s + 1>(rx, rw, L, lw, vx, vw, sx0, sw0, rot);
}
template <int s> __device__ __forceinline__ void ssm_loader_step(rsrc_t rx, rsrc_t rb, LAS unsigned char* L, int lw, int vx, int vb,
                                                               int sx0, int sb0, int rot) {
  wait_vm<ssm_wait(s)>();
  BARRIER();
  if constexpr (s >= 2 && (s & 1) == 0 && s / 2 + 2 <= 7) {
    constexpr int T = s / 2 + 2;
    const int k0 = ((T + rot) & 7) * 128;
#pragma unroll
    for (int n = 0; n < 4; ++n) glds(rx, L + (T % 3) * SX_SLOT + lw * 1024 + n * 4096, vx, sx0 + k0 + n * 8192);
#pragma unroll
    for (int n = 0; n < 8; ++n)
      glds(rb, L + SB_BASE + (T % 3) * SB_SLOT + (lw >> 1) * 16384 + (lw & 1) * 1024 + n * 2048, vb, sb0 + k0 + n * 16384);
  }
  if constexpr (s < 15) ssm_loader_step<s + 1>(rx, rb, L, lw, vx, vb, sx0, sb0, rot);
}

__device__ __forceinline__ void retile_b2(const Params& p, int b, int c, int lane) {
  const int h = lane >> 5, i = lane & 31;
  rsrc_t rws = mk_rsrc(p.ws + WS_B2, B2_BYTES);
  {
    const int N = b & 7, e = (b >> 3) & 1, mat = (b >> 4) & 1, line = b >> 5;
    const int cg = 2 * line + (c >> 1), lo = c & 1;
    rsrc_t rc = mk_rsrc(mat ? p.Cim : p.Cre, D_ * P_ * 2);
    const int d = 64 * N + 2 * i + e;
    v4u v = __builtin_amdgcn_raw_buffer_load_b128(rc, d * (P_ * 2) + (32 * cg + 16 * lo + 8 * h) * 2, 0, 0);
    const int kap = 112 + 4 * cg + 2 * mat + lo;
    const int id = 4 * b + c;
    unsigned w[8];
    const int kg = id >> 3, Ng = id & 7;
    rsrc_t rd = mk_rsrc(p.Wd, id < 896 ? F_ * D_ * 2 : 0);
#pragma unroll
    for (int j = 0; j < 8; ++j) {
      const int f = 16 * kg + (j & 3) + 8 * (j >> 2) + 4 * h;
      w[j] = __builtin_amdgcn_raw_buffer_load_b32(rd, f * (D_ * 2) + (64 * Ng + 2 * i) * 2, 0, 0);
    }
    __builtin_amdgcn_raw_buffer_store_b128(v, rws, (N * NK + kap) * 2048 + e * 1024 + lane * 16, 0, 0);
    if (id < 896) {
      v4u f0, f1;
#pragma unroll
      for (int q = 0; q < 4; ++q) {
        f0[q] = __builtin_amdgcn_perm(w[2 * q + 1], w[2 * q], 0x05040100u);
        f1[q] = __builtin_amdgcn_perm(w[2 * q + 1], w[2 * q], 0x07060302u);
      }
      __builtin_amdgcn_raw_buffer_store_b128(f0, rws, (Ng * NK + kg) * 2048 + lane * 16, 0, 0);
      __builtin_amdgcn_raw_buffer_store_b128(f1, rws, (Ng * NK + kg) * 2048 + 1024 + lane * 16, 0, 0);
    }
  }
}

__device__ __forceinline__ void cmul(float ar, float ai, float br, float bi, float& cr, float& ci) {
  cr = ar * br - ai * bi; ci = ar * bi + ai * br;
}

__global__ __launch_bounds__(512, 2) void k_phase1(Params p) {
  extern __shared__ __attribute__((aligned(16))) unsigned char lds_[];
  LAS unsigned char* L = (LAS unsigned char*)lds_;
  const int b = blockIdx.x, tid = threadIdx.x, lane = tid & 63;
  const int w = __builtin_amdgcn_readfirstlane(tid >> 6);
  const int xcd = b & 7, r = b >> 3, H = xcd >> 2, Q = xcd & 3;
  const int nq = H ? 9 : 7;
  int kind, pn = 0, cb = 0;
  if (r < nq) { kind = 1; pn = (H ? 7 : 0) + r; cb = Q; }
  else {
    const int r2 = r - nq;
    const int cnt = H ? 21 : (Q < 2 ? 25 : 24), st = H ? 21 * Q : (Q < 2 ? 25 * Q : 50 + 24 * (Q - 2));
    if (r2 < cnt) { const int idx = st + r2, np = H ? 6 : 7; kind = 0; cb = idx / np; pn = (H ? 7 : 0) + idx % np; }
    else { kind = 2; pn = H ? 2 + 2 * Q + (r2 - 21) : (Q - 2); }
  }
  const int rot = 2 * ((pn + cb) & 3);
  STAMP(0);
  rsrc_t rx = mk_rsrc(p.x, S_ * D_ * 2);

  if (kind == 2) {
    if (w < 4) retile_b2(p, b, w, lane);
    rsrc_t rg = mk_rsrc(p.gw, D_ * 2);
    v4u gv = __builtin_amdgcn_raw_buffer_load_b128(rg, lane * 16, 0, 0);
    const int wid = pn * 8 + w;
    float* G = (float*)(p.ws + WS_G);
    v4u xv[26];
#pragma unroll
    for (int i = 0; i < 26; ++i) xv[i] = __builtin_amdgcn_raw_buffer_load_b128(rx, lane * 16, (wid + 80 * i) * 1024, 0);
#pragma unroll
    for (int i = 0; i < 26; ++i) {
      float s = 0.f;
#pragma unroll
      for (int q = 0; q < 4; ++q) { s += bf2f(xv[i][q] & 0xFFFFu) * bf2f(gv[q] & 0xFFFFu); s += bfhi(xv[i][q]) * bfhi(gv[q]); }
#pragma unroll
      for (int o = 32; o > 0; o >>= 1) s += __shfl_xor(s, o);
      const int t = wid + 80 * i;
      if (lane == 0 && t < S_) G[t] = 1.f / (1.f + __expf(-s));
    }
    STAMP(1);
    return;
  }

  if (w >= 4) {
    const int lw = w - 4;
    if (kind == 0) {
      const int row = 8 * lw + (lane >> 3);
      const int vx = row * 1024 + (((lane & 7) ^ ((row >> 1) & 7)) << 4);
      const int sx0 = pn * 160 * 1024;
      const int m = lw >> 1, kq = lane >> 4;
      rsrc_t rw = mk_rsrc(m ? p.Wu : p.Wg, D_ * F_ * 2);
      const int vw = ((lw & 1) * 16 + kq) * (F_ * 2) + (((lane & 15) ^ ((kq << 2) | (m << 1))) << 4);
      const int sw0 = cb * 256;
      auto ldx = [&](int T) {
        const int k0 = ((T + rot) & 7) * 128;
#pragma unroll
        for (int n = 0; n < 5; ++n) glds(rx, L + T * GX_SLOT + lw * 1024 + n * 4096, vx, sx0 + k0 + n * 32768);
      };
      auto ldw = [&](int t) {
        const int k0 = ((((t >> 1) + rot) & 7) * 64 + (t & 1) * 32) * (F_ * 2);
#pragma unroll
        for (int i = 0; i < 4; ++i) glds(rw, L + GW_BASE + t * GW_SLOT + lw * 4096 + i * 1024, vw, sw0 + k0 + i * 4 * (F_ * 2));
      };
      ldx(0); ldw(0); ldw(1); ldx(1); ldw(2); ldw(3); ldx(2);
      glu_loader_step<0>(rx, rw, L, lw, vx, vw, sx0, sw0, rot);
    } else {
      const int rr = 8 * lw + (lane >> 3);
      const int tok = 64 * ((rr >> 2) & 1) + 32 * (rr & 1) + ((rr >> 1) & 1) + 2 * (rr >> 3);
      const int vx = tok * 1024 + (((lane & 7) ^ ((rr >> 1) & 7)) << 4);
      const int sx0 = pn * 128 * 1024;
      rsrc_t rb = mk_rsrc((lw >> 1) ? p.Bim : p.Bre, P_ * D_ * 2);
      const int brow = 8 * (lw & 1) + (lane >> 3);
      const int vb = brow * 1024 + (((lane & 7) ^ ((brow >> 1) & 7)) << 4);
      const int sb0 = cb * 128 * 1024;
#pragma unroll
      for (int T = 0; T < 3; ++T) {
        const int k0 = ((T + rot) & 7) * 128;
#pragma unroll
        for (int n = 0; n < 4; ++n) glds(rx, L + T * SX_SLOT + lw * 1024 + n * 4096, vx, sx0 + k0 + n * 8192);
#pragma unroll
        for (int n = 0; n < 8; ++n)
          glds(rb, L + SB_BASE + T * SB_SLOT + (lw >> 1) * 16384 + (lw & 1) * 1024 + n * 2048, vb, sb0 + k0 + n * 16384);
      }
      ssm_loader_step<0>(rx, rb, L, lw, vx, vb, sx0, sb0, rot);
    }
    STAMP(1);
    return;
  }

  const int c = w, h = lane >> 5, i31 = lane & 31;
  retile_b2(p, b, c, lane);
  rsrc_t ra2 = mk_rsrc(p.ws + WS_A2, A2_BYTES);
  int xa[4];
#pragma unroll
  for (int ks = 0; ks < 4; ++ks) xa[ks] = i31 * 128 + (((2 * ks + h) ^ ((i31 >> 1) & 7)) << 4);

  if (kind == 0) {
    const int m = (lane >> 4) & 1, q = (lane & 15) >> 2, pp = lane & 3;
    int wa[2];
#pragma unroll
    for (int u = 0; u < 2; ++u)
      wa[u] = GW_BASE + (m * 32 + 8 * h + q) * 256 + (((2 * (2 * c + u) + (pp >> 1)) ^ ((q << 2) | (m << 1))) << 4) + 8 * (pp & 1);
    f32x16 acc[2][5];
#pragma unroll
    for (int u = 0; u < 2; ++u)
#pragma unroll
      for (int rt = 0; rt < 5; ++rt) acc[u][rt] = f32x16{};
    v4u xf[2][5], wf[2][2];
    auto rd = [&](int n) {
      const int bq = n & 1, s = n >> 1, T = n >> 2;
#pragma unroll
      for (int u = 0; u < 2; ++u) {
        v2u lo = lds_rdtr(L + wa[u] + (s & 3) * GW_SLOT + (16 * (n & 1)) * 256);
        v2u hi = lds_rdtr(L + wa[u] + (s & 3) * GW_SLOT + (16 * (n & 1) + 4) * 256);
        wf[bq][u] = v4u{lo[0], lo[1], hi[0], hi[1]};
      }
#pragma unroll
      for (int rt = 0; rt < 5; ++rt) xf[bq][rt] = lds_rd128(L + xa[n & 3] + (T % 3) * GX_SLOT + rt * 4096);
    };
    STAMP(1);
    BARRIER();
    STAMP(2); CSTAMP(2);
    rd(0);
#pragma unroll
    for (int n = 0; n < 32; ++n) {
      SCHED();
      if (n > 0 && (n & 1) == 0) { BARRIER(); SCHED(); }
      if (n + 1 < 32) rd(n + 1);
#pragma unroll
      for (int rt = 0; rt < 5; ++rt)
#pragma unroll
        for (int u = 0; u < 2; ++u) acc[u][rt] = mfma32(wf[n & 1][u], xf[n & 1][rt], acc[u][rt]);
      if (n + 1 < 32) {
#pragma unroll
        for (int g = 0; g < 9; ++g) {
          __builtin_amdgcn_sched_group_barrier(0x8, 1, 0);
          __builtin_amdgcn_sched_group_barrier(0x100, 1, 0);
        }
        __builtin_amdgcn_sched_group_barrier(0x8, 1, 0);
      }
    }
    SCHED();
    STAMP(3); CSTAMP(3);
#pragma unroll
    for (int rt = 0; rt < 5; ++rt) {
      const int RT = 5 * pn + rt;
#pragma unroll
      for (int u = 0; u < 2; ++u) {
        float hv[8];
#pragma unroll
        for (int j = 0; j < 8; ++j) {
          const float e = __builtin_amdgcn_exp2f(acc[u][rt][j] * -1.44269504088896341f);
          hv[j] = acc[u][rt][8 + j] * __builtin_amdgcn_rcpf(1.f + e);
        }
        v4u o = {pk_bf16(hv[0], hv[1]), pk_bf16(hv[2], hv[3]), pk_bf16(hv[4], hv[5]), pk_bf16(hv[6], hv[7])};
        const int kap = 8 * cb + 2 * c + u;
        __builtin_amdgcn_raw_buffer_store_b128(o, ra2, lane * 16, ((RT >> 1) * NK + kap) * 2048 + (RT & 1) * 1024, 0);
      }
    }
    STAMP(4);
    return;
  }

  {
    const int cg = 4 * cb + c, ch = 32 * cg + i31;
    GAS unsigned* ep = (GAS unsigned*)(p.ws + WS_EPOCH + cg * 128);
    const unsigned tag = __hip_atomic_load(ep, __ATOMIC_RELAXED, __HIP_MEMORY_SCOPE_AGENT) + 1u;
    const float lr = bf2f(p.Lre[ch]), li = bf2f(p.Lim[ch]);
    float dt = expf(bf2f(p.ldt[ch]));
    dt = bf2f(pk_bf16(dt, 0.f) & 0xFFFFu);
    float sn, cs;
    sincosf(dt * li, &sn, &cs);
    const float ex = expf(dt * lr);
    const float ar = ex * cs, ai = ex * sn;
    const float den = 1.f / (lr * lr + li * li);
    const float cr = ((ar - 1.f) * lr + ai * li) * den, ci = (ai * lr - (ar - 1.f) * li) * den;
    float p32r = ar, p32i = ai;
#pragma unroll
    for (int z = 0; z < 5; ++z) { float t0, t1; cmul(p32r, p32i, p32r, p32i, t0, t1); p32r = t0; p32i = t1; }
    float p128r = p32r, p128i = p32i;
#pragma unroll
    for (int z = 0; z < 2; ++z) { float t0, t1; cmul(p128r, p128i, p128r, p128i, t0, t1); p128r = t0; p128i = t1; }

    f32x16 acc[2][4];
#pragma unroll
    for (int m = 0; m < 2; ++m)
#pragma unroll
      for (int rt = 0; rt < 4; ++rt) acc[m][rt] = f32x16{};
    v4u xf[2][4], bfr[2][2];
    auto rd = [&](int n) {
      const int bq = n & 1, T = n >> 2;
#pragma unroll
      for (int m = 0; m < 2; ++m) bfr[bq][m] = lds_rd128(L + SB_BASE + c * 4096 + xa[n & 3] + (T % 3) * SB_SLOT + m * 16384);
#pragma unroll
      for (int rt = 0; rt < 4; ++rt) xf[bq][rt] = lds_rd128(L + xa[n & 3] + (T % 3) * SX_SLOT + rt * 4096);
    };
    STAMP(1);
    BARRIER();
    STAMP(2); CSTAMP(2);
    rd(0);
#pragma unroll
    for (int n = 0; n < 32; ++n) {
      SCHED();
      if (n > 0 && (n & 1) == 0) { BARRIER(); SCHED(); }
      if (n + 1 < 32) rd(n + 1);
#pragma unroll
      for (int rt = 0; rt < 4; ++rt)
#pragma unroll
        for (int m = 0; m < 2; ++m) acc[m][rt] = mfma32(xf[n & 1][rt], bfr[n & 1][m], acc[m][rt]);
      if (n + 1 < 32) {
#pragma unroll
        for (int g = 0; g < 6; ++g) {
          __builtin_amdgcn_sched_group_barrier(0x8, 1, 0);
          __builtin_amdgcn_sched_group_barrier(0x100, 1, 0);
        }
        __builtin_amdgcn_sched_group_barrier(0x8, 2, 0);
      }
    }
    SCHED();
    STAMP(3); CSTAMP(3);
    const f32x2 a2 = {ar, ar}, b2 = {ai, ai}, nb2 = {-ai, -ai};
    f32x2 er = {0.f, 0.f}, ei = {0.f, 0.f};
#pragma unroll
    for (int rt = 0; rt < 4; ++rt)
#pragma unroll
      for (int z = 0; z < 8; ++z) {
        const f32x2 ur = {acc[0][rt][2 * z], acc[0][rt][2 * z + 1]}, ui = {acc[1][rt][2 * z], acc[1][rt][2 * z + 1]};
        f32x2 nr = __builtin_elementwise_fma(a2, er, ur), ni = __builtin_elementwise_fma(a2, ei, ui);
        nr = __builtin_elementwise_fma(nb2, ei, nr); ni = __builtin_elementwise_fma(b2, er, ni);
        er = nr; ei = ni;
      }
    const float o0r = __shfl_xor(er[0], 32), o0i = __shfl_xor(ei[0], 32), o1r = __shfl_xor(er[1], 32), o1i = __shfl_xor(ei[1], 32);
    const float e0r = h ? o0r : er[0], e0i = h ? o0i : ei[0], e1r = h ? o1r : er[1], e1i = h ? o1i : ei[1];
    const float e2r = h ? er[0] : o0r, e2i = h ? ei[0] : o0i, e3r = h ? er[1] : o1r, e3i = h ? ei[1] : o1i;
    rsrc_t rcar = mk_rsrc(p.ws + WS_CARRY, 16 * P_ * 16);
    {
      float tr, ti, Er, Ei;
      cmul(e0r, e0i, p32r, p32i, tr, ti); tr += e1r; ti += e1i;
      cmul(tr, ti, p32r, p32i, Er, Ei); Er += e2r; Ei += e2i;
      cmul(Er, Ei, p32r, p32i, tr, ti); tr += e3r; ti += e3i;
      if (h == 0) {
        v4u gq = {__float_as_uint(tr), tag, __float_as_uint(ti), tag};
        __builtin_amdgcn_raw_buffer_store_b128(gq, rcar, ch * 16, pn * (P_ * 16), AUX_SC1);
      }
    }
    STAMP(4);
    float Hr = 0.f, Hi = 0.f;
    if (pn > 0) {
      v4u cv[15];
      for (unsigned spins = 0; spins < (1u << 20); ++spins) {
        bool ok = true;
#pragma unroll
        for (int z = 0; z < 15; ++z) {
          const int zz = z < pn ? z : 0;
          cv[z] = __builtin_amdgcn_raw_buffer_load_b128(rcar, ch * 16, zz * (P_ * 16), AUX_SC1);
        }
#pragma unroll
        for (int z = 0; z < 15; ++z) ok = ok && cv[z][1] == tag && cv[z][3] == tag;
        if (__all(ok)) break;
        __builtin_amdgcn_s_sleep(2);
      }
#pragma unroll
      for (int z = 0; z < 15; ++z)
        if (z < pn) {
          float tr, ti;
          cmul(Hr, Hi, p128r, p128i, tr, ti);
          Hr = tr + __uint_as_float(cv[z][0]); Hi = ti + __uint_as_float(cv[z][2]);
        }
      if (pn == 15 && lane == 0) __hip_atomic_store(ep, tag, __ATOMIC_RELAXED, __HIP_MEMORY_SCOPE_AGENT);
    }
    STAMP(5);
    float c1r, c1i, c2r, c2i, c3r, c3i;
    cmul(Hr, Hi, p32r, p32i, c1r, c1i); c1r += e0r; c1i += e0i;
    cmul(c1r, c1i, p32r, p32i, c2r, c2i); c2r += e1r; c2i += e1i;
    cmul(c2r, c2i, p32r, p32i, c3r, c3i); c3r += e2r; c3i += e2i;
    f32x2 sr = {h ? c2r : Hr, h ? c3r : c1r}, si = {h ? c2i : Hi, h ? c3i : c1i};
    const f32x2 cr2 = {cr, cr}, nci2 = {-ci, -ci}, ncr2 = {-cr, -cr};
    LAS unsigned char* T = L + SB_BASE + (c >> 1) * (2 * SB_SLOT) + (c & 1) * 16384;
    const int swz = ((i31 & 3) << 2) | ((i31 >> 1) & 3);
#pragma unroll
    for (int rt = 0; rt < 4; ++rt) {
      f32x2 orr[8], oi[8];
#pragma unroll
      for (int z = 0; z < 8; ++z) {
        const f32x2 ur = {acc[0][rt][2 * z], acc[0][rt][2 * z + 1]}, ui = {acc[1][rt][2 * z], acc[1][rt][2 * z + 1]};
        f32x2 nr = __builtin_elementwise_fma(a2, sr, ur), ni = __builtin_elementwise_fma(a2, si, ui);
        nr = __builtin_elementwise_fma(nb2, si, nr); ni = __builtin_elementwise_fma(b2, sr, ni);
        sr = nr; si = ni;
        orr[z] = __builtin_elementwise_fma(nci2, si, cr2 * sr);
        oi[z] = __builtin_elementwise_fma(nci2, sr, ncr2 * si);
      }
#pragma unroll
      for (int par = 0; par < 2; ++par) {
        const int chunk = (2 * h + par) * 4 + rt;
        v4u vr = {pk_bf16(orr[0][par], orr[1][par]), pk_bf16(orr[2][par], orr[3][par]), pk_bf16(orr[4][par], orr[5][par]), pk_bf16(orr[6][par], orr[7][par])};
        v4u vi = {pk_bf16(oi[0][par], oi[1][par]), pk_bf16(oi[2][par], oi[3][par]), pk_bf16(oi[4][par], oi[5][par]), pk_bf16(oi[6][par], oi[7][par])};
        *(LAS v4u*)(T + i31 * 256 + ((chunk ^ swz) << 4)) = vr;
        *(LAS v4u*)(T + (32 + i31) * 256 + ((chunk ^ swz) << 4)) = vi;
      }
    }
    {
      const int th = (lane >> 4) & 1, q = (lane & 15) >> 2, pp = lane & 3;
#pragma unroll
      for (int sg = 0; sg < 4; ++sg)
#pragma unroll
        for (int mk = 0; mk < 4; ++mk) {
          v2u part[2];
#pragma unroll
          for (int t = 0; t < 2; ++t) {
            const int row = 16 * mk + 8 * h + 4 * t + q;
            const int chunk = 4 * sg + 2 * th + (pp >> 1);
            part[t] = lds_rdtr(T + row * 256 + ((chunk ^ ((q << 2) | (2 * t + (q >> 1)))) << 4) + 8 * (pp & 1));
          }
          const int RT = 4 * pn + sg, kap = 112 + 4 * cg + mk;
          v4u o = {part[0][0], part[0][1], part[1][0], part[1][1]};
          __builtin_amdgcn_raw_buffer_store_b128(o, ra2, lane * 16, ((RT >> 1) * NK + kap) * 2048 + (RT & 1) * 1024, 0);
        }
    }
    STAMP(6);
  }
}

__global__ __launch_bounds__(512, 2) void k_phase2(Params p) {
  extern __shared__ __attribute__((aligned(16))) unsigned char lds_[];
  LAS unsigned char* L = (LAS unsigned char*)lds_;
  const int b = blockIdx.x, tid = threadIdx.x, lane = tid & 63;
  const int w = __builtin_amdgcn_readfirstlane(tid >> 6);
  const int xcd = b & 7, r = b >> 3;
  const int R = xcd * 4 + (r & 3), N = r >> 2;
  STAMP(8); CSTAMP(8);
  const int k0 = w == 0 ? 0 : w == 1 ? 23 : w == 2 ? 46 : w == 3 ? 68 : w == 4 ? 112 : w == 5 ? 133 : w == 6 ? 154 : 90;
  const int nk = w < 2 ? 23 : (w == 4 || w == 5) ? 21 : 22;
  rsrc_t ra = mk_rsrc(p.ws + WS_A2 + (R * NK + k0) * 2048, nk * 2048);
  rsrc_t rb = mk_rsrc(p.ws + WS_B2 + (N * NK + k0) * 2048, nk * 2048);
  const int rot = ((N + 2 * (r & 3)) & 7) * 21 / 8;
  const int tok = 64 * R + (tid >> 3), chunk = tid & 7;
  rsrc_t rx = mk_rsrc(p.x, S_ * D_ * 2), rdk = mk_rsrc(p.Dsk, D_ * 2);
  const v4u xv = __builtin_amdgcn_raw_buffer_load_b128(rx, tok * 1024 + (64 * N + 8 * chunk) * 2, 0, 0);
  const v4u dv = __builtin_amdgcn_raw_buffer_load_b128(rdk, (64 * N + 8 * chunk) * 2, 0, 0);
  const float g = ((const float*)(p.ws + WS_G))[tok];

  constexpr int W = 6;
  v4u af[W][2], bf[W][2];
  auto so = [&](int i) { int t = i + rot; t = t >= nk ? t - nk : t; return i < nk ? t * 2048 : (1 << 20); };
  auto ld = [&](int i) {
    const int o = so(i);
#pragma unroll
    for (int e = 0; e < 2; ++e) {
      af[i % W][e] = __builtin_amdgcn_raw_buffer_load_b128(ra, lane * 16 + e * 1024, o, 0);
      bf[i % W][e] = __builtin_amdgcn_raw_buffer_load_b128(rb, lane * 16 + e * 1024, o, 0);
    }
  };
#pragma unroll
  for (int i = 0; i < W; ++i) ld(i);
  SCHED();
  f32x16 acc[2][2];
#pragma unroll
  for (int e = 0; e < 2; ++e)
#pragma unroll
    for (int rt = 0; rt < 2; ++rt) acc[e][rt] = f32x16{};
#pragma unroll
  for (int i = 0; i < 23; ++i) {
#pragma unroll
    for (int e = 0; e < 2; ++e)
#pragma unroll
      for (int rt = 0; rt < 2; ++rt) acc[e][rt] = mfma32(bf[i % W][e], af[i % W][rt], acc[e][rt]);
    SCHED();
    if (i + W < 23) ld(i + W);
    SCHED();
  }
  STAMP(9);
  const int h = lane >> 5;
#pragma unroll
  for (int rt = 0; rt < 2; ++rt)
#pragma unroll
    for (int e = 0; e < 2; ++e)
#pragma unroll
      for (int gq = 0; gq < 4; ++gq) {
        f32x4 v = {acc[e][rt][4 * gq], acc[e][rt][4 * gq + 1], acc[e][rt][4 * gq + 2], acc[e][rt][4 * gq + 3]};
        const int pos = lane ^ (2 * (2 * gq + h));
        *(LAS f32x4*)(L + w * 16384 + (rt * 8 + e * 4 + gq) * 1024 + pos * 16) = v;
      }
  __syncthreads();
  {
    const int t = tid >> 3, rt = t >> 5, gq = chunk >> 1, hh = chunk & 1;
    const int pos = ((t & 31) + 32 * hh) ^ (2 * chunk);
    f32x4 sg[2] = {{0.f, 0.f, 0.f, 0.f}, {0.f, 0.f, 0.f, 0.f}}, ss[2] = {{0.f, 0.f, 0.f, 0.f}, {0.f, 0.f, 0.f, 0.f}};
#pragma unroll
    for (int ww = 0; ww < 8; ++ww)
#pragma unroll
      for (int e = 0; e < 2; ++e) {
        const f32x4 v = *(LAS f32x4*)(L + ww * 16384 + (rt * 8 + e * 4 + gq) * 1024 + pos * 16);
        if (ww >= 4 && ww <= 6) ss[e] += v; else sg[e] += v;
      }
    float o[8];
#pragma unroll
    for (int q = 0; q < 4; ++q) {
      const float x0 = bf2f(xv[q] & 0xFFFFu), x1 = bfhi(xv[q]), d0 = bf2f(dv[q] & 0xFFFFu), d1 = bfhi(dv[q]);
      o[2 * q] = g * (2.f * ss[0][q] + x0 * d0) + (1.f - g) * sg[0][q];
      o[2 * q + 1] = g * (2.f * ss[1][q] + x1 * d1) + (1.f - g) * sg[1][q];
    }
    rsrc_t ro = mk_rsrc(p.out, S_ * D_ * 4);
    const int oo = tok * 2048 + (64 * N + 8 * chunk) * 4;
    v4u o0 = {__float_as_uint(o[0]), __float_as_uint(o[1]), __float_as_uint(o[2]), __float_as_uint(o[3])};
    v4u o1 = {__float_as_uint(o[4]), __float_as_uint(o[5]), __float_as_uint(o[6]), __float_as_uint(o[7])};
    __builtin_amdgcn_raw_buffer_store_b128(o0, ro, oo, 0, 0);
    __builtin_amdgcn_raw_buffer_store_b128(o1, ro, oo + 16, 0, 0);
  }
  STAMP(10); CSTAMP(10);
}

extern "C" void kernel_launch(void* const* d_in, const int* in_sizes, int n_in, void* d_out, int out_size, void* d_ws, size_t ws_size,
                              hipStream_t stream) {
  Params p;
  p.x = (const u16*)d_in[0]; p.Lre = (const u16*)d_in[1]; p.Lim = (const u16*)d_in[2]; p.Bre = (const u16*)d_in[3];
  p.Bim = (const u16*)d_in[4]; p.Cre = (const u16*)d_in[5]; p.Cim = (const u16*)d_in[6]; p.ldt = (const u16*)d_in[7];
  p.Dsk = (const u16*)d_in[8]; p.Wg = (const u16*)d_in[10]; p.Wu = (const u16*)d_in[11]; p.Wd = (const u16*)d_in[12];
  p.gw = (const u16*)d_in[13]; p.out = (float*)d_out; p.ws = (unsigned char*)d_ws;
  (void)hipFuncSetAttribute((const void*)k_phase1, hipFuncAttributeMaxDynamicSharedMemorySize, LDS_BYTES);
  (void)hipFuncSetAttribute((const void*)k_phase2, hipFuncAttributeMaxDynamicSharedMemorySize, LDS_BYTES);
  k_phase1<<<256, 512, LDS_BYTES, stream>>>(p);
  k_phase2<<<256, 512, 128 * 1024, stream>>>(p);
}
```

```cpp
#include <hip/hip_runtime.h>
#include <stdint.h>

typedef unsigned short u16;
typedef unsigned long long u64;
typedef unsigned v4u __attribute__((ext_vector_type(4)));
typedef unsigned v2u __attribute__((ext_vector_type(2)));
typedef short s16x4 __attribute__((ext_vector_type(4)));
typedef __bf16 bf16x8 __attribute__((ext_vector_type(8)));
typedef __bf16 bf16x2 __attribute__((ext_vector_type(2)));
typedef float f32x2 __attribute__((ext_vector_type(2)));
typedef float f32x4 __attribute__((ext_vector_type(4)));
typedef float f32x16 __attribute__((ext_vector_type(16)));
#define LAS __attribute__((address_space(3)))
#define GAS __attribute__((address_space(1)))
#define RLX_AGENT __ATOMIC_RELAXED, __HIP_MEMORY_SCOPE_AGENT
typedef __amdgpu_buffer_rsrc_t rsrc_t;

constexpr int S_ = 2048, D_ = 512, P_ = 512, F_ = 1792;
constexpr int NK = 88;
constexpr int AUX_SC1 = 16;
constexpr int WS_EPOCH = 0;
constexpr int WS_FB2 = 4096;
constexpr int WS_FA2 = 16384;
constexpr int WS_G = 32768;
constexpr int WS_CARRY = 65536;
constexpr int WS_B2 = 262144;
constexpr int B2_BYTES = 8 * NK * 4096;
constexpr int WS_A2 = 4 << 20;
constexpr int A2_BYTES = 32 * NK * 4096;
constexpr int WS_STAMP = 32 << 20;
constexpr int LDS_BYTES = 160 * 1024;
constexpr int LDS_TAG = LDS_BYTES - 16, LDS_RDY = LDS_BYTES - 8, LDS_DONE = LDS_BYTES - 32;
constexpr int OOB = 1 << 28;
constexpr int CTRL_BYTES = 32768;
constexpr int CODE_BYTES = 42 * 1024;
constexpr unsigned SPIN_CAP = 1u << 18;

struct Params {
  const u16 *x, *Wg, *Wu, *Bre, *Bim, *Cre, *Cim, *Wd;
  unsigned char* ws;
  const u16 *Lre, *Lim, *ldt, *Dsk, *gw;
  float* out;
  u64 pad;
  unsigned map[256];
};

__device__ __forceinline__ rsrc_t mk_rsrc(const void* p, int bytes) {
  return __builtin_amdgcn_make_buffer_rsrc((void*)p, 0, bytes, 0x00020000);
}
__device__ __forceinline__ float bf2f(unsigned v) { return __uint_as_float(v << 16); }
__device__ __forceinline__ float bfhi(unsigned v) { return __uint_as_float(v & 0xFFFF0000u); }
__device__ __forceinline__ unsigned pk_bf16(float lo, float hi) {
  f32x2 t = {lo, hi};
  return __builtin_bit_cast(unsigned, __builtin_convertvector(t, bf16x2));
}
__device__ __forceinline__ f32x16 mfma32(v4u a, v4u b, f32x16 c) {
  return __builtin_amdgcn_mfma_f32_32x32x16_bf16(__builtin_bit_cast(bf16x8, a), __builtin_bit_cast(bf16x8, b), c, 0, 0, 0);
}
__device__ __forceinline__ f32x4 mfma16(v4u a, v4u b, f32x4 c) {
  return __builtin_amdgcn_mfma_f32_16x16x32_bf16(__builtin_bit_cast(bf16x8, a), __builtin_bit_cast(bf16x8, b), c, 0, 0, 0);
}
__device__ __forceinline__ v4u lds_rd128(LAS unsigned char* p) { return *(LAS v4u*)p; }
__device__ __forceinline__ v2u lds_rdtr(LAS unsigned char* p) {
  return __builtin_bit_cast(v2u, __builtin_amdgcn_ds_read_tr16_b64_v4i16((LAS s16x4*)p));
}
__device__ __forceinline__ void glds(rsrc_t rs, LAS unsigned char* dst, int voff, int soff) {
  __builtin_amdgcn_raw_ptr_buffer_load_lds(rs, (LAS void*)dst, 16, voff, soff, 0, 0);
}
__device__ __forceinline__ v4u ld_sc1(rsrc_t rs, int voff, int soff) { return __builtin_amdgcn_raw_buffer_load_b128(rs, voff, soff, AUX_SC1); }
__device__ __forceinline__ void st_sc1(v4u v, rsrc_t rs, int voff, int soff) { __builtin_amdgcn_raw_buffer_store_b128(v, rs, voff, soff, AUX_SC1); }
__device__ __forceinline__ u64 tag64(unsigned tag) { return ((u64)(~tag) << 32) | tag; }
__device__ __forceinline__ void st_flag(unsigned char* ws, int off, unsigned tag, bool on) {
  __builtin_amdgcn_raw_buffer_store_b64(v2u{tag, ~tag}, mk_rsrc(ws, CTRL_BYTES), on ? 0 : OOB, off, AUX_SC1);
}
__device__ __forceinline__ unsigned lds_word(LAS unsigned char* p) { return __builtin_amdgcn_readfirstlane(*(volatile LAS unsigned*)p); }
#define BARRIER() asm volatile("s_barrier" ::: "memory")
#define SCHED() __builtin_amdgcn_sched_barrier(0)
#define CFENCE() asm volatile("" ::: "memory")
template <int N> __device__ __forceinline__ void wait_vm() { asm volatile("s_waitcnt vmcnt(%c0)" ::"i"(N) : "memory"); }

#define STAMP(i) do {} while (0)

__device__ __forceinline__ void poll_flags(GAS u64* f, bool valid, u64 want) {
#pragma nounroll
  for (unsigned spins = 0; spins < SPIN_CAP; ++spins) {
    CFENCE();
    const u64 v = __hip_atomic_load(f, RLX_AGENT);
    if (__all(!valid || v == want)) break;
    __builtin_amdgcn_s_sleep(4);
  }
  CFENCE();
}

constexpr int NB = 8;
constexpr int S_RT = 3;
constexpr int S_CHK = 9;
#define NROT 2
struct Sched {
  int n;
  int type[40], idx[40];
  int lo[17], hi[17];
  int b2lo[17], b2hi[17];
  int wait[17];
};
constexpr Sched make_sched(int kind) {
  Sched s{};
  int cnt[40] = {}, need[40] = {}, avail[40] = {};
  int n = 0;
  auto add = [&](int ty, int ix, int c, int nd, int av) { s.type[n] = ty; s.idx[n] = ix; cnt[n] = c; need[n] = nd; avail[n] = av; ++n; };
  if (kind == 0) {
    add(0, 0, 3, 0, -1); add(1, 0, 2, 0, -1); add(2, 0, 4, 0, -1); add(2, 1, 4, 0, -1);
    add(0, 1, 3, 1, -1); add(1, 1, 2, 1, -1); add(2, 2, 4, 1, -1); add(2, 3, 4, 2, -1);
    add(0, 2, 3, 3, -1); add(1, 2, 2, 3, -1);
    for (int t = 4; t < 16; ++t) {
      add(2, t, 4, t - 1, t - 3);
      const int b = t - 3, T = b / 2 + 2;
      if (b >= 2 && T <= 7) add(b & 1, T, (b & 1) ? 2 : 3, 2 * T - 1, b);
    }
  } else {
    for (int T = 0; T < 8; ++T)
      for (int ty = 0; ty < 2; ++ty) add(ty, T, 6, T == 0 ? 0 : 2 * T - 1, T < 3 ? -1 : 2 * T - 4 + ty);
  }
  s.n = n;
  const int LIM = 8;
  int endpos[40] = {};
  int next = 0, total = 0, b2 = 0;
  for (int b = 0; b <= 16; ++b) {
    s.lo[b] = next;
    for (;;) {
      if (next >= n || avail[next] > b - 1) break;
      int lastneed = 0;
      for (int i = 0; i < next; ++i) if (need[i] <= b) lastneed = endpos[i];
      if (b < 4 && need[next] > b && total + cnt[next] - lastneed > LIM + 2 * b) break;
      total += cnt[next]; endpos[next] = total; ++next;
    }
    s.hi[b] = next;
    s.b2lo[b] = b2;
    if (b >= S_CHK + 2 && b2 < NB) { ++b2; total += 4; }
    if (b >= 14 && b2 < NB) { ++b2; total += 4; }
    if (b == 16) { total += 4 * (NB - b2); b2 = NB; }
    s.b2hi[b] = b2;
    int lastneed = 0;
    for (int i = 0; i < next; ++i) if (need[i] <= b) lastneed = endpos[i];
    for (int i = next; i < n; ++i) if (need[i] <= b) lastneed = 1 << 20;
    s.wait[b] = total - lastneed;
    if (s.wait[b] > 63) s.wait[b] = 63;
  }
  return s;
}
constexpr bool sched_ok(const Sched& s) { for (int b = 0; b <= 16; ++b) if (s.wait[b] < 0) return false; return s.hi[16] == s.n; }
constexpr Sched SCH[2] = {make_sched(0), make_sched(1)};
static_assert(sched_ok(SCH[0]) && sched_ok(SCH[1]), "every ring item must be issued in time");

constexpr int GX_SLOT = 20480, GW_BASE = 61440, GW_SLOT = 16384;
constexpr int SX_SLOT = 16384, SB_BASE = 49152, SB_SLOT = 32768;

struct P2Ctx {
  rsrc_t rb;
  int nk, rot, lane16;
};
__device__ __forceinline__ int p2_off(const P2Ctx& c, int i) {
  int t = i + c.rot; t = t >= c.nk ? t - c.nk : t;
  return i < c.nk ? t * 4096 : OOB;
}
__device__ __forceinline__ void ld_b2(const P2Ctx& c, v4u (&b2)[NB][4], int i) {
  const int o = p2_off(c, i);
#pragma unroll
  for (int e = 0; e < 4; ++e) b2[i % NB][e] = ld_sc1(c.rb, c.lane16 + e * 1024, o);
}

__device__ __forceinline__ void retile_load(const Params& p, int unit, int lane, v4u (&f)[4]) {
  const int N = unit / NK, kap = unit - N * NK, q = lane >> 4, i = lane & 15;
  if (kap < 56) {
    rsrc_t rd = mk_rsrc(p.Wd, F_ * D_ * 2);
#pragma unroll
    for (int j = 0; j < 8; ++j) {
      const v2u t = __builtin_amdgcn_raw_buffer_load_b64(rd, (4 * q + (j & 3) + 16 * (j >> 2)) * (D_ * 2) + i * 8, kap * (32 * D_ * 2) + N * 128, 0);
      f[j >> 1][2 * (j & 1)] = t[0]; f[j >> 1][2 * (j & 1) + 1] = t[1];
    }
  } else {
    const int cg = (kap - 56) >> 1, m = kap & 1;
    rsrc_t rc = mk_rsrc(m ? p.Cim : p.Cre, D_ * P_ * 2);
#pragma unroll
    for (int e = 0; e < 4; ++e) f[e] = __builtin_amdgcn_raw_buffer_load_b128(rc, (4 * i + e) * (P_ * 2) + q * 16, N * (64 * P_ * 2) + cg * 64, 0);
  }
}
__device__ __forceinline__ void retile_store(const Params& p, int unit, int lane, const v4u (&f)[4]) {
  const int kap = unit % NK;
  rsrc_t rws = mk_rsrc(p.ws + WS_B2, B2_BYTES);
  if (kap < 56) {
#pragma unroll
    for (int e = 0; e < 4; ++e) {
      v4u o;
#pragma unroll
      for (int z = 0; z < 4; ++z) o[z] = __builtin_amdgcn_perm(f[z][2 + (e >> 1)], f[z][e >> 1], (e & 1) ? 0x07060302u : 0x05040100u);
      st_sc1(o, rws, lane * 16 + e * 1024, unit * 4096);
    }
  } else {
#pragma unroll
    for (int e = 0; e < 4; ++e) st_sc1(f[e], rws, lane * 16 + e * 1024, unit * 4096);
  }
}

struct LdCtx { rsrc_t rx, rw; int lw, vx, vw0, vw1, sx0, sw0, rot; GAS u64* fb2; int unit, lane; mutable unsigned sv; };

template <int KIND, int I> __device__ __forceinline__ void ld_item(const LdCtx& c, LAS unsigned char* L) {
  constexpr int ty = SCH[KIND].type[I], t = SCH[KIND].idx[I];
  if constexpr (KIND == 0 && ty < 2) {
    const int k0 = ((t + c.rot) & 7) * 128;
#pragma unroll
    for (int n = 3 * ty; n < 3 + 2 * ty; ++n) glds(c.rx, L + (t % 3) * GX_SLOT + c.lw * 1024 + n * 4096, c.vx, c.sx0 + k0 + n * 32768);
  } else if constexpr (KIND == 0) {
    const int k0 = ((((t >> 1) + c.rot) & 7) * 64 + (t & 1) * 32) * (F_ * 2);
#pragma unroll
    for (int i = 0; i < 4; ++i)
      glds(c.rw, L + GW_BASE + (t & 3) * GW_SLOT + c.lw * 4096 + i * 1024, (i >> 1) ? c.vw1 : c.vw0, c.sw0 + k0 + i * 4 * (F_ * 2));
  } else {
    const int k0 = ((t + c.rot) & 7) * 128;
#pragma unroll
    for (int n = 2 * ty; n < 2 * ty + 2; ++n) glds(c.rx, L + (t % 3) * SX_SLOT + c.lw * 1024 + n * 4096, c.vx, c.sx0 + k0 + n * 8192);
#pragma unroll
    for (int n = 4 * ty; n < 4 * ty + 4; ++n)
      glds(c.rw, L + SB_BASE + (t % 3) * SB_SLOT + (c.lw >> 1) * 16384 + (c.lw & 1) * 1024 + n * 2048, c.vw0, c.sw0 + k0 + n * 16384);
  }
  SCHED();
}
template <int KIND, int LO, int HI> __device__ __forceinline__ void ld_items(const LdCtx& c, LAS unsigned char* L) {
  if constexpr (LO < HI) { ld_item<KIND, LO>(c, L); ld_items<KIND, LO + 1, HI>(c, L); }
}
template <int KIND, int B>
__device__ __forceinline__ void loader_steps(const Params& p, const LdCtx& c, LAS unsigned char* L, const P2Ctx& pc, v4u (&b2)[NB][4], v4u (&rt)[4],
                                             unsigned& tag) {
  if constexpr (B == S_RT) { if (c.unit < 8 * NK) retile_load(p, c.unit, c.lane, rt); SCHED(); }
  if constexpr (B == S_RT + 2) { if (c.unit < 8 * NK) retile_store(p, c.unit, c.lane, rt); SCHED(); }
  if constexpr (B == S_RT + 4) {
    tag = lds_word(L + LDS_TAG);
    wait_vm<0>();
    st_flag(p.ws, WS_FB2 + c.unit * 8, tag, c.unit < 8 * NK && c.lane == 0);
    SCHED();
  }
  if constexpr (B == S_CHK + 1) poll_flags(c.fb2, true, tag64(tag));
  if constexpr (B > 0) ld_items<KIND, SCH[KIND].lo[B], SCH[KIND].hi[B]>(c, L);
#pragma unroll
  for (int i = SCH[KIND].b2lo[B]; i < SCH[KIND].b2hi[B]; ++i) ld_b2(pc, b2, i);
  SCHED();
  wait_vm<SCH[KIND].wait[B]>();
  BARRIER();
  if constexpr (B < 16) loader_steps<KIND, B + 1>(p, c, L, pc, b2, rt, tag);
}
__device__ __forceinline__ bool all_done(LAS unsigned char* L) {
  const v4u d = *(volatile LAS v4u*)(L + LDS_DONE);
  return __builtin_amdgcn_readfirstlane(d[0] & d[1] & d[2] & d[3]) != 0u;
}
__device__ __forceinline__ void publish_block(LAS unsigned char* L, unsigned char* ws, int off, unsigned tag, int c, int lane) {
  wait_vm<0>();
  *(volatile LAS unsigned*)(L + LDS_DONE + 4 * c) = 1u;
  const bool last = all_done(L);
  st_flag(ws, off, tag, last && lane == 0);
  CFENCE();
}
__device__ __forceinline__ void lds_wait(LAS unsigned char* word) {
#pragma nounroll
  for (unsigned spins = 0; spins < (SPIN_CAP << 4); ++spins) {
    if (lds_word(word) != 0u) break;
    __builtin_amdgcn_s_sleep(1);
  }
  CFENCE();
}

__device__ __forceinline__ void cmul(float ar, float ai, float br, float bi, float& cr, float& ci) {
  cr = ar * br - ai * bi; ci = ar * bi + ai * br;
}

__global__ __launch_bounds__(512, 2) void k_fused(Params p) {
  extern __shared__ __attribute__((aligned(16))) unsigned char lds_[];
  LAS unsigned char* L = (LAS unsigned char*)lds_;
  STAMP(0);
  const int b = blockIdx.x, tid = threadIdx.x, lane = tid & 63;
  const int w = __builtin_amdgcn_readfirstlane(tid >> 6);
  const int xcd = b & 7, r = b >> 3;
  const unsigned mp = p.map[b];
  unsigned zpf = 0;
  if (w < 4) {
    const u64 pcv = __builtin_amdgcn_s_getpc();
    rsrc_t rcode = mk_rsrc((const void*)(pcv & ~(u64)127), CODE_BYTES);
    zpf = __builtin_amdgcn_raw_buffer_load_b32(rcode, lane < 4 ? lane * 16384 : OOB, (r * 4 + w) * 128, 0);
  }
  const int kind = mp & 3, pn = (mp >> 2) & 63, cb = mp >> 8;
  const int rot = (8 / NROT) * ((pn + cb) & (NROT - 1));
  const int R = xcd * 4 + (r & 3), N = r >> 2;
  const int k0 = (int)(0x22170C002D4D4238ull >> (8 * w)) & 255;
  P2Ctx pc;
  pc.nk = (0xBBBCBBBAu >> (4 * w)) & 15;
  pc.rot = (N * 5) >> 2;
  pc.lane16 = lane * 16;
  pc.rb = mk_rsrc(p.ws + WS_B2 + (N * NK + k0) * 4096, pc.nk * 4096);
  rsrc_t rx = mk_rsrc(p.x, S_ * D_ * 2);
  v4u b2[NB][4];
  unsigned tag;

  if (__builtin_expect(w >= 4 && kind != 2, 1)) {
    LdCtx c;
    c.rx = rx; c.lw = w - 4; c.rot = rot;
    const int rr = 8 * c.lw + (lane >> 3);
    const int sw = ((lane & 7) ^ ((rr >> 1) & 7)) << 4;
    if (kind == 0) {
      const int m = c.lw >> 1, kq = lane >> 4;
      c.vx = rr * 1024 + sw;
      c.sx0 = pn * 160 * 1024;
      c.rw = mk_rsrc(m ? p.Wu : p.Wg, D_ * F_ * 2);
      c.vw0 = ((c.lw & 1) * 16 + kq) * (F_ * 2) + (((lane & 15) ^ (kq << 2)) << 4);
      c.vw1 = c.vw0 ^ 32;
      c.sw0 = cb * 256;
      STAMP(1);
      ld_items<0, 0, SCH[0].hi[0]>(c, L);
    } else {
      const int tok = 64 * ((rr >> 2) & 1) + 32 * (rr & 1) + ((rr >> 1) & 1) + 2 * (rr >> 3);
      c.vx = tok * 1024 + sw;
      c.sx0 = pn * 128 * 1024;
      c.rw = mk_rsrc((c.lw >> 1) ? p.Bim : p.Bre, P_ * D_ * 2);
      const int brow = 8 * (c.lw & 1) + (lane >> 3);
      c.vw0 = brow * 1024 + (((lane & 7) ^ ((brow >> 1) & 7)) << 4);
      c.vw1 = 0;
      c.sw0 = cb * 128 * 1024;
      STAMP(1);
      ld_items<1, 0, SCH[1].hi[0]>(c, L);
    }
    STAMP(2);
    int kind2 = kind;
    asm volatile("" : "+s"(kind2));
    c.fb2 = (GAS u64*)(p.ws + WS_FB2) + N * NK + c.lw * 22 + (lane < 22 ? lane : 0);
    c.unit = c.lw * 256 + b; c.lane = lane; c.sv = 0;
    v4u rt[4];
    if (kind2 == 0) loader_steps<0, 0>(p, c, L, pc, b2, rt, tag);
    else loader_steps<1, 0>(p, c, L, pc, b2, rt, tag);
    STAMP(3);
  } else {
    if (w == 3) {
      const unsigned e = __hip_atomic_load((GAS unsigned*)(p.ws + WS_EPOCH), RLX_AGENT) + 1u;
      *(volatile LAS v4u*)(L + LDS_TAG) = v4u{e, 0u, 0u, 0u};
      *(volatile LAS v4u*)(L + LDS_DONE) = v4u{0u, 0u, 0u, 0u};
      asm volatile("s_waitcnt lgkmcnt(0)" ::: "memory");
    }
    asm volatile("" ::"v"(zpf));
    rsrc_t ra2 = mk_rsrc(p.ws + WS_A2, A2_BYTES);
    const int fa2 = WS_FA2 + ((kind * 16 + pn) * 16 + cb) * 8;
    const int c = w;

    if (kind == 0) {
      const int q = lane >> 4, i15 = lane & 15;
      int xa[2], wa[2];
#pragma unroll
      for (int ks = 0; ks < 2; ++ks) xa[ks] = i15 * 128 + (((4 * ks + q) ^ ((i15 >> 1) & 7)) << 4);
      {
        const int qq = i15 >> 2, pp = lane & 3;
#pragma unroll
        for (int u = 0; u < 2; ++u)
          wa[u] = GW_BASE + (8 * q + qq) * 256 + (((2 * (2 * c + u) + (pp >> 1)) ^ ((qq << 2) | ((q & 1) << 1))) << 4) + 8 * (pp & 1);
      }
      f32x4 acc[4][10];
#pragma unroll
      for (int a = 0; a < 4; ++a)
#pragma unroll
        for (int tt = 0; tt < 10; ++tt) acc[a][tt] = f32x4{0.f, 0.f, 0.f, 0.f};
      v4u xf[4];
      v2u wf[2][8];
      auto rdx = [&](int it) {
        const int s = it / 10, tt = it % 10;
        xf[it & 3] = lds_rd128(L + xa[s & 1] + ((s >> 1) % 3) * GX_SLOT + tt * 2048);
      };
      auto rdw = [&](int s, int z) {
        const int m = z >> 2, u = (z >> 1) & 1, t = z & 1;
        wf[s & 1][z] = lds_rdtr(L + wa[u] + (s & 3) * GW_SLOT + m * 8192 + t * 1024);
      };
      STAMP(1);
      BARRIER();
      STAMP(2);
      tag = lds_word(L + LDS_TAG);
#pragma unroll
      for (int z = 0; z < 8; ++z) rdw(0, z);
      rdx(0); rdx(1); rdx(2);
#pragma clang loop unroll(full)
      for (int s = 0; s < 16; ++s)
#pragma clang loop unroll(full)
      for (int tt = 0; tt < 10; ++tt) {
        const int it = 10 * s + tt;
        SCHED();
        if (tt == 0 && s > 0) { BARRIER(); SCHED(); }
        if (it + 3 < 160) rdx(it + 3);
        if (tt < 8 && s < 15) rdw(s + 1, tt);
#pragma unroll
        for (int a = 0; a < 4; ++a) {
          const v4u wv = {wf[s & 1][2 * a][0], wf[s & 1][2 * a][1], wf[s & 1][2 * a + 1][0], wf[s & 1][2 * a + 1][1]};
          acc[a][tt] = mfma16(wv, xf[it & 3], acc[a][tt]);
        }
        __builtin_amdgcn_sched_group_barrier(0x8, 1, 0);
        __builtin_amdgcn_sched_group_barrier(0x100, 1, 0);
        __builtin_amdgcn_sched_group_barrier(0x8, 1, 0);
        __builtin_amdgcn_sched_group_barrier(0x100, 1, 0);
        __builtin_amdgcn_sched_group_barrier(0x8, 2, 0);
      }
      SCHED();
      BARRIER();
      STAMP(3);
      const int kap = 4 * cb + c;
#pragma unroll
      for (int tt = 0; tt < 10; ++tt) {
        float hv[8];
#pragma unroll
        for (int j = 0; j < 8; ++j) {
          const float e = __builtin_amdgcn_exp2f(acc[j >> 2][tt][j & 3] * -1.44269504088896341f);
          hv[j] = acc[2 + (j >> 2)][tt][j & 3] * __builtin_amdgcn_rcpf(1.f + e);
        }
        const v4u o = {pk_bf16(hv[0], hv[1]), pk_bf16(hv[2], hv[3]), pk_bf16(hv[4], hv[5]), pk_bf16(hv[6], hv[7])};
        const int TT = 10 * pn + tt;
        st_sc1(o, ra2, lane * 16, ((TT >> 2) * NK + kap) * 4096 + (TT & 3) * 1024);
      }
      SCHED();
      STAMP(4);
      publish_block(L, p.ws, fa2, tag, c, lane);
      STAMP(5);
#pragma unroll
      for (int i = 0; i < NB; ++i) ld_b2(pc, b2, i);
      SCHED();
    } else if (kind == 1) {
      const int h = lane >> 5, i31 = lane & 31;
      int xa[4];
#pragma unroll
      for (int ks = 0; ks < 4; ++ks) xa[ks] = i31 * 128 + (((2 * ks + h) ^ ((i31 >> 1) & 7)) << 4);
      const int cg = 4 * cb + c, ch = 32 * cg + i31;
      const float lr = bf2f(p.Lre[ch]), li = bf2f(p.Lim[ch]);
      float dt = expf(bf2f(p.ldt[ch]));
      dt = bf2f(pk_bf16(dt, 0.f) & 0xFFFFu);
      float sn, cs;
      sincosf(dt * li, &sn, &cs);
      const float ex = expf(dt * lr);
      const float ar = ex * cs, ai = ex * sn;
      const float den = 1.f / (lr * lr + li * li);
      const float cr = ((ar - 1.f) * lr + ai * li) * den, ci = (ai * lr - (ar - 1.f) * li) * den;
      float p32r = ar, p32i = ai;
#pragma unroll
      for (int z = 0; z < 5; ++z) { float t0, t1; cmul(p32r, p32i, p32r, p32i, t0, t1); p32r = t0; p32i = t1; }
      float p128r = p32r, p128i = p32i;
#pragma unroll
      for (int z = 0; z < 2; ++z) { float t0, t1; cmul(p128r, p128i, p128r, p128i, t0, t1); p128r = t0; p128i = t1; }

      f32x16 acc[2][4];
#pragma unroll
      for (int m = 0; m < 2; ++m)
#pragma unroll
        for (int rt = 0; rt < 4; ++rt) acc[m][rt] = f32x16{};
      v4u xf[2][4], bfr[2][2];
      auto rd = [&](int n) {
        const int bq = n & 1, T = n >> 2;
#pragma unroll
        for (int m = 0; m < 2; ++m) bfr[bq][m] = lds_rd128(L + SB_BASE + c * 4096 + xa[n & 3] + (T % 3) * SB_SLOT + m * 16384);
#pragma unroll
        for (int rt = 0; rt < 4; ++rt) xf[bq][rt] = lds_rd128(L + xa[n & 3] + (T % 3) * SX_SLOT + rt * 4096);
      };
      STAMP(1);
      BARRIER();
      STAMP(2);
      tag = lds_word(L + LDS_TAG);
      rd(0);
#pragma unroll
      for (int n = 0; n < 32; ++n) {
        SCHED();
        if (n > 0 && (n & 1) == 0) { BARRIER(); SCHED(); }
        if (n + 1 < 32) rd(n + 1);
#pragma unroll
        for (int rt = 0; rt < 4; ++rt)
#pragma unroll
          for (int m = 0; m < 2; ++m) acc[m][rt] = mfma32(xf[n & 1][rt], bfr[n & 1][m], acc[m][rt]);
        if (n + 1 < 32) {
#pragma unroll
          for (int g = 0; g < 6; ++g) {
            __builtin_amdgcn_sched_group_barrier(0x8, 1, 0);
            __builtin_amdgcn_sched_group_barrier(0x100, 1, 0);
          }
          __builtin_amdgcn_sched_group_barrier(0x8, 2, 0);
        }
      }
      SCHED();
      BARRIER();
      STAMP(3);
      const f32x2 a2 = {ar, ar}, bb2 = {ai, ai}, nb2 = {-ai, -ai};
      f32x2 er = {0.f, 0.f}, ei = {0.f, 0.f};
#pragma unroll
      for (int rt = 0; rt < 4; ++rt)
#pragma unroll
        for (int z = 0; z < 8; ++z) {
          const f32x2 ur = {acc[0][rt][2 * z], acc[0][rt][2 * z + 1]}, ui = {acc[1][rt][2 * z], acc[1][rt][2 * z + 1]};
          f32x2 nr = __builtin_elementwise_fma(a2, er, ur), ni = __builtin_elementwise_fma(a2, ei, ui);
          nr = __builtin_elementwise_fma(nb2, ei, nr); ni = __builtin_elementwise_fma(bb2, er, ni);
          er = nr; ei = ni;
        }
      const float o0r = __shfl_xor(er[0], 32), o0i = __shfl_xor(ei[0], 32), o1r = __shfl_xor(er[1], 32), o1i = __shfl_xor(ei[1], 32);
      const float e0r = h ? o0r : er[0], e0i = h ? o0i : ei[0], e1r = h ? o1r : er[1], e1i = h ? o1i : ei[1];
      const float e2r = h ? er[0] : o0r, e2i = h ? ei[0] : o0i, e3r = h ? er[1] : o1r, e3i = h ? ei[1] : o1i;
      rsrc_t rcar = mk_rsrc(p.ws + WS_CARRY, 16 * P_ * 16);
      {
        float tr, ti, Er, Ei;
        cmul(e0r, e0i, p32r, p32i, tr, ti); tr += e1r; ti += e1i;
        cmul(tr, ti, p32r, p32i, Er, Ei); Er += e2r; Ei += e2i;
        cmul(Er, Ei, p32r, p32i, tr, ti); tr += e3r; ti += e3i;
        const v4u gq = {__float_as_uint(tr), tag, __float_as_uint(ti), ~tag};
        st_sc1(gq, rcar, h ? OOB : ch * 16, (15 - pn) * (P_ * 16));
      }
      STAMP(4);
      float Hr = 0.f, Hi = 0.f;
      {
        v4u cv[15];
#pragma nounroll
        for (unsigned spins = 0; spins < SPIN_CAP; ++spins) {
          CFENCE();
          int good = 0;
#pragma unroll
          for (int z = 0; z < 15; ++z) cv[z] = ld_sc1(rcar, ch * 16 + (16 - pn) * (P_ * 16), z * (P_ * 16));
#pragma unroll
          for (int z = 0; z < 15; ++z) good += ((cv[z][1] ^ tag) | (cv[z][3] ^ ~tag)) == 0u;
          if (__all(good == pn)) break;
          __builtin_amdgcn_s_sleep(2);
        }
#pragma unroll
        for (int z = 14; z >= 0; --z) {
          float tr, ti;
          cmul(Hr, Hi, p128r, p128i, tr, ti);
          Hr = tr + __uint_as_float(cv[z][0]); Hi = ti + __uint_as_float(cv[z][2]);
        }
      }
      STAMP(5);
      float c1r, c1i, c2r, c2i, c3r, c3i;
      cmul(Hr, Hi, p32r, p32i, c1r, c1i); c1r += e0r; c1i += e0i;
      cmul(c1r, c1i, p32r, p32i, c2r, c2i); c2r += e1r; c2i += e1i;
      cmul(c2r, c2i, p32r, p32i, c3r, c3i); c3r += e2r; c3i += e2i;
      f32x2 sr = {h ? c2r : Hr, h ? c3r : c1r}, si = {h ? c2i : Hi, h ? c3i : c1i};
      const f32x2 cr2 = {cr, cr}, nci2 = {-ci, -ci}, ncr2 = {-cr, -cr};
      LAS unsigned char* T = L + c * 16384;
      const int swz = ((i31 & 3) << 2) | (((i31 >> 3) & 1) << 1) | ((i31 >> 2) & 1);
#pragma unroll
      for (int rt = 0; rt < 4; ++rt) {
        f32x2 orr[8], oi[8];
#pragma unroll
        for (int z = 0; z < 8; ++z) {
          const f32x2 ur = {acc[0][rt][2 * z], acc[0][rt][2 * z + 1]}, ui = {acc[1][rt][2 * z], acc[1][rt][2 * z + 1]};
          f32x2 nr = __builtin_elementwise_fma(a2, sr, ur), ni = __builtin_elementwise_fma(a2, si, ui);
          nr = __builtin_elementwise_fma(nb2, si, nr); ni = __builtin_elementwise_fma(bb2, sr, ni);
          sr = nr; si = ni;
          orr[z] = __builtin_elementwise_fma(nci2, si, cr2 * sr);
          oi[z] = __builtin_elementwise_fma(nci2, sr, ncr2 * si);
        }
#pragma unroll
        for (int par = 0; par < 2; ++par) {
          const int chunk = (2 * h + par) * 4 + rt;
          const v4u vr = {pk_bf16(orr[0][par], orr[1][par]), pk_bf16(orr[2][par], orr[3][par]), pk_bf16(orr[4][par], orr[5][par]), pk_bf16(orr[6][par], orr[7][par])};
          const v4u vi = {pk_bf16(oi[0][par], oi[1][par]), pk_bf16(oi[2][par], oi[3][par]), pk_bf16(oi[4][par], oi[5][par]), pk_bf16(oi[6][par], oi[7][par])};
          *(LAS v4u*)(T + i31 * 256 + ((chunk ^ swz) << 4)) = vr;
          *(LAS v4u*)(T + (32 + i31) * 256 + ((chunk ^ swz) << 4)) = vi;
        }
      }
      {
        const int q = lane >> 4, qq = (lane & 15) >> 2, pp = lane & 3;
#pragma unroll
        for (int tt = 0; tt < 8; ++tt)
#pragma unroll
          for (int m = 0; m < 2; ++m) {
            v2u part[2];
#pragma unroll
            for (int t = 0; t < 2; ++t) {
              const int row = 32 * m + 8 * q + 4 * t + qq;
              const int chunk = 2 * tt + (pp >> 1);
              part[t] = lds_rdtr(T + row * 256 + ((chunk ^ ((qq << 2) | ((q & 1) << 1) | t)) << 4) + 8 * (pp & 1));
            }
            const int TT = 8 * pn + tt;
            const v4u o = {part[0][0], part[0][1], part[1][0], part[1][1]};
            st_sc1(o, ra2, lane * 16, ((TT >> 2) * NK + 56 + 2 * cg + m) * 4096 + (TT & 3) * 1024);
          }
      }
      SCHED();
      STAMP(6);
      publish_block(L, p.ws, fa2, tag, c, lane);
      STAMP(7);
#pragma unroll
      for (int i = 0; i < NB; ++i) ld_b2(pc, b2, i);
      SCHED();
    } else {
      const int unit = w * 256 + b;
      const bool has_unit = w < 3 && unit < 8 * NK;
      if (has_unit) { v4u rt[4]; retile_load(p, unit, lane, rt); retile_store(p, unit, lane, rt); }
      wait_vm<0>();
      BARRIER();
      tag = lds_word(L + LDS_TAG);
      st_flag(p.ws, WS_FB2 + unit * 8, tag, has_unit && lane == 0);
      const int wid = pn * 8 + w;
      if (wid < 64) {
        rsrc_t rg = mk_rsrc(p.gw, D_ * 2);
        v4u gv[8], xv[4][8];
#pragma unroll
        for (int z = 0; z < 8; ++z) gv[z] = __builtin_amdgcn_raw_buffer_load_b128(rg, (lane & 7) * 16, z * 128, 0);
#pragma unroll
        for (int gi = 0; gi < 4; ++gi)
#pragma unroll
          for (int z = 0; z < 8; ++z)
            xv[gi][z] = __builtin_amdgcn_raw_buffer_load_b128(rx, (lane >> 3) * 1024 + (lane & 7) * 16, (wid * 32 + gi * 8) * 1024 + z * 128, 0);
        rsrc_t rgo = mk_rsrc(p.ws + WS_G, S_ * 16);
#pragma unroll
        for (int gi = 0; gi < 4; ++gi) {
          float s = 0.f;
#pragma unroll
          for (int z = 0; z < 8; ++z)
#pragma unroll
            for (int y = 0; y < 4; ++y) {
              const unsigned xa = xv[gi][z][y], ga = gv[z][y];
              s = __builtin_amdgcn_fdot2_f32_bf16(__builtin_bit_cast(bf16x2, xa), __builtin_bit_cast(bf16x2, ga), s, false);
            }
          s += __shfl_xor(s, 1); s += __shfl_xor(s, 2); s += __shfl_xor(s, 4);
          const float g = 1.f / (1.f + __expf(-s));
          const v4u gq = {__float_as_uint(g), tag, ~tag, 0u};
          st_sc1(gq, rgo, (lane & 7) ? OOB : (lane >> 3) * 16, (wid * 32 + gi * 8) * 16);
        }
      }
      STAMP(3);
      poll_flags((GAS u64*)(p.ws + WS_FB2) + N * NK + k0 + (lane < pc.nk ? lane : 0), true, tag64(tag));
#pragma unroll
      for (int i = 0; i < NB; ++i) ld_b2(pc, b2, i);
      SCHED();
    }
  }

  if (w == 4) {
    if (kind == 0) {
#pragma nounroll
      for (unsigned spins = 0; spins < (SPIN_CAP << 4); ++spins) { if (all_done(L)) break; __builtin_amdgcn_s_sleep(1); }
    }
    const int p0 = (4 * R * 205) >> 11, p1 = ((4 * R + 3) * 205) >> 11, kk = lane & 15;
    poll_flags((GAS u64*)(p.ws + WS_FA2) + ((lane & 16) ? p1 : p0) * 16 + (kk < 14 ? kk : 0), true, tag64(tag));
    *(volatile LAS unsigned*)(L + LDS_RDY) = 1u;
  } else if (w == 0) {
    poll_flags((GAS u64*)(p.ws + WS_FA2) + (16 + (R >> 1)) * 16 + (lane & 3), true, tag64(tag));
    *(volatile LAS unsigned*)(L + LDS_RDY + 4) = 1u;
  } else {
    lds_wait(L + LDS_RDY + (w < 3 ? 4 : 0));
  }
  STAMP(8);
  rsrc_t ra = mk_rsrc(p.ws + WS_A2 + (R * NK + k0) * 4096, pc.nk * 4096);
  constexpr int WA = 2;
  v4u af[WA][4];
#pragma unroll
  for (int i = 0; i < WA; ++i) {
    const int o = p2_off(pc, i);
#pragma unroll
    for (int tt = 0; tt < 4; ++tt) af[i][tt] = ld_sc1(ra, pc.lane16 + tt * 1024, o);
  }
  SCHED();
  f32x4 acc[4][4];
#pragma unroll
  for (int e = 0; e < 4; ++e)
#pragma unroll
    for (int tt = 0; tt < 4; ++tt) acc[e][tt] = f32x4{0.f, 0.f, 0.f, 0.f};
#pragma unroll
  for (int i = 0; i < 12; ++i) {
    if (i < 10 || i < pc.nk) {
      const int o = p2_off(pc, i + WA);
#pragma unroll
      for (int tt = 0; tt < 4; ++tt) {
#pragma unroll
        for (int e = 0; e < 4; ++e) acc[e][tt] = mfma16(b2[i % NB][e], af[i % WA][tt], acc[e][tt]);
        SCHED();
        if (i + WA < 12) af[i % WA][tt] = ld_sc1(ra, pc.lane16 + tt * 1024, o);
        SCHED();
      }
      if (i + NB < 12) ld_b2(pc, b2, i + NB);
      SCHED();
    }
  }
  STAMP(9);
  const int tk = tid >> 3, ck = tid & 7, tok = 64 * R + tk;
  rsrc_t rdk = mk_rsrc(p.Dsk, D_ * 2), rgi = mk_rsrc(p.ws + WS_G, S_ * 16);
  const v4u xv = __builtin_amdgcn_raw_buffer_load_b128(rx, tok * 1024 + ck * 16, N * 128, 0);
  const v4u dv = __builtin_amdgcn_raw_buffer_load_b128(rdk, ck * 16, N * 128, 0);
  v4u gq = ld_sc1(rgi, tok * 16, 0);
#pragma unroll
  for (int tt = 0; tt < 4; ++tt)
#pragma unroll
    for (int e = 0; e < 4; ++e) *(LAS f32x4*)(L + w * 16384 + (tt * 4 + e) * 1024 + ((lane ^ ((lane >> 4) << 2)) << 4)) = acc[e][tt];
#pragma nounroll
  for (unsigned spins = 0; spins < SPIN_CAP; ++spins) {
    if (__all(gq[1] == tag && gq[2] == ~tag)) break;
    __builtin_amdgcn_s_sleep(4);
    CFENCE();
    gq = ld_sc1(rgi, tok * 16, 0);
  }
  if (b == 0 && w == 7) {
#pragma nounroll
    for (int z = 0; z < 4; ++z) poll_flags((GAS u64*)(p.ws + WS_FB2) + z * 64 + lane, true, tag64(tag));
    st_flag(p.ws, WS_EPOCH, tag, lane == 0);
  }
  asm volatile("s_waitcnt lgkmcnt(0)" ::: "memory");
  BARRIER();
  STAMP(10);
  {
    const int q = ck >> 1, hf = ck & 1, ln = (tk & 15) + 16 * q;
    const int base = (tk >> 4) * 4096 + ((ln ^ (q << 2)) << 4) + hf * 8;
    f32x2 sg[4], ss[4];
#pragma unroll
    for (int e = 0; e < 4; ++e) { sg[e] = f32x2{0.f, 0.f}; ss[e] = f32x2{0.f, 0.f}; }
#pragma unroll
    for (int ww = 0; ww < 8; ++ww)
#pragma unroll
      for (int e = 0; e < 4; ++e) {
        const f32x2 v = *(LAS f32x2*)(L + ww * 16384 + e * 1024 + base);
        if (ww < 3) ss[e] += v; else sg[e] += v;
      }
    const float g = __uint_as_float(gq[0]);
    float o[8];
#pragma unroll
    for (int j = 0; j < 8; ++j) {
      const float xj = (j & 1) ? bfhi(xv[j >> 1]) : bf2f(xv[j >> 1] & 0xFFFFu);
      const float dj = (j & 1) ? bfhi(dv[j >> 1]) : bf2f(dv[j >> 1] & 0xFFFFu);
      o[j] = g * (2.f * ss[j & 3][j >> 2] + xj * dj) + (1.f - g) * sg[j & 3][j >> 2];
    }
    rsrc_t ro = mk_rsrc(p.out, S_ * D_ * 4);
    const v4u o0 = {__float_as_uint(o[0]), __float_as_uint(o[1]), __float_as_uint(o[2]), __float_as_uint(o[3])};
    const v4u o1 = {__float_as_uint(o[4]), __float_as_uint(o[5]), __float_as_uint(o[6]), __float_as_uint(o[7])};
    __builtin_amdgcn_raw_buffer_store_b128(o0, ro, tok * 2048 + ck * 32, N * 256, 0);
    __builtin_amdgcn_raw_buffer_store_b128(o1, ro, tok * 2048 + ck * 32 + 16, N * 256, 0);
  }
  STAMP(11);
}

extern "C" void kernel_launch(void* const* d_in, const int* in_sizes, int n_in, void* d_out, int out_size, void* d_ws, size_t ws_size,
                              hipStream_t stream) {
  Params p;
  p.x = (const u16*)d_in[0]; p.Lre = (const u16*)d_in[1]; p.Lim = (const u16*)d_in[2]; p.Bre = (const u16*)d_in[3];
  p.Bim = (const u16*)d_in[4]; p.Cre = (const u16*)d_in[5]; p.Cim = (const u16*)d_in[6]; p.ldt = (const u16*)d_in[7];
  p.Dsk = (const u16*)d_in[8]; p.Wg = (const u16*)d_in[10]; p.Wu = (const u16*)d_in[11]; p.Wd = (const u16*)d_in[12];
  p.gw = (const u16*)d_in[13]; p.out = (float*)d_out; p.ws = (unsigned char*)d_ws;
  p.pad = 0;
  for (int b = 0; b < 256; ++b) {
    const int xcd = b & 7, r = b >> 3, H = xcd >> 2, Q = xcd & 3, nq = 7 + 2 * H, r2 = r - nq;
    const int cnt = H ? 21 : (Q < 2 ? 25 : 24), st = H ? 21 * Q : (Q < 2 ? 25 * Q : 24 * Q + 2);
    unsigned kind, pn, cb = 0;
    if (r2 < 0) { kind = 1; pn = 7 * H + r; cb = Q; }
    else if (r2 < cnt) { kind = 0; cb = (st + r2) / (7 - H); pn = 7 * H + (st + r2) % (7 - H); }
    else { kind = 2; pn = H ? 2 * Q + r2 - 19 : Q - 2; }
    p.map[b] = kind | pn << 2 | cb << 8;
  }
  (void)hipFuncSetAttribute((const void*)k_fused, hipFuncAttributeMaxDynamicSharedMemorySize, LDS_BYTES);
  k_fused<<<256, 512, LDS_BYTES, stream>>>(p);
}
```

```cpp
#include <hip/hip_runtime.h>
#include <stdint.h>

typedef unsigned short u16;
typedef unsigned long long u64;
typedef unsigned v4u __attribute__((ext_vector_type(4)));
typedef unsigned v2u __attribute__((ext_vector_type(2)));
typedef short s16x4 __attribute__((ext_vector_type(4)));
typedef __bf16 bf16x8 __attribute__((ext_vector_type(8)));
typedef __bf16 bf16x2 __attribute__((ext_vector_type(2)));
typedef float f32x2 __attribute__((ext_vector_type(2)));
typedef float f32x4 __attribute__((ext_vector_type(4)));
typedef float f32x16 __attribute__((ext_vector_type(16)));
#define LAS __attribute__((address_space(3)))
#define GAS __attribute__((address_space(1)))
#define RLX_AGENT __ATOMIC_RELAXED, __HIP_MEMORY_SCOPE_AGENT
typedef __amdgpu_buffer_rsrc_t rsrc_t;

constexpr int S_ = 2048, D_ = 512, P_ = 512, F_ = 1792;
constexpr int NK = 88;
constexpr int AUX_SC1 = 16;
constexpr int WS_EPOCH = 0;
constexpr int WS_FB2 = 4096;
constexpr int WS_FA2 = 16384;
constexpr int WS_G = 32768;
constexpr int WS_CARRY = 65536;
constexpr int WS_B2 = 262144;
constexpr int B2_BYTES = 8 * NK * 4096;
constexpr int WS_A2 = 4 << 20;
constexpr int A2_BYTES = 32 * NK * 4096;
constexpr int WS_STAMP = 32 << 20;
constexpr int LDS_BYTES = 160 * 1024;
constexpr int LDS_TAG = LDS_BYTES - 16, LDS_RDY = LDS_BYTES - 8, LDS_DONE = LDS_BYTES - 32;
constexpr int OOB = 1 << 28;
constexpr int CTRL_BYTES = 32768;
constexpr int CODE_BYTES = 42 * 1024;
constexpr unsigned SPIN_CAP = 1u << 18;

struct Params {
  const u16 *x, *Wg, *Wu, *Bre, *Bim, *Cre, *Cim, *Wd;
  unsigned char* ws;
  const u16 *Lre, *Lim, *ldt, *Dsk, *gw;
  float* out;
  u64 pad;
  unsigned map[256];
};

__device__ __forceinline__ rsrc_t mk_rsrc(const void* p, int bytes) {
  return __builtin_amdgcn_make_buffer_rsrc((void*)p, 0, bytes, 0x00020000);
}
__device__ __forceinline__ float bf2f(unsigned v) { return __uint_as_float(v << 16); }
__device__ __forceinline__ float bfhi(unsigned v) { return __uint_as_float(v & 0xFFFF0000u); }
__device__ __forceinline__ unsigned pk_bf16(float lo, float hi) {
  f32x2 t = {lo, hi};
  return __builtin_bit_cast(unsigned, __builtin_convertvector(t, bf16x2));
}
__device__ __forceinline__ f32x16 mfma32(v4u a, v4u b, f32x16 c) {
  return __builtin_amdgcn_mfma_f32_32x32x16_bf16(__builtin_bit_cast(bf16x8, a), __builtin_bit_cast(bf16x8, b), c, 0, 0, 0);
}
__device__ __forceinline__ f32x4 mfma16(v4u a, v4u b, f32x4 c) {
  return __builtin_amdgcn_mfma_f32_16x16x32_bf16(__builtin_bit_cast(bf16x8, a), __builtin_bit_cast(bf16x8, b), c, 0, 0, 0);
}
__device__ __forceinline__ v4u lds_rd128(LAS unsigned char* p) { return *(LAS v4u*)p; }
__device__ __forceinline__ v2u lds_rdtr(LAS unsigned char* p) {
  return __builtin_bit_cast(v2u, __builtin_amdgcn_ds_read_tr16_b64_v4i16((LAS s16x4*)p));
}
__device__ __forceinline__ void glds(rsrc_t rs, LAS unsigned char* dst, int voff, int soff) {
  __builtin_amdgcn_raw_ptr_buffer_load_lds(rs, (LAS void*)dst, 16, voff, soff, 0, 0);
}
__device__ __forceinline__ v4u ld_sc1(rsrc_t rs, int voff, int soff) { return __builtin_amdgcn_raw_buffer_load_b128(rs, voff, soff, AUX_SC1); }
__device__ __forceinline__ void st_sc1(v4u v, rsrc_t rs, int voff, int soff) { __builtin_amdgcn_raw_buffer_store_b128(v, rs, voff, soff, AUX_SC1); }
__device__ __forceinline__ u64 tag64(unsigned tag) { return ((u64)(~tag) << 32) | tag; }
__device__ __forceinline__ void st_flag(unsigned char* ws, int off, unsigned tag, bool on) {
  __builtin_amdgcn_raw_buffer_store_b64(v2u{tag, ~tag}, mk_rsrc(ws, CTRL_BYTES), on ? 0 : OOB, off, AUX_SC1);
}
__device__ __forceinline__ unsigned lds_word(LAS unsigned char* p) { return __builtin_amdgcn_readfirstlane(*(volatile LAS unsigned*)p); }
#define BARRIER() asm volatile("s_barrier" ::: "memory")
#define SCHED() __builtin_amdgcn_sched_barrier(0)
#define CFENCE() asm volatile("" ::: "memory")
template <int N> __device__ __forceinline__ void wait_vm() { asm volatile("s_waitcnt vmcnt(%c0)" ::"i"(N) : "memory"); }

#define STAMP(i) do {} while (0)

__device__ __forceinline__ void poll_flags(GAS u64* f, bool valid, u64 want) {
#pragma nounroll
  for (unsigned spins = 0; spins < SPIN_CAP; ++spins) {
    CFENCE();
    const u64 v = __hip_atomic_load(f, RLX_AGENT);
    if (__all(!valid || v == want)) break;
    __builtin_amdgcn_s_sleep(4);
  }
  CFENCE();
}

constexpr int NB = 8;
constexpr int S_RT = 3;
constexpr int S_CHK = 9;
#define NROT 2
struct Sched {
  int n;
  int type[40], idx[40];
  int lo[17], hi[17];
  int b2lo[17], b2hi[17];
  int wait[17];
};
constexpr Sched make_sched(int kind) {
  Sched s{};
  int cnt[40] = {}, need[40] = {}, avail[40] = {};
  int n = 0;
  auto add = [&](int ty, int ix, int c, int nd, int av) { s.type[n] = ty; s.idx[n] = ix; cnt[n] = c; need[n] = nd; avail[n] = av; ++n; };
  if (kind == 0) {
    add(0, 0, 3, 0, -1); add(1, 0, 2, 0, -1); add(2, 0, 4, 0, -1); add(2, 1, 4, 0, -1);
    add(0, 1, 3, 1, -1); add(1, 1, 2, 1, -1); add(2, 2, 4, 1, -1); add(2, 3, 4, 2, -1);
    add(0, 2, 3, 3, -1); add(1, 2, 2, 3, -1);
    for (int t = 4; t < 16; ++t) {
      add(2, t, 4, t - 1, t - 3);
      const int b = t - 3, T = b / 2 + 2;
      if (b >= 2 && T <= 7) add(b & 1, T, (b & 1) ? 2 : 3, 2 * T - 1, b);
    }
  } else {
    for (int T = 0; T < 8; ++T)
      for (int ty = 0; ty < 2; ++ty) add(ty, T, 6, T == 0 ? 0 : 2 * T - 1, T < 3 ? -1 : 2 * T - 4 + ty);
  }
  s.n = n;
  const int LIM = 8;
  int endpos[40] = {};
  int next = 0, total = 0, b2 = 0;
  for (int b = 0; b <= 16; ++b) {
    s.lo[b] = next;
    for (;;) {
      if (next >= n || avail[next] > b - 1) break;
      int lastneed = 0;
      for (int i = 0; i < next; ++i) if (need[i] <= b) lastneed = endpos[i];
      if (b < 4 && need[next] > b && total + cnt[next] - lastneed > LIM + 2 * b) break;
      total += cnt[next]; endpos[next] = total; ++next;
    }
    s.hi[b] = next;
    s.b2lo[b] = b2;
    if (b >= S_CHK + 2 && b2 < NB) { ++b2; total += 4; }
    if (b >= 14 && b2 < NB) { ++b2; total += 4; }
    if (b == 16) { total += 4 * (NB - b2); b2 = NB; }
    s.b2hi[b] = b2;
    int lastneed = 0;
    for (int i = 0; i < next; ++i) if (need[i] <= b) lastneed = endpos[i];
    for (int i = next; i < n; ++i) if (need[i] <= b) lastneed = 1 << 20;
    s.wait[b] = total - lastneed;
    if (s.wait[b] > 63) s.wait[b] = 63;
  }
  return s;
}
constexpr bool sched_ok(const Sched& s) { for (int b = 0; b <= 16; ++b) if (s.wait[b] < 0) return false; return s.hi[16] == s.n; }
constexpr Sched SCH[2] = {make_sched(0), make_sched(1)};
static_assert(sched_ok(SCH[0]) && sched_ok(SCH[1]), "every ring item must be issued in time");

constexpr int GX_SLOT = 20480, GW_BASE = 61440, GW_SLOT = 16384;
constexpr int SX_SLOT = 16384, SB_BASE = 49152, SB_SLOT = 32768;

struct P2Ctx {
  rsrc_t rb;
  int nk, rot, lane16;
};
__device__ __forceinline__ int p2_off(const P2Ctx& c, int i) {
  int t = i + c.rot; t = t >= c.nk ? t - c.nk : t;
  return i < c.nk ? t * 4096 : OOB;
}
__device__ __forceinline__ void ld_b2(const P2Ctx& c, v4u (&b2)[NB][4], int i) {
  const int o = p2_off(c, i);
#pragma unroll
  for (int e = 0; e < 4; ++e) b2[i % NB][e] = ld_sc1(c.rb, c.lane16 + e * 1024, o);
}

__device__ __forceinline__ void retile_load(const Params& p, int unit, int lane, v4u (&f)[4]) {
  const int N = unit / NK, kap = unit - N * NK, q = lane >> 4, i = lane & 15;
  if (kap < 56) {
    rsrc_t rd = mk_rsrc(p.Wd, F_ * D_ * 2);
#pragma unroll
    for (int j = 0; j < 8; ++j) {
      const v2u t = __builtin_amdgcn_raw_buffer_load_b64(rd, (4 * q + (j & 3) + 16 * (j >> 2)) * (D_ * 2) + i * 8, kap * (32 * D_ * 2) + N * 128, 0);
      f[j >> 1][2 * (j & 1)] = t[0]; f[j >> 1][2 * (j & 1) + 1] = t[1];
    }
  } else {
    const int cg = (kap - 56) >> 1, m = kap & 1;
    rsrc_t rc = mk_rsrc(m ? p.Cim : p.Cre, D_ * P_ * 2);
#pragma unroll
    for (int e = 0; e < 4; ++e) f[e] = __builtin_amdgcn_raw_buffer_load_b128(rc, (4 * i + e) * (P_ * 2) + q * 16, N * (64 * P_ * 2) + cg * 64, 0);
  }
}
__device__ __forceinline__ void retile_store(const Params& p, int unit, int lane, const v4u (&f)[4]) {
  const int kap = unit % NK;
  rsrc_t rws = mk_rsrc(p.ws + WS_B2, B2_BYTES);
  if (kap < 56) {
#pragma unroll
    for (int e = 0; e < 4; ++e) {
      v4u o;
#pragma unroll
      for (int z = 0; z < 4; ++z) o[z] = __builtin_amdgcn_perm(f[z][2 + (e >> 1)], f[z][e >> 1], (e & 1) ? 0x07060302u : 0x05040100u);
      st_sc1(o, rws, lane * 16 + e * 1024, unit * 4096);
    }
  } else {
#pragma unroll
    for (int e = 0; e < 4; ++e) st_sc1(f[e], rws, lane * 16 + e * 1024, unit * 4096);
  }
}

struct LdCtx { rsrc_t rx, rw; int lw, vx, vw0, vw1, sx0, sw0, rot; GAS u64* fb2; int unit, lane; mutable unsigned sv; };

template <int KIND, int I> __device__ __forceinline__ void ld_item(const LdCtx& c, LAS unsigned char* L) {
  constexpr int ty = SCH[KIND].type[I], t = SCH[KIND].idx[I];
  if constexpr (KIND == 0 && ty < 2) {
    const int k0 = ((t + c.rot) & 7) * 128;
#pragma unroll
    for (int n = 3 * ty; n < 3 + 2 * ty; ++n) glds(c.rx, L + (t % 3) * GX_SLOT + c.lw * 1024 + n * 4096, c.vx, c.sx0 + k0 + n * 32768);
  } else if constexpr (KIND == 0) {
    const int k0 = ((((t >> 1) + c.rot) & 7) * 64 + (t & 1) * 32) * (F_ * 2);
#pragma unroll
    for (int i = 0; i < 4; ++i)
      glds(c.rw, L + GW_BASE + (t & 3) * GW_SLOT + c.lw * 4096 + i * 1024, (i >> 1) ? c.vw1 : c.vw0, c.sw0 + k0 + i * 4 * (F_ * 2));
  } else {
    const int k0 = ((t + c.rot) & 7) * 128;
#pragma unroll
    for (int n = 2 * ty; n < 2 * ty + 2; ++n) glds(c.rx, L + (t % 3) * SX_SLOT + c.lw * 1024 + n * 4096, c.vx, c.sx0 + k0 + n * 8192);
#pragma unroll
    for (int n = 4 * ty; n < 4 * ty + 4; ++n)
      glds(c.rw, L + SB_BASE + (t % 3) * SB_SLOT + (c.lw >> 1) * 16384 + (c.lw & 1) * 1024 + n * 2048, c.vw0, c.sw0 + k0 + n * 16384);
  }
  SCHED();
}
template <int KIND, int LO, int HI> __device__ __forceinline__ void ld_items(const LdCtx& c, LAS unsigned char* L) {
  if constexpr (LO < HI) { ld_item<KIND, LO>(c, L); ld_items<KIND, LO + 1, HI>(c, L); }
}
template <int KIND, int B>
__device__ __forceinline__ void loader_steps(const Params& p, const LdCtx& c, LAS unsigned char* L, const P2Ctx& pc, v4u (&b2)[NB][4], v4u (&rt)[4],
                                             unsigned& tag) {
  if constexpr (B == S_RT) { if (c.unit < 8 * NK) retile_load(p, c.unit, c.lane, rt); SCHED(); }
  if constexpr (B == S_RT + 2) { if (c.unit < 8 * NK) retile_store(p, c.unit, c.lane, rt); SCHED(); }
  if constexpr (B == S_RT + 4) {
    tag = lds_word(L + LDS_TAG);
    wait_vm<0>();
    st_flag(p.ws, WS_FB2 + c.unit * 8, tag, c.unit < 8 * NK && c.lane == 0);
    SCHED();
  }
  if constexpr (B == S_CHK + 1) poll_flags(c.fb2, true, tag64(tag));
  if constexpr (B > 0) ld_items<KIND, SCH[KIND].lo[B], SCH[KIND].hi[B]>(c, L);
#pragma unroll
  for (int i = SCH[KIND].b2lo[B]; i < SCH[KIND].b2hi[B]; ++i) ld_b2(pc, b2, i);
  SCHED();
  wait_vm<SCH[KIND].wait[B]>();
  BARRIER();
  if constexpr (B < 16) loader_steps<KIND, B + 1>(p, c, L, pc, b2, rt, tag);
}
__device__ __forceinline__ bool all_done(LAS unsigned char* L) {
  const v4u d = *(volatile LAS v4u*)(L + LDS_DONE);
  return __builtin_amdgcn_readfirstlane(d[0] & d[1] & d[2] & d[3]) != 0u;
}
__device__ __forceinline__ void publish_block(LAS unsigned char* L, unsigned char* ws, int off, unsigned tag, int c, int lane) {
  wait_vm<0>();
  *(volatile LAS unsigned*)(L + LDS_DONE + 4 * c) = 1u;
  const bool last = all_done(L);
  st_flag(ws, off, tag, last && lane == 0);
  CFENCE();
}
__device__ __forceinline__ void lds_wait(LAS unsigned char* word) {
#pragma nounroll
  for (unsigned spins = 0; spins < (SPIN_CAP << 4); ++spins) {
    if (lds_word(word) != 0u) break;
    __builtin_amdgcn_s_sleep(1);
  }
  CFENCE();
}

__device__ __forceinline__ void cmul(float ar, float ai, float br, float bi, float& cr, float& ci) {
  cr = ar * br - ai * bi; ci = ar * bi + ai * br;
}

__global__ __launch_bounds__(512, 2) void k_fused(Params p) {
  extern __shared__ __attribute__((aligned(16))) unsigned char lds_[];
  LAS unsigned char* L = (LAS unsigned char*)lds_;
  STAMP(0);
  const int b = blockIdx.x, tid = threadIdx.x, lane = tid & 63;
  const int w = __builtin_amdgcn_readfirstlane(tid >> 6);
  const int xcd = b & 7, r = b >> 3;
  const unsigned mp = p.map[b];
  unsigned zpf = 0;
  if (w < 4) {
    const u64 pcv = __builtin_amdgcn_s_getpc();
    rsrc_t rcode = mk_rsrc((const void*)(pcv & ~(u64)127), CODE_BYTES);
    zpf = __builtin_amdgcn_raw_buffer_load_b32(rcode, lane < 4 ? lane * 16384 : OOB, (r * 4 + w) * 128, 0);
  }
  const int kind = mp & 3, pn = (mp >> 2) & 63, cb = mp >> 8;
  const int rot = (8 / NROT) * ((pn + cb) & (NROT - 1));
  const int R = xcd * 4 + (r & 3), N = r >> 2;
  const int k0 = (int)(0x22170C002D4D4238ull >> (8 * w)) & 255;
  P2Ctx pc;
  pc.nk = (0xBBBCBBBAu >> (4 * w)) & 15;
  pc.rot = (N * 5) >> 2;
  pc.lane16 = lane * 16;
  pc.rb = mk_rsrc(p.ws + WS_B2 + (N * NK + k0) * 4096, pc.nk * 4096);
  rsrc_t rx = mk_rsrc(p.x, S_ * D_ * 2);
  v4u b2[NB][4];
  unsigned tag;

  if (__builtin_expect(w >= 4 && kind != 2, 1)) {
    LdCtx c;
    c.rx = rx; c.lw = w - 4; c.rot = rot;
    const int rr = 8 * c.lw + (lane >> 3);
    const int sw = ((lane & 7) ^ ((rr >> 1) & 7)) << 4;
    if (kind == 0) {
      const int m = c.lw >> 1, kq = lane >> 4;
      c.vx = rr * 1024 + sw;
      c.sx0 = pn * 160 * 1024;
      c.rw = mk_rsrc(m ? p.Wu : p.Wg, D_ * F_ * 2);
      c.vw0 = ((c.lw & 1) * 16 + kq) * (F_ * 2) + (((lane & 15) ^ (kq << 2)) << 4);
      c.vw1 = c.vw0 ^ 32;
      c.sw0 = cb * 256;
      STAMP(1);
      ld_items<0, 0, SCH[0].hi[0]>(c, L);
    } else {
      const int tok = 64 * ((rr >> 2) & 1) + 32 * (rr & 1) + ((rr >> 1) & 1) + 2 * (rr >> 3);
      c.vx = tok * 1024 + sw;
      c.sx0 = pn * 128 * 1024;
      c.rw = mk_rsrc((c.lw >> 1) ? p.Bim : p.Bre, P_ * D_ * 2);
      const int brow = 8 * (c.lw & 1) + (lane >> 3);
      c.vw0 = brow * 1024 + (((lane & 7) ^ ((brow >> 1) & 7)) << 4);
      c.vw1 = 0;
      c.sw0 = cb * 128 * 1024;
      STAMP(1);
      ld_items<1, 0, SCH[1].hi[0]>(c, L);
    }
    STAMP(2);
    int kind2 = kind;
    asm volatile("" : "+s"(kind2));
    c.fb2 = (GAS u64*)(p.ws + WS_FB2) + N * NK + c.lw * 22 + (lane < 22 ? lane : 0);
    c.unit = c.lw * 256 + b; c.lane = lane; c.sv = 0;
    v4u rt[4];
    if (kind2 == 0) loader_steps<0, 0>(p, c, L, pc, b2, rt, tag);
    else loader_steps<1, 0>(p, c, L, pc, b2, rt, tag);
    STAMP(3);
  } else {
    if (w == 3) {
      const unsigned e = __hip_atomic_load((GAS unsigned*)(p.ws + WS_EPOCH), RLX_AGENT) + 1u;
      *(volatile LAS v4u*)(L + LDS_TAG) = v4u{e, 0u, 0u, 0u};
      *(volatile LAS v4u*)(L + LDS_DONE) = v4u{0u, 0u, 0u, 0u};
      asm volatile("s_waitcnt lgkmcnt(0)" ::: "memory");
    }
    if (w == 2) {
      const int g = lane >> 3, j = lane & 7;
      int off = OOB;
      if (g == 0) off = WS_A2 + R * NK * 4096 + (j < 6 ? j : 0) * 65536;
      if (g == 1) off = WS_B2 + N * NK * 4096 + (j < 6 ? j : 0) * 65536;
      if (g == 2) { const int R0 = kind == 0 ? (10 * pn) >> 2 : 2 * pn; const int Rq = R0 + (j & 3) < 32 ? R0 + (j & 3) : 31;
                    off = WS_A2 + (Rq * NK + (kind == 0 ? 4 * cb : 56 + 8 * cb) + (j >> 2) * 3) * 4096; }
      if (g == 3) off = j * 32768;
      zpf ^= __builtin_amdgcn_raw_buffer_load_b32(mk_rsrc(p.ws, WS_A2 + A2_BYTES), off, 0, 0);
      zpf ^= __builtin_amdgcn_raw_buffer_load_b32(mk_rsrc(p.out, S_ * D_ * 4), lane < 4 ? lane * 32768 + N * 256 : OOB, R * 131072, 0);
      zpf ^= __builtin_amdgcn_raw_buffer_load_b32(mk_rsrc(p.Dsk, D_ * 2), lane < 1 ? 0 : OOB, 0, 0);
    }
    asm volatile("" ::"v"(zpf));
    rsrc_t ra2 = mk_rsrc(p.ws + WS_A2, A2_BYTES);
    const int fa2 = WS_FA2 + ((kind * 16 + pn) * 16 + cb) * 8;
    const int c = w;

    if (kind == 0) {
      const int q = lane >> 4, i15 = lane & 15;
      int xa[2], wa[2];
#pragma unroll
      for (int ks = 0; ks < 2; ++ks) xa[ks] = i15 * 128 + (((4 * ks + q) ^ ((i15 >> 1) & 7)) << 4);
      {
        const int qq = i15 >> 2, pp = lane & 3;
#pragma unroll
        for (int u = 0; u < 2; ++u)
          wa[u] = GW_BASE + (8 * q + qq) * 256 + (((2 * (2 * c + u) + (pp >> 1)) ^ ((qq << 2) | ((q & 1) << 1))) << 4) + 8 * (pp & 1);
      }
      f32x4 acc[4][10];
#pragma unroll
      for (int a = 0; a < 4; ++a)
#pragma unroll
        for (int tt = 0; tt < 10; ++tt) acc[a][tt] = f32x4{0.f, 0.f, 0.f, 0.f};
      v4u xf[4];
      v2u wf[2][8];
      auto rdx = [&](int it) {
        const int s = it / 10, tt = it % 10;
        xf[it & 3] = lds_rd128(L + xa[s & 1] + ((s >> 1) % 3) * GX_SLOT + tt * 2048);
      };
      auto rdw = [&](int s, int z) {
        const int m = z >> 2, u = (z >> 1) & 1, t = z & 1;
        wf[s & 1][z] = lds_rdtr(L + wa[u] + (s & 3) * GW_SLOT + m * 8192 + t * 1024);
      };
      STAMP(1);
      BARRIER();
      STAMP(2);
      tag = lds_word(L + LDS_TAG);
#pragma unroll
      for (int z = 0; z < 8; ++z) rdw(0, z);
      rdx(0); rdx(1); rdx(2);
#pragma clang loop unroll(full)
      for (int s = 0; s < 16; ++s)
#pragma clang loop unroll(full)
      for (int tt = 0; tt < 10; ++tt) {
        const int it = 10 * s + tt;
        SCHED();
        if (tt == 0 && s > 0) { BARRIER(); SCHED(); }
        if (it + 3 < 160) rdx(it + 3);
        if (tt < 8 && s < 15) rdw(s + 1, tt);
#pragma unroll
        for (int a = 0; a < 4; ++a) {
          const v4u wv = {wf[s & 1][2 * a][0], wf[s & 1][2 * a][1], wf[s & 1][2 * a + 1][0], wf[s & 1][2 * a + 1][1]};
          acc[a][tt] = mfma16(wv, xf[it & 3], acc[a][tt]);
        }
        __builtin_amdgcn_sched_group_barrier(0x8, 1, 0);
        __builtin_amdgcn_sched_group_barrier(0x100, 1, 0);
        __builtin_amdgcn_sched_group_barrier(0x8, 1, 0);
        __builtin_amdgcn_sched_group_barrier(0x100, 1, 0);
        __builtin_amdgcn_sched_group_barrier(0x8, 2, 0);
      }
      SCHED();
      BARRIER();
      STAMP(3);
      const int kap = 4 * cb + c;
#pragma unroll
      for (int tt = 0; tt < 10; ++tt) {
        float hv[8];
#pragma unroll
        for (int j = 0; j < 8; ++j) {
          const float e = __builtin_amdgcn_exp2f(acc[j >> 2][tt][j & 3] * -1.44269504088896341f);
          hv[j] = acc[2 + (j >> 2)][tt][j & 3] * __builtin_amdgcn_rcpf(1.f + e);
        }
        const v4u o = {pk_bf16(hv[0], hv[1]), pk_bf16(hv[2], hv[3]), pk_bf16(hv[4], hv[5]), pk_bf16(hv[6], hv[7])};
        const int TT = 10 * pn + tt;
        st_sc1(o, ra2, lane * 16, ((TT >> 2) * NK + kap) * 4096 + (TT & 3) * 1024);
      }
      SCHED();
      STAMP(4);
      publish_block(L, p.ws, fa2, tag, c, lane);
      STAMP(5);
#pragma unroll
      for (int i = 0; i < NB; ++i) ld_b2(pc, b2, i);
      SCHED();
    } else if (kind == 1) {
      const int h = lane >> 5, i31 = lane & 31;
      int xa[4];
#pragma unroll
      for (int ks = 0; ks < 4; ++ks) xa[ks] = i31 * 128 + (((2 * ks + h) ^ ((i31 >> 1) & 7)) << 4);
      const int cg = 4 * cb + c, ch = 32 * cg + i31;
      const float lr = bf2f(p.Lre[ch]), li = bf2f(p.Lim[ch]);
      float dt = expf(bf2f(p.ldt[ch]));
      dt = bf2f(pk_bf16(dt, 0.f) & 0xFFFFu);
      float sn, cs;
      sincosf(dt * li, &sn, &cs);
      const float ex = expf(dt * lr);
      const float ar = ex * cs, ai = ex * sn;
      const float den = 1.f / (lr * lr + li * li);
      const float cr = ((ar - 1.f) * lr + ai * li) * den, ci = (ai * lr - (ar - 1.f) * li) * den;
      float p32r = ar, p32i = ai;
#pragma unroll
      for (int z = 0; z < 5; ++z) { float t0, t1; cmul(p32r, p32i, p32r, p32i, t0, t1); p32r = t0; p32i = t1; }
      float p128r = p32r, p128i = p32i;
#pragma unroll
      for (int z = 0; z < 2; ++z) { float t0, t1; cmul(p128r, p128i, p128r, p128i, t0, t1); p128r = t0; p128i = t1; }

      f32x16 acc[2][4];
#pragma unroll
      for (int m = 0; m < 2; ++m)
#pragma unroll
        for (int rt = 0; rt < 4; ++rt) acc[m][rt] = f32x16{};
      v4u xf[2][4], bfr[2][2];
      auto rd = [&](int n) {
        const int bq = n & 1, T = n >> 2;
#pragma unroll
        for (int m = 0; m < 2; ++m) bfr[bq][m] = lds_rd128(L + SB_BASE + c * 4096 + xa[n & 3] + (T % 3) * SB_SLOT + m * 16384);
#pragma unroll
        for (int rt = 0; rt < 4; ++rt) xf[bq][rt] = lds_rd128(L + xa[n & 3] + (T % 3) * SX_SLOT + rt * 4096);
      };
      STAMP(1);
      BARRIER();
      STAMP(2);
      tag = lds_word(L + LDS_TAG);
      rd(0);
#pragma unroll
      for (int n = 0; n < 32; ++n) {
        SCHED();
        if (n > 0 && (n & 1) == 0) { BARRIER(); SCHED(); }
        if (n + 1 < 32) rd(n + 1);
#pragma unroll
        for (int rt = 0; rt < 4; ++rt)
#pragma unroll
          for (int m = 0; m < 2; ++m) acc[m][rt] = mfma32(xf[n & 1][rt], bfr[n & 1][m], acc[m][rt]);
        if (n + 1 < 32) {
#pragma unroll
          for (int g = 0; g < 6; ++g) {
            __builtin_amdgcn_sched_group_barrier(0x8, 1, 0);
            __builtin_amdgcn_sched_group_barrier(0x100, 1, 0);
          }
          __builtin_amdgcn_sched_group_barrier(0x8, 2, 0);
        }
      }
      SCHED();
      BARRIER();
      STAMP(3);
      const f32x2 a2 = {ar, ar}, bb2 = {ai, ai}, nb2 = {-ai, -ai};
      f32x2 er = {0.f, 0.f}, ei = {0.f, 0.f};
#pragma unroll
      for (int rt = 0; rt < 4; ++rt)
#pragma unroll
        for (int z = 0; z < 8; ++z) {
          const f32x2 ur = {acc[0][rt][2 * z], acc[0][rt][2 * z + 1]}, ui = {acc[1][rt][2 * z], acc[1][rt][2 * z + 1]};
          f32x2 nr = __builtin_elementwise_fma(a2, er, ur), ni = __builtin_elementwise_fma(a2, ei, ui);
          nr = __builtin_elementwise_fma(nb2, ei, nr); ni = __builtin_elementwise_fma(bb2, er, ni);
          er = nr; ei = ni;
        }
      const float o0r = __shfl_xor(er[0], 32), o0i = __shfl_xor(ei[0], 32), o1r = __shfl_xor(er[1], 32), o1i = __shfl_xor(ei[1], 32);
      const float e0r = h ? o0r : er[0], e0i = h ? o0i : ei[0], e1r = h ? o1r : er[1], e1i = h ? o1i : ei[1];
      const float e2r = h ? er[0] : o0r, e2i = h ? ei[0] : o0i, e3r = h ? er[1] : o1r, e3i = h ? ei[1] : o1i;
      rsrc_t rcar = mk_rsrc(p.ws + WS_CARRY, 16 * P_ * 16);
      {
        float tr, ti, Er, Ei;
        cmul(e0r, e0i, p32r, p32i, tr, ti); tr += e1r; ti += e1i;
        cmul(tr, ti, p32r, p32i, Er, Ei); Er += e2r; Ei += e2i;
        cmul(Er, Ei, p32r, p32i, tr, ti); tr += e3r; ti += e3i;
        const v4u gq = {__float_as_uint(tr), tag, __float_as_uint(ti), ~tag};
        st_sc1(gq, rcar, h ? OOB : ch * 16, (15 - pn) * (P_ * 16));
      }
      STAMP(4);
      float Hr = 0.f, Hi = 0.f;
      {
        v4u cv[15];
#pragma nounroll
        for (unsigned spins = 0; spins < SPIN_CAP; ++spins) {
          CFENCE();
          int good = 0;
#pragma unroll
          for (int z = 0; z < 15; ++z) cv[z] = ld_sc1(rcar, ch * 16 + (16 - pn) * (P_ * 16), z * (P_ * 16));
#pragma unroll
          for (int z = 0; z < 15; ++z) good += ((cv[z][1] ^ tag) | (cv[z][3] ^ ~tag)) == 0u;
          if (__all(good == pn)) break;
          __builtin_amdgcn_s_sleep(2);
        }
#pragma unroll
        for (int z = 14; z >= 0; --z) {
          float tr, ti;
          cmul(Hr, Hi, p128r, p128i, tr, ti);
          Hr = tr + __uint_as_float(cv[z][0]); Hi = ti + __uint_as_float(cv[z][2]);
        }
      }
      STAMP(5);
      float c1r, c1i, c2r, c2i, c3r, c3i;
      cmul(Hr, Hi, p32r, p32i, c1r, c1i); c1r += e0r; c1i += e0i;
      cmul(c1r, c1i, p32r, p32i, c2r, c2i); c2r += e1r; c2i += e1i;
      cmul(c2r, c2i, p32r, p32i, c3r, c3i); c3r += e2r; c3i += e2i;
      f32x2 sr = {h ? c2r : Hr, h ? c3r : c1r}, si = {h ? c2i : Hi, h ? c3i : c1i};
      const f32x2 cr2 = {cr, cr}, nci2 = {-ci, -ci}, ncr2 = {-cr, -cr};
      LAS unsigned char* T = L + c * 16384;
      const int swz = ((i31 & 3) << 2) | (((i31 >> 3) & 1) << 1) | ((i31 >> 2) & 1);
#pragma unroll
      for (int rt = 0; rt < 4; ++rt) {
        f32x2 orr[8], oi[8];
#pragma unroll
        for (int z = 0; z < 8; ++z) {
          const f32x2 ur = {acc[0][rt][2 * z], acc[0][rt][2 * z + 1]}, ui = {acc[1][rt][2 * z], acc[1][rt][2 * z + 1]};
          f32x2 nr = __builtin_elementwise_fma(a2, sr, ur), ni = __builtin_elementwise_fma(a2, si, ui);
          nr = __builtin_elementwise_fma(nb2, si, nr); ni = __builtin_elementwise_fma(bb2, sr, ni);
          sr = nr; si = ni;
          orr[z] = __builtin_elementwise_fma(nci2, si, cr2 * sr);
          oi[z] = __builtin_elementwise_fma(nci2, sr, ncr2 * si);
        }
#pragma unroll
        for (int par = 0; par < 2; ++par) {
          const int chunk = (2 * h + par) * 4 + rt;
          const v4u vr = {pk_bf16(orr[0][par], orr[1][par]), pk_bf16(orr[2][par], orr[3][par]), pk_bf16(orr[4][par], orr[5][par]), pk_bf16(orr[6][par], orr[7][par])};
          const v4u vi = {pk_bf16(oi[0][par], oi[1][par]), pk_bf16(oi[2][par], oi[3][par]), pk_bf16(oi[4][par], oi[5][par]), pk_bf16(oi[6][par], oi[7][par])};
          *(LAS v4u*)(T + i31 * 256 + ((chunk ^ swz) << 4)) = vr;
          *(LAS v4u*)(T + (32 + i31) * 256 + ((chunk ^ swz) << 4)) = vi;
        }
      }
      {
        const int q = lane >> 4, qq = (lane & 15) >> 2, pp = lane & 3;
#pragma unroll
        for (int tt = 0; tt < 8; ++tt)
#pragma unroll
          for (int m = 0; m < 2; ++m) {
            v2u part[2];
#pragma unroll
            for (int t = 0; t < 2; ++t) {
              const int row = 32 * m + 8 * q + 4 * t + qq;
              const int chunk = 2 * tt + (pp >> 1);
              part[t] = lds_rdtr(T + row * 256 + ((chunk ^ ((qq << 2) | ((q & 1) << 1) | t)) << 4) + 8 * (pp & 1));
            }
            const int TT = 8 * pn + tt;
            const v4u o = {part[0][0], part[0][1], part[1][0], part[1][1]};
            st_sc1(o, ra2, lane * 16, ((TT >> 2) * NK + 56 + 2 * cg + m) * 4096 + (TT & 3) * 1024);
          }
      }
      SCHED();
      STAMP(6);
      publish_block(L, p.ws, fa2, tag, c, lane);
      STAMP(7);
#pragma unroll
      for (int i = 0; i < NB; ++i) ld_b2(pc, b2, i);
      SCHED();
    } else {
      const int unit = w * 256 + b;
      const bool has_unit = w < 3 && unit < 8 * NK;
      if (has_unit) { v4u rt[4]; retile_load(p, unit, lane, rt); retile_store(p, unit, lane, rt); }
      wait_vm<0>();
      BARRIER();
      tag = lds_word(L + LDS_TAG);
      st_flag(p.ws, WS_FB2 + unit * 8, tag, has_unit && lane == 0);
      const int wid = pn * 8 + w;
      if (wid < 64) {
        rsrc_t rg = mk_rsrc(p.gw, D_ * 2);
        v4u gv[8], xv[4][8];
#pragma unroll
        for (int z = 0; z < 8; ++z) gv[z] = __builtin_amdgcn_raw_buffer_load_b128(rg, (lane & 7) * 16, z * 128, 0);
#pragma unroll
        for (int gi = 0; gi < 4; ++gi)
#pragma unroll
          for (int z = 0; z < 8; ++z)
            xv[gi][z] = __builtin_amdgcn_raw_buffer_load_b128(rx, (lane >> 3) * 1024 + (lane & 7) * 16, (wid * 32 + gi * 8) * 1024 + z * 128, 0);
        rsrc_t rgo = mk_rsrc(p.ws + WS_G, S_ * 16);
#pragma unroll
        for (int gi = 0; gi < 4; ++gi) {
          float s = 0.f;
#pragma unroll
          for (int z = 0; z < 8; ++z)
#pragma unroll
            for (int y = 0; y < 4; ++y) {
              const unsigned xa = xv[gi][z][y], ga = gv[z][y];
              s = __builtin_amdgcn_fdot2_f32_bf16(__builtin_bit_cast(bf16x2, xa), __builtin_bit_cast(bf16x2, ga), s, false);
            }
          s += __shfl_xor(s, 1); s += __shfl_xor(s, 2); s += __shfl_xor(s, 4);
          const float g = 1.f / (1.f + __expf(-s));
          const v4u gq = {__float_as_uint(g), tag, ~tag, 0u};
          st_sc1(gq, rgo, (lane & 7) ? OOB : (lane >> 3) * 16, (wid * 32 + gi * 8) * 16);
        }
      }
      STAMP(3);
      poll_flags((GAS u64*)(p.ws + WS_FB2) + N * NK + k0 + (lane < pc.nk ? lane : 0), true, tag64(tag));
#pragma unroll
      for (int i = 0; i < NB; ++i) ld_b2(pc, b2, i);
      SCHED();
    }
  }

  if (w == 4) {
    if (kind == 0) {
#pragma nounroll
      for (unsigned spins = 0; spins < (SPIN_CAP << 4); ++spins) { if (all_done(L)) break; __builtin_amdgcn_s_sleep(1); }
    }
    const int p0 = (4 * R * 205) >> 11, p1 = ((4 * R + 3) * 205) >> 11, kk = lane & 15;
    poll_flags((GAS u64*)(p.ws + WS_FA2) + ((lane & 16) ? p1 : p0) * 16 + (kk < 14 ? kk : 0), true, tag64(tag));
    *(volatile LAS unsigned*)(L + LDS_RDY) = 1u;
  } else if (w == 0) {
    poll_flags((GAS u64*)(p.ws + WS_FA2) + (16 + (R >> 1)) * 16 + (lane & 3), true, tag64(tag));
    *(volatile LAS unsigned*)(L + LDS_RDY + 4) = 1u;
  } else {
    lds_wait(L + LDS_RDY + (w < 3 ? 4 : 0));
  }
  STAMP(8);
  rsrc_t ra = mk_rsrc(p.ws + WS_A2 + (R * NK + k0) * 4096, pc.nk * 4096);
  constexpr int WA = 2;
  v4u af[WA][4];
#pragma unroll
  for (int i = 0; i < WA; ++i) {
    const int o = p2_off(pc, i);
#pragma unroll
    for (int tt = 0; tt < 4; ++tt) af[i][tt] = ld_sc1(ra, pc.lane16 + tt * 1024, o);
  }
  SCHED();
  f32x4 acc[4][4];
#pragma unroll
  for (int e = 0; e < 4; ++e)
#pragma unroll
    for (int tt = 0; tt < 4; ++tt) acc[e][tt] = f32x4{0.f, 0.f, 0.f, 0.f};
#pragma unroll
  for (int i = 0; i < 12; ++i) {
    if (i < 10 || i < pc.nk) {
      const int o = p2_off(pc, i + WA);
#pragma unroll
      for (int tt = 0; tt < 4; ++tt) {
#pragma unroll
        for (int e = 0; e < 4; ++e) acc[e][tt] = mfma16(b2[i % NB][e], af[i % WA][tt], acc[e][tt]);
        SCHED();
        if (i + WA < 12) af[i % WA][tt] = ld_sc1(ra, pc.lane16 + tt * 1024, o);
        SCHED();
      }
      if (i + NB < 12) ld_b2(pc, b2, i + NB);
      SCHED();
    }
  }
  STAMP(9);
  const int tk = tid >> 3, ck = tid & 7, tok = 64 * R + tk;
  rsrc_t rdk = mk_rsrc(p.Dsk, D_ * 2), rgi = mk_rsrc(p.ws + WS_G, S_ * 16);
  const v4u xv = __builtin_amdgcn_raw_buffer_load_b128(rx, tok * 1024 + ck * 16, N * 128, 0);
  const v4u dv = __builtin_amdgcn_raw_buffer_load_b128(rdk, ck * 16, N * 128, 0);
  v4u gq = ld_sc1(rgi, tok * 16, 0);
#pragma unroll
  for (int tt = 0; tt < 4; ++tt)
#pragma unroll
    for (int e = 0; e < 4; ++e) *(LAS f32x4*)(L + w * 16384 + (tt * 4 + e) * 1024 + ((lane ^ ((lane >> 4) << 2)) << 4)) = acc[e][tt];
#pragma nounroll
  for (unsigned spins = 0; spins < SPIN_CAP; ++spins) {
    if (__all(gq[1] == tag && gq[2] == ~tag)) break;
    __builtin_amdgcn_s_sleep(4);
    CFENCE();
    gq = ld_sc1(rgi, tok * 16, 0);
  }
  if (b == 0 && w == 7) {
#pragma nounroll
    for (int z = 0; z < 4; ++z) poll_flags((GAS u64*)(p.ws + WS_FB2) + z * 64 + lane, true, tag64(tag));
    st_flag(p.ws, WS_EPOCH, tag, lane == 0);
  }
  asm volatile("s_waitcnt lgkmcnt(0)" ::: "memory");
  BARRIER();
  STAMP(10);
  {
    const int q = ck >> 1, hf = ck & 1, ln = (tk & 15) + 16 * q;
    const int base = (tk >> 4) * 4096 + ((ln ^ (q << 2)) << 4) + hf * 8;
    f32x2 sg[4], ss[4];
#pragma unroll
    for (int e = 0; e < 4; ++e) { sg[e] = f32x2{0.f, 0.f}; ss[e] = f32x2{0.f, 0.f}; }
#pragma unroll
    for (int ww = 0; ww < 8; ++ww)
#pragma unroll
      for (int e = 0; e < 4; ++e) {
        const f32x2 v = *(LAS f32x2*)(L + ww * 16384 + e * 1024 + base);
        if (ww < 3) ss[e] += v; else sg[e] += v;
      }
    const float g = __uint_as_float(gq[0]);
    float o[8];
#pragma unroll
    for (int j = 0; j < 8; ++j) {
      const float xj = (j & 1) ? bfhi(xv[j >> 1]) : bf2f(xv[j >> 1] & 0xFFFFu);
      const float dj = (j & 1) ? bfhi(dv[j >> 1]) : bf2f(dv[j >> 1] & 0xFFFFu);
      o[j] = g * (2.f * ss[j & 3][j >> 2] + xj * dj) + (1.f - g) * sg[j & 3][j >> 2];
    }
    rsrc_t ro = mk_rsrc(p.out, S_ * D_ * 4);
    const v4u o0 = {__float_as_uint(o[0]), __float_as_uint(o[1]), __float_as_uint(o[2]), __float_as_uint(o[3])};
    const v4u o1 = {__float_as_uint(o[4]), __float_as_uint(o[5]), __float_as_uint(o[6]), __float_as_uint(o[7])};
    __builtin_amdgcn_raw_buffer_store_b128(o0, ro, tok * 2048 + ck * 32, N * 256, 0);
    __builtin_amdgcn_raw_buffer_store_b128(o1, ro, tok * 2048 + ck * 32 + 16, N * 256, 0);
  }
  STAMP(11);
}

extern "C" void kernel_launch(void* const* d_in, const int* in_sizes, int n_in, void* d_out, int out_size, void* d_ws, size_t ws_size,
                              hipStream_t stream) {
  Params p;
  p.x = (const u16*)d_in[0]; p.Lre = (const u16*)d_in[1]; p.Lim = (const u16*)d_in[2]; p.Bre = (const u16*)d_in[3];
  p.Bim = (const u16*)d_in[4]; p.Cre = (const u16*)d_in[5]; p.Cim = (const u16*)d_in[6]; p.ldt = (const u16*)d_in[7];
  p.Dsk = (const u16*)d_in[8]; p.Wg = (const u16*)d_in[10]; p.Wu = (const u16*)d_in[11]; p.Wd = (const u16*)d_in[12];
  p.gw = (const u16*)d_in[13]; p.out = (float*)d_out; p.ws = (unsigned char*)d_ws;
  p.pad = 0;
  for (int b = 0; b < 256; ++b) {
    const int xcd = b & 7, r = b >> 3, H = xcd >> 2, Q = xcd & 3, nq = 7 + 2 * H, r2 = r - nq;
    const int cnt = H ? 21 : (Q < 2 ? 25 : 24), st = H ? 21 * Q : (Q < 2 ? 25 * Q : 24 * Q + 2);
    unsigned kind, pn, cb = 0;
    if (r2 < 0) { kind = 1; pn = 7 * H + r; cb = Q; }
    else if (r2 < cnt) { kind = 0; cb = (st + r2) / (7 - H); pn = 7 * H + (st + r2) % (7 - H); }
    else { kind = 2; pn = H ? 2 * Q + r2 - 19 : Q - 2; }
    p.map[b] = kind | pn << 2 | cb << 8;
  }
  (void)hipFuncSetAttribute((const void*)k_fused, hipFuncAttributeMaxDynamicSharedMemorySize, LDS_BYTES);
  k_fused<<<256, 512, LDS_BYTES, stream>>>(p);
}
```

```cpp
#define PROBE 0
#define PB 100
#define PW 0
#include <hip/hip_runtime.h>
#include <stdint.h>

typedef unsigned short u16;
typedef unsigned long long u64;
typedef unsigned v4u __attribute__((ext_vector_type(4)));
typedef unsigned v2u __attribute__((ext_vector_type(2)));
typedef short s16x4 __attribute__((ext_vector_type(4)));
typedef __bf16 bf16x8 __attribute__((ext_vector_type(8)));
typedef __bf16 bf16x2 __attribute__((ext_vector_type(2)));
typedef float f32x2 __attribute__((ext_vector_type(2)));
typedef float f32x4 __attribute__((ext_vector_type(4)));
typedef float f32x16 __attribute__((ext_vector_type(16)));
#define LAS __attribute__((address_space(3)))
#define GAS __attribute__((address_space(1)))
#define RLX_AGENT __ATOMIC_RELAXED, __HIP_MEMORY_SCOPE_AGENT
typedef __amdgpu_buffer_rsrc_t rsrc_t;

constexpr int S_ = 2048, D_ = 512, P_ = 512, F_ = 1792;
constexpr int NK = 88;
constexpr int AUX_SC1 = 16;
constexpr int WS_EPOCH = 0;
constexpr int WS_FB2 = 4096;
constexpr int WS_FA2 = 16384;
constexpr int WS_G = 32768;
constexpr int WS_CARRY = 65536;
constexpr int WS_B2 = 262144;
constexpr int B2_BYTES = 8 * NK * 4096;
constexpr int WS_A2 = 4 << 20;
constexpr int A2_BYTES = 32 * NK * 4096;
constexpr int WS_STAMP = 32 << 20;
constexpr int LDS_BYTES = 160 * 1024;
constexpr int LDS_TAG = LDS_BYTES - 16, LDS_RDY = LDS_BYTES - 8, LDS_DONE = LDS_BYTES - 32;
constexpr int OOB = 1 << 28;
constexpr int CTRL_BYTES = 32768;
constexpr int CODE_BYTES = 42 * 1024;
constexpr unsigned SPIN_CAP = 1u << 18;

struct Params {
  const u16 *x, *Wg, *Wu, *Bre, *Bim, *Cre, *Cim, *Wd;
  unsigned char* ws;
  const u16 *Lre, *Lim, *ldt, *Dsk, *gw;
  float* out;
  u64 pad;
  unsigned map[256];
};

__device__ __forceinline__ rsrc_t mk_rsrc(const void* p, int bytes) {
  return __builtin_amdgcn_make_buffer_rsrc((void*)p, 0, bytes, 0x00020000);
}
__device__ __forceinline__ float bf2f(unsigned v) { return __uint_as_float(v << 16); }
__device__ __forceinline__ float bfhi(unsigned v) { return __uint_as_float(v & 0xFFFF0000u); }
__device__ __forceinline__ unsigned pk_bf16(float lo, float hi) {
  f32x2 t = {lo, hi};
  return __builtin_bit_cast(unsigned, __builtin_convertvector(t, bf16x2));
}
__device__ __forceinline__ f32x16 mfma32(v4u a, v4u b, f32x16 c) {
  return __builtin_amdgcn_mfma_f32_32x32x16_bf16(__builtin_bit_cast(bf16x8, a), __builtin_bit_cast(bf16x8, b), c, 0, 0, 0);
}
__device__ __forceinline__ f32x4 mfma16(v4u a, v4u b, f32x4 c) {
  return __builtin_amdgcn_mfma_f32_16x16x32_bf16(__builtin_bit_cast(bf16x8, a), __builtin_bit_cast(bf16x8, b), c, 0, 0, 0);
}
__device__ __forceinline__ v4u lds_rd128(LAS unsigned char* p) { return *(LAS v4u*)p; }
__device__ __forceinline__ v2u lds_rdtr(LAS unsigned char* p) {
  return __builtin_bit_cast(v2u, __builtin_amdgcn_ds_read_tr16_b64_v4i16((LAS s16x4*)p));
}
__device__ __forceinline__ void glds(rsrc_t rs, LAS unsigned char* dst, int voff, int soff) {
  __builtin_amdgcn_raw_ptr_buffer_load_lds(rs, (LAS void*)dst, 16, voff, soff, 0, 0);
}
__device__ __forceinline__ v4u ld_sc1(rsrc_t rs, int voff, int soff) { return __builtin_amdgcn_raw_buffer_load_b128(rs, voff, soff, AUX_SC1); }
__device__ __forceinline__ void st_sc1(v4u v, rsrc_t rs, int voff, int soff) { __builtin_amdgcn_raw_buffer_store_b128(v, rs, voff, soff, AUX_SC1); }
__device__ __forceinline__ u64 tag64(unsigned tag) { return ((u64)(~tag) << 32) | tag; }
__device__ __forceinline__ void st_flag(unsigned char* ws, int off, unsigned tag, bool on) {
  __builtin_amdgcn_raw_buffer_store_b64(v2u{tag, ~tag}, mk_rsrc(ws, CTRL_BYTES), on ? 0 : OOB, off, AUX_SC1);
}
__device__ __forceinline__ unsigned lds_word(LAS unsigned char* p) { return __builtin_amdgcn_readfirstlane(*(volatile LAS unsigned*)p); }
#define BARRIER() asm volatile("s_barrier" ::: "memory")
#define SCHED() __builtin_amdgcn_sched_barrier(0)
#define CFENCE() asm volatile("" ::: "memory")
template <int N> __device__ __forceinline__ void wait_vm() { asm volatile("s_waitcnt vmcnt(%c0)" ::"i"(N) : "memory"); }

#define STAMP(i) do { if ((i) == 0 || (i) == PROBE) stamp_[i] = __builtin_amdgcn_s_memrealtime(); } while (0)

__device__ __forceinline__ void poll_flags(GAS u64* f, bool valid, u64 want) {
#pragma nounroll
  for (unsigned spins = 0; spins < SPIN_CAP; ++spins) {
    CFENCE();
    const u64 v = __hip_atomic_load(f, RLX_AGENT);
    if (__all(!valid || v == want)) break;
    __builtin_amdgcn_s_sleep(4);
  }
  CFENCE();
}

constexpr int NB = 8;
constexpr int S_RT = 3;
constexpr int S_CHK = 9;
#define NROT 2
struct Sched {
  int n;
  int type[40], idx[40];
  int lo[17], hi[17];
  int b2lo[17], b2hi[17];
  int wait[17];
};
constexpr Sched make_sched(int kind) {
  Sched s{};
  int cnt[40] = {}, need[40] = {}, avail[40] = {};
  int n = 0;
  auto add = [&](int ty, int ix, int c, int nd, int av) { s.type[n] = ty; s.idx[n] = ix; cnt[n] = c; need[n] = nd; avail[n] = av; ++n; };
  if (kind == 0) {
    add(0, 0, 3, 0, -1); add(1, 0, 2, 0, -1); add(2, 0, 4, 0, -1); add(2, 1, 4, 0, -1);
    add(0, 1, 3, 1, -1); add(1, 1, 2, 1, -1); add(2, 2, 4, 1, -1); add(2, 3, 4, 2, -1);
    add(0, 2, 3, 3, -1); add(1, 2, 2, 3, -1);
    for (int t = 4; t < 16; ++t) {
      add(2, t, 4, t - 1, t - 3);
      const int b = t - 3, T = b / 2 + 2;
      if (b >= 2 && T <= 7) add(b & 1, T, (b & 1) ? 2 : 3, 2 * T - 1, b);
    }
  } else {
    for (int T = 0; T < 8; ++T)
      for (int ty = 0; ty < 2; ++ty) add(ty, T, 6, T == 0 ? 0 : 2 * T - 1, T < 3 ? -1 : 2 * T - 4 + ty);
  }
  s.n = n;
  const int LIM = 8;
  int endpos[40] = {};
  int next = 0, total = 0, b2 = 0;
  for (int b = 0; b <= 16; ++b) {
    s.lo[b] = next;
    for (;;) {
      if (next >= n || avail[next] > b - 1) break;
      int lastneed = 0;
      for (int i = 0; i < next; ++i) if (need[i] <= b) lastneed = endpos[i];
      if (b < 4 && need[next] > b && total + cnt[next] - lastneed > LIM + 2 * b) break;
      total += cnt[next]; endpos[next] = total; ++next;
    }
    s.hi[b] = next;
    s.b2lo[b] = b2;
    if (b >= S_CHK + 2 && b2 < NB) { ++b2; total += 4; }
    if (b >= 14 && b2 < NB) { ++b2; total += 4; }
    if (b == 16) { total += 4 * (NB - b2); b2 = NB; }
    s.b2hi[b] = b2;
    int lastneed = 0;
    for (int i = 0; i < next; ++i) if (need[i] <= b) lastneed = endpos[i];
    for (int i = next; i < n; ++i) if (need[i] <= b) lastneed = 1 << 20;
    s.wait[b] = total - lastneed;
    if (s.wait[b] > 63) s.wait[b] = 63;
  }
  return s;
}
constexpr bool sched_ok(const Sched& s) { for (int b = 0; b <= 16; ++b) if (s.wait[b] < 0) return false; return s.hi[16] == s.n; }
constexpr Sched SCH[2] = {make_sched(0), make_sched(1)};
static_assert(sched_ok(SCH[0]) && sched_ok(SCH[1]), "every ring item must be issued in time");

constexpr int GX_SLOT = 20480, GW_BASE = 61440, GW_SLOT = 16384;
constexpr int SX_SLOT = 16384, SB_BASE = 49152, SB_SLOT = 32768;

struct P2Ctx {
  rsrc_t rb;
  int nk, rot, lane16;
};
__device__ __forceinline__ int p2_off(const P2Ctx& c, int i) {
  int t = i + c.rot; t = t >= c.nk ? t - c.nk : t;
  return i < c.nk ? t * 4096 : OOB;
}
__device__ __forceinline__ void ld_b2(const P2Ctx& c, v4u (&b2)[NB][4], int i) {
  const int o = p2_off(c, i);
#pragma unroll
  for (int e = 0; e < 4; ++e) b2[i % NB][e] = ld_sc1(c.rb, c.lane16 + e * 1024, o);
}

__device__ __forceinline__ void retile_load(const Params& p, int unit, int lane, v4u (&f)[4]) {
  const int N = unit / NK, kap = unit - N * NK, q = lane >> 4, i = lane & 15;
  if (kap < 56) {
    rsrc_t rd = mk_rsrc(p.Wd, F_ * D_ * 2);
#pragma unroll
    for (int j = 0; j < 8; ++j) {
      const v2u t = __builtin_amdgcn_raw_buffer_load_b64(rd, (4 * q + (j & 3) + 16 * (j >> 2)) * (D_ * 2) + i * 8, kap * (32 * D_ * 2) + N * 128, 0);
      f[j >> 1][2 * (j & 1)] = t[0]; f[j >> 1][2 * (j & 1) + 1] = t[1];
    }
  } else {
    const int cg = (kap - 56) >> 1, m = kap & 1;
    rsrc_t rc = mk_rsrc(m ? p.Cim : p.Cre, D_ * P_ * 2);
#pragma unroll
    for (int e = 0; e < 4; ++e) f[e] = __builtin_amdgcn_raw_buffer_load_b128(rc, (4 * i + e) * (P_ * 2) + q * 16, N * (64 * P_ * 2) + cg * 64, 0);
  }
}
__device__ __forceinline__ void retile_store(const Params& p, int unit, int lane, const v4u (&f)[4]) {
  const int kap = unit % NK;
  rsrc_t rws = mk_rsrc(p.ws + WS_B2, B2_BYTES);
  if (kap < 56) {
#pragma unroll
    for (int e = 0; e < 4; ++e) {
      v4u o;
#pragma unroll
      for (int z = 0; z < 4; ++z) o[z] = __builtin_amdgcn_perm(f[z][2 + (e >> 1)], f[z][e >> 1], (e & 1) ? 0x07060302u : 0x05040100u);
      st_sc1(o, rws, lane * 16 + e * 1024, unit * 4096);
    }
  } else {
#pragma unroll
    for (int e = 0; e < 4; ++e) st_sc1(f[e], rws, lane * 16 + e * 1024, unit * 4096);
  }
}

struct LdCtx { rsrc_t rx, rw; int lw, vx, vw0, vw1, sx0, sw0, rot; GAS u64* fb2; int unit, lane; mutable unsigned sv; };

template <int KIND, int I> __device__ __forceinline__ void ld_item(const LdCtx& c, LAS unsigned char* L) {
  constexpr int ty = SCH[KIND].type[I], t = SCH[KIND].idx[I];
  if constexpr (KIND == 0 && ty < 2) {
    const int k0 = ((t + c.rot) & 7) * 128;
#pragma unroll
    for (int n = 3 * ty; n < 3 + 2 * ty; ++n) glds(c.rx, L + (t % 3) * GX_SLOT + c.lw * 1024 + n * 4096, c.vx, c.sx0 + k0 + n * 32768);
  } else if constexpr (KIND == 0) {
    const int k0 = ((((t >> 1) + c.rot) & 7) * 64 + (t & 1) * 32) * (F_ * 2);
#pragma unroll
    for (int i = 0; i < 4; ++i)
      glds(c.rw, L + GW_BASE + (t & 3) * GW_SLOT + c.lw * 4096 + i * 1024, (i >> 1) ? c.vw1 : c.vw0, c.sw0 + k0 + i * 4 * (F_ * 2));
  } else {
    const int k0 = ((t + c.rot) & 7) * 128;
#pragma unroll
    for (int n = 2 * ty; n < 2 * ty + 2; ++n) glds(c.rx, L + (t % 3) * SX_SLOT + c.lw * 1024 + n * 4096, c.vx, c.sx0 + k0 + n * 8192);
#pragma unroll
    for (int n = 4 * ty; n < 4 * ty + 4; ++n)
      glds(c.rw, L + SB_BASE + (t % 3) * SB_SLOT + (c.lw >> 1) * 16384 + (c.lw & 1) * 1024 + n * 2048, c.vw0, c.sw0 + k0 + n * 16384);
  }
  SCHED();
}
template <int KIND, int LO, int HI> __device__ __forceinline__ void ld_items(const LdCtx& c, LAS unsigned char* L) {
  if constexpr (LO < HI) { ld_item<KIND, LO>(c, L); ld_items<KIND, LO + 1, HI>(c, L); }
}
template <int KIND, int B>
__device__ __forceinline__ void loader_steps(const Params& p, const LdCtx& c, LAS unsigned char* L, const P2Ctx& pc, v4u (&b2)[NB][4], v4u (&rt)[4],
                                             unsigned& tag) {
  if constexpr (B == S_RT) { if (c.unit < 8 * NK) retile_load(p, c.unit, c.lane, rt); SCHED(); }
  if constexpr (B == S_RT + 2) { if (c.unit < 8 * NK) retile_store(p, c.unit, c.lane, rt); SCHED(); }
  if constexpr (B == S_RT + 4) {
    tag = lds_word(L + LDS_TAG);
    wait_vm<0>();
    st_flag(p.ws, WS_FB2 + c.unit * 8, tag, c.unit < 8 * NK && c.lane == 0);
    SCHED();
  }
  if constexpr (B == S_CHK + 1) poll_flags(c.fb2, true, tag64(tag));
  if constexpr (B > 0) ld_items<KIND, SCH[KIND].lo[B], SCH[KIND].hi[B]>(c, L);
#pragma unroll
  for (int i = SCH[KIND].b2lo[B]; i < SCH[KIND].b2hi[B]; ++i) ld_b2(pc, b2, i);
  SCHED();
  wait_vm<SCH[KIND].wait[B]>();
  BARRIER();
  if constexpr (B < 16) loader_steps<KIND, B + 1>(p, c, L, pc, b2, rt, tag);
}
__device__ __forceinline__ bool all_done(LAS unsigned char* L) {
  const v4u d = *(volatile LAS v4u*)(L + LDS_DONE);
  return __builtin_amdgcn_readfirstlane(d[0] & d[1] & d[2] & d[3]) != 0u;
}
__device__ __forceinline__ void publish_block(LAS unsigned char* L, unsigned char* ws, int off, unsigned tag, int c, int lane) {
  wait_vm<0>();
  *(volatile LAS unsigned*)(L + LDS_DONE + 4 * c) = 1u;
  const bool last = all_done(L);
  st_flag(ws, off, tag, last && lane == 0);
  CFENCE();
}
__device__ __forceinline__ void lds_wait(LAS unsigned char* word) {
#pragma nounroll
  for (unsigned spins = 0; spins < (SPIN_CAP << 4); ++spins) {
    if (lds_word(word) != 0u) break;
    __builtin_amdgcn_s_sleep(1);
  }
  CFENCE();
}

__device__ __forceinline__ void cmul(float ar, float ai, float br, float bi, float& cr, float& ci) {
  cr = ar * br - ai * bi; ci = ar * bi + ai * br;
}

__global__ __launch_bounds__(512, 2) void k_fused(Params p) {
  extern __shared__ __attribute__((aligned(16))) unsigned char lds_[];
  LAS unsigned char* L = (LAS unsigned char*)lds_;
  u64 stamp_[16] = {};
  STAMP(0);
  const int b = blockIdx.x, tid = threadIdx.x, lane = tid & 63;
  const int w = __builtin_amdgcn_readfirstlane(tid >> 6);
  const int xcd = b & 7, r = b >> 3;
  const unsigned mp = p.map[b];
  unsigned zpf = 0;
  if (w < 4) {
    const u64 pcv = __builtin_amdgcn_s_getpc();
    rsrc_t rcode = mk_rsrc((const void*)(pcv & ~(u64)127), CODE_BYTES);
    zpf = __builtin_amdgcn_raw_buffer_load_b32(rcode, lane < 4 ? lane * 16384 : OOB, (r * 4 + w) * 128, 0);
  }
  const int kind = mp & 3, pn = (mp >> 2) & 63, cb = mp >> 8;
  const int rot = (8 / NROT) * ((pn + cb) & (NROT - 1));
  const int R = xcd * 4 + (r & 3), N = r >> 2;
  const int k0 = (int)(0x22170C002D4D4238ull >> (8 * w)) & 255;
  P2Ctx pc;
  pc.nk = (0xBBBCBBBAu >> (4 * w)) & 15;
  pc.rot = (N * 5) >> 2;
  pc.lane16 = lane * 16;
  pc.rb = mk_rsrc(p.ws + WS_B2 + (N * NK + k0) * 4096, pc.nk * 4096);
  rsrc_t rx = mk_rsrc(p.x, S_ * D_ * 2);
  v4u b2[NB][4];
  unsigned tag;

  if (__builtin_expect(w >= 4 && kind != 2, 1)) {
    LdCtx c;
    c.rx = rx; c.lw = w - 4; c.rot = rot;
    const int rr = 8 * c.lw + (lane >> 3);
    const int sw = ((lane & 7) ^ ((rr >> 1) & 7)) << 4;
    if (kind == 0) {
      const int m = c.lw >> 1, kq = lane >> 4;
      c.vx = rr * 1024 + sw;
      c.sx0 = pn * 160 * 1024;
      c.rw = mk_rsrc(m ? p.Wu : p.Wg, D_ * F_ * 2);
      c.vw0 = ((c.lw & 1) * 16 + kq) * (F_ * 2) + (((lane & 15) ^ (kq << 2)) << 4);
      c.vw1 = c.vw0 ^ 32;
      c.sw0 = cb * 256;
      STAMP(1);
      ld_items<0, 0, SCH[0].hi[0]>(c, L);
    } else {
      const int tok = 64 * ((rr >> 2) & 1) + 32 * (rr & 1) + ((rr >> 1) & 1) + 2 * (rr >> 3);
      c.vx = tok * 1024 + sw;
      c.sx0 = pn * 128 * 1024;
      c.rw = mk_rsrc((c.lw >> 1) ? p.Bim : p.Bre, P_ * D_ * 2);
      const int brow = 8 * (c.lw & 1) + (lane >> 3);
      c.vw0 = brow * 1024 + (((lane & 7) ^ ((brow >> 1) & 7)) << 4);
      c.vw1 = 0;
      c.sw0 = cb * 128 * 1024;
      STAMP(1);
      ld_items<1, 0, SCH[1].hi[0]>(c, L);
    }
    STAMP(2);
    int kind2 = kind;
    asm volatile("" : "+s"(kind2));
    c.fb2 = (GAS u64*)(p.ws + WS_FB2) + N * NK + c.lw * 22 + (lane < 22 ? lane : 0);
    c.unit = c.lw * 256 + b; c.lane = lane; c.sv = 0;
    v4u rt[4];
    if (kind2 == 0) loader_steps<0, 0>(p, c, L, pc, b2, rt, tag);
    else loader_steps<1, 0>(p, c, L, pc, b2, rt, tag);
    STAMP(3);
  } else {
    if (w == 3) {
      const unsigned e = __hip_atomic_load((GAS unsigned*)(p.ws + WS_EPOCH), RLX_AGENT) + 1u;
      *(volatile LAS v4u*)(L + LDS_TAG) = v4u{e, 0u, 0u, 0u};
      *(volatile LAS v4u*)(L + LDS_DONE) = v4u{0u, 0u, 0u, 0u};
      asm volatile("s_waitcnt lgkmcnt(0)" ::: "memory");
    }
    if (w == 2) {
      const int g = lane >> 3, j = lane & 7;
      int off = OOB;
      if (g == 0) off = WS_A2 + R * NK * 4096 + (j < 6 ? j : 0) * 65536;
      if (g == 1) off = WS_B2 + N * NK * 4096 + (j < 6 ? j : 0) * 65536;
      if (g == 2) { const int R0 = kind == 0 ? (10 * pn) >> 2 : 2 * pn; const int Rq = R0 + (j & 3) < 32 ? R0 + (j & 3) : 31;
                    off = WS_A2 + (Rq * NK + (kind == 0 ? 4 * cb : 56 + 8 * cb) + (j >> 2) * 3) * 4096; }
      if (g == 3) off = j * 32768;
      zpf ^= __builtin_amdgcn_raw_buffer_load_b32(mk_rsrc(p.ws, WS_A2 + A2_BYTES), off, 0, 0);
      zpf ^= __builtin_amdgcn_raw_buffer_load_b32(mk_rsrc(p.out, S_ * D_ * 4), lane < 4 ? lane * 32768 + N * 256 : OOB, R * 131072, 0);
      zpf ^= __builtin_amdgcn_raw_buffer_load_b32(mk_rsrc(p.Dsk, D_ * 2), lane < 1 ? 0 : OOB, 0, 0);
    }
    asm volatile("" ::"v"(zpf));
    rsrc_t ra2 = mk_rsrc(p.ws + WS_A2, A2_BYTES);
    const int fa2 = WS_FA2 + ((kind * 16 + pn) * 16 + cb) * 8;
    const int c = w;

    if (kind == 0) {
      const int q = lane >> 4, i15 = lane & 15;
      int xa[2], wa[2];
#pragma unroll
      for (int ks = 0; ks < 2; ++ks) xa[ks] = i15 * 128 + (((4 * ks + q) ^ ((i15 >> 1) & 7)) << 4);
      {
        const int qq = i15 >> 2, pp = lane & 3;
#pragma unroll
        for (int u = 0; u < 2; ++u)
          wa[u] = GW_BASE + (8 * q + qq) * 256 + (((2 * (2 * c + u) + (pp >> 1)) ^ ((qq << 2) | ((q & 1) << 1))) << 4) + 8 * (pp & 1);
      }
      f32x4 acc[4][10];
#pragma unroll
      for (int a = 0; a < 4; ++a)
#pragma unroll
        for (int tt = 0; tt < 10; ++tt) acc[a][tt] = f32x4{0.f, 0.f, 0.f, 0.f};
      v4u xf[4];
      v2u wf[2][8];
      auto rdx = [&](int it) {
        const int s = it / 10, tt = it % 10;
        xf[it & 3] = lds_rd128(L + xa[s & 1] + ((s >> 1) % 3) * GX_SLOT + tt * 2048);
      };
      auto rdw = [&](int s, int z) {
        const int m = z >> 2, u = (z >> 1) & 1, t = z & 1;
        wf[s & 1][z] = lds_rdtr(L + wa[u] + (s & 3) * GW_SLOT + m * 8192 + t * 1024);
      };
      STAMP(1);
      BARRIER();
      STAMP(2);
      tag = lds_word(L + LDS_TAG);
#pragma unroll
      for (int z = 0; z < 8; ++z) rdw(0, z);
      rdx(0); rdx(1); rdx(2);
#pragma clang loop unroll(full)
      for (int s = 0; s < 16; ++s)
#pragma clang loop unroll(full)
      for (int tt = 0; tt < 10; ++tt) {
        const int it = 10 * s + tt;
        SCHED();
        if (tt == 0 && s > 0) { BARRIER(); SCHED(); }
        if (it + 3 < 160) rdx(it + 3);
        if (tt < 8 && s < 15) rdw(s + 1, tt);
#pragma unroll
        for (int a = 0; a < 4; ++a) {
          const v4u wv = {wf[s & 1][2 * a][0], wf[s & 1][2 * a][1], wf[s & 1][2 * a + 1][0], wf[s & 1][2 * a + 1][1]};
          acc[a][tt] = mfma16(wv, xf[it & 3], acc[a][tt]);
        }
        __builtin_amdgcn_sched_group_barrier(0x8, 1, 0);
        __builtin_amdgcn_sched_group_barrier(0x100, 1, 0);
        __builtin_amdgcn_sched_group_barrier(0x8, 1, 0);
        __builtin_amdgcn_sched_group_barrier(0x100, 1, 0);
        __builtin_amdgcn_sched_group_barrier(0x8, 2, 0);
      }
      SCHED();
      BARRIER();
      STAMP(3);
      const int kap = 4 * cb + c;
#pragma unroll
      for (int tt = 0; tt < 10; ++tt) {
        float hv[8];
#pragma unroll
        for (int j = 0; j < 8; ++j) {
          const float e = __builtin_amdgcn_exp2f(acc[j >> 2][tt][j & 3] * -1.44269504088896341f);
          hv[j] = acc[2 + (j >> 2)][tt][j & 3] * __builtin_amdgcn_rcpf(1.f + e);
        }
        const v4u o = {pk_bf16(hv[0], hv[1]), pk_bf16(hv[2], hv[3]), pk_bf16(hv[4], hv[5]), pk_bf16(hv[6], hv[7])};
        const int TT = 10 * pn + tt;
        st_sc1(o, ra2, lane * 16, ((TT >> 2) * NK + kap) * 4096 + (TT & 3) * 1024);
      }
      SCHED();
      STAMP(4);
      publish_block(L, p.ws, fa2, tag, c, lane);
      STAMP(5);
#pragma unroll
      for (int i = 0; i < NB; ++i) ld_b2(pc, b2, i);
      SCHED();
    } else if (kind == 1) {
      const int h = lane >> 5, i31 = lane & 31;
      int xa[4];
#pragma unroll
      for (int ks = 0; ks < 4; ++ks) xa[ks] = i31 * 128 + (((2 * ks + h) ^ ((i31 >> 1) & 7)) << 4);
      const int cg = 4 * cb + c, ch = 32 * cg + i31;
      const float lr = bf2f(p.Lre[ch]), li = bf2f(p.Lim[ch]);
      float dt = expf(bf2f(p.ldt[ch]));
      dt = bf2f(pk_bf16(dt, 0.f) & 0xFFFFu);
      float sn, cs;
      sincosf(dt * li, &sn, &cs);
      const float ex = expf(dt * lr);
      const float ar = ex * cs, ai = ex * sn;
      const float den = 1.f / (lr * lr + li * li);
      const float cr = ((ar - 1.f) * lr + ai * li) * den, ci = (ai * lr - (ar - 1.f) * li) * den;
      float p32r = ar, p32i = ai;
#pragma unroll
      for (int z = 0; z < 5; ++z) { float t0, t1; cmul(p32r, p32i, p32r, p32i, t0, t1); p32r = t0; p32i = t1; }
      float p128r = p32r, p128i = p32i;
#pragma unroll
      for (int z = 0; z < 2; ++z) { float t0, t1; cmul(p128r, p128i, p128r, p128i, t0, t1); p128r = t0; p128i = t1; }

      f32x16 acc[2][4];
#pragma unroll
      for (int m = 0; m < 2; ++m)
#pragma unroll
        for (int rt = 0; rt < 4; ++rt) acc[m][rt] = f32x16{};
      v4u xf[2][4], bfr[2][2];
      auto rd = [&](int n) {
        const int bq = n & 1, T = n >> 2;
#pragma unroll
        for (int m = 0; m < 2; ++m) bfr[bq][m] = lds_rd128(L + SB_BASE + c * 4096 + xa[n & 3] + (T % 3) * SB_SLOT + m * 16384);
#pragma unroll
        for (int rt = 0; rt < 4; ++rt) xf[bq][rt] = lds_rd128(L + xa[n & 3] + (T % 3) * SX_SLOT + rt * 4096);
      };
      STAMP(1);
      BARRIER();
      STAMP(2);
      tag = lds_word(L + LDS_TAG);
      rd(0);
#pragma unroll
      for (int n = 0; n < 32; ++n) {
        SCHED();
        if (n > 0 && (n & 1) == 0) { BARRIER(); SCHED(); }
        if (n + 1 < 32) rd(n + 1);
#pragma unroll
        for (int rt = 0; rt < 4; ++rt)
#pragma unroll
          for (int m = 0; m < 2; ++m) acc[m][rt] = mfma32(xf[n & 1][rt], bfr[n & 1][m], acc[m][rt]);
        if (n + 1 < 32) {
#pragma unroll
          for (int g = 0; g < 6; ++g) {
            __builtin_amdgcn_sched_group_barrier(0x8, 1, 0);
            __builtin_amdgcn_sched_group_barrier(0x100, 1, 0);
          }
          __builtin_amdgcn_sched_group_barrier(0x8, 2, 0);
        }
      }
      SCHED();
      BARRIER();
      STAMP(3);
      const f32x2 a2 = {ar, ar}, bb2 = {ai, ai}, nb2 = {-ai, -ai};
      f32x2 er = {0.f, 0.f}, ei = {0.f, 0.f};
#pragma unroll
      for (int rt = 0; rt < 4; ++rt)
#pragma unroll
        for (int z = 0; z < 8; ++z) {
          const f32x2 ur = {acc[0][rt][2 * z], acc[0][rt][2 * z + 1]}, ui = {acc[1][rt][2 * z], acc[1][rt][2 * z + 1]};
          f32x2 nr = __builtin_elementwise_fma(a2, er, ur), ni = __builtin_elementwise_fma(a2, ei, ui);
          nr = __builtin_elementwise_fma(nb2, ei, nr); ni = __builtin_elementwise_fma(bb2, er, ni);
          er = nr; ei = ni;
        }
      const float o0r = __shfl_xor(er[0], 32), o0i = __shfl_xor(ei[0], 32), o1r = __shfl_xor(er[1], 32), o1i = __shfl_xor(ei[1], 32);
      const float e0r = h ? o0r : er[0], e0i = h ? o0i : ei[0], e1r = h ? o1r : er[1], e1i = h ? o1i : ei[1];
      const float e2r = h ? er[0] : o0r, e2i = h ? ei[0] : o0i, e3r = h ? er[1] : o1r, e3i = h ? ei[1] : o1i;
      rsrc_t rcar = mk_rsrc(p.ws + WS_CARRY, 16 * P_ * 16);
      {
        float tr, ti, Er, Ei;
        cmul(e0r, e0i, p32r, p32i, tr, ti); tr += e1r; ti += e1i;
        cmul(tr, ti, p32r, p32i, Er, Ei); Er += e2r; Ei += e2i;
        cmul(Er, Ei, p32r, p32i, tr, ti); tr += e3r; ti += e3i;
        const v4u gq = {__float_as_uint(tr), tag, __float_as_uint(ti), ~tag};
        st_sc1(gq, rcar, h ? OOB : ch * 16, (15 - pn) * (P_ * 16));
      }
      STAMP(4);
      float Hr = 0.f, Hi = 0.f;
      {
        v4u cv[15];
#pragma nounroll
        for (unsigned spins = 0; spins < SPIN_CAP; ++spins) {
          CFENCE();
          int good = 0;
#pragma unroll
          for (int z = 0; z < 15; ++z) cv[z] = ld_sc1(rcar, ch * 16 + (16 - pn) * (P_ * 16), z * (P_ * 16));
#pragma unroll
          for (int z = 0; z < 15; ++z) good += ((cv[z][1] ^ tag) | (cv[z][3] ^ ~tag)) == 0u;
          if (__all(good == pn)) break;
          __builtin_amdgcn_s_sleep(2);
        }
#pragma unroll
        for (int z = 14; z >= 0; --z) {
          float tr, ti;
          cmul(Hr, Hi, p128r, p128i, tr, ti);
          Hr = tr + __uint_as_float(cv[z][0]); Hi = ti + __uint_as_float(cv[z][2]);
        }
      }
      STAMP(5);
      float c1r, c1i, c2r, c2i, c3r, c3i;
      cmul(Hr, Hi, p32r, p32i, c1r, c1i); c1r += e0r; c1i += e0i;
      cmul(c1r, c1i, p32r, p32i, c2r, c2i); c2r += e1r; c2i += e1i;
      cmul(c2r, c2i, p32r, p32i, c3r, c3i); c3r += e2r; c3i += e2i;
      f32x2 sr = {h ? c2r : Hr, h ? c3r : c1r}, si = {h ? c2i : Hi, h ? c3i : c1i};
      const f32x2 cr2 = {cr, cr}, nci2 = {-ci, -ci}, ncr2 = {-cr, -cr};
      LAS unsigned char* T = L + c * 16384;
      const int swz = ((i31 & 3) << 2) | (((i31 >> 3) & 1) << 1) | ((i31 >> 2) & 1);
#pragma unroll
      for (int rt = 0; rt < 4; ++rt) {
        f32x2 orr[8], oi[8];
#pragma unroll
        for (int z = 0; z < 8; ++z) {
          const f32x2 ur = {acc[0][rt][2 * z], acc[0][rt][2 * z + 1]}, ui = {acc[1][rt][2 * z], acc[1][rt][2 * z + 1]};
          f32x2 nr = __builtin_elementwise_fma(a2, sr, ur), ni = __builtin_elementwise_fma(a2, si, ui);
          nr = __builtin_elementwise_fma(nb2, si, nr); ni = __builtin_elementwise_fma(bb2, sr, ni);
          sr = nr; si = ni;
          orr[z] = __builtin_elementwise_fma(nci2, si, cr2 * sr);
          oi[z] = __builtin_elementwise_fma(nci2, sr, ncr2 * si);
        }
#pragma unroll
        for (int par = 0; par < 2; ++par) {
          const int chunk = (2 * h + par) * 4 + rt;
          const v4u vr = {pk_bf16(orr[0][par], orr[1][par]), pk_bf16(orr[2][par], orr[3][par]), pk_bf16(orr[4][par], orr[5][par]), pk_bf16(orr[6][par], orr[7][par])};
          const v4u vi = {pk_bf16(oi[0][par], oi[1][par]), pk_bf16(oi[2][par], oi[3][par]), pk_bf16(oi[4][par], oi[5][par]), pk_bf16(oi[6][par], oi[7][par])};
          *(LAS v4u*)(T + i31 * 256 + ((chunk ^ swz) << 4)) = vr;
          *(LAS v4u*)(T + (32 + i31) * 256 + ((chunk ^ swz) << 4)) = vi;
        }
      }
      {
        const int q = lane >> 4, qq = (lane & 15) >> 2, pp = lane & 3;
#pragma unroll
        for (int tt = 0; tt < 8; ++tt)
#pragma unroll
          for (int m = 0; m < 2; ++m) {
            v2u part[2];
#pragma unroll
            for (int t = 0; t < 2; ++t) {
              const int row = 32 * m + 8 * q + 4 * t + qq;
              const int chunk = 2 * tt + (pp >> 1);
              part[t] = lds_rdtr(T + row * 256 + ((chunk ^ ((qq << 2) | ((q & 1) << 1) | t)) << 4) + 8 * (pp & 1));
            }
            const int TT = 8 * pn + tt;
            const v4u o = {part[0][0], part[0][1], part[1][0], part[1][1]};
            st_sc1(o, ra2, lane * 16, ((TT >> 2) * NK + 56 + 2 * cg + m) * 4096 + (TT & 3) * 1024);
          }
      }
      SCHED();
      STAMP(6);
      publish_block(L, p.ws, fa2, tag, c, lane);
      STAMP(7);
#pragma unroll
      for (int i = 0; i < NB; ++i) ld_b2(pc, b2, i);
      SCHED();
    } else {
      const int unit = w * 256 + b;
      const bool has_unit = w < 3 && unit < 8 * NK;
      if (has_unit) { v4u rt[4]; retile_load(p, unit, lane, rt); retile_store(p, unit, lane, rt); }
      wait_vm<0>();
      BARRIER();
      tag = lds_word(L + LDS_TAG);
      st_flag(p.ws, WS_FB2 + unit * 8, tag, has_unit && lane == 0);
      const int wid = pn * 8 + w;
      if (wid < 64) {
        rsrc_t rg = mk_rsrc(p.gw, D_ * 2);
        v4u gv[8], xv[4][8];
#pragma unroll
        for (int z = 0; z < 8; ++z) gv[z] = __builtin_amdgcn_raw_buffer_load_b128(rg, (lane & 7) * 16, z * 128, 0);
#pragma unroll
        for (int gi = 0; gi < 4; ++gi)
#pragma unroll
          for (int z = 0; z < 8; ++z)
            xv[gi][z] = __builtin_amdgcn_raw_buffer_load_b128(rx, (lane >> 3) * 1024 + (lane & 7) * 16, (wid * 32 + gi * 8) * 1024 + z * 128, 0);
        rsrc_t rgo = mk_rsrc(p.ws + WS_G, S_ * 16);
#pragma unroll
        for (int gi = 0; gi < 4; ++gi) {
          float s = 0.f;
#pragma unroll
          for (int z = 0; z < 8; ++z)
#pragma unroll
            for (int y = 0; y < 4; ++y) {
              const unsigned xa = xv[gi][z][y], ga = gv[z][y];
              s = __builtin_amdgcn_fdot2_f32_bf16(__builtin_bit_cast(bf16x2, xa), __builtin_bit_cast(bf16x2, ga), s, false);
            }
          s += __shfl_xor(s, 1); s += __shfl_xor(s, 2); s += __shfl_xor(s, 4);
          const float g = 1.f / (1.f + __expf(-s));
          const v4u gq = {__float_as_uint(g), tag, ~tag, 0u};
          st_sc1(gq, rgo, (lane & 7) ? OOB : (lane >> 3) * 16, (wid * 32 + gi * 8) * 16);
        }
      }
      STAMP(3);
      poll_flags((GAS u64*)(p.ws + WS_FB2) + N * NK + k0 + (lane < pc.nk ? lane : 0), true, tag64(tag));
#pragma unroll
      for (int i = 0; i < NB; ++i) ld_b2(pc, b2, i);
      SCHED();
    }
  }

  if (w == 4) {
    if (kind == 0) {
#pragma nounroll
      for (unsigned spins = 0; spins < (SPIN_CAP << 4); ++spins) { if (all_done(L)) break; __builtin_amdgcn_s_sleep(1); }
    }
    const int p0 = (4 * R * 205) >> 11, p1 = ((4 * R + 3) * 205) >> 11, kk = lane & 15;
    poll_flags((GAS u64*)(p.ws + WS_FA2) + ((lane & 16) ? p1 : p0) * 16 + (kk < 14 ? kk : 0), true, tag64(tag));
    *(volatile LAS unsigned*)(L + LDS_RDY) = 1u;
  } else if (w == 0) {
    poll_flags((GAS u64*)(p.ws + WS_FA2) + (16 + (R >> 1)) * 16 + (lane & 3), true, tag64(tag));
    *(volatile LAS unsigned*)(L + LDS_RDY + 4) = 1u;
  } else {
    lds_wait(L + LDS_RDY + (w < 3 ? 4 : 0));
  }
  STAMP(8);
  rsrc_t ra = mk_rsrc(p.ws + WS_A2 + (R * NK + k0) * 4096, pc.nk * 4096);
  constexpr int WA = 2;
  v4u af[WA][4];
#pragma unroll
  for (int i = 0; i < WA; ++i) {
    const int o = p2_off(pc, i);
#pragma unroll
    for (int tt = 0; tt < 4; ++tt) af[i][tt] = ld_sc1(ra, pc.lane16 + tt * 1024, o);
  }
  SCHED();
  f32x4 acc[4][4];
#pragma unroll
  for (int e = 0; e < 4; ++e)
#pragma unroll
    for (int tt = 0; tt < 4; ++tt) acc[e][tt] = f32x4{0.f, 0.f, 0.f, 0.f};
#pragma unroll
  for (int i = 0; i < 12; ++i) {
    if (i < 10 || i < pc.nk) {
      const int o = p2_off(pc, i + WA);
#pragma unroll
      for (int tt = 0; tt < 4; ++tt) {
#pragma unroll
        for (int e = 0; e < 4; ++e) acc[e][tt] = mfma16(b2[i % NB][e], af[i % WA][tt], acc[e][tt]);
        SCHED();
        if (i + WA < 12) af[i % WA][tt] = ld_sc1(ra, pc.lane16 + tt * 1024, o);
        SCHED();
      }
      if (i + NB < 12) ld_b2(pc, b2, i + NB);
      SCHED();
    }
  }
  STAMP(9);
  const int tk = tid >> 3, ck = tid & 7, tok = 64 * R + tk;
  rsrc_t rdk = mk_rsrc(p.Dsk, D_ * 2), rgi = mk_rsrc(p.ws + WS_G, S_ * 16);
  const v4u xv = __builtin_amdgcn_raw_buffer_load_b128(rx, tok * 1024 + ck * 16, N * 128, 0);
  const v4u dv = __builtin_amdgcn_raw_buffer_load_b128(rdk, ck * 16, N * 128, 0);
  v4u gq = ld_sc1(rgi, tok * 16, 0);
#pragma unroll
  for (int tt = 0; tt < 4; ++tt)
#pragma unroll
    for (int e = 0; e < 4; ++e) *(LAS f32x4*)(L + w * 16384 + (tt * 4 + e) * 1024 + ((lane ^ ((lane >> 4) << 2)) << 4)) = acc[e][tt];
#pragma nounroll
  for (unsigned spins = 0; spins < SPIN_CAP; ++spins) {
    if (__all(gq[1] == tag && gq[2] == ~tag)) break;
    __builtin_amdgcn_s_sleep(4);
    CFENCE();
    gq = ld_sc1(rgi, tok * 16, 0);
  }
  if (b == 0 && w == 7) {
#pragma nounroll
    for (int z = 0; z < 4; ++z) poll_flags((GAS u64*)(p.ws + WS_FB2) + z * 64 + lane, true, tag64(tag));
    st_flag(p.ws, WS_EPOCH, tag, lane == 0);
  }
  asm volatile("s_waitcnt lgkmcnt(0)" ::: "memory");
  BARRIER();
  STAMP(10);
  {
    const int q = ck >> 1, hf = ck & 1, ln = (tk & 15) + 16 * q;
    const int base = (tk >> 4) * 4096 + ((ln ^ (q << 2)) << 4) + hf * 8;
    f32x2 sg[4], ss[4];
#pragma unroll
    for (int e = 0; e < 4; ++e) { sg[e] = f32x2{0.f, 0.f}; ss[e] = f32x2{0.f, 0.f}; }
#pragma unroll
    for (int ww = 0; ww < 8; ++ww)
#pragma unroll
      for (int e = 0; e < 4; ++e) {
        const f32x2 v = *(LAS f32x2*)(L + ww * 16384 + e * 1024 + base);
        if (ww < 3) ss[e] += v; else sg[e] += v;
      }
    const float g = __uint_as_float(gq[0]);
    float o[8];
#pragma unroll
    for (int j = 0; j < 8; ++j) {
      const float xj = (j & 1) ? bfhi(xv[j >> 1]) : bf2f(xv[j >> 1] & 0xFFFFu);
      const float dj = (j & 1) ? bfhi(dv[j >> 1]) : bf2f(dv[j >> 1] & 0xFFFFu);
      o[j] = g * (2.f * ss[j & 3][j >> 2] + xj * dj) + (1.f - g) * sg[j & 3][j >> 2];
    }
    rsrc_t ro = mk_rsrc(p.out, S_ * D_ * 4);
    const v4u o0 = {__float_as_uint(o[0]), __float_as_uint(o[1]), __float_as_uint(o[2]), __float_as_uint(o[3])};
    const v4u o1 = {__float_as_uint(o[4]), __float_as_uint(o[5]), __float_as_uint(o[6]), __float_as_uint(o[7])};
    __builtin_amdgcn_raw_buffer_store_b128(o0, ro, tok * 2048 + ck * 32, N * 256, 0);
    __builtin_amdgcn_raw_buffer_store_b128(o1, ro, tok * 2048 + ck * 32 + 16, N * 256, 0);
  }
  STAMP(11);
  if (b == PB && w == PW) { const u64 tend = stamp_[PROBE] + 6000; for (int z = 0; z < 1000000; ++z) { if (__builtin_amdgcn_s_memrealtime() >= tend) break; __builtin_amdgcn_s_sleep(1); } }
}

extern "C" void kernel_launch(void* const* d_in, const int* in_sizes, int n_in, void* d_out, int out_size, void* d_ws, size_t ws_size,
                              hipStream_t stream) {
  Params p;
  p.x = (const u16*)d_in[0]; p.Lre = (const u16*)d_in[1]; p.Lim = (const u16*)d_in[2]; p.Bre = (const u16*)d_in[3];
  p.Bim = (const u16*)d_in[4]; p.Cre = (const u16*)d_in[5]; p.Cim = (const u16*)d_in[6]; p.ldt = (const u16*)d_in[7];
  p.Dsk = (const u16*)d_in[8]; p.Wg = (const u16*)d_in[10]; p.Wu = (const u16*)d_in[11]; p.Wd = (const u16*)d_in[12];
  p.gw = (const u16*)d_in[13]; p.out = (float*)d_out; p.ws = (unsigned char*)d_ws;
  p.pad = 0;
  for (int b = 0; b < 256; ++b) {
    const int xcd = b & 7, r = b >> 3, H = xcd >> 2, Q = xcd & 3, nq = 7 + 2 * H, r2 = r - nq;
    const int cnt = H ? 21 : (Q < 2 ? 25 : 24), st = H ? 21 * Q : (Q < 2 ? 25 * Q : 24 * Q + 2);
    unsigned kind, pn, cb = 0;
    if (r2 < 0) { kind = 1; pn = 7 * H + r; cb = Q; }
    else if (r2 < cnt) { kind = 0; cb = (st + r2) / (7 - H); pn = 7 * H + (st + r2) % (7 - H); }
    else { kind = 2; pn = H ? 2 * Q + r2 - 19 : Q - 2; }
    p.map[b] = kind | pn << 2 | cb << 8;
  }
  (void)hipFuncSetAttribute((const void*)k_fused, hipFuncAttributeMaxDynamicSharedMemorySize, LDS_BYTES);
  k_fused<<<256, 512, LDS_BYTES, stream>>>(p);
}
```

```cpp
#include <hip/hip_runtime.h>
#include <stdint.h>

typedef unsigned short u16;
typedef unsigned long long u64;
typedef unsigned v4u __attribute__((ext_vector_type(4)));
typedef unsigned v2u __attribute__((ext_vector_type(2)));
typedef short s16x4 __attribute__((ext_vector_type(4)));
typedef __bf16 bf16x8 __attribute__((ext_vector_type(8)));
typedef __bf16 bf16x2 __attribute__((ext_vector_type(2)));
typedef float f32x2 __attribute__((ext_vector_type(2)));
typedef float f32x4 __attribute__((ext_vector_type(4)));
typedef float f32x16 __attribute__((ext_vector_type(16)));
#define LAS __attribute__((address_space(3)))
#define GAS __attribute__((address_space(1)))
#define RLX_AGENT __ATOMIC_RELAXED, __HIP_MEMORY_SCOPE_AGENT
typedef __amdgpu_buffer_rsrc_t rsrc_t;

constexpr int S_ = 2048, D_ = 512, P_ = 512, F_ = 1792;
constexpr int NK = 88;
constexpr int AUX_SC1 = 16;
constexpr int WS_EPOCH = 0;
constexpr int WS_FB2 = 4096;
constexpr int WS_FA2 = 16384;
constexpr int WS_G = 32768;
constexpr int WS_CARRY = 65536;
constexpr int WS_B2 = 262144;
constexpr int B2_BYTES = 8 * NK * 4096;
constexpr int WS_A2 = 4 << 20;
constexpr int A2_BYTES = 32 * NK * 4096;
constexpr int WS_STAMP = 32 << 20;
constexpr int LDS_BYTES = 160 * 1024;
constexpr int LDS_TAG = LDS_BYTES - 16, LDS_RDY = LDS_BYTES - 8, LDS_DONE = LDS_BYTES - 32;
constexpr int OOB = 1 << 28;
constexpr int CTRL_BYTES = 32768;
constexpr int CODE_BYTES = 42 * 1024;
constexpr unsigned SPIN_CAP = 1u << 18;

struct Params {
  const u16 *x, *Wg, *Wu, *Bre, *Bim, *Cre, *Cim, *Wd;
  unsigned char* ws;
  const u16 *Lre, *Lim, *ldt, *Dsk, *gw;
  float* out;
  u64 pad;
  unsigned map[256];
};

__device__ __forceinline__ rsrc_t mk_rsrc(const void* p, int bytes) {
  return __builtin_amdgcn_make_buffer_rsrc((void*)p, 0, bytes, 0x00020000);
}
__device__ __forceinline__ float bf2f(unsigned v) { return __uint_as_float(v << 16); }
__device__ __forceinline__ float bfhi(unsigned v) { return __uint_as_float(v & 0xFFFF0000u); }
__device__ __forceinline__ unsigned pk_bf16(float lo, float hi) {
  f32x2 t = {lo, hi};
  return __builtin_bit_cast(unsigned, __builtin_convertvector(t, bf16x2));
}
__device__ __forceinline__ f32x16 mfma32(v4u a, v4u b, f32x16 c) {
  return __builtin_amdgcn_mfma_f32_32x32x16_bf16(__builtin_bit_cast(bf16x8, a), __builtin_bit_cast(bf16x8, b), c, 0, 0, 0);
}
__device__ __forceinline__ f32x4 mfma16(v4u a, v4u b, f32x4 c) {
  return __builtin_amdgcn_mfma_f32_16x16x32_bf16(__builtin_bit_cast(bf16x8, a), __builtin_bit_cast(bf16x8, b), c, 0, 0, 0);
}
__device__ __forceinline__ v4u lds_rd128(LAS unsigned char* p) { return *(LAS v4u*)p; }
__device__ __forceinline__ v2u lds_rdtr(LAS unsigned char* p) {
  return __builtin_bit_cast(v2u, __builtin_amdgcn_ds_read_tr16_b64_v4i16((LAS s16x4*)p));
}
__device__ __forceinline__ void glds(rsrc_t rs, LAS unsigned char* dst, int voff, int soff) {
  __builtin_amdgcn_raw_ptr_buffer_load_lds(rs, (LAS void*)dst, 16, voff, soff, 0, 0);
}
__device__ __forceinline__ v4u ld_sc1(rsrc_t rs, int voff, int soff) { return __builtin_amdgcn_raw_buffer_load_b128(rs, voff, soff, AUX_SC1); }
__device__ __forceinline__ void st_sc1(v4u v, rsrc_t rs, int voff, int soff) { __builtin_amdgcn_raw_buffer_store_b128(v, rs, voff, soff, AUX_SC1); }
__device__ __forceinline__ u64 tag64(unsigned tag) { return ((u64)(~tag) << 32) | tag; }
__device__ __forceinline__ void st_flag(unsigned char* ws, int off, unsigned tag, bool on) {
  __builtin_amdgcn_raw_buffer_store_b64(v2u{tag, ~tag}, mk_rsrc(ws, CTRL_BYTES), on ? 0 : OOB, off, AUX_SC1);
}
__device__ __forceinline__ unsigned lds_word(LAS unsigned char* p) { return __builtin_amdgcn_readfirstlane(*(volatile LAS unsigned*)p); }
#define BARRIER() asm volatile("s_barrier" ::: "memory")
#define SCHED() __builtin_amdgcn_sched_barrier(0)
#define CFENCE() asm volatile("" ::: "memory")
template <int N> __device__ __forceinline__ void wait_vm() { asm volatile("s_waitcnt vmcnt(%c0)" ::"i"(N) : "memory"); }

#define STAMP(i) do {} while (0)

__device__ __forceinline__ void poll_flags(GAS u64* f, bool valid, u64 want) {
#pragma nounroll
  for (unsigned spins = 0; spins < SPIN_CAP; ++spins) {
    CFENCE();
    const u64 v = __hip_atomic_load(f, RLX_AGENT);
    if (__all(!valid || v == want)) break;
    __builtin_amdgcn_s_sleep(4);
  }
  CFENCE();
}

constexpr int NB = 8;
constexpr int S_RT = 3;
constexpr int S_CHK = 9;
#define NROT 2
struct Sched {
  int n;
  int type[40], idx[40];
  int lo[17], hi[17];
  int b2lo[17], b2hi[17];
  int wait[17];
};
constexpr Sched make_sched(int kind) {
  Sched s{};
  int cnt[40] = {}, need[40] = {}, avail[40] = {};
  int n = 0;
  auto add = [&](int ty, int ix, int c, int nd, int av) { s.type[n] = ty; s.idx[n] = ix; cnt[n] = c; need[n] = nd; avail[n] = av; ++n; };
  if (kind == 0) {
    add(0, 0, 3, 0, -1); add(1, 0, 2, 0, -1); add(2, 0, 4, 0, -1); add(2, 1, 4, 0, -1);
    add(0, 1, 3, 1, -1); add(1, 1, 2, 1, -1); add(2, 2, 4, 1, -1); add(2, 3, 4, 2, -1);
    add(0, 2, 3, 3, -1); add(1, 2, 2, 3, -1);
    for (int t = 4; t < 16; ++t) {
      add(2, t, 4, t - 1, t - 3);
      const int b = t - 3, T = b / 2 + 2;
      if (b >= 2 && T <= 7) add(b & 1, T, (b & 1) ? 2 : 3, 2 * T - 1, b);
    }
  } else {
    for (int T = 0; T < 8; ++T)
      for (int ty = 0; ty < 2; ++ty) add(ty, T, 6, T == 0 ? 0 : 2 * T - 1, T < 3 ? -1 : 2 * T - 4 + ty);
  }
  s.n = n;
  const int LIM = 8;
  int endpos[40] = {};
  int next = 0, total = 0, b2 = 0;
  for (int b = 0; b <= 16; ++b) {
    s.lo[b] = next;
    for (;;) {
      if (next >= n || avail[next] > b - 1) break;
      int lastneed = 0;
      for (int i = 0; i < next; ++i) if (need[i] <= b) lastneed = endpos[i];
      if (b < 4 && need[next] > b && total + cnt[next] - lastneed > LIM + 2 * b) break;
      total += cnt[next]; endpos[next] = total; ++next;
    }
    s.hi[b] = next;
    s.b2lo[b] = b2;
    if (b >= S_CHK + 2 && b2 < NB) { ++b2; total += 4; }
    if (b >= 14 && b2 < NB) { ++b2; total += 4; }
    if (b == 16) { total += 4 * (NB - b2); b2 = NB; }
    s.b2hi[b] = b2;
    int lastneed = 0;
    for (int i = 0; i < next; ++i) if (need[i] <= b) lastneed = endpos[i];
    for (int i = next; i < n; ++i) if (need[i] <= b) lastneed = 1 << 20;
    s.wait[b] = total - lastneed;
    if (s.wait[b] > 63) s.wait[b] = 63;
  }
  return s;
}
constexpr bool sched_ok(const Sched& s) { for (int b = 0; b <= 16; ++b) if (s.wait[b] < 0) return false; return s.hi[16] == s.n; }
constexpr Sched SCH[2] = {make_sched(0), make_sched(1)};
static_assert(sched_ok(SCH[0]) && sched_ok(SCH[1]), "every ring item must be issued in time");

constexpr int GX_SLOT = 20480, GW_BASE = 61440, GW_SLOT = 16384;
constexpr int SX_SLOT = 16384, SB_BASE = 49152, SB_SLOT = 32768;

struct P2Ctx {
  rsrc_t rb;
  int nk, rot, lane16;
};
__device__ __forceinline__ int p2_off(const P2Ctx& c, int i) {
  int t = i + c.rot; t = t >= c.nk ? t - c.nk : t;
  return i < c.nk ? t * 4096 : OOB;
}
__device__ __forceinline__ void ld_b2(const P2Ctx& c, v4u (&b2)[NB][4], int i) {
  const int o = p2_off(c, i);
#pragma unroll
  for (int e = 0; e < 4; ++e) b2[i % NB][e] = ld_sc1(c.rb, c.lane16 + e * 1024, o);
}

__device__ __forceinline__ void retile_load(const Params& p, int unit, int lane, v4u (&f)[4]) {
  const int N = unit / NK, kap = unit - N * NK, q = lane >> 4, i = lane & 15;
  if (kap < 56) {
    rsrc_t rd = mk_rsrc(p.Wd, F_ * D_ * 2);
#pragma unroll
    for (int j = 0; j < 8; ++j) {
      const v2u t = __builtin_amdgcn_raw_buffer_load_b64(rd, (4 * q + (j & 3) + 16 * (j >> 2)) * (D_ * 2) + i * 8, kap * (32 * D_ * 2) + N * 128, 0);
      f[j >> 1][2 * (j & 1)] = t[0]; f[j >> 1][2 * (j & 1) + 1] = t[1];
    }
  } else {
    const int cg = (kap - 56) >> 1, m = kap & 1;
    rsrc_t rc = mk_rsrc(m ? p.Cim : p.Cre, D_ * P_ * 2);
#pragma unroll
    for (int e = 0; e < 4; ++e) f[e] = __builtin_amdgcn_raw_buffer_load_b128(rc, (4 * i + e) * (P_ * 2) + q * 16, N * (64 * P_ * 2) + cg * 64, 0);
  }
}
__device__ __forceinline__ void retile_store(const Params& p, int unit, int lane, const v4u (&f)[4]) {
  const int kap = unit % NK;
  rsrc_t rws = mk_rsrc(p.ws + WS_B2, B2_BYTES);
  if (kap < 56) {
#pragma unroll
    for (int e = 0; e < 4; ++e) {
      v4u o;
#pragma unroll
      for (int z = 0; z < 4; ++z) o[z] = __builtin_amdgcn_perm(f[z][2 + (e >> 1)], f[z][e >> 1], (e & 1) ? 0x07060302u : 0x05040100u);
      st_sc1(o, rws, lane * 16 + e * 1024, unit * 4096);
    }
  } else {
#pragma unroll
    for (int e = 0; e < 4; ++e) st_sc1(f[e], rws, lane * 16 + e * 1024, unit * 4096);
  }
}

struct LdCtx { rsrc_t rx, rw; int lw, vx, vw0, vw1, sx0, sw0, rot; GAS u64* fb2; int unit, lane; mutable unsigned sv; };

template <int KIND, int I> __device__ __forceinline__ void ld_item(const LdCtx& c, LAS unsigned char* L) {
  constexpr int ty = SCH[KIND].type[I], t = SCH[KIND].idx[I];
  if constexpr (KIND == 0 && ty < 2) {
    const int k0 = ((t + c.rot) & 7) * 128;
#pragma unroll
    for (int n = 3 * ty; n < 3 + 2 * ty; ++n) glds(c.rx, L + (t % 3) * GX_SLOT + c.lw * 1024 + n * 4096, c.vx, c.sx0 + k0 + n * 32768);
  } else if constexpr (KIND == 0) {
    const int k0 = ((((t >> 1) + c.rot) & 7) * 64 + (t & 1) * 32) * (F_ * 2);
#pragma unroll
    for (int i = 0; i < 4; ++i)
      glds(c.rw, L + GW_BASE + (t & 3) * GW_SLOT + c.lw * 4096 + i * 1024, (i >> 1) ? c.vw1 : c.vw0, c.sw0 + k0 + i * 4 * (F_ * 2));
  } else {
    const int k0 = ((t + c.rot) & 7) * 128;
#pragma unroll
    for (int n = 2 * ty; n < 2 * ty + 2; ++n) glds(c.rx, L + (t % 3) * SX_SLOT + c.lw * 1024 + n * 4096, c.vx, c.sx0 + k0 + n * 8192);
#pragma unroll
    for (int n = 4 * ty; n < 4 * ty + 4; ++n)
      glds(c.rw, L + SB_BASE + (t % 3) * SB_SLOT + (c.lw >> 1) * 16384 + (c.lw & 1) * 1024 + n * 2048, c.vw0, c.sw0 + k0 + n * 16384);
  }
  SCHED();
}
template <int KIND, int LO, int HI> __device__ __forceinline__ void ld_items(const LdCtx& c, LAS unsigned char* L) {
  if constexpr (LO < HI) { ld_item<KIND, LO>(c, L); ld_items<KIND, LO + 1, HI>(c, L); }
}
template <int KIND, int B>
__device__ __forceinline__ void loader_steps(const Params& p, const LdCtx& c, LAS unsigned char* L, const P2Ctx& pc, v4u (&b2)[NB][4], v4u (&rt)[4],
                                             unsigned& tag) {
  if constexpr (B == S_RT) { if (c.unit < 8 * NK) retile_load(p, c.unit, c.lane, rt); SCHED(); }
  if constexpr (B == S_RT + 2) { if (c.unit < 8 * NK) retile_store(p, c.unit, c.lane, rt); SCHED(); }
  if constexpr (B == S_RT + 4) {
    tag = lds_word(L + LDS_TAG);
    wait_vm<0>();
    st_flag(p.ws, WS_FB2 + c.unit * 8, tag, c.unit < 8 * NK && c.lane == 0);
    SCHED();
  }
  if constexpr (B == S_CHK + 1) poll_flags(c.fb2, true, tag64(tag));
  if constexpr (B > 0) ld_items<KIND, SCH[KIND].lo[B], SCH[KIND].hi[B]>(c, L);
#pragma unroll
  for (int i = SCH[KIND].b2lo[B]; i < SCH[KIND].b2hi[B]; ++i) ld_b2(pc, b2, i);
  SCHED();
  wait_vm<SCH[KIND].wait[B]>();
  BARRIER();
  if constexpr (B < 16) loader_steps<KIND, B + 1>(p, c, L, pc, b2, rt, tag);
}
__device__ __forceinline__ bool all_done(LAS unsigned char* L) {
  const v4u d = *(volatile LAS v4u*)(L + LDS_DONE);
  return __builtin_amdgcn_readfirstlane(d[0] & d[1] & d[2] & d[3]) != 0u;
}
__device__ __forceinline__ void publish_block(LAS unsigned char* L, unsigned char* ws, int off, unsigned tag, int c, int lane) {
  wait_vm<0>();
  *(volatile LAS unsigned*)(L + LDS_DONE + 4 * c) = 1u;
  const bool last = all_done(L);
  st_flag(ws, off, tag, last && lane == 0);
  CFENCE();
}
__device__ __forceinline__ void lds_wait(LAS unsigned char* word) {
#pragma nounroll
  for (unsigned spins = 0; spins < (SPIN_CAP << 4); ++spins) {
    if (lds_word(word) != 0u) break;
    __builtin_amdgcn_s_sleep(1);
  }
  CFENCE();
}

__device__ __forceinline__ void cmul(float ar, float ai, float br, float bi, float& cr, float& ci) {
  cr = ar * br - ai * bi; ci = ar * bi + ai * br;
}

__global__ __launch_bounds__(512, 2) void k_fused(Params p) {
  extern __shared__ __attribute__((aligned(16))) unsigned char lds_[];
  LAS unsigned char* L = (LAS unsigned char*)lds_;
  STAMP(0);
  const int b = blockIdx.x, tid = threadIdx.x, lane = tid & 63;
  const int w = __builtin_amdgcn_readfirstlane(tid >> 6);
  const int xcd = b & 7, r = b >> 3;
  const unsigned mp = p.map[b];
  unsigned zpf = 0;
  if (w < 4) {
    const u64 pcv = __builtin_amdgcn_s_getpc();
    rsrc_t rcode = mk_rsrc((const void*)(pcv & ~(u64)127), CODE_BYTES);
    zpf = __builtin_amdgcn_raw_buffer_load_b32(rcode, lane < 4 ? lane * 16384 : OOB, (r * 4 + w) * 128, 0);
  }
  const int kind = mp & 3, pn = (mp >> 2) & 63, cb = mp >> 8;
  const int rot = (8 / NROT) * ((pn + cb) & (NROT - 1));
  const int R = xcd * 4 + (r & 3), N = r >> 2;
  const int k0 = (int)(0x22170C002D4D4238ull >> (8 * w)) & 255;
  P2Ctx pc;
  pc.nk = (0xBBBCBBBAu >> (4 * w)) & 15;
  pc.rot = (N * 5) >> 2;
  pc.lane16 = lane * 16;
  pc.rb = mk_rsrc(p.ws + WS_B2 + (N * NK + k0) * 4096, pc.nk * 4096);
  rsrc_t rx = mk_rsrc(p.x, S_ * D_ * 2);
  v4u b2[NB][4];
  unsigned tag;

  if (__builtin_expect(w >= 4 && kind != 2, 1)) {
    LdCtx c;
    c.rx = rx; c.lw = w - 4; c.rot = rot;
    const int rr = 8 * c.lw + (lane >> 3);
    const int sw = ((lane & 7) ^ ((rr >> 1) & 7)) << 4;
    if (kind == 0) {
      const int m = c.lw >> 1, kq = lane >> 4;
      c.vx = rr * 1024 + sw;
      c.sx0 = pn * 160 * 1024;
      c.rw = mk_rsrc(m ? p.Wu : p.Wg, D_ * F_ * 2);
      c.vw0 = ((c.lw & 1) * 16 + kq) * (F_ * 2) + (((lane & 15) ^ (kq << 2)) << 4);
      c.vw1 = c.vw0 ^ 32;
      c.sw0 = cb * 256;
      STAMP(1);
      ld_items<0, 0, SCH[0].hi[0]>(c, L);
    } else {
      const int tok = 64 * ((rr >> 2) & 1) + 32 * (rr & 1) + ((rr >> 1) & 1) + 2 * (rr >> 3);
      c.vx = tok * 1024 + sw;
      c.sx0 = pn * 128 * 1024;
      c.rw = mk_rsrc((c.lw >> 1) ? p.Bim : p.Bre, P_ * D_ * 2);
      const int brow = 8 * (c.lw & 1) + (lane >> 3);
      c.vw0 = brow * 1024 + (((lane & 7) ^ ((brow >> 1) & 7)) << 4);
      c.vw1 = 0;
      c.sw0 = cb * 128 * 1024;
      STAMP(1);
      ld_items<1, 0, SCH[1].hi[0]>(c, L);
    }
    STAMP(2);
    int kind2 = kind;
    asm volatile("" : "+s"(kind2));
    c.fb2 = (GAS u64*)(p.ws + WS_FB2) + N * NK + c.lw * 22 + (lane < 22 ? lane : 0);
    c.unit = c.lw * 256 + b; c.lane = lane; c.sv = 0;
    v4u rt[4];
    if (kind2 == 0) loader_steps<0, 0>(p, c, L, pc, b2, rt, tag);
    else loader_steps<1, 0>(p, c, L, pc, b2, rt, tag);
    STAMP(3);
  } else {
    if (w == 3) {
      const unsigned e = __hip_atomic_load((GAS unsigned*)(p.ws + WS_EPOCH), RLX_AGENT) + 1u;
      *(volatile LAS v4u*)(L + LDS_TAG) = v4u{e, 0u, 0u, 0u};
      *(volatile LAS v4u*)(L + LDS_DONE) = v4u{0u, 0u, 0u, 0u};
      asm volatile("s_waitcnt lgkmcnt(0)" ::: "memory");
    }
    if (w == 2) {
      const int g = lane >> 3, j = lane & 7;
      int off = OOB;
      if (g == 0) off = WS_A2 + R * NK * 4096 + (j < 6 ? j : 0) * 65536;
      if (g == 1) off = WS_B2 + N * NK * 4096 + (j < 6 ? j : 0) * 65536;
      if (g == 2) { const int R0 = kind == 0 ? (10 * pn) >> 2 : 2 * pn; const int Rq = R0 + (j & 3) < 32 ? R0 + (j & 3) : 31;
                    off = WS_A2 + (Rq * NK + (kind == 0 ? 4 * cb : 56 + 8 * cb) + (j >> 2) * 3) * 4096; }
      if (g == 3) off = j * 32768;
      zpf ^= __builtin_amdgcn_raw_buffer_load_b32(mk_rsrc(p.ws, WS_A2 + A2_BYTES), off, 0, 0);
      zpf ^= __builtin_amdgcn_raw_buffer_load_b32(mk_rsrc(p.out, S_ * D_ * 4), lane < 4 ? lane * 32768 + N * 256 : OOB, R * 131072, 0);
      zpf ^= __builtin_amdgcn_raw_buffer_load_b32(mk_rsrc(p.Dsk, D_ * 2), lane < 1 ? 0 : OOB, 0, 0);
    }
    asm volatile("" ::"v"(zpf));
    rsrc_t ra2 = mk_rsrc(p.ws + WS_A2, A2_BYTES);
    const int fa2 = WS_FA2 + ((kind * 16 + pn) * 16 + cb) * 8;
    const int c = w;

    if (kind == 0) {
      const int q = lane >> 4, i15 = lane & 15;
      int xa[2], wa[2];
#pragma unroll
      for (int ks = 0; ks < 2; ++ks) xa[ks] = i15 * 128 + (((4 * ks + q) ^ ((i15 >> 1) & 7)) << 4);
      {
        const int qq = i15 >> 2, pp = lane & 3;
#pragma unroll
        for (int u = 0; u < 2; ++u)
          wa[u] = GW_BASE + (8 * q + qq) * 256 + (((2 * (2 * c + u) + (pp >> 1)) ^ ((qq << 2) | ((q & 1) << 1))) << 4) + 8 * (pp & 1);
      }
      f32x4 acc[4][10];
#pragma unroll
      for (int a = 0; a < 4; ++a)
#pragma unroll
        for (int tt = 0; tt < 10; ++tt) acc[a][tt] = f32x4{0.f, 0.f, 0.f, 0.f};
      v4u xf[4];
      v2u wf[2][8];
      auto rdx = [&](int it) {
        const int s = it / 10, tt = it % 10;
        xf[it & 3] = lds_rd128(L + xa[s & 1] + ((s >> 1) % 3) * GX_SLOT + tt * 2048);
      };
      auto rdw = [&](int s, int z) {
        const int m = z >> 2, u = (z >> 1) & 1, t = z & 1;
        wf[s & 1][z] = lds_rdtr(L + wa[u] + (s & 3) * GW_SLOT + m * 8192 + t * 1024);
      };
      STAMP(1);
      BARRIER();
      STAMP(2);
      tag = lds_word(L + LDS_TAG);
#pragma unroll
      for (int z = 0; z < 8; ++z) rdw(0, z);
      rdx(0); rdx(1); rdx(2);
#pragma clang loop unroll(full)
      for (int s = 0; s < 16; ++s)
#pragma clang loop unroll(full)
      for (int tt = 0; tt < 10; ++tt) {
        const int it = 10 * s + tt;
        SCHED();
        if (tt == 0 && s > 0) { BARRIER(); SCHED(); }
        if (it + 3 < 160) rdx(it + 3);
        if (tt < 8 && s < 15) rdw(s + 1, tt);
#pragma unroll
        for (int a = 0; a < 4; ++a) {
          const v4u wv = {wf[s & 1][2 * a][0], wf[s & 1][2 * a][1], wf[s & 1][2 * a + 1][0], wf[s & 1][2 * a + 1][1]};
          acc[a][tt] = mfma16(wv, xf[it & 3], acc[a][tt]);
        }
        __builtin_amdgcn_sched_group_barrier(0x8, 1, 0);
        __builtin_amdgcn_sched_group_barrier(0x100, 1, 0);
        __builtin_amdgcn_sched_group_barrier(0x8, 1, 0);
        __builtin_amdgcn_sched_group_barrier(0x100, 1, 0);
        __builtin_amdgcn_sched_group_barrier(0x8, 2, 0);
      }
      SCHED();
      BARRIER();
      STAMP(3);
      const int kap = 4 * cb + c;
#pragma unroll
      for (int tt = 0; tt < 10; ++tt) {
        float hv[8];
#pragma unroll
        for (int j = 0; j < 8; ++j) {
          const float e = __builtin_amdgcn_exp2f(acc[j >> 2][tt][j & 3] * -1.44269504088896341f);
          hv[j] = acc[2 + (j >> 2)][tt][j & 3] * __builtin_amdgcn_rcpf(1.f + e);
        }
        const v4u o = {pk_bf16(hv[0], hv[1]), pk_bf16(hv[2], hv[3]), pk_bf16(hv[4], hv[5]), pk_bf16(hv[6], hv[7])};
        const int TT = 10 * pn + tt;
        st_sc1(o, ra2, lane * 16, ((TT >> 2) * NK + kap) * 4096 + (TT & 3) * 1024);
      }
      SCHED();
      STAMP(4);
      publish_block(L, p.ws, fa2, tag, c, lane);
      STAMP(5);
#pragma unroll
      for (int i = 0; i < NB; ++i) ld_b2(pc, b2, i);
      SCHED();
    } else if (kind == 1) {
      const int h = lane >> 5, i31 = lane & 31;
      int xa[4];
#pragma unroll
      for (int ks = 0; ks < 4; ++ks) xa[ks] = i31 * 128 + (((2 * ks + h) ^ ((i31 >> 1) & 7)) << 4);
      const int cg = 4 * cb + c, ch = 32 * cg + i31;
      const float lr = bf2f(p.Lre[ch]), li = bf2f(p.Lim[ch]);
      float dt = expf(bf2f(p.ldt[ch]));
      dt = bf2f(pk_bf16(dt, 0.f) & 0xFFFFu);
      float sn, cs;
      sincosf(dt * li, &sn, &cs);
      const float ex = expf(dt * lr);
      const float ar = ex * cs, ai = ex * sn;
      const float den = 1.f / (lr * lr + li * li);
      const float cr = ((ar - 1.f) * lr + ai * li) * den, ci = (ai * lr - (ar - 1.f) * li) * den;
      float p32r = ar, p32i = ai;
#pragma unroll
      for (int z = 0; z < 5; ++z) { float t0, t1; cmul(p32r, p32i, p32r, p32i, t0, t1); p32r = t0; p32i = t1; }
      float p128r = p32r, p128i = p32i;
#pragma unroll
      for (int z = 0; z < 2; ++z) { float t0, t1; cmul(p128r, p128i, p128r, p128i, t0, t1); p128r = t0; p128i = t1; }

      f32x16 acc[2][4];
#pragma unroll
      for (int m = 0; m < 2; ++m)
#pragma unroll
        for (int rt = 0; rt < 4; ++rt) acc[m][rt] = f32x16{};
      v4u xf[2][4], bfr[2][2];
      auto rd = [&](int n) {
        const int bq = n & 1, T = n >> 2;
#pragma unroll
        for (int m = 0; m < 2; ++m) bfr[bq][m] = lds_rd128(L + SB_BASE + c * 4096 + xa[n & 3] + (T % 3) * SB_SLOT + m * 16384);
#pragma unroll
        for (int rt = 0; rt < 4; ++rt) xf[bq][rt] = lds_rd128(L + xa[n & 3] + (T % 3) * SX_SLOT + rt * 4096);
      };
      STAMP(1);
      BARRIER();
      STAMP(2);
      tag = lds_word(L + LDS_TAG);
      rd(0);
#pragma unroll
      for (int n = 0; n < 32; ++n) {
        SCHED();
        if (n > 0 && (n & 1) == 0) { BARRIER(); SCHED(); }
        if (n + 1 < 32) rd(n + 1);
#pragma unroll
        for (int rt = 0; rt < 4; ++rt)
#pragma unroll
          for (int m = 0; m < 2; ++m) acc[m][rt] = mfma32(xf[n & 1][rt], bfr[n & 1][m], acc[m][rt]);
        if (n + 1 < 32) {
#pragma unroll
          for (int g = 0; g < 6; ++g) {
            __builtin_amdgcn_sched_group_barrier(0x8, 1, 0);
            __builtin_amdgcn_sched_group_barrier(0x100, 1, 0);
          }
          __builtin_amdgcn_sched_group_barrier(0x8, 2, 0);
        }
      }
      SCHED();
      BARRIER();
      STAMP(3);
      const f32x2 a2 = {ar, ar}, bb2 = {ai, ai}, nb2 = {-ai, -ai};
      f32x2 er = {0.f, 0.f}, ei = {0.f, 0.f};
#pragma unroll
      for (int rt = 0; rt < 4; ++rt)
#pragma unroll
        for (int z = 0; z < 8; ++z) {
          const f32x2 ur = {acc[0][rt][2 * z], acc[0][rt][2 * z + 1]}, ui = {acc[1][rt][2 * z], acc[1][rt][2 * z + 1]};
          f32x2 nr = __builtin_elementwise_fma(a2, er, ur), ni = __builtin_elementwise_fma(a2, ei, ui);
          nr = __builtin_elementwise_fma(nb2, ei, nr); ni = __builtin_elementwise_fma(bb2, er, ni);
          er = nr; ei = ni;
        }
      const float o0r = __shfl_xor(er[0], 32), o0i = __shfl_xor(ei[0], 32), o1r = __shfl_xor(er[1], 32), o1i = __shfl_xor(ei[1], 32);
      const float e0r = h ? o0r : er[0], e0i = h ? o0i : ei[0], e1r = h ? o1r : er[1], e1i = h ? o1i : ei[1];
      const float e2r = h ? er[0] : o0r, e2i = h ? ei[0] : o0i, e3r = h ? er[1] : o1r, e3i = h ? ei[1] : o1i;
      rsrc_t rcar = mk_rsrc(p.ws + WS_CARRY, 16 * P_ * 16);
      {
        float tr, ti, Er, Ei;
        cmul(e0r, e0i, p32r, p32i, tr, ti); tr += e1r; ti += e1i;
        cmul(tr, ti, p32r, p32i, Er, Ei); Er += e2r; Ei += e2i;
        cmul(Er, Ei, p32r, p32i, tr, ti); tr += e3r; ti += e3i;
        const v4u gq = {__float_as_uint(tr), tag, __float_as_uint(ti), ~tag};
        st_sc1(gq, rcar, h ? OOB : ch * 16, (15 - pn) * (P_ * 16));
      }
      STAMP(4);
      float Hr = 0.f, Hi = 0.f;
      {
        v4u cv[15];
#pragma nounroll
        for (unsigned spins = 0; spins < SPIN_CAP; ++spins) {
          CFENCE();
          int good = 0;
#pragma unroll
          for (int z = 0; z < 15; ++z) cv[z] = ld_sc1(rcar, ch * 16 + (16 - pn) * (P_ * 16), z * (P_ * 16));
#pragma unroll
          for (int z = 0; z < 15; ++z) good += ((cv[z][1] ^ tag) | (cv[z][3] ^ ~tag)) == 0u;
          if (__all(good == pn)) break;
          __builtin_amdgcn_s_sleep(2);
        }
#pragma unroll
        for (int z = 14; z >= 0; --z) {
          float tr, ti;
          cmul(Hr, Hi, p128r, p128i, tr, ti);
          Hr = tr + __uint_as_float(cv[z][0]); Hi = ti + __uint_as_float(cv[z][2]);
        }
      }
      STAMP(5);
      float c1r, c1i, c2r, c2i, c3r, c3i;
      cmul(Hr, Hi, p32r, p32i, c1r, c1i); c1r += e0r; c1i += e0i;
      cmul(c1r, c1i, p32r, p32i, c2r, c2i); c2r += e1r; c2i += e1i;
      cmul(c2r, c2i, p32r, p32i, c3r, c3i); c3r += e2r; c3i += e2i;
      f32x2 sr = {h ? c2r : Hr, h ? c3r : c1r}, si = {h ? c2i : Hi, h ? c3i : c1i};
      const f32x2 cr2 = {cr, cr}, nci2 = {-ci, -ci}, ncr2 = {-cr, -cr};
      LAS unsigned char* T = L + c * 16384;
      const int swz = ((i31 & 3) << 2) | (((i31 >> 3) & 1) << 1) | ((i31 >> 2) & 1);
#pragma unroll
      for (int rt = 0; rt < 4; ++rt) {
        f32x2 orr[8], oi[8];
#pragma unroll
        for (int z = 0; z < 8; ++z) {
          const f32x2 ur = {acc[0][rt][2 * z], acc[0][rt][2 * z + 1]}, ui = {acc[1][rt][2 * z], acc[1][rt][2 * z + 1]};
          f32x2 nr = __builtin_elementwise_fma(a2, sr, ur), ni = __builtin_elementwise_fma(a2, si, ui);
          nr = __builtin_elementwise_fma(nb2, si, nr); ni = __builtin_elementwise_fma(bb2, sr, ni);
          sr = nr; si = ni;
          orr[z] = __builtin_elementwise_fma(nci2, si, cr2 * sr);
          oi[z] = __builtin_elementwise_fma(nci2, sr, ncr2 * si);
        }
#pragma unroll
        for (int par = 0; par < 2; ++par) {
          const int chunk = (2 * h + par) * 4 + rt;
          const v4u vr = {pk_bf16(orr[0][par], orr[1][par]), pk_bf16(orr[2][par], orr[3][par]), pk_bf16(orr[4][par], orr[5][par]), pk_bf16(orr[6][par], orr[7][par])};
          const v4u vi = {pk_bf16(oi[0][par], oi[1][par]), pk_bf16(oi[2][par], oi[3][par]), pk_bf16(oi[4][par], oi[5][par]), pk_bf16(oi[6][par], oi[7][par])};
          *(LAS v4u*)(T + i31 * 256 + ((chunk ^ swz) << 4)) = vr;
          *(LAS v4u*)(T + (32 + i31) * 256 + ((chunk ^ swz) << 4)) = vi;
        }
      }
      {
        const int q = lane >> 4, qq = (lane & 15) >> 2, pp = lane & 3;
#pragma unroll
        for (int tt = 0; tt < 8; ++tt)
#pragma unroll
          for (int m = 0; m < 2; ++m) {
            v2u part[2];
#pragma unroll
            for (int t = 0; t < 2; ++t) {
              const int row = 32 * m + 8 * q + 4 * t + qq;
              const int chunk = 2 * tt + (pp >> 1);
              part[t] = lds_rdtr(T + row * 256 + ((chunk ^ ((qq << 2) | ((q & 1) << 1) | t)) << 4) + 8 * (pp & 1));
            }
            const int TT = 8 * pn + tt;
            const v4u o = {part[0][0], part[0][1], part[1][0], part[1][1]};
            st_sc1(o, ra2, lane * 16, ((TT >> 2) * NK + 56 + 2 * cg + m) * 4096 + (TT & 3) * 1024);
          }
      }
      SCHED();
      STAMP(6);
      publish_block(L, p.ws, fa2, tag, c, lane);
      STAMP(7);
#pragma unroll
      for (int i = 0; i < NB; ++i) ld_b2(pc, b2, i);
      SCHED();
    } else {
      const int unit = w * 256 + b;
      const bool has_unit = w < 3 && unit < 8 * NK;
      if (has_unit) { v4u rt[4]; retile_load(p, unit, lane, rt); retile_store(p, unit, lane, rt); }
      wait_vm<0>();
      BARRIER();
      tag = lds_word(L + LDS_TAG);
      st_flag(p.ws, WS_FB2 + unit * 8, tag, has_unit && lane == 0);
      const int wid = pn * 8 + w;
      if (wid < 64) {
        rsrc_t rg = mk_rsrc(p.gw, D_ * 2);
        v4u gv[8], xv[4][8];
#pragma unroll
        for (int z = 0; z < 8; ++z) gv[z] = __builtin_amdgcn_raw_buffer_load_b128(rg, (lane & 7) * 16, z * 128, 0);
#pragma unroll
        for (int gi = 0; gi < 4; ++gi)
#pragma unroll
          for (int z = 0; z < 8; ++z)
            xv[gi][z] = __builtin_amdgcn_raw_buffer_load_b128(rx, (lane >> 3) * 1024 + (lane & 7) * 16, (wid * 32 + gi * 8) * 1024 + z * 128, 0);
        rsrc_t rgo = mk_rsrc(p.ws + WS_G, S_ * 16);
#pragma unroll
        for (int gi = 0; gi < 4; ++gi) {
          float s = 0.f;
#pragma unroll
          for (int z = 0; z < 8; ++z)
#pragma unroll
            for (int y = 0; y < 4; ++y) {
              const unsigned xa = xv[gi][z][y], ga = gv[z][y];
              s = __builtin_amdgcn_fdot2_f32_bf16(__builtin_bit_cast(bf16x2, xa), __builtin_bit_cast(bf16x2, ga), s, false);
            }
          s += __shfl_xor(s, 1); s += __shfl_xor(s, 2); s += __shfl_xor(s, 4);
          const float g = 1.f / (1.f + __expf(-s));
          const v4u gq = {__float_as_uint(g), tag, ~tag, 0u};
          st_sc1(gq, rgo, (lane & 7) ? OOB : (lane >> 3) * 16, (wid * 32 + gi * 8) * 16);
        }
      }
      STAMP(3);
      poll_flags((GAS u64*)(p.ws + WS_FB2) + N * NK + k0 + (lane < pc.nk ? lane : 0), true, tag64(tag));
#pragma unroll
      for (int i = 0; i < NB; ++i) ld_b2(pc, b2, i);
      SCHED();
    }
  }

  if (w == 4) {
    if (kind == 0) {
#pragma nounroll
      for (unsigned spins = 0; spins < (SPIN_CAP << 4); ++spins) { if (all_done(L)) break; __builtin_amdgcn_s_sleep(1); }
    }
    const int p0 = (4 * R * 205) >> 11, p1 = ((4 * R + 3) * 205) >> 11, kk = lane & 15;
    poll_flags((GAS u64*)(p.ws + WS_FA2) + ((lane & 16) ? p1 : p0) * 16 + (kk < 14 ? kk : 0), true, tag64(tag));
    *(volatile LAS unsigned*)(L + LDS_RDY) = 1u;
  } else if (w == 0) {
    poll_flags((GAS u64*)(p.ws + WS_FA2) + (16 + (R >> 1)) * 16 + (lane & 3), true, tag64(tag));
    *(volatile LAS unsigned*)(L + LDS_RDY + 4) = 1u;
  } else {
    lds_wait(L + LDS_RDY + (w < 3 ? 4 : 0));
  }
  STAMP(8);
  rsrc_t ra = mk_rsrc(p.ws + WS_A2 + (R * NK + k0) * 4096, pc.nk * 4096);
  constexpr int WA = 2;
  v4u af[WA][4];
#pragma unroll
  for (int i = 0; i < WA; ++i) {
    const int o = p2_off(pc, i);
#pragma unroll
    for (int tt = 0; tt < 4; ++tt) af[i][tt] = ld_sc1(ra, pc.lane16 + tt * 1024, o);
  }
  SCHED();
  f32x4 acc[4][4];
#pragma unroll
  for (int e = 0; e < 4; ++e)
#pragma unroll
    for (int tt = 0; tt < 4; ++tt) acc[e][tt] = f32x4{0.f, 0.f, 0.f, 0.f};
#pragma unroll
  for (int i = 0; i < 12; ++i) {
    if (i < 10 || i < pc.nk) {
      const int o = p2_off(pc, i + WA);
#pragma unroll
      for (int tt = 0; tt < 4; ++tt) {
#pragma unroll
        for (int e = 0; e < 4; ++e) acc[e][tt] = mfma16(b2[i % NB][e], af[i % WA][tt], acc[e][tt]);
        SCHED();
        if (i + WA < 12) af[i % WA][tt] = ld_sc1(ra, pc.lane16 + tt * 1024, o);
        SCHED();
      }
      if (i + NB < 12) ld_b2(pc, b2, i + NB);
      SCHED();
    }
  }
  STAMP(9);
  const int tk = tid >> 3, ck = tid & 7, tok = 64 * R + tk;
  rsrc_t rdk = mk_rsrc(p.Dsk, D_ * 2), rgi = mk_rsrc(p.ws + WS_G, S_ * 16);
  const v4u xv = __builtin_amdgcn_raw_buffer_load_b128(rx, tok * 1024 + ck * 16, N * 128, 0);
  const v4u dv = __builtin_amdgcn_raw_buffer_load_b128(rdk, ck * 16, N * 128, 0);
  v4u gq = ld_sc1(rgi, tok * 16, 0);
#pragma unroll
  for (int tt = 0; tt < 4; ++tt)
#pragma unroll
    for (int e = 0; e < 4; ++e) *(LAS f32x4*)(L + w * 16384 + (tt * 4 + e) * 1024 + ((lane ^ ((lane >> 4) << 2)) << 4)) = acc[e][tt];
#pragma nounroll
  for (unsigned spins = 0; spins < SPIN_CAP; ++spins) {
    if (__all(gq[1] == tag && gq[2] == ~tag)) break;
    __builtin_amdgcn_s_sleep(4);
    CFENCE();
    gq = ld_sc1(rgi, tok * 16, 0);
  }
  if (b == 0 && w == 7) {
#pragma nounroll
    for (int z = 0; z < 4; ++z) poll_flags((GAS u64*)(p.ws + WS_FB2) + z * 64 + lane, true, tag64(tag));
    st_flag(p.ws, WS_EPOCH, tag, lane == 0);
  }
  asm volatile("s_waitcnt lgkmcnt(0)" ::: "memory");
  BARRIER();
  STAMP(10);
  {
    const int q = ck >> 1, hf = ck & 1, ln = (tk & 15) + 16 * q;
    const int base = (tk >> 4) * 4096 + ((ln ^ (q << 2)) << 4) + hf * 8;
    f32x2 sg[4], ss[4];
#pragma unroll
    for (int e = 0; e < 4; ++e) { sg[e] = f32x2{0.f, 0.f}; ss[e] = f32x2{0.f, 0.f}; }
#pragma unroll
    for (int ww = 0; ww < 8; ++ww)
#pragma unroll
      for (int e = 0; e < 4; ++e) {
        const f32x2 v = *(LAS f32x2*)(L + ww * 16384 + e * 1024 + base);
        if (ww < 3) ss[e] += v; else sg[e] += v;
      }
    const float g = __uint_as_float(gq[0]);
    float o[8];
#pragma unroll
    for (int j = 0; j < 8; ++j) {
      const float xj = (j & 1) ? bfhi(xv[j >> 1]) : bf2f(xv[j >> 1] & 0xFFFFu);
      const float dj = (j & 1) ? bfhi(dv[j >> 1]) : bf2f(dv[j >> 1] & 0xFFFFu);
      o[j] = g * (2.f * ss[j & 3][j >> 2] + xj * dj) + (1.f - g) * sg[j & 3][j >> 2];
    }
    rsrc_t ro = mk_rsrc(p.out, S_ * D_ * 4);
    const v4u o0 = {__float_as_uint(o[0]), __float_as_uint(o[1]), __float_as_uint(o[2]), __float_as_uint(o[3])};
    const v4u o1 = {__float_as_uint(o[4]), __float_as_uint(o[5]), __float_as_uint(o[6]), __float_as_uint(o[7])};
    __builtin_amdgcn_raw_buffer_store_b128(o0, ro, tok * 2048 + ck * 32, N * 256, 17);
    __builtin_amdgcn_raw_buffer_store_b128(o1, ro, tok * 2048 + ck * 32 + 16, N * 256, 17);
  }
  STAMP(11);
}

extern "C" void kernel_launch(void* const* d_in, const int* in_sizes, int n_in, void* d_out, int out_size, void* d_ws, size_t ws_size,
                              hipStream_t stream) {
  Params p;
  p.x = (const u16*)d_in[0]; p.Lre = (const u16*)d_in[1]; p.Lim = (const u16*)d_in[2]; p.Bre = (const u16*)d_in[3];
  p.Bim = (const u16*)d_in[4]; p.Cre = (const u16*)d_in[5]; p.Cim = (const u16*)d_in[6]; p.ldt = (const u16*)d_in[7];
  p.Dsk = (const u16*)d_in[8]; p.Wg = (const u16*)d_in[10]; p.Wu = (const u16*)d_in[11]; p.Wd = (const u16*)d_in[12];
  p.gw = (const u16*)d_in[13]; p.out = (float*)d_out; p.ws = (unsigned char*)d_ws;
  p.pad = 0;
  for (int b = 0; b < 256; ++b) {
    const int xcd = b & 7, r = b >> 3, H = xcd >> 2, Q = xcd & 3, nq = 7 + 2 * H, r2 = r - nq;
    const int cnt = H ? 21 : (Q < 2 ? 25 : 24), st = H ? 21 * Q : (Q < 2 ? 25 * Q : 24 * Q + 2);
    unsigned kind, pn, cb = 0;
    if (r2 < 0) { kind = 1; pn = 7 * H + r; cb = Q; }
    else if (r2 < cnt) { kind = 0; cb = (st + r2) / (7 - H); pn = 7 * H + (st + r2) % (7 - H); }
    else { kind = 2; pn = H ? 2 * Q + r2 - 19 : Q - 2; }
    p.map[b] = kind | pn << 2 | cb << 8;
  }
  (void)hipFuncSetAttribute((const void*)k_fused, hipFuncAttributeMaxDynamicSharedMemorySize, LDS_BYTES);
  k_fused<<<256, 512, LDS_BYTES, stream>>>(p);
}
```

```cpp
#include <hip/hip_runtime.h>
#include <stdint.h>

typedef unsigned short u16;
typedef unsigned long long u64;
typedef unsigned v4u __attribute__((ext_vector_type(4)));
typedef unsigned v2u __attribute__((ext_vector_type(2)));
typedef short s16x4 __attribute__((ext_vector_type(4)));
typedef __bf16 bf16x8 __attribute__((ext_vector_type(8)));
typedef __bf16 bf16x2 __attribute__((ext_vector_type(2)));
typedef float f32x2 __attribute__((ext_vector_type(2)));
typedef float f32x4 __attribute__((ext_vector_type(4)));
typedef float f32x16 __attribute__((ext_vector_type(16)));
#define LAS __attribute__((address_space(3)))
#define GAS __attribute__((address_space(1)))
#define RLX_AGENT __ATOMIC_RELAXED, __HIP_MEMORY_SCOPE_AGENT
typedef __amdgpu_buffer_rsrc_t rsrc_t;

constexpr int S_ = 2048, D_ = 512, P_ = 512, F_ = 1792;
constexpr int NK = 88;
constexpr int AUX_SC1 = 16;
#define OUT_AUX 0
constexpr int WS_EPOCH = 0;
constexpr int WS_G = 8192;
constexpr int WS_FA2 = 65536;
constexpr int CTRL_BYTES = 131072;
constexpr int WS_CARRY = 131072;
constexpr int WS_A2 = 524288;
constexpr int A2_BYTES = 32 * NK * 4096;
constexpr int WS_STAMP = 32 << 20;
constexpr int LDS_BYTES = 160 * 1024;
constexpr int X_SLOT = 16384, W_BASE = 65536, W_SLOT = 16384, B_BASE = 131072, B_SLOT = 8192;
constexpr int LDS_DONE = 155648, LDS_PF = LDS_DONE + 32, LDS_MD = LDS_DONE + 48, LDS_TAG = LDS_DONE + 64;
constexpr int LDS_DUMMY = LDS_DONE + 256, LDS_CONST = LDS_DONE + 512;
constexpr int OOB = 1 << 28;
constexpr int CODE_BYTES = 31 * 1024;
constexpr unsigned SPIN_CAP = 1u << 18;

struct Params {
  const u16 *x, *Wg, *Wu, *Bre, *Bim, *Cre, *Cim, *Wd;
  unsigned char* ws;
  const u16 *Lre, *Lim, *ldt, *Dsk, *gw;
  float* out;
};

#if defined(__HIP_DEVICE_COMPILE__)
#pragma clang attribute push(__attribute__((target("no-packed-fp32-ops"))), apply_to = function)
#endif
__device__ __forceinline__ float u2f(unsigned v) { return __builtin_bit_cast(float, v); }
__device__ __forceinline__ unsigned f2u(float v) { return __builtin_bit_cast(unsigned, v); }
__device__ __forceinline__ bool wall(bool p) { return __builtin_amdgcn_ballot_w64(!p) == 0; }
__device__ __forceinline__ bool wany(bool p) { return __builtin_amdgcn_ballot_w64(p) != 0; }
__device__ __forceinline__ int imin(int a, int b) { return a < b ? a : b; }
__device__ __forceinline__ int imax(int a, int b) { return a > b ? a : b; }
template <int CTRL> __device__ __forceinline__ float dpp(float v) { return u2f(__builtin_amdgcn_mov_dpp(f2u(v), CTRL, 0xF, 0xF, true)); }
__device__ __forceinline__ rsrc_t mk_rsrc(const void* p, int bytes) {
  return __builtin_amdgcn_make_buffer_rsrc((void*)p, 0, bytes, 0x00020000);
}
__device__ __forceinline__ float bf2f(unsigned v) { return u2f(v << 16); }
__device__ __forceinline__ float bfhi(unsigned v) { return u2f(v & 0xFFFF0000u); }
__device__ __forceinline__ unsigned pk_bf16(float lo, float hi) {
  f32x2 t = {lo, hi};
  return __builtin_bit_cast(unsigned, __builtin_convertvector(t, bf16x2));
}
__device__ __forceinline__ void mfma16t(f32x4& c, v4u a, v4u b) {
  asm volatile("v_mfma_f32_16x16x32_bf16 %0, %1, %2, %0" : "+v"(c) : "v"(a), "v"(b));
}
__device__ __forceinline__ void mfma32t(f32x16& c, v4u a, v4u b) {
  asm volatile("v_mfma_f32_32x32x16_bf16 %0, %1, %2, %0" : "+v"(c) : "v"(a), "v"(b));
}
__device__ __forceinline__ v4u lds_rd128(LAS unsigned char* p) { return *(LAS v4u*)p; }
__device__ __forceinline__ v2u lds_rdtr(LAS unsigned char* p) {
  return __builtin_bit_cast(v2u, __builtin_amdgcn_ds_read_tr16_b64_v4i16((LAS s16x4*)p));
}
__device__ __forceinline__ v4u mk_rs(const void* p, unsigned bytes) {
  const u64 a = (u64)p;
  unsigned lo = (unsigned)a, hi = (unsigned)(a >> 32) & 0xFFFFu;
  asm volatile("" : "+s"(lo), "+s"(hi));
  return v4u{lo, hi, bytes, 0x00020000u};
}
template <int LI, int SI> __device__ __forceinline__ void glds(v4u rs, int lds, int voff, int soff) {
  int t;
  asm volatile("s_add_u32 m0, %1, %5\n\ts_add_u32 %0, %4, %6\n\tbuffer_load_dwordx4 %2, %3, %0 offen lds"
               : "=&s"(t) : "s"(lds), "v"(voff), "s"(rs), "s"(soff), "i"(LI), "i"(SI) : "memory", "scc");
}
typedef const __attribute__((address_space(4))) Params* KP;
__device__ __forceinline__ KP kargs() {
  KP k = (KP)__builtin_amdgcn_kernarg_segment_ptr();
  asm volatile("" : "+s"(k));
  return k;
}
__device__ __forceinline__ void warm(v4u rs, int voff, int soff) {
  asm volatile("s_mov_b32 m0, %3\n\ts_nop 0\n\tbuffer_load_dword %0, %1, %2 offen lds" : : "v"(voff), "s"(rs), "s"(soff), "i"(LDS_DUMMY) : "memory");
}
__device__ __forceinline__ v4u ld_sc1(rsrc_t rs, int voff, int soff) { return __builtin_amdgcn_raw_buffer_load_b128(rs, voff, soff, AUX_SC1); }
__device__ __forceinline__ void st_sc1(v4u v, rsrc_t rs, int voff, int soff) { __builtin_amdgcn_raw_buffer_store_b128(v, rs, voff + soff, 0, AUX_SC1); }
__device__ __forceinline__ u64 tag64(unsigned tag) { return ((u64)(~tag) << 32) | tag; }
__device__ __forceinline__ void st_flag(unsigned char* ws, int off, unsigned tag, int voff) {
  __builtin_amdgcn_raw_buffer_store_b64(v2u{tag, ~tag}, mk_rsrc(ws, CTRL_BYTES), voff, off, AUX_SC1);
}
__device__ __forceinline__ unsigned lds_word(LAS unsigned char* p) { return __builtin_amdgcn_readfirstlane(*(volatile LAS unsigned*)p); }
#define BARRIER() asm volatile("s_barrier" ::: "memory")
#define SCHED() __builtin_amdgcn_sched_barrier(0)
#define CFENCE() asm volatile("" ::: "memory")
#define WAIT_LDS() asm volatile("s_waitcnt lgkmcnt(0)" ::: "memory")
template <int N> __device__ __forceinline__ void wait_vm() { asm volatile("s_waitcnt vmcnt(%c0)" ::"i"(N) : "memory"); }

#define STAMP(i) do {} while (0)
#define POLL_SLEEP 4
#define POLL_IDLE 16

__device__ __forceinline__ void poll_flags(GAS u64* f, u64 want, bool care, bool late) {
#pragma nounroll
  for (unsigned spins = 0; spins < SPIN_CAP; ++spins) {
    CFENCE();
    const u64 v = __hip_atomic_load(f, RLX_AGENT);
    if (wall(!care || v == want)) break;
    if (wany(late && v == want)) __builtin_amdgcn_s_sleep(POLL_SLEEP); else __builtin_amdgcn_s_sleep(POLL_IDLE);
  }
  CFENCE();
}
__device__ __forceinline__ void csq(float& r, float& i, int n) {
#pragma unroll
  for (int z = 0; z < n; ++z) { const float t = r * r - i * i; i = 2.f * r * i; r = t; }
}

#define NROT 100
#define WARM0 3
#define CONST_AT 13
constexpr int LASTB = 13;
struct Sched {
  int n;
  int type[40], idx[40];
  int lo[LASTB + 1], hi[LASTB + 1];
  int wait[LASTB + 1];
  bool ok;
};
constexpr Sched make_sched(int kind) {
  Sched s{};
  int cnt[40] = {}, need[40] = {}, avail[40] = {};
  int n = 0;
  auto add = [&](int ty, int ix, int nd, int av) {
    if (kind && ty == 2) return;
    s.type[n] = ty; s.idx[n] = ix; cnt[n] = ty == 2 ? 4 : 3; need[n] = nd < LASTB ? nd : LASTB; avail[n] = av; ++n;
  };
  add(0, 0, 0, -1); add(1, 0, 0, -1); add(2, 0, 0, -1); add(2, 1, 0, -1);
  add(0, 1, 1, -1); add(1, 1, 1, -1); add(2, 2, 1, -1); add(2, 3, 2, -1);
  add(0, 2, 3, -1); add(1, 2, 3, -1);
  add(2, 4, 3, 1); add(2, 5, 4, 1);
  for (int b = 2; b < 12; ++b) {
    const int T = b / 2 + 2;
    add(b & 1, T, 2 * T - 1, kind ? (b & ~1) : b);
    add(2, b + 4, b + 3, b);
  }
  s.n = n;
  s.ok = true;
  const int LIM = 8;
  int endpos[40] = {};
  int next = 0, total = 0;
  for (int b = 0; b <= LASTB; ++b) {
    s.lo[b] = next;
    const int start = total;
    for (;;) {
      if (next >= n || avail[next] > b - 1) break;
      int lastneed = 0;
      for (int i = 0; i < next; ++i) if (need[i] <= b) lastneed = endpos[i];
      if (b < 3 && need[next] > b && total + cnt[next] - lastneed > LIM + 2 * b) break;
      if (b >= 3 && need[next] > b + 1 && total + cnt[next] - start > 11) break;
      total += cnt[next]; endpos[next] = total; ++next;
    }
    s.hi[b] = next;
    if (kind == 0 && b >= WARM0 && b < WARM0 + 11) total += 1;
    int lastneed = 0;
    for (int i = 0; i < next; ++i) if (need[i] <= b) lastneed = endpos[i];
    for (int i = next; i < n; ++i) if (need[i] <= b) s.ok = false;
    s.wait[b] = total - lastneed;
  }
  if (next != n || WARM0 + 10 > LASTB) s.ok = false;
  return s;
}
constexpr Sched SCH[2] = {make_sched(0), make_sched(1)};
static_assert(SCH[0].ok && SCH[1].ok, "every ring item must be issued in time");

struct P2Ctx { u64 a0, a1; int so0, sh, g0, nd, rot; unsigned bytes; };
__device__ __forceinline__ P2Ctx p2_ctx(u64 Wd, u64 Cre, u64 Cim, int w, int N) {
  P2Ctx c;
  const int ssm = w < 4;
  const u64 m = 0 - (u64)ssm;
  c.a0 = (Cre & m) | (Wd & ~m); c.a1 = (Cim & m) | (Wd & ~m);
  c.sh = ssm;
  c.so0 = N * 128 + ssm * (N * (65536 - 128) - 14 * 128);
  c.bytes = F_ * D_ * 2u - ssm * (F_ * D_ * 2u - D_ * P_ * 2u);
  c.g0 = 7 * w - 28 + ssm * (56 - 3 * w);
  c.nd = 7 - 3 * ssm;
#define P2ROT 1
  c.rot = P2ROT == 0 ? 0 : (P2ROT == 1 ? (N & 1) * (3 - ssm) : (P2ROT == 2 ? (N & 3) * (2 - ssm) : (N * c.nd) >> 3));
  return c;
}
__device__ __forceinline__ int p2_ds(const P2Ctx& c, int d) { const int t = d + c.rot; return t >= c.nd ? t - c.nd : t; }
__device__ __forceinline__ void b2_src(const P2Ctx& c, int d, v4u& rs, int& so) {
  const int g = c.g0 + p2_ds(c, d);
  const u64 x0 = c.a0, x1 = c.a1;
  rs = mk_rs((const void*)((g & 1) ? x1 : x0), c.bytes);
  so = c.so0 + (c.sh ? (g >> 1) * 128 : g * 65536);
}
template <int LI, int N0, int N1> __device__ __forceinline__ void b2_rows(v4u rs, int lds, int vb, int so) {
  if constexpr (N0 < N1) { glds<LI, N0 * 8192>(rs, lds, (N0 & 1) ? vb ^ 16 : vb, so); b2_rows<LI + 1024, N0 + 1, N1>(rs, lds, vb, so); }
}
template <int LI, int N0, int N1> __device__ __forceinline__ void b2_load(const P2Ctx& c, int vb, int d, int lds) {
  v4u rs; int so;
  b2_src(c, d, rs, so);
  b2_rows<LI, N0, N1>(rs, lds, vb, so);
}
__device__ __forceinline__ void b2_read(bool ssm, LAS unsigned char* img, int lane, v4u (&f)[2][4]) {
  if (ssm) {
    const int i = lane & 31, sw = ((i & 2) << 1) | ((i >> 1) & 2) | ((i >> 3) & 1);
#pragma unroll
    for (int h = 0; h < 2; ++h)
#pragma unroll
      for (int z = 0; z < 4; ++z) f[h][z] = lds_rd128(img + i * 128 + (((4 * h + (z & 2) + (lane >> 5)) ^ sw) << 4) + (z & 1) * 4096);
  } else {
    const int qq = (lane >> 2) & 3, hi = lane >> 5;
    const int c0 = (((lane >> 4) & 1) * 2 + ((lane >> 1) & 1)) ^ (((qq & 2) << 1) | (hi << 1));
#pragma unroll
    for (int h = 0; h < 2; ++h)
#pragma unroll
      for (int z = 0; z < 4; ++z) {
        LAS unsigned char* a = img + (4 * hi + qq) * 128 + ((c0 ^ ((z & 1) << 2) ^ (z >> 1)) << 4) + (lane & 1) * 8 + h * 4096 + (z >> 1) * 1024;
        const v2u lo = lds_rdtr(a), hi2 = lds_rdtr(a + 2048);
        f[h][z] = v4u{lo[0], lo[1], hi2[0], hi2[1]};
      }
  }
}

struct LdCtx { v4u rx, rw, rb; u64 Wd, Cre, Cim; int lw, lb, vx, vw0, vw1, vbb, rot, lane, vb, vwarm, xcd, ch; mutable unsigned sv, lrb, lib, ldb, ep; };
__device__ __forceinline__ void warm_chunk(const LdCtx& c, int slot) {
  int xc = c.xcd;
  asm volatile("" : "+s"(xc));
  int i = slot < 6 ? slot + xc : slot - 6 + xc;
  if (slot < 6) { i = i >= 12 ? i - 12 : (i >= 6 ? i - 6 : i); } else { i = 6 + (i >= 10 ? i - 10 : (i >= 5 ? i - 5 : i)); }
  const u64 x0 = c.Wd, x1 = c.Cre, x2 = c.Cim;
  const u64 src = i < 7 ? x0 : (i < 9 ? x1 : x2);
  const unsigned bytes = i < 7 ? F_ * D_ * 2 : D_ * P_ * 2;
  warm(mk_rs((const void*)src, bytes), c.vwarm, (i < 7 ? i : ((i - 7) & 1)) * 262144);
}

template <int KIND, int I> __device__ __forceinline__ void ld_item(const LdCtx& c) {
  constexpr int ty = SCH[KIND].type[I], t = SCH[KIND].idx[I];
  int rot = c.rot;
  asm volatile("" : "+s"(rot));
  if constexpr (ty < 2) {
    const int k0 = ((t + rot) & 7) * 128;
    glds<(t % 3) * X_SLOT + 2 * ty * 4096, ty * 65536>(c.rx, c.lw * 1024, c.vx, k0);
    glds<(t % 3) * X_SLOT + (2 * ty + 1) * 4096, ty * 65536 + 8192>(c.rx, c.lw * 1024, c.vx, k0);
    glds<B_BASE + (t % 3) * B_SLOT + ty * 2048, ty * 16384>(c.rb, c.lb, c.vbb, k0);
  } else {
    const int k0 = (((t >> 1) + rot) & 7) * (64 * F_ * 2);
    constexpr int LI = W_BASE + (t & 3) * W_SLOT, SI = (t & 1) * 32 * F_ * 2;
    glds<LI, SI>(c.rw, c.lw * 4096, c.vw0, k0);
    glds<LI + 1024, SI + 4 * F_ * 2>(c.rw, c.lw * 4096, c.vw0, k0);
    glds<LI + 2048, SI + 8 * F_ * 2>(c.rw, c.lw * 4096, c.vw1, k0);
    glds<LI + 3072, SI + 12 * F_ * 2>(c.rw, c.lw * 4096, c.vw1, k0);
  }
}
template <int KIND, int LO, int HI> __device__ __forceinline__ void ld_items(const LdCtx& c) {
  if constexpr (LO < HI) { ld_item<KIND, LO>(c); ld_items<KIND, LO + 1, HI>(c); }
}
template <int KIND, int B> __device__ __forceinline__ void loader_steps(const LdCtx& c, LAS unsigned char* L) {
  if constexpr (B == CONST_AT) {
    asm volatile("" : "+v"(c.lrb), "+v"(c.lib), "+v"(c.ldb), "+v"(c.ep) : : "memory");
    const float lr = bf2f(c.lrb), li = bf2f(c.lib);
    float dt = __builtin_amdgcn_exp2f(bf2f(c.ldb) * 1.44269504088896341f);
    dt = bf2f(pk_bf16(dt, 0.f) & 0xFFFFu);
    const float rev = __builtin_amdgcn_fractf(dt * li * 0.15915494309189535f);
    const float ex = __builtin_amdgcn_exp2f(dt * lr * 1.44269504088896341f);
    const float ar = ex * __builtin_amdgcn_cosf(rev), ai = ex * __builtin_amdgcn_sinf(rev);
    const float den = 1.f / (lr * lr + li * li);
    float p16r = ar, p16i = ai;
    csq(p16r, p16i, 4);
    float p32r = p16r, p32i = p16i;
    csq(p32r, p32i, 1);
    LAS f32x4* d = (LAS f32x4*)(L + LDS_CONST + c.lw * 512 + (c.lane & 15) * 32);
    d[0] = f32x4{ar, ai, ((ar - 1.f) * lr + ai * li) * den, (ai * lr - (ar - 1.f) * li) * den};
    d[1] = f32x4{p16r, p16i, p32r, p32i};
    *(volatile LAS unsigned*)(L + LDS_TAG + 4 * (3 - c.lw)) = c.ep + 1u;
    WAIT_LDS();
  }
  if constexpr (B == 0) {
    constexpr int FIRST = KIND ? 2 : 4;
    ld_items<KIND, 0, FIRST>(c);
    KP kp = kargs();
    c.lrb = kp->Lre[c.ch]; c.lib = kp->Lim[c.ch]; c.ldb = kp->ldt[c.ch];
    c.ep = __builtin_amdgcn_raw_buffer_load_b32(mk_rsrc(kp->ws + WS_EPOCH, c.lw == 3 ? 4 : 0), 0, 0, AUX_SC1);
    CFENCE();
    ld_items<KIND, FIRST, SCH[KIND].hi[0]>(c);
  } else {
    ld_items<KIND, SCH[KIND].lo[B], SCH[KIND].hi[B]>(c);
  }
  if constexpr (KIND == 0 && B >= WARM0 && B < WARM0 + 11) warm_chunk(c, B - WARM0);
  SCHED();
  wait_vm<SCH[KIND].wait[B]>();
  BARRIER();
  if constexpr (B < LASTB) loader_steps<KIND, B + 1>(c, L);
}

__device__ __forceinline__ bool all4(LAS unsigned char* p) {
  const v4u d = *(volatile LAS v4u*)p;
  return __builtin_amdgcn_readfirstlane(d[0] & d[1] & d[2] & d[3]) != 0u;
}
__device__ __forceinline__ void lds_wait4(LAS unsigned char* p) {
#pragma nounroll
  for (unsigned spins = 0; spins < (SPIN_CAP << 4); ++spins) {
    if (all4(p)) break;
    __builtin_amdgcn_s_sleep(2);
  }
  CFENCE();
}
__device__ __forceinline__ void lds_wait(LAS unsigned char* word) {
#pragma nounroll
  for (unsigned spins = 0; spins < (SPIN_CAP << 4); ++spins) {
    if (lds_word(word) != 0u) break;
    __builtin_amdgcn_s_sleep(2);
  }
  CFENCE();
}
__device__ __forceinline__ void cmul(float ar, float ai, float br, float bi, float& cr, float& ci) {
  cr = ar * br - ai * bi; ci = ar * bi + ai * br;
}

__global__ __launch_bounds__(512, 2) void k_fused(Params) {
  extern __shared__ __attribute__((aligned(16))) unsigned char lds_[];
  LAS unsigned char* L = (LAS unsigned char*)lds_;
  STAMP(0);
  KP kp = kargs();
  u64 pX = (u64)kp->x, pWg = (u64)kp->Wg, pWu = (u64)kp->Wu, pBre = (u64)kp->Bre, pBim = (u64)kp->Bim, pCre = (u64)kp->Cre, pCim = (u64)kp->Cim, pWd = (u64)kp->Wd;
  const int b = __builtin_amdgcn_workgroup_id_x(), tid = __builtin_amdgcn_workitem_id_x(), lane = tid & 63;
  const int w = __builtin_amdgcn_readfirstlane(tid >> 6);
  const int xcd = b & 7, r = b >> 3;
  warm(mk_rs((const void*)(__builtin_amdgcn_s_getpc() & ~(u64)127), CODE_BYTES), lane < 2 ? lane * 32768 : OOB, (r * 8 + w) * 128);
  const bool light = (r & 7) == 7;
  const int ih = 28 * (xcd & 3) + r - (r >> 3), il = 112 + 4 * (xcd & 3) + (r >> 3);
  const int idx = ih + (il - ih) * (int)light;
  const int pn = 8 * (xcd >> 2) + (idx & 7), cb = idx >> 3;
  const int rot = pn & 7;
  const int R = xcd * 4 + (r & 3), N = r >> 2;
  asm volatile("" : "+s"(pX), "+s"(pWg), "+s"(pWu), "+s"(pBre), "+s"(pBim), "+s"(pCre), "+s"(pCim), "+s"(pWd));
  const int vb = (lane >> 3) * 1024 + (((lane & 7) ^ (((lane >> 2) & 4) | ((lane >> 4) & 2))) << 4);
  v4u b2[4][2][4];
  unsigned tag = 0;
  P2Ctx pc;

  if (__builtin_expect(w >= 4, 1)) {
    LdCtx c;
    c.lw = w - 4; c.rot = rot; c.lane = lane; c.vb = vb; c.sv = 0;
    c.Wd = pWd; c.Cre = pCre; c.Cim = pCim; c.vwarm = lane < 16 ? lane * 128 + (r * 4 + c.lw) * 2048 : OOB; c.xcd = xcd; c.ch = 32 * cb + 16 * (c.lw >> 1) + (lane & 15);
    const int l3 = lane >> 3, kq = lane >> 4;
    c.rx = mk_rs((const void*)pX, S_ * D_ * 2);
    c.vx = (32 * (c.lw & 1) + 16 * (l3 >> 2) + 4 * (c.lw >> 1) + (l3 & 3)) * 1024 + (((lane & 7) ^ ((4 * c.lw + kq) & 7)) << 4) + pn * 128 * 1024;
    c.rb = mk_rs((const void*)((c.lw >> 1) ? pBim : pBre), P_ * D_ * 2);
    c.vbb = (8 * (c.lw & 1) + l3) * 1024 + (((lane & 7) ^ (4 * (c.lw & 1) + kq)) << 4) + cb * 32 * 1024;
    c.lb = (c.lw >> 1) * 4096 + (c.lw & 1) * 1024;
    c.rw = mk_rs((const void*)((c.lw >> 1) ? pWu : pWg), D_ * F_ * 2);
    c.vw0 = ((c.lw & 1) * 16 + kq) * (F_ * 2) + (((lane & 15) ^ (kq << 2)) << 4) + cb * 256;
    c.vw1 = c.vw0 ^ 32;
    STAMP(1);
    if (__builtin_expect(!light, 1)) {
      loader_steps<0, 0>(c, L);
      STAMP(2);
      pc = p2_ctx(pWd, pCre, pCim, w, N);
      const int wA = c.lw < 2 ? w : c.lw, wB = wA ^ 4;
      b2_load<0, 0, 8>(pc, vb, 0, wA * 16384);
      b2_load<8192, 0, 8>(pc, vb, 1, wA * 16384);
      const P2Ctx pA = p2_ctx(pWd, pCre, pCim, wA, N), pB = p2_ctx(pWd, pCre, pCim, wB, N);
#pragma unroll
      for (int d = 0; d < 4; ++d) {
        wait_vm<8>();
        b2_read(false, L + wA * 16384 + (d & 1) * 8192, lane, b2[d]);
        WAIT_LDS();
        if (d < 2) b2_load<0, 0, 8>(pc, vb, d + 2, wA * 16384 + (d & 1) * 8192);
        else b2_load<0, 0, 8>(pA, vb, (wA & 4) + (d & 1), wA * 16384 + (d & 1) * 8192);
      }
      STAMP(3);
      lds_wait4(L + LDS_MD);
      b2_load<0, 0, 8>(pB, vb, wB & 4, wB * 16384);
      b2_load<8192, 0, 8>(pB, vb, (wB & 4) + 1, wB * 16384);
      wait_vm<0>();
      *(volatile LAS unsigned*)(L + LDS_PF + 4 * c.lw) = 1u;
      STAMP(4);
    } else {
      loader_steps<1, 0>(c, L);
      BARRIER();
      STAMP(3);
#pragma unroll
      for (int i = 0; i < 11; ++i) warm_chunk(c, i);
      lds_wait4(L + LDS_DONE + 16);
    }
    tag = lds_word(L + LDS_TAG);
  } else {
    const int c = w, q = lane >> 4, i15 = lane & 15, G = c & 1;
    const int ch = 32 * cb + 16 * (c >> 1) + i15;
    if (w == 3) {
#pragma unroll
      for (int z = 0; z < 4; ++z) *(volatile LAS v4u*)(L + LDS_DONE + 16 * z) = v4u{0u, 0u, 0u, 0u};
      WAIT_LDS();
    }
    f32x4 k0 = {0.f, 0.f, 0.f, 0.f}, k1 = {0.f, 0.f, 0.f, 0.f};
    unsigned tagv = 0;
    auto rd_const = [&]() {
      k0 = *(LAS f32x4*)(L + LDS_CONST + c * 512 + i15 * 32); k1 = *(LAS f32x4*)(L + LDS_CONST + c * 512 + i15 * 32 + 16);
      tagv = *(volatile LAS unsigned*)(L + LDS_TAG);
    };
    u64 pws = (u64)kp->ws;
    asm volatile("" : "+s"(pws));
    if (w == 2) {
      const int g = lane >> 3, j = lane & 7;
      int off = j * 65536;
      off = g == 1 ? WS_A2 + R * NK * 4096 + imin(j, 5) * 65536 : off;
      off = g == 2 ? WS_A2 + ((2 * pn + (j & 1)) * NK + ((j & 2) ? 56 + 2 * cb : 4 * cb)) * 4096 : off;
      off = g > 2 ? OOB : off;
      warm(mk_rs(kp->ws, WS_A2 + A2_BYTES), off, 0);
      warm(mk_rs(kp->out, S_ * D_ * 4), lane < 4 ? lane * 32768 + N * 256 : OOB, R * 131072);
      warm(mk_rs(kp->Dsk, D_ * 2), lane < 1 ? 0 : OOB, 0);
    }
    int xa[2];
#pragma unroll
    for (int ks = 0; ks < 2; ++ks) xa[ks] = i15 * 128 + (((4 * ks + q) ^ ((i15 >> 1) & 7)) << 4);
    asm volatile("" : "+v"(xa[0]), "+v"(xa[1]));
    const int bo = B_BASE + (c >> 1) * 2048;
    f32x4 accs[2][4], acc[4][8];
    v4u bfr[2];
    auto zero_accs = [&]() {
#pragma unroll
      for (int m = 0; m < 2; ++m)
#pragma unroll
        for (int t = 0; t < 4; ++t) { accs[m][t] = f32x4{0.f, 0.f, 0.f, 0.f}; asm volatile("" : "+v"(accs[m][t])); }
    };
    auto sc = [](int v) { asm volatile("" : "+s"(v)); return v; };

    if (!light) {
      int wa[2];
      {
        const int qq = i15 >> 2, pp = lane & 3;
#pragma unroll
        for (int u = 0; u < 2; ++u)
          wa[u] = W_BASE + (8 * q + qq) * 256 + (((2 * (2 * c + u) + (pp >> 1)) ^ ((qq << 2) | ((q & 1) << 1))) << 4) + 8 * (pp & 1);
      }
      asm volatile("" : "+v"(wa[0]), "+v"(wa[1]));
#pragma unroll
      for (int a = 0; a < 4; ++a)
#pragma unroll
        for (int u = 0; u < 8; ++u) { acc[a][u] = f32x4{0.f, 0.f, 0.f, 0.f}; asm volatile("" : "+v"(acc[a][u])); }
      zero_accs();
      v4u xf[4];
      v2u wf[2][8];
      auto rdw = [&](int s, int z) {
        const int m = z >> 2, u = (z >> 1) & 1, t = z & 1;
        wf[s & 1][z] = lds_rdtr(L + wa[u] + (s & 3) * W_SLOT + m * 8192 + t * 1024);
      };
      STAMP(1);
      BARRIER();
      STAMP(2);
#pragma unroll
      for (int z = 0; z < 8; ++z) rdw(0, z);
#pragma unroll
      for (int z = 0; z < 3; ++z) xf[z] = lds_rd128(L + xa[0] + G * 8192 + z * 2048);
#pragma unroll
      for (int m = 0; m < 2; ++m) bfr[m] = lds_rd128(L + xa[0] + bo + m * 4096);
      int xo0 = 0;
#pragma nounroll
      for (int o = 0; o < 4; ++o) {
        const int xo1 = xo0 == 2 * X_SLOT ? 0 : xo0 + X_SLOT, xo2 = xo1 == 2 * X_SLOT ? 0 : xo1 + X_SLOT;
        const int xo[5] = {xo0, xo0, xo1, xo1, xo2};
        int xb = 0, bb = 0;
#pragma unroll
        for (int s = 0; s < 4; ++s)
#pragma unroll
          for (int u = 0; u < 8; ++u) {
            const int it = 8 * s + u, sn = (it + 3) >> 3, un = (it + 3) & 7;
            SCHED();
            if (u == 0) {
              if (s == 0) { if (o > 0) BARRIER(); }
              else if (s == 1) BARRIER();
              else if (o < 3) BARRIER();
              if (s == 3) rd_const();
              SCHED();
            }
#pragma unroll
            for (int a = 0; a < 4; ++a) {
              const v4u wv = {wf[s & 1][2 * a][0], wf[s & 1][2 * a][1], wf[s & 1][2 * a + 1][0], wf[s & 1][2 * a + 1][1]};
              mfma16t(acc[a][u], wv, xf[it & 3]);
              SCHED();
              if (a == 0) {
                if ((un & 3) == 0 || it == 0) xb = xa[sn & 1] + sc(xo[sn] + ((un >> 2) ^ G) * 8192);
                xf[(it + 3) & 3] = lds_rd128(L + xb + (un & 3) * 2048);
              }
              if ((a == 1 || a == 2) && u < 4) rdw(s + 1, 2 * u + a - 1);
              if (a == 2 && (u == 4 || u == 5)) {
                if (u == 4) bb = xa[(s + 1) & 1] + sc(bo + (xo[s + 1] >> 1));
                bfr[u - 4] = lds_rd128(L + bb + (u - 4) * 4096);
              }
              SCHED();
            }
            if (u < 4) { mfma16t(accs[0][u], xf[it & 3], bfr[0]); mfma16t(accs[1][u], xf[it & 3], bfr[1]); }
          }
        xo0 = xo2;
      }
      asm volatile("s_nop 15" : "+v"(acc[0][7]), "+v"(acc[1][7]), "+v"(acc[2][7]), "+v"(acc[3][7]), "+v"(acc[0][6]), "+v"(acc[1][6]), "+v"(acc[2][6]), "+v"(acc[3][6]));
      *(volatile LAS unsigned*)(L + LDS_MD + 4 * w) = 1u;
    } else {
      zero_accs();
      int xo = 0;
#pragma nounroll
      for (int T = 0; T < 8; ++T) {
#pragma unroll
        for (int ks = 0; ks < 2; ++ks) {
          if (T < 7) BARRIER();
          v4u xf[4];
#pragma unroll
          for (int t = 0; t < 4; ++t) xf[t] = lds_rd128(L + (xa[ks] + xo + G * 8192) + t * 2048);
#pragma unroll
          for (int m = 0; m < 2; ++m) bfr[m] = lds_rd128(L + (xa[ks] + bo + (xo >> 1)) + m * 4096);
#pragma unroll
          for (int t = 0; t < 4; ++t) { mfma16t(accs[0][t], xf[t], bfr[0]); mfma16t(accs[1][t], xf[t], bfr[1]); }
          SCHED();
        }
        xo = xo == 2 * X_SLOT ? 0 : xo + X_SLOT;
      }
      asm volatile("s_nop 15" : "+v"(accs[0][0]), "+v"(accs[1][0]), "+v"(accs[0][1]), "+v"(accs[1][1]), "+v"(accs[0][2]), "+v"(accs[1][2]), "+v"(accs[0][3]), "+v"(accs[1][3]));
      rd_const();
      BARRIER();
    }
    SCHED();
    STAMP(3);
    tag = __builtin_amdgcn_readfirstlane(tagv);
    const float ar = k0[0], ai = k0[1], cr = k0[2], ci = k0[3], p16r = k1[0], p16i = k1[1], p32r = k1[2], p32i = k1[3];
    __builtin_amdgcn_s_setprio(2);
    rsrc_t ra2 = mk_rsrc((const void*)(pws + WS_A2), A2_BYTES), rcar = mk_rsrc((const void*)(pws + WS_CARRY), 32 * P_ * 16);
    const int ck = 2 * pn + G;
    float vr[16], vi[16];
    vr[0] = accs[0][0][0]; vi[0] = accs[1][0][0];
#pragma unroll
    for (int z = 1; z < 16; ++z) {
      const float t0 = __builtin_fmaf(ar, vr[z - 1], accs[0][z >> 2][z & 3]), t1 = __builtin_fmaf(ar, vi[z - 1], accs[1][z >> 2][z & 3]);
      SCHED();
      vr[z] = __builtin_fmaf(-ai, vi[z - 1], t0); vi[z] = __builtin_fmaf(ai, vr[z - 1], t1);
      SCHED();
    }
    float pr, pi;
    {
      float tr, ti;
      const auto xr = __builtin_amdgcn_permlane16_swap(f2u(vr[15]), f2u(vr[15]), false, false);
      const auto xi = __builtin_amdgcn_permlane16_swap(f2u(vi[15]), f2u(vi[15]), false, false);
      const float e0r = u2f(xr[0]), e1r = u2f(xr[1]), e0i = u2f(xi[0]), e1i = u2f(xi[1]);
      cmul(e0r, e0i, p16r, p16i, tr, ti); tr += e1r; ti += e1i;
      const auto yr = __builtin_amdgcn_permlane32_swap(f2u(tr), f2u(tr), false, false);
      const auto yi = __builtin_amdgcn_permlane32_swap(f2u(ti), f2u(ti), false, false);
      const float f0r = u2f(yr[0]), f1r = u2f(yr[1]), f0i = u2f(yi[0]), f1i = u2f(yi[1]);
      float Er, Ei;
      cmul(f0r, f0i, p32r, p32i, Er, Ei); Er += f1r; Ei += f1i;
      const float br = (q & 2) ? f0r : 0.f, bi = (q & 2) ? f0i : 0.f;
      cmul(br, bi, p16r, p16i, tr, ti);
      pr = (q & 1) ? tr + e0r : br; pi = (q & 1) ? ti + e0i : bi;
      const v4u gq = {f2u(Er), tag, f2u(Ei), ~tag};
      st_sc1(gq, rcar, q ? OOB : ch * 16, (31 - ck) * (P_ * 16));
    }
    asm volatile("" : "+v"(pr), "+v"(pi) : : "memory");
    SCHED();
    STAMP(4);
    v4u cv[8];
    const int cvo = ch * 16 + (32 - ck + 8 * q) * (P_ * 16);
    auto ld_carry = [&]() {
#pragma unroll
      for (int z = 0; z < 8; ++z) cv[z] = ld_sc1(rcar, cvo, z * (P_ * 16));
    };
    auto publish = [&](int part) {
      *(volatile LAS unsigned*)(L + LDS_DONE + 16 * part + 4 * w) = 1u;
      const bool last = all4(L + LDS_DONE + 16 * part);
      st_flag((unsigned char*)pws, WS_FA2 + part * 32768 + pn * 128 + cb * 8, tag, last && lane < 16 ? lane * 2048 : OOB);
      CFENCE();
    };
    if (!light) {
#pragma unroll
      for (int u = 0; u < 8; ++u) {
        float hv[8];
#pragma unroll
        for (int j = 0; j < 8; ++j) hv[j] = acc[j >> 2][u][j & 3] * -1.44269504088896341f;
        SCHED();
#pragma unroll
        for (int j = 0; j < 8; ++j) hv[j] = __builtin_amdgcn_exp2f(hv[j]);
        SCHED();
#pragma unroll
        for (int j = 0; j < 8; ++j) hv[j] += 1.f;
        SCHED();
#pragma unroll
        for (int j = 0; j < 8; ++j) hv[j] = __builtin_amdgcn_rcpf(hv[j]);
        SCHED();
#pragma unroll
        for (int j = 0; j < 8; ++j) hv[j] *= acc[2 + (j >> 2)][u][j & 3];
        SCHED();
        st_sc1(v4u{pk_bf16(hv[0], hv[1]), pk_bf16(hv[2], hv[3]), pk_bf16(hv[4], hv[5]), pk_bf16(hv[6], hv[7])}, ra2, lane * 16,
               ((2 * pn + ((u >> 2) ^ G)) * NK + 4 * cb + c) * 4096 + (u & 3) * 1024);
        if (u + 1 < 8) asm volatile("" : "+v"(acc[0][u + 1]), "+v"(acc[1][u + 1]) : : "memory");
      }
    } else {
      kp = kargs();
      rsrc_t rx = mk_rsrc(kp->x, S_ * D_ * 2), rg = mk_rsrc(kp->gw, D_ * 2), rgo = mk_rsrc(kp->ws + WS_G, S_ * 4);
      const int t0 = 128 * pn + 64 * (cb - 14) + 16 * c;
      v4u gv[8], xv[2][8];
#pragma unroll
      for (int z = 0; z < 8; ++z) gv[z] = __builtin_amdgcn_raw_buffer_load_b128(rg, (lane & 7) * 16, z * 128, 0);
#pragma unroll
      for (int gi = 0; gi < 2; ++gi)
#pragma unroll
        for (int z = 0; z < 8; ++z) xv[gi][z] = __builtin_amdgcn_raw_buffer_load_b128(rx, (lane >> 3) * 1024 + (lane & 7) * 16, (t0 + gi * 8) * 1024 + z * 128, 0);
#pragma unroll
      for (int gi = 0; gi < 2; ++gi) {
        float s = 0.f;
#pragma unroll
        for (int z = 0; z < 8; ++z)
#pragma unroll
          for (int y = 0; y < 4; ++y) {
            const unsigned xa_ = xv[gi][z][y], ga = gv[z][y];
            s = __builtin_amdgcn_fdot2_f32_bf16(__builtin_bit_cast(bf16x2, xa_), __builtin_bit_cast(bf16x2, ga), s, false);
          }
        s += dpp<0xB1>(s); s += dpp<0x4E>(s); s += dpp<0x141>(s);
        const float g = __builtin_amdgcn_rcpf(1.f + __builtin_amdgcn_exp2f(s * -1.44269504088896341f));
        __builtin_amdgcn_raw_buffer_store_b32(f2u(g), rgo, (lane & 7) ? OOB : (lane >> 3) * 4, (t0 + gi * 8) * 4, AUX_SC1);
      }
    }
    CFENCE();
    SCHED();
    STAMP(5);
    ld_carry();
    CFENCE();
    SCHED();
#pragma unroll
    for (int z = 0; z < 16; ++z) {
      const float t = cr * vr[z] - ci * vi[z];
      vi[z] = -cr * vi[z] - ci * vr[z]; vr[z] = t;
    }
    float qr[4], qi[4];
    qr[0] = ar; qi[0] = ai; qr[1] = ar; qi[1] = ai; csq(qr[1], qi[1], 1);
    cmul(qr[1], qi[1], ar, ai, qr[2], qi[2]); qr[3] = qr[1]; qi[3] = qi[1]; csq(qr[3], qi[3], 1);
    const float p4r = qr[3], p4i = qi[3];
    float p64r = p32r, p64i = p32i; csq(p64r, p64i, 1);
    float p512r = p64r, p512i = p64i; csq(p512r, p512i, 3);
    float p1kr = p512r, p1ki = p512i; csq(p1kr, p1ki, 1);
    float sgr, sgi, wgr, wgi;
    cmul((q & 1) ? p16r : 1.f, (q & 1) ? p16i : 0.f, (q & 2) ? p32r : 1.f, (q & 2) ? p32i : 0.f, sgr, sgi);
    cmul((q & 1) ? p512r : 1.f, (q & 1) ? p512i : 0.f, (q & 2) ? p1kr : 1.f, (q & 2) ? p1ki : 0.f, wgr, wgi);
#pragma unroll
    for (int z = 0; z < 16; ++z) asm volatile("" : "+v"(vr[z]), "+v"(vi[z]));
    asm volatile("" : "+v"(sgr), "+v"(sgi), "+v"(wgr), "+v"(wgi), "+v"(p64r), "+v"(p64i));
    SCHED();
    const int want = imin(imax(ck - 8 * q, 0), 8);
    auto carry_ok = [&]() {
      int good = 0;
#pragma unroll
      for (int z = 0; z < 8; ++z) good += ((cv[z][1] ^ tag) | (cv[z][3] ^ ~tag)) == 0u;
      return wall(good == want);
    };
    wait_vm<0>();
    publish(0);
    STAMP(14);
    if (__builtin_expect(!carry_ok(), 0)) {
#pragma nounroll
      for (unsigned spins = 0; spins < SPIN_CAP; ++spins) {
        __builtin_amdgcn_s_sleep(2);
        CFENCE();
        ld_carry();
        if (carry_ok()) break;
      }
    }
    STAMP(6);
    float Gr, Gi;
    {
      float Hr = 0.f, Hi = 0.f, tr, ti;
#pragma unroll
      for (int z = 7; z >= 0; --z) { cmul(Hr, Hi, p64r, p64i, tr, ti); Hr = tr + u2f(cv[z][0]); Hi = ti + u2f(cv[z][2]); }
      cmul(Hr, Hi, wgr, wgi, tr, ti);
      { const auto a = __builtin_amdgcn_permlane16_swap(f2u(tr), f2u(tr), false, false);
        const auto c2 = __builtin_amdgcn_permlane16_swap(f2u(ti), f2u(ti), false, false);
        tr = u2f(a[0]) + u2f(a[1]); ti = u2f(c2[0]) + u2f(c2[1]); }
      { const auto a = __builtin_amdgcn_permlane32_swap(f2u(tr), f2u(tr), false, false);
        const auto c2 = __builtin_amdgcn_permlane32_swap(f2u(ti), f2u(ti), false, false);
        tr = u2f(a[0]) + u2f(a[1]); ti = u2f(c2[0]) + u2f(c2[1]); }
      cmul(tr, ti, sgr, sgi, Hr, Hi);
      cmul(Hr + pr, Hi + pi, cr, ci, Gr, Gi);
    }
    {
      LAS unsigned char* T = L + B_BASE + (c >> 1) * (2 * B_SLOT) + (c & 1) * 4096;
      const int s32 = ((i15 >> 1) & 1) | ((i15 >> 2) & 2), s8 = (i15 & 1) | ((i15 >> 1) & 2);
#pragma unroll
      for (int t = 0; t < 4; ++t) {
        float orr[4], oi[4];
#pragma unroll
        for (int z = 0; z < 4; ++z) {
          orr[z] = __builtin_fmaf(-qi[z], Gi, __builtin_fmaf(qr[z], Gr, vr[4 * t + z]));
          oi[z] = __builtin_fmaf(-qi[z], Gr, __builtin_fmaf(-qr[z], Gi, vi[4 * t + z]));
        }
        if (t < 3) { float tr, ti; cmul(Gr, Gi, p4r, p4i, tr, ti); Gr = tr; Gi = ti; }
        LAS unsigned char* a = T + i15 * 128 + ((t ^ s32) << 5) + ((q ^ s8) << 3);
        *(LAS v2u*)a = v2u{pk_bf16(orr[0], orr[1]), pk_bf16(orr[2], orr[3])};
        *(LAS v2u*)(a + 2048) = v2u{pk_bf16(oi[0], oi[1]), pk_bf16(oi[2], oi[3])};
      }
      const int qq = i15 >> 2, pp = lane & 3;
      const int so = (ck * NK + 56 + 4 * (cb >> 1) + (cb & 1)) * 4096 + (c >> 1) * 512;
#pragma unroll
      for (int t = 0; t < 4; ++t) {
        v2u part[2];
#pragma unroll
        for (int hh = 0; hh < 2; ++hh)
          part[hh] = lds_rdtr(T + (8 * q + 4 * hh + qq) * 128 + ((t ^ ((qq >> 1) | ((q & 1) << 1))) << 5) + ((pp ^ ((qq & 1) | (hh << 1))) << 3));
        st_sc1(v4u{part[0][0], part[0][1], part[1][0], part[1][1]}, ra2, (lane >> 5) * 8192 + (lane & 31) * 16, so + t * 1024);
      }
    }
    SCHED();
    STAMP(7);
    if (!light) {
      lds_wait(L + LDS_PF + 4 * w);
      b2_read(true, L + w * 16384, lane, b2[2]);
      b2_read(true, L + w * 16384 + 8192, lane, b2[3]);
    }
    wait_vm<0>();
    publish(1);
    __builtin_amdgcn_s_setprio(0);
    STAMP(12);
  }
  kp = kargs();
  pc = p2_ctx(pWd, pCre, pCim, w, N);
  const int d0 = 2 * pc.sh;
  if (light) {
#pragma unroll
    for (int d = 0; d < 4; d += 2)
      if (d == 2 || w >= 4) {
        WAIT_LDS();
        b2_load<0, 0, 8>(pc, vb, d - d0, w * 16384);
        b2_load<8192, 0, 8>(pc, vb, d + 1 - d0, w * 16384);
        wait_vm<0>();
        b2_read(w < 4, L + w * 16384, lane, b2[d]);
        b2_read(w < 4, L + w * 16384 + 8192, lane, b2[d + 1]);
      }
  }
  if (w < 4 || light) {
    WAIT_LDS();
    b2_load<0, 0, 8>(pc, vb, 4 - d0, w * 16384);
    b2_load<8192, 0, 8>(pc, vb, 5 - d0, w * 16384);
    STAMP(13);
  }

  {
    lds_wait4(L + LDS_DONE);
    const int cl = lane & 15, lo = pc.sh ? (pc.g0 - 28) & ~1 : pc.g0 >> 1, hi = pc.sh ? (pc.g0 + pc.nd - 29) | 1 : (pc.g0 + pc.nd - 1) >> 1;
    poll_flags((GAS u64*)(kp->ws + WS_FA2) + ((pc.sh * 16 + (R & 1) * 8 + N) * 16 + (R >> 1)) * 16 + cl, tag64(tag), (cl >= lo && cl <= hi) || cl >= 14, cl < 14);
  }
  STAMP(8);
  rsrc_t ra = mk_rsrc(kp->ws + WS_A2 + (R * NK + 2 * pc.g0) * 4096, pc.nd * 8192);
  const int va = ((lane >> 4) & 1) * 1024 + (lane >> 5) * 256 + (lane & 15) * 16;
  v4u af[8];
  auto ld_a = [&](int n, int o) { af[n] = ld_sc1(ra, va + (n & 1) * 2048 + ((n >> 1) & 1) * 512, o + (n >> 2) * 4096); };
  {
    const int o = p2_ds(pc, 0) * 8192;
#pragma unroll
    for (int n = 0; n < 8; ++n) ld_a(n, o);
  }
  SCHED();
  f32x16 acc[2][2];
#pragma unroll
  for (int e = 0; e < 2; ++e)
#pragma unroll
    for (int tt = 0; tt < 2; ++tt) { acc[e][tt] = f32x16{}; asm volatile("" : "+v"(acc[e][tt])); }
  asm volatile("s_nop 1");
#pragma unroll
  for (int d = 0; d < 7; ++d) {
    if ((d >= 2 && d < 6) || w >= 4) {
      if (d == 3 && w >= 4) { WAIT_LDS(); b2_load<0, 0, 8>(pc, vb, 6, w * 16384); }
      const int o = d + 1 - d0 < pc.nd ? p2_ds(pc, d + 1 - d0) * 8192 : OOB;
#pragma unroll
      for (int n = 0; n < 8; ++n) {
        mfma32t(acc[0][n & 1], b2[d & 3][n >> 2][n & 2], af[n]);
        mfma32t(acc[1][n & 1], b2[d & 3][n >> 2][(n & 2) + 1], af[n]);
        SCHED();
        if (d < 6) ld_a(n, o);
        SCHED();
      }
      if (d == 2 || d == 3) b2_read(w < 4, L + w * 16384 + (d & 1) * 8192, lane, b2[d & 1]);
      if (d == 4) b2_read(false, L + w * 16384, lane, b2[2]);
      SCHED();
    }
  }
  asm volatile("s_nop 15\n\ts_nop 7" : "+v"(acc[0][0]), "+v"(acc[1][0]), "+v"(acc[0][1]), "+v"(acc[1][1]));
  SCHED();
  STAMP(9);
  kp = kargs();
  const int tok = 64 * R + 32 * (w & 1) + 4 * (w >> 1) + (lane >> 4);
  rsrc_t rx = mk_rsrc(kp->x, S_ * D_ * 2), rdk = mk_rsrc(kp->Dsk, D_ * 2), rgi = mk_rsrc(kp->ws + WS_G, S_ * 4);
  const v2u xv0 = __builtin_amdgcn_raw_buffer_load_b64(rx, tok * 1024 + (lane & 15) * 8, N * 128, 0);
  const v2u xv1 = __builtin_amdgcn_raw_buffer_load_b64(rx, tok * 1024 + (lane & 15) * 8, N * 128 + 16384, 0);
  const v2u dv = __builtin_amdgcn_raw_buffer_load_b64(rdk, (lane & 15) * 8, N * 128, 0);
  const float g0 = u2f(__builtin_amdgcn_raw_buffer_load_b32(rgi, tok * 4, 0, AUX_SC1));
  const float g1 = u2f(__builtin_amdgcn_raw_buffer_load_b32(rgi, tok * 4, 64, AUX_SC1));
  {
    const int t3 = lane & 3, g3 = ((t3 & 1) * 3) ^ (t3 >> 1), hi = lane >> 5;
#pragma unroll
    for (int T = 0; T < 2; ++T)
#pragma unroll
      for (int E = 0; E < 2; ++E)
#pragma unroll
        for (int g = 0; g < 4; ++g) {
          const int e = 2 * E + (g >> 1), q = 2 * (g & 1) + hi;
          *(LAS f32x4*)(L + w * 16384 + ((2 * T + ((lane >> 4) & 1)) * 4 + e) * 1024 + ((16 * q + (((((lane >> 2) & 3) ^ q) << 2) | (e ^ g3))) << 4)) =
              f32x4{acc[E][T][4 * g], acc[E][T][4 * g + 1], acc[E][T][4 * g + 2], acc[E][T][4 * g + 3]};
        }
  }
  if (b == 255 && w == 0) {
    rsrc_t rf = mk_rsrc(kp->ws + WS_FA2 + 32768, 256 * 8);
#pragma nounroll
    for (unsigned spins = 0; spins < SPIN_CAP; ++spins) {
      CFENCE();
      const v4u f0 = ld_sc1(rf, lane * 32, 0), f1 = ld_sc1(rf, lane * 32, 16);
      if (wall(((f0[0] ^ tag) | (f0[1] ^ ~tag) | (f0[2] ^ tag) | (f0[3] ^ ~tag) | (f1[0] ^ tag) | (f1[1] ^ ~tag) | (f1[2] ^ tag) | (f1[3] ^ ~tag)) == 0u)) break;
      __builtin_amdgcn_s_sleep(8);
    }
    st_flag(kp->ws, WS_EPOCH, tag, lane == 0 ? 0 : OOB);
  }
  WAIT_LDS();
  BARRIER();
  STAMP(10);
  {
    const int q = lane & 3, e = (lane >> 2) & 3, t3 = lane >> 4;
    const int base = ((w >> 1) * 4 + e) * 1024 + q * 256 + ((e ^ ((t3 & 1) * 3) ^ (t3 >> 1)) << 4);
    rsrc_t ro = mk_rsrc(kp->out, S_ * D_ * 4);
    f32x4 res[2];
#pragma unroll
    for (int hf = 0; hf < 2; ++hf) {
      f32x4 sg = {0.f, 0.f, 0.f, 0.f}, ss = {0.f, 0.f, 0.f, 0.f};
      const int a = base + (((2 * (w & 1) + hf) ^ q) << 6);
#pragma unroll
      for (int ww = 0; ww < 8; ++ww) {
        const f32x4 v = *(LAS f32x4*)(L + ww * 16384 + a);
        if (ww < 4) ss += v; else sg += v;
      }
      const float g = hf ? g1 : g0;
      f32x4 m = ss * (2.f * g) + sg * (1.f - g);
      const v2u xv = hf ? xv1 : xv0;
      m[0] += g * (bf2f(xv[0] & 0xFFFFu) * bf2f(dv[0] & 0xFFFFu)); m[1] += g * (bfhi(xv[0]) * bfhi(dv[0]));
      m[2] += g * (bf2f(xv[1] & 0xFFFFu) * bf2f(dv[1] & 0xFFFFu)); m[3] += g * (bfhi(xv[1]) * bfhi(dv[1]));
      res[hf] = m;
    }
    asm volatile("" : "+v"(res[0]), "+v"(res[1]));
#pragma unroll
    for (int hf = 0; hf < 2; ++hf)
      __builtin_amdgcn_raw_buffer_store_b128(__builtin_bit_cast(v4u, res[hf]), ro, tok * 2048 + (lane & 15) * 16 + hf * 32768 + N * 256, 0, OUT_AUX);
  }
  STAMP(11);
}

#if defined(__HIP_DEVICE_COMPILE__)
#pragma clang attribute pop
#endif

extern "C" void kernel_launch(void* const* d_in, const int* in_sizes, int n_in, void* d_out, int out_size, void* d_ws, size_t ws_size,
                              hipStream_t stream) {
  Params p;
  p.x = (const u16*)d_in[0]; p.Lre = (const u16*)d_in[1]; p.Lim = (const u16*)d_in[2]; p.Bre = (const u16*)d_in[3];
  p.Bim = (const u16*)d_in[4]; p.Cre = (const u16*)d_in[5]; p.Cim = (const u16*)d_in[6]; p.ldt = (const u16*)d_in[7];
  p.Dsk = (const u16*)d_in[8]; p.Wg = (const u16*)d_in[10]; p.Wu = (const u16*)d_in[11]; p.Wd = (const u16*)d_in[12];
  p.gw = (const u16*)d_in[13]; p.out = (float*)d_out; p.ws = (unsigned char*)d_ws;
  (void)hipFuncSetAttribute((const void*)k_fused, hipFuncAttributeMaxDynamicSharedMemorySize, LDS_BYTES);
  k_fused<<<256, 512, LDS_BYTES, stream>>>(p);
}
```

```cpp
#define PROBE 2
#define PB 130
#define PW 0
#define EXTRA 0
#include <hip/hip_runtime.h>
#include <stdint.h>

typedef unsigned short u16;
typedef unsigned long long u64;
typedef unsigned v4u __attribute__((ext_vector_type(4)));
typedef unsigned v2u __attribute__((ext_vector_type(2)));
typedef short s16x4 __attribute__((ext_vector_type(4)));
typedef __bf16 bf16x8 __attribute__((ext_vector_type(8)));
typedef __bf16 bf16x2 __attribute__((ext_vector_type(2)));
typedef float f32x2 __attribute__((ext_vector_type(2)));
typedef float f32x4 __attribute__((ext_vector_type(4)));
typedef float f32x16 __attribute__((ext_vector_type(16)));
#define LAS __attribute__((address_space(3)))
#define GAS __attribute__((address_space(1)))
#define RLX_AGENT __ATOMIC_RELAXED, __HIP_MEMORY_SCOPE_AGENT
typedef __amdgpu_buffer_rsrc_t rsrc_t;

constexpr int S_ = 2048, D_ = 512, P_ = 512, F_ = 1792;
constexpr int NK = 88;
constexpr int AUX_SC1 = 16;
#define OUT_AUX 0
constexpr int WS_EPOCH = 0;
constexpr int WS_G = 8192;
constexpr int WS_FA2 = 65536;
constexpr int CTRL_BYTES = 131072;
constexpr int WS_CARRY = 131072;
constexpr int WS_A2 = 524288;
constexpr int A2_BYTES = 32 * NK * 4096;
constexpr int WS_STAMP = 32 << 20;
constexpr int LDS_BYTES = 160 * 1024;
constexpr int X_SLOT = 16384, W_BASE = 65536, W_SLOT = 16384, B_BASE = 131072, B_SLOT = 8192;
constexpr int LDS_DONE = 155648, LDS_PF = LDS_DONE + 32, LDS_MD = LDS_DONE + 48, LDS_TAG = LDS_DONE + 64;
constexpr int LDS_DUMMY = LDS_DONE + 256, LDS_CONST = LDS_DONE + 512;
constexpr int OOB = 1 << 28;
constexpr int CODE_BYTES = 31 * 1024;
constexpr unsigned SPIN_CAP = 1u << 18;

struct Params {
  const u16 *x, *Wg, *Wu, *Bre, *Bim, *Cre, *Cim, *Wd;
  unsigned char* ws;
  const u16 *Lre, *Lim, *ldt, *Dsk, *gw;
  float* out;
};

#if defined(__HIP_DEVICE_COMPILE__)
#pragma clang attribute push(__attribute__((target("no-packed-fp32-ops"))), apply_to = function)
#endif
__device__ __forceinline__ float u2f(unsigned v) { return __builtin_bit_cast(float, v); }
__device__ __forceinline__ unsigned f2u(float v) { return __builtin_bit_cast(unsigned, v); }
__device__ __forceinline__ bool wall(bool p) { return __builtin_amdgcn_ballot_w64(!p) == 0; }
__device__ __forceinline__ bool wany(bool p) { return __builtin_amdgcn_ballot_w64(p) != 0; }
__device__ __forceinline__ int imin(int a, int b) { return a < b ? a : b; }
__device__ __forceinline__ int imax(int a, int b) { return a > b ? a : b; }
template <int CTRL> __device__ __forceinline__ float dpp(float v) { return u2f(__builtin_amdgcn_mov_dpp(f2u(v), CTRL, 0xF, 0xF, true)); }
__device__ __forceinline__ rsrc_t mk_rsrc(const void* p, int bytes) {
  return __builtin_amdgcn_make_buffer_rsrc((void*)p, 0, bytes, 0x00020000);
}
__device__ __forceinline__ float bf2f(unsigned v) { return u2f(v << 16); }
__device__ __forceinline__ float bfhi(unsigned v) { return u2f(v & 0xFFFF0000u); }
__device__ __forceinline__ unsigned pk_bf16(float lo, float hi) {
  f32x2 t = {lo, hi};
  return __builtin_bit_cast(unsigned, __builtin_convertvector(t, bf16x2));
}
__device__ __forceinline__ void mfma16t(f32x4& c, v4u a, v4u b) {
  asm volatile("v_mfma_f32_16x16x32_bf16 %0, %1, %2, %0" : "+v"(c) : "v"(a), "v"(b));
}
__device__ __forceinline__ void mfma32t(f32x16& c, v4u a, v4u b) {
  asm volatile("v_mfma_f32_32x32x16_bf16 %0, %1, %2, %0" : "+v"(c) : "v"(a), "v"(b));
}
__device__ __forceinline__ v4u lds_rd128(LAS unsigned char* p) { return *(LAS v4u*)p; }
__device__ __forceinline__ v2u lds_rdtr(LAS unsigned char* p) {
  return __builtin_bit_cast(v2u, __builtin_amdgcn_ds_read_tr16_b64_v4i16((LAS s16x4*)p));
}
__device__ __forceinline__ v4u mk_rs(const void* p, unsigned bytes) {
  const u64 a = (u64)p;
  unsigned lo = (unsigned)a, hi = (unsigned)(a >> 32) & 0xFFFFu;
  asm volatile("" : "+s"(lo), "+s"(hi));
  return v4u{lo, hi, bytes, 0x00020000u};
}
template <int LI, int SI> __device__ __forceinline__ void glds(v4u rs, int lds, int voff, int soff) {
  int t;
  asm volatile("s_add_u32 m0, %1, %5\n\ts_add_u32 %0, %4, %6\n\tbuffer_load_dwordx4 %2, %3, %0 offen lds"
               : "=&s"(t) : "s"(lds), "v"(voff), "s"(rs), "s"(soff), "i"(LI), "i"(SI) : "memory", "scc");
}
typedef const __attribute__((address_space(4))) Params* KP;
__device__ __forceinline__ KP kargs() {
  KP k = (KP)__builtin_amdgcn_kernarg_segment_ptr();
  asm volatile("" : "+s"(k));
  return k;
}
__device__ __forceinline__ void warm(v4u rs, int voff, int soff) {
  asm volatile("s_mov_b32 m0, %3\n\ts_nop 0\n\tbuffer_load_dword %0, %1, %2 offen lds" : : "v"(voff), "s"(rs), "s"(soff), "i"(LDS_DUMMY) : "memory");
}
__device__ __forceinline__ v4u ld_sc1(rsrc_t rs, int voff, int soff) { return __builtin_amdgcn_raw_buffer_load_b128(rs, voff, soff, AUX_SC1); }
__device__ __forceinline__ void st_sc1(v4u v, rsrc_t rs, int voff, int soff) { __builtin_amdgcn_raw_buffer_store_b128(v, rs, voff + soff, 0, AUX_SC1); }
__device__ __forceinline__ u64 tag64(unsigned tag) { return ((u64)(~tag) << 32) | tag; }
__device__ __forceinline__ void st_flag(unsigned char* ws, int off, unsigned tag, int voff) {
  __builtin_amdgcn_raw_buffer_store_b64(v2u{tag, ~tag}, mk_rsrc(ws, CTRL_BYTES), voff, off, AUX_SC1);
}
__device__ __forceinline__ unsigned lds_word(LAS unsigned char* p) { return __builtin_amdgcn_readfirstlane(*(volatile LAS unsigned*)p); }
#define BARRIER() asm volatile("s_barrier" ::: "memory")
#define SCHED() __builtin_amdgcn_sched_barrier(0)
#define CFENCE() asm volatile("" ::: "memory")
#define WAIT_LDS() asm volatile("s_waitcnt lgkmcnt(0)" ::: "memory")
template <int N> __device__ __forceinline__ void wait_vm() { asm volatile("s_waitcnt vmcnt(%c0)" ::"i"(N) : "memory"); }

#define STAMP(i) do { if ((i) == PROBE) stamp_[i] = __builtin_amdgcn_s_memrealtime(); } while (0)
#define POLL_SLEEP 4
#define POLL_IDLE 16

__device__ __forceinline__ void poll_flags(GAS u64* f, u64 want, bool care, bool late) {
#pragma nounroll
  for (unsigned spins = 0; spins < SPIN_CAP; ++spins) {
    CFENCE();
    const u64 v = __hip_atomic_load(f, RLX_AGENT);
    if (wall(!care || v == want)) break;
    if (wany(late && v == want)) __builtin_amdgcn_s_sleep(POLL_SLEEP); else __builtin_amdgcn_s_sleep(POLL_IDLE);
  }
  CFENCE();
}
__device__ __forceinline__ void csq(float& r, float& i, int n) {
#pragma unroll
  for (int z = 0; z < n; ++z) { const float t = r * r - i * i; i = 2.f * r * i; r = t; }
}

#define NROT 100
#define WARM0 3
#define CONST_AT 13
constexpr int LASTB = 13;
struct Sched {
  int n;
  int type[40], idx[40];
  int lo[LASTB + 1], hi[LASTB + 1];
  int wait[LASTB + 1];
  bool ok;
};
constexpr Sched make_sched(int kind) {
  Sched s{};
  int cnt[40] = {}, need[40] = {}, avail[40] = {};
  int n = 0;
  auto add = [&](int ty, int ix, int nd, int av) {
    if (kind && ty == 2) return;
    s.type[n] = ty; s.idx[n] = ix; cnt[n] = ty == 2 ? 4 : 3; need[n] = nd < LASTB ? nd : LASTB; avail[n] = av; ++n;
  };
  add(0, 0, 0, -1); add(1, 0, 0, -1); add(2, 0, 0, -1); add(2, 1, 0, -1);
  add(0, 1, 1, -1); add(1, 1, 1, -1); add(2, 2, 1, -1); add(2, 3, 2, -1);
  add(0, 2, 3, -1); add(1, 2, 3, -1);
  add(2, 4, 3, 1); add(2, 5, 4, 1);
  for (int b = 2; b < 12; ++b) {
    const int T = b / 2 + 2;
    add(b & 1, T, 2 * T - 1, kind ? (b & ~1) : b);
    add(2, b + 4, b + 3, b);
  }
  s.n = n;
  s.ok = true;
  const int LIM = 8;
  int endpos[40] = {};
  int next = 0, total = 0;
  for (int b = 0; b <= LASTB; ++b) {
    s.lo[b] = next;
    const int start = total;
    for (;;) {
      if (next >= n || avail[next] > b - 1) break;
      int lastneed = 0;
      for (int i = 0; i < next; ++i) if (need[i] <= b) lastneed = endpos[i];
      if (b < 3 && need[next] > b && total + cnt[next] - lastneed > LIM + 2 * b) break;
      if (b >= 3 && need[next] > b + 1 && total + cnt[next] - start > 11) break;
      total += cnt[next]; endpos[next] = total; ++next;
    }
    s.hi[b] = next;
    if (kind == 0 && b >= WARM0 && b < WARM0 + 11) total += 1;
    int lastneed = 0;
    for (int i = 0; i < next; ++i) if (need[i] <= b) lastneed = endpos[i];
    for (int i = next; i < n; ++i) if (need[i] <= b) s.ok = false;
    s.wait[b] = total - lastneed;
  }
  if (next != n || WARM0 + 10 > LASTB) s.ok = false;
  return s;
}
constexpr Sched SCH[2] = {make_sched(0), make_sched(1)};
static_assert(SCH[0].ok && SCH[1].ok, "every ring item must be issued in time");

struct P2Ctx { u64 a0, a1; int so0, sh, g0, nd, rot; unsigned bytes; };
__device__ __forceinline__ P2Ctx p2_ctx(u64 Wd, u64 Cre, u64 Cim, int w, int N) {
  P2Ctx c;
  const int ssm = w < 4;
  const u64 m = 0 - (u64)ssm;
  c.a0 = (Cre & m) | (Wd & ~m); c.a1 = (Cim & m) | (Wd & ~m);
  c.sh = ssm;
  c.so0 = N * 128 + ssm * (N * (65536 - 128) - 14 * 128);
  c.bytes = F_ * D_ * 2u - ssm * (F_ * D_ * 2u - D_ * P_ * 2u);
  c.g0 = 7 * w - 28 + ssm * (56 - 3 * w);
  c.nd = 7 - 3 * ssm;
#define P2ROT 1
  c.rot = P2ROT == 0 ? 0 : (P2ROT == 1 ? (N & 1) * (3 - ssm) : (P2ROT == 2 ? (N & 3) * (2 - ssm) : (N * c.nd) >> 3));
  return c;
}
__device__ __forceinline__ int p2_ds(const P2Ctx& c, int d) { const int t = d + c.rot; return t >= c.nd ? t - c.nd : t; }
__device__ __forceinline__ void b2_src(const P2Ctx& c, int d, v4u& rs, int& so) {
  const int g = c.g0 + p2_ds(c, d);
  const u64 x0 = c.a0, x1 = c.a1;
  rs = mk_rs((const void*)((g & 1) ? x1 : x0), c.bytes);
  so = c.so0 + (c.sh ? (g >> 1) * 128 : g * 65536);
}
template <int LI, int N0, int N1> __device__ __forceinline__ void b2_rows(v4u rs, int lds, int vb, int so) {
  if constexpr (N0 < N1) { glds<LI, N0 * 8192>(rs, lds, (N0 & 1) ? vb ^ 16 : vb, so); b2_rows<LI + 1024, N0 + 1, N1>(rs, lds, vb, so); }
}
template <int LI, int N0, int N1> __device__ __forceinline__ void b2_load(const P2Ctx& c, int vb, int d, int lds) {
  v4u rs; int so;
  b2_src(c, d, rs, so);
  b2_rows<LI, N0, N1>(rs, lds, vb, so);
}
__device__ __forceinline__ void b2_read(bool ssm, LAS unsigned char* img, int lane, v4u (&f)[2][4]) {
  if (ssm) {
    const int i = lane & 31, sw = ((i & 2) << 1) | ((i >> 1) & 2) | ((i >> 3) & 1);
#pragma unroll
    for (int h = 0; h < 2; ++h)
#pragma unroll
      for (int z = 0; z < 4; ++z) f[h][z] = lds_rd128(img + i * 128 + (((4 * h + (z & 2) + (lane >> 5)) ^ sw) << 4) + (z & 1) * 4096);
  } else {
    const int qq = (lane >> 2) & 3, hi = lane >> 5;
    const int c0 = (((lane >> 4) & 1) * 2 + ((lane >> 1) & 1)) ^ (((qq & 2) << 1) | (hi << 1));
#pragma unroll
    for (int h = 0; h < 2; ++h)
#pragma unroll
      for (int z = 0; z < 4; ++z) {
        LAS unsigned char* a = img + (4 * hi + qq) * 128 + ((c0 ^ ((z & 1) << 2) ^ (z >> 1)) << 4) + (lane & 1) * 8 + h * 4096 + (z >> 1) * 1024;
        const v2u lo = lds_rdtr(a), hi2 = lds_rdtr(a + 2048);
        f[h][z] = v4u{lo[0], lo[1], hi2[0], hi2[1]};
      }
  }
}

struct LdCtx { v4u rx, rw, rb; u64 Wd, Cre, Cim; int lw, lb, vx, vw0, vw1, vbb, rot, lane, vb, vwarm, xcd, ch; mutable unsigned sv, lrb, lib, ldb, ep; };
__device__ __forceinline__ void warm_chunk(const LdCtx& c, int slot) {
  int xc = c.xcd;
  asm volatile("" : "+s"(xc));
  int i = slot < 6 ? slot + xc : slot - 6 + xc;
  if (slot < 6) { i = i >= 12 ? i - 12 : (i >= 6 ? i - 6 : i); } else { i = 6 + (i >= 10 ? i - 10 : (i >= 5 ? i - 5 : i)); }
  const u64 x0 = c.Wd, x1 = c.Cre, x2 = c.Cim;
  const u64 src = i < 7 ? x0 : (i < 9 ? x1 : x2);
  const unsigned bytes = i < 7 ? F_ * D_ * 2 : D_ * P_ * 2;
  warm(mk_rs((const void*)src, bytes), c.vwarm, (i < 7 ? i : ((i - 7) & 1)) * 262144);
}

template <int KIND, int I> __device__ __forceinline__ void ld_item(const LdCtx& c) {
  constexpr int ty = SCH[KIND].type[I], t = SCH[KIND].idx[I];
  int rot = c.rot;
  asm volatile("" : "+s"(rot));
  if constexpr (ty < 2) {
    const int k0 = ((t + rot) & 7) * 128;
    glds<(t % 3) * X_SLOT + 2 * ty * 4096, ty * 65536>(c.rx, c.lw * 1024, c.vx, k0);
    glds<(t % 3) * X_SLOT + (2 * ty + 1) * 4096, ty * 65536 + 8192>(c.rx, c.lw * 1024, c.vx, k0);
    glds<B_BASE + (t % 3) * B_SLOT + ty * 2048, ty * 16384>(c.rb, c.lb, c.vbb, k0);
  } else {
    const int k0 = (((t >> 1) + rot) & 7) * (64 * F_ * 2);
    constexpr int LI = W_BASE + (t & 3) * W_SLOT, SI = (t & 1) * 32 * F_ * 2;
    glds<LI, SI>(c.rw, c.lw * 4096, c.vw0, k0);
    glds<LI + 1024, SI + 4 * F_ * 2>(c.rw, c.lw * 4096, c.vw0, k0);
    glds<LI + 2048, SI + 8 * F_ * 2>(c.rw, c.lw * 4096, c.vw1, k0);
    glds<LI + 3072, SI + 12 * F_ * 2>(c.rw, c.lw * 4096, c.vw1, k0);
  }
}
template <int KIND, int LO, int HI> __device__ __forceinline__ void ld_items(const LdCtx& c) {
  if constexpr (LO < HI) { ld_item<KIND, LO>(c); ld_items<KIND, LO + 1, HI>(c); }
}
template <int KIND, int B> __device__ __forceinline__ void loader_steps(const LdCtx& c, LAS unsigned char* L) {
  if constexpr (B == CONST_AT) {
    asm volatile("" : "+v"(c.lrb), "+v"(c.lib), "+v"(c.ldb), "+v"(c.ep) : : "memory");
    const float lr = bf2f(c.lrb), li = bf2f(c.lib);
    float dt = __builtin_amdgcn_exp2f(bf2f(c.ldb) * 1.44269504088896341f);
    dt = bf2f(pk_bf16(dt, 0.f) & 0xFFFFu);
    const float rev = __builtin_amdgcn_fractf(dt * li * 0.15915494309189535f);
    const float ex = __builtin_amdgcn_exp2f(dt * lr * 1.44269504088896341f);
    const float ar = ex * __builtin_amdgcn_cosf(rev), ai = ex * __builtin_amdgcn_sinf(rev);
    const float den = 1.f / (lr * lr + li * li);
    float p16r = ar, p16i = ai;
    csq(p16r, p16i, 4);
    float p32r = p16r, p32i = p16i;
    csq(p32r, p32i, 1);
    LAS f32x4* d = (LAS f32x4*)(L + LDS_CONST + c.lw * 512 + (c.lane & 15) * 32);
    d[0] = f32x4{ar, ai, ((ar - 1.f) * lr + ai * li) * den, (ai * lr - (ar - 1.f) * li) * den};
    d[1] = f32x4{p16r, p16i, p32r, p32i};
    *(volatile LAS unsigned*)(L + LDS_TAG + 4 * (3 - c.lw)) = c.ep + 1u;
    WAIT_LDS();
  }
  if constexpr (B == 0) {
    constexpr int FIRST = KIND ? 2 : 4;
    ld_items<KIND, 0, FIRST>(c);
    KP kp = kargs();
    c.lrb = kp->Lre[c.ch]; c.lib = kp->Lim[c.ch]; c.ldb = kp->ldt[c.ch];
    c.ep = __builtin_amdgcn_raw_buffer_load_b32(mk_rsrc(kp->ws + WS_EPOCH, c.lw == 3 ? 4 : 0), 0, 0, AUX_SC1);
    CFENCE();
    ld_items<KIND, FIRST, SCH[KIND].hi[0]>(c);
  } else {
    ld_items<KIND, SCH[KIND].lo[B], SCH[KIND].hi[B]>(c);
  }
  if constexpr (KIND == 0 && B >= WARM0 && B < WARM0 + 11) warm_chunk(c, B - WARM0);
  SCHED();
  wait_vm<SCH[KIND].wait[B]>();
  BARRIER();
  if constexpr (B < LASTB) loader_steps<KIND, B + 1>(c, L);
}

__device__ __forceinline__ bool all4(LAS unsigned char* p) {
  const v4u d = *(volatile LAS v4u*)p;
  return __builtin_amdgcn_readfirstlane(d[0] & d[1] & d[2] & d[3]) != 0u;
}
__device__ __forceinline__ void lds_wait4(LAS unsigned char* p) {
#pragma nounroll
  for (unsigned spins = 0; spins < (SPIN_CAP << 4); ++spins) {
    if (all4(p)) break;
    __builtin_amdgcn_s_sleep(2);
  }
  CFENCE();
}
__device__ __forceinline__ void lds_wait(LAS unsigned char* word) {
#pragma nounroll
  for (unsigned spins = 0; spins < (SPIN_CAP << 4); ++spins) {
    if (lds_word(word) != 0u) break;
    __builtin_amdgcn_s_sleep(2);
  }
  CFENCE();
}
__device__ __forceinline__ void cmul(float ar, float ai, float br, float bi, float& cr, float& ci) {
  cr = ar * br - ai * bi; ci = ar * bi + ai * br;
}

__global__ __launch_bounds__(512, 2) void k_fused(Params) {
  extern __shared__ __attribute__((aligned(16))) unsigned char lds_[];
  LAS unsigned char* L = (LAS unsigned char*)lds_;
  u64 stamp_[16] = {};
  STAMP(0);
  KP kp = kargs();
  u64 pX = (u64)kp->x, pWg = (u64)kp->Wg, pWu = (u64)kp->Wu, pBre = (u64)kp->Bre, pBim = (u64)kp->Bim, pCre = (u64)kp->Cre, pCim = (u64)kp->Cim, pWd = (u64)kp->Wd;
  const int b = __builtin_amdgcn_workgroup_id_x(), tid = __builtin_amdgcn_workitem_id_x(), lane = tid & 63;
  const int w = __builtin_amdgcn_readfirstlane(tid >> 6);
  const int xcd = b & 7, r = b >> 3;
  warm(mk_rs((const void*)(__builtin_amdgcn_s_getpc() & ~(u64)127), CODE_BYTES), lane < 2 ? lane * 32768 : OOB, (r * 8 + w) * 128);
  const bool light = (r & 7) == 7;
  const int ih = 28 * (xcd & 3) + r - (r >> 3), il = 112 + 4 * (xcd & 3) + (r >> 3);
  const int idx = ih + (il - ih) * (int)light;
  const int pn = 8 * (xcd >> 2) + (idx & 7), cb = idx >> 3;
  const int rot = pn & 7;
  const int R = xcd * 4 + (r & 3), N = r >> 2;
  asm volatile("" : "+s"(pX), "+s"(pWg), "+s"(pWu), "+s"(pBre), "+s"(pBim), "+s"(pCre), "+s"(pCim), "+s"(pWd));
  const int vb = (lane >> 3) * 1024 + (((lane & 7) ^ (((lane >> 2) & 4) | ((lane >> 4) & 2))) << 4);
  v4u b2[4][2][4];
  unsigned tag = 0;
  P2Ctx pc;

  if (__builtin_expect(w >= 4, 1)) {
    LdCtx c;
    c.lw = w - 4; c.rot = rot; c.lane = lane; c.vb = vb; c.sv = 0;
    c.Wd = pWd; c.Cre = pCre; c.Cim = pCim; c.vwarm = lane < 16 ? lane * 128 + (r * 4 + c.lw) * 2048 : OOB; c.xcd = xcd; c.ch = 32 * cb + 16 * (c.lw >> 1) + (lane & 15);
    const int l3 = lane >> 3, kq = lane >> 4;
    c.rx = mk_rs((const void*)pX, S_ * D_ * 2);
    c.vx = (32 * (c.lw & 1) + 16 * (l3 >> 2) + 4 * (c.lw >> 1) + (l3 & 3)) * 1024 + (((lane & 7) ^ ((4 * c.lw + kq) & 7)) << 4) + pn * 128 * 1024;
    c.rb = mk_rs((const void*)((c.lw >> 1) ? pBim : pBre), P_ * D_ * 2);
    c.vbb = (8 * (c.lw & 1) + l3) * 1024 + (((lane & 7) ^ (4 * (c.lw & 1) + kq)) << 4) + cb * 32 * 1024;
    c.lb = (c.lw >> 1) * 4096 + (c.lw & 1) * 1024;
    c.rw = mk_rs((const void*)((c.lw >> 1) ? pWu : pWg), D_ * F_ * 2);
    c.vw0 = ((c.lw & 1) * 16 + kq) * (F_ * 2) + (((lane & 15) ^ (kq << 2)) << 4) + cb * 256;
    c.vw1 = c.vw0 ^ 32;
    STAMP(1);
    if (__builtin_expect(!light, 1)) {
      loader_steps<0, 0>(c, L);
      STAMP(2);
      pc = p2_ctx(pWd, pCre, pCim, w, N);
      const int wA = c.lw < 2 ? w : c.lw, wB = wA ^ 4;
      b2_load<0, 0, 8>(pc, vb, 0, wA * 16384);
      b2_load<8192, 0, 8>(pc, vb, 1, wA * 16384);
      const P2Ctx pA = p2_ctx(pWd, pCre, pCim, wA, N), pB = p2_ctx(pWd, pCre, pCim, wB, N);
#pragma unroll
      for (int d = 0; d < 4; ++d) {
        wait_vm<8>();
        b2_read(false, L + wA * 16384 + (d & 1) * 8192, lane, b2[d]);
        WAIT_LDS();
        if (d < 2) b2_load<0, 0, 8>(pc, vb, d + 2, wA * 16384 + (d & 1) * 8192);
        else b2_load<0, 0, 8>(pA, vb, (wA & 4) + (d & 1), wA * 16384 + (d & 1) * 8192);
      }
      STAMP(3);
      lds_wait4(L + LDS_MD);
      b2_load<0, 0, 8>(pB, vb, wB & 4, wB * 16384);
      b2_load<8192, 0, 8>(pB, vb, (wB & 4) + 1, wB * 16384);
      wait_vm<0>();
      *(volatile LAS unsigned*)(L + LDS_PF + 4 * c.lw) = 1u;
      STAMP(4);
    } else {
      loader_steps<1, 0>(c, L);
      BARRIER();
      STAMP(3);
#pragma unroll
      for (int i = 0; i < 11; ++i) warm_chunk(c, i);
      lds_wait4(L + LDS_DONE + 16);
    }
    tag = lds_word(L + LDS_TAG);
  } else {
    const int c = w, q = lane >> 4, i15 = lane & 15, G = c & 1;
    const int ch = 32 * cb + 16 * (c >> 1) + i15;
    if (w == 3) {
#pragma unroll
      for (int z = 0; z < 4; ++z) *(volatile LAS v4u*)(L + LDS_DONE + 16 * z) = v4u{0u, 0u, 0u, 0u};
      WAIT_LDS();
    }
    f32x4 k0 = {0.f, 0.f, 0.f, 0.f}, k1 = {0.f, 0.f, 0.f, 0.f};
    unsigned tagv = 0;
    auto rd_const = [&]() {
      k0 = *(LAS f32x4*)(L + LDS_CONST + c * 512 + i15 * 32); k1 = *(LAS f32x4*)(L + LDS_CONST + c * 512 + i15 * 32 + 16);
      tagv = *(volatile LAS unsigned*)(L + LDS_TAG);
    };
    u64 pws = (u64)kp->ws;
    asm volatile("" : "+s"(pws));
    if (w == 2) {
      const int g = lane >> 3, j = lane & 7;
      int off = j * 65536;
      off = g == 1 ? WS_A2 + R * NK * 4096 + imin(j, 5) * 65536 : off;
      off = g == 2 ? WS_A2 + ((2 * pn + (j & 1)) * NK + ((j & 2) ? 56 + 2 * cb : 4 * cb)) * 4096 : off;
      off = g > 2 ? OOB : off;
      warm(mk_rs(kp->ws, WS_A2 + A2_BYTES), off, 0);
      warm(mk_rs(kp->out, S_ * D_ * 4), lane < 4 ? lane * 32768 + N * 256 : OOB, R * 131072);
      warm(mk_rs(kp->Dsk, D_ * 2), lane < 1 ? 0 : OOB, 0);
    }
    int xa[2];
#pragma unroll
    for (int ks = 0; ks < 2; ++ks) xa[ks] = i15 * 128 + (((4 * ks + q) ^ ((i15 >> 1) & 7)) << 4);
    asm volatile("" : "+v"(xa[0]), "+v"(xa[1]));
    const int bo = B_BASE + (c >> 1) * 2048;
    f32x4 accs[2][4], acc[4][8];
    v4u bfr[2];
    auto zero_accs = [&]() {
#pragma unroll
      for (int m = 0; m < 2; ++m)
#pragma unroll
        for (int t = 0; t < 4; ++t) { accs[m][t] = f32x4{0.f, 0.f, 0.f, 0.f}; asm volatile("" : "+v"(accs[m][t])); }
    };
    auto sc = [](int v) { asm volatile("" : "+s"(v)); return v; };

    if (!light) {
      int wa[2];
      {
        const int qq = i15 >> 2, pp = lane & 3;
#pragma unroll
        for (int u = 0; u < 2; ++u)
          wa[u] = W_BASE + (8 * q + qq) * 256 + (((2 * (2 * c + u) + (pp >> 1)) ^ ((qq << 2) | ((q & 1) << 1))) << 4) + 8 * (pp & 1);
      }
      asm volatile("" : "+v"(wa[0]), "+v"(wa[1]));
#pragma unroll
      for (int a = 0; a < 4; ++a)
#pragma unroll
        for (int u = 0; u < 8; ++u) { acc[a][u] = f32x4{0.f, 0.f, 0.f, 0.f}; asm volatile("" : "+v"(acc[a][u])); }
      zero_accs();
      v4u xf[4];
      v2u wf[2][8];
      auto rdw = [&](int s, int z) {
        const int m = z >> 2, u = (z >> 1) & 1, t = z & 1;
        wf[s & 1][z] = lds_rdtr(L + wa[u] + (s & 3) * W_SLOT + m * 8192 + t * 1024);
      };
      STAMP(1);
      BARRIER();
      STAMP(2);
#pragma unroll
      for (int z = 0; z < 8; ++z) rdw(0, z);
#pragma unroll
      for (int z = 0; z < 3; ++z) xf[z] = lds_rd128(L + xa[0] + G * 8192 + z * 2048);
#pragma unroll
      for (int m = 0; m < 2; ++m) bfr[m] = lds_rd128(L + xa[0] + bo + m * 4096);
      int xo0 = 0;
#pragma nounroll
      for (int o = 0; o < 4; ++o) {
        const int xo1 = xo0 == 2 * X_SLOT ? 0 : xo0 + X_SLOT, xo2 = xo1 == 2 * X_SLOT ? 0 : xo1 + X_SLOT;
        const int xo[5] = {xo0, xo0, xo1, xo1, xo2};
        int xb = 0, bb = 0;
#pragma unroll
        for (int s = 0; s < 4; ++s)
#pragma unroll
          for (int u = 0; u < 8; ++u) {
            const int it = 8 * s + u, sn = (it + 3) >> 3, un = (it + 3) & 7;
            SCHED();
            if (u == 0) {
              if (s == 0) { if (o > 0) BARRIER(); }
              else if (s == 1) BARRIER();
              else if (o < 3) BARRIER();
              if (s == 3) rd_const();
              SCHED();
            }
#pragma unroll
            for (int a = 0; a < 4; ++a) {
              const v4u wv = {wf[s & 1][2 * a][0], wf[s & 1][2 * a][1], wf[s & 1][2 * a + 1][0], wf[s & 1][2 * a + 1][1]};
              mfma16t(acc[a][u], wv, xf[it & 3]);
              SCHED();
              if (a == 0) {
                if ((un & 3) == 0 || it == 0) xb = xa[sn & 1] + sc(xo[sn] + ((un >> 2) ^ G) * 8192);
                xf[(it + 3) & 3] = lds_rd128(L + xb + (un & 3) * 2048);
              }
              if ((a == 1 || a == 2) && u < 4) rdw(s + 1, 2 * u + a - 1);
              if (a == 2 && (u == 4 || u == 5)) {
                if (u == 4) bb = xa[(s + 1) & 1] + sc(bo + (xo[s + 1] >> 1));
                bfr[u - 4] = lds_rd128(L + bb + (u - 4) * 4096);
              }
              SCHED();
            }
            if (u < 4) { mfma16t(accs[0][u], xf[it & 3], bfr[0]); mfma16t(accs[1][u], xf[it & 3], bfr[1]); }
          }
        xo0 = xo2;
      }
      asm volatile("s_nop 15" : "+v"(acc[0][7]), "+v"(acc[1][7]), "+v"(acc[2][7]), "+v"(acc[3][7]), "+v"(acc[0][6]), "+v"(acc[1][6]), "+v"(acc[2][6]), "+v"(acc[3][6]));
      *(volatile LAS unsigned*)(L + LDS_MD + 4 * w) = 1u;
    } else {
      zero_accs();
      int xo = 0;
#pragma nounroll
      for (int T = 0; T < 8; ++T) {
#pragma unroll
        for (int ks = 0; ks < 2; ++ks) {
          if (T < 7) BARRIER();
          v4u xf[4];
#pragma unroll
          for (int t = 0; t < 4; ++t) xf[t] = lds_rd128(L + (xa[ks] + xo + G * 8192) + t * 2048);
#pragma unroll
          for (int m = 0; m < 2; ++m) bfr[m] = lds_rd128(L + (xa[ks] + bo + (xo >> 1)) + m * 4096);
#pragma unroll
          for (int t = 0; t < 4; ++t) { mfma16t(accs[0][t], xf[t], bfr[0]); mfma16t(accs[1][t], xf[t], bfr[1]); }
          SCHED();
        }
        xo = xo == 2 * X_SLOT ? 0 : xo + X_SLOT;
      }
      asm volatile("s_nop 15" : "+v"(accs[0][0]), "+v"(accs[1][0]), "+v"(accs[0][1]), "+v"(accs[1][1]), "+v"(accs[0][2]), "+v"(accs[1][2]), "+v"(accs[0][3]), "+v"(accs[1][3]));
      rd_const();
      BARRIER();
    }
    SCHED();
    STAMP(3);
    tag = __builtin_amdgcn_readfirstlane(tagv);
    const float ar = k0[0], ai = k0[1], cr = k0[2], ci = k0[3], p16r = k1[0], p16i = k1[1], p32r = k1[2], p32i = k1[3];
    __builtin_amdgcn_s_setprio(2);
    rsrc_t ra2 = mk_rsrc((const void*)(pws + WS_A2), A2_BYTES), rcar = mk_rsrc((const void*)(pws + WS_CARRY), 32 * P_ * 16);
    const int ck = 2 * pn + G;
    float vr[16], vi[16];
    vr[0] = accs[0][0][0]; vi[0] = accs[1][0][0];
#pragma unroll
    for (int z = 1; z < 16; ++z) {
      const float t0 = __builtin_fmaf(ar, vr[z - 1], accs[0][z >> 2][z & 3]), t1 = __builtin_fmaf(ar, vi[z - 1], accs[1][z >> 2][z & 3]);
      SCHED();
      vr[z] = __builtin_fmaf(-ai, vi[z - 1], t0); vi[z] = __builtin_fmaf(ai, vr[z - 1], t1);
      SCHED();
    }
    float pr, pi;
    {
      float tr, ti;
      const auto xr = __builtin_amdgcn_permlane16_swap(f2u(vr[15]), f2u(vr[15]), false, false);
      const auto xi = __builtin_amdgcn_permlane16_swap(f2u(vi[15]), f2u(vi[15]), false, false);
      const float e0r = u2f(xr[0]), e1r = u2f(xr[1]), e0i = u2f(xi[0]), e1i = u2f(xi[1]);
      cmul(e0r, e0i, p16r, p16i, tr, ti); tr += e1r; ti += e1i;
      const auto yr = __builtin_amdgcn_permlane32_swap(f2u(tr), f2u(tr), false, false);
      const auto yi = __builtin_amdgcn_permlane32_swap(f2u(ti), f2u(ti), false, false);
      const float f0r = u2f(yr[0]), f1r = u2f(yr[1]), f0i = u2f(yi[0]), f1i = u2f(yi[1]);
      float Er, Ei;
      cmul(f0r, f0i, p32r, p32i, Er, Ei); Er += f1r; Ei += f1i;
      const float br = (q & 2) ? f0r : 0.f, bi = (q & 2) ? f0i : 0.f;
      cmul(br, bi, p16r, p16i, tr, ti);
      pr = (q & 1) ? tr + e0r : br; pi = (q & 1) ? ti + e0i : bi;
      const v4u gq = {f2u(Er), tag, f2u(Ei), ~tag};
      st_sc1(gq, rcar, q ? OOB : ch * 16, (31 - ck) * (P_ * 16));
    }
    asm volatile("" : "+v"(pr), "+v"(pi) : : "memory");
    SCHED();
    STAMP(4);
    v4u cv[8];
    const int cvo = ch * 16 + (32 - ck + 8 * q) * (P_ * 16);
    auto ld_carry = [&]() {
#pragma unroll
      for (int z = 0; z < 8; ++z) cv[z] = ld_sc1(rcar, cvo, z * (P_ * 16));
    };
    auto publish = [&](int part) {
      *(volatile LAS unsigned*)(L + LDS_DONE + 16 * part + 4 * w) = 1u;
      const bool last = all4(L + LDS_DONE + 16 * part);
      st_flag((unsigned char*)pws, WS_FA2 + part * 32768 + pn * 128 + cb * 8, tag, last && lane < 16 ? lane * 2048 : OOB);
      CFENCE();
    };
    if (!light) {
#pragma unroll
      for (int u = 0; u < 8; ++u) {
        float hv[8];
#pragma unroll
        for (int j = 0; j < 8; ++j) hv[j] = acc[j >> 2][u][j & 3] * -1.44269504088896341f;
        SCHED();
#pragma unroll
        for (int j = 0; j < 8; ++j) hv[j] = __builtin_amdgcn_exp2f(hv[j]);
        SCHED();
#pragma unroll
        for (int j = 0; j < 8; ++j) hv[j] += 1.f;
        SCHED();
#pragma unroll
        for (int j = 0; j < 8; ++j) hv[j] = __builtin_amdgcn_rcpf(hv[j]);
        SCHED();
#pragma unroll
        for (int j = 0; j < 8; ++j) hv[j] *= acc[2 + (j >> 2)][u][j & 3];
        SCHED();
        st_sc1(v4u{pk_bf16(hv[0], hv[1]), pk_bf16(hv[2], hv[3]), pk_bf16(hv[4], hv[5]), pk_bf16(hv[6], hv[7])}, ra2, lane * 16,
               ((2 * pn + ((u >> 2) ^ G)) * NK + 4 * cb + c) * 4096 + (u & 3) * 1024);
        if (u + 1 < 8) asm volatile("" : "+v"(acc[0][u + 1]), "+v"(acc[1][u + 1]) : : "memory");
      }
    } else {
      kp = kargs();
      rsrc_t rx = mk_rsrc(kp->x, S_ * D_ * 2), rg = mk_rsrc(kp->gw, D_ * 2), rgo = mk_rsrc(kp->ws + WS_G, S_ * 4);
      const int t0 = 128 * pn + 64 * (cb - 14) + 16 * c;
      v4u gv[8], xv[2][8];
#pragma unroll
      for (int z = 0; z < 8; ++z) gv[z] = __builtin_amdgcn_raw_buffer_load_b128(rg, (lane & 7) * 16, z * 128, 0);
#pragma unroll
      for (int gi = 0; gi < 2; ++gi)
#pragma unroll
        for (int z = 0; z < 8; ++z) xv[gi][z] = __builtin_amdgcn_raw_buffer_load_b128(rx, (lane >> 3) * 1024 + (lane & 7) * 16, (t0 + gi * 8) * 1024 + z * 128, 0);
#pragma unroll
      for (int gi = 0; gi < 2; ++gi) {
        float s = 0.f;
#pragma unroll
        for (int z = 0; z < 8; ++z)
#pragma unroll
          for (int y = 0; y < 4; ++y) {
            const unsigned xa_ = xv[gi][z][y], ga = gv[z][y];
            s = __builtin_amdgcn_fdot2_f32_bf16(__builtin_bit_cast(bf16x2, xa_), __builtin_bit_cast(bf16x2, ga), s, false);
          }
        s += dpp<0xB1>(s); s += dpp<0x4E>(s); s += dpp<0x141>(s);
        const float g = __builtin_amdgcn_rcpf(1.f + __builtin_amdgcn_exp2f(s * -1.44269504088896341f));
        __builtin_amdgcn_raw_buffer_store_b32(f2u(g), rgo, (lane & 7) ? OOB : (lane >> 3) * 4, (t0 + gi * 8) * 4, AUX_SC1);
      }
    }
    CFENCE();
    SCHED();
    STAMP(5);
    ld_carry();
    CFENCE();
    SCHED();
#pragma unroll
    for (int z = 0; z < 16; ++z) {
      const float t = cr * vr[z] - ci * vi[z];
      vi[z] = -cr * vi[z] - ci * vr[z]; vr[z] = t;
    }
    float qr[4], qi[4];
    qr[0] = ar; qi[0] = ai; qr[1] = ar; qi[1] = ai; csq(qr[1], qi[1], 1);
    cmul(qr[1], qi[1], ar, ai, qr[2], qi[2]); qr[3] = qr[1]; qi[3] = qi[1]; csq(qr[3], qi[3], 1);
    const float p4r = qr[3], p4i = qi[3];
    float p64r = p32r, p64i = p32i; csq(p64r, p64i, 1);
    float p512r = p64r, p512i = p64i; csq(p512r, p512i, 3);
    float p1kr = p512r, p1ki = p512i; csq(p1kr, p1ki, 1);
    float sgr, sgi, wgr, wgi;
    cmul((q & 1) ? p16r : 1.f, (q & 1) ? p16i : 0.f, (q & 2) ? p32r : 1.f, (q & 2) ? p32i : 0.f, sgr, sgi);
    cmul((q & 1) ? p512r : 1.f, (q & 1) ? p512i : 0.f, (q & 2) ? p1kr : 1.f, (q & 2) ? p1ki : 0.f, wgr, wgi);
#pragma unroll
    for (int z = 0; z < 16; ++z) asm volatile("" : "+v"(vr[z]), "+v"(vi[z]));
    asm volatile("" : "+v"(sgr), "+v"(sgi), "+v"(wgr), "+v"(wgi), "+v"(p64r), "+v"(p64i));
    SCHED();
    const int want = imin(imax(ck - 8 * q, 0), 8);
    auto carry_ok = [&]() {
      int good = 0;
#pragma unroll
      for (int z = 0; z < 8; ++z) good += ((cv[z][1] ^ tag) | (cv[z][3] ^ ~tag)) == 0u;
      return wall(good == want);
    };
    wait_vm<0>();
    publish(0);
    STAMP(14);
    if (__builtin_expect(!carry_ok(), 0)) {
#pragma nounroll
      for (unsigned spins = 0; spins < SPIN_CAP; ++spins) {
        __builtin_amdgcn_s_sleep(2);
        CFENCE();
        ld_carry();
        if (carry_ok()) break;
      }
    }
    STAMP(6);
    float Gr, Gi;
    {
      float Hr = 0.f, Hi = 0.f, tr, ti;
#pragma unroll
      for (int z = 7; z >= 0; --z) { cmul(Hr, Hi, p64r, p64i, tr, ti); Hr = tr + u2f(cv[z][0]); Hi = ti + u2f(cv[z][2]); }
      cmul(Hr, Hi, wgr, wgi, tr, ti);
      { const auto a = __builtin_amdgcn_permlane16_swap(f2u(tr), f2u(tr), false, false);
        const auto c2 = __builtin_amdgcn_permlane16_swap(f2u(ti), f2u(ti), false, false);
        tr = u2f(a[0]) + u2f(a[1]); ti = u2f(c2[0]) + u2f(c2[1]); }
      { const auto a = __builtin_amdgcn_permlane32_swap(f2u(tr), f2u(tr), false, false);
        const auto c2 = __builtin_amdgcn_permlane32_swap(f2u(ti), f2u(ti), false, false);
        tr = u2f(a[0]) + u2f(a[1]); ti = u2f(c2[0]) + u2f(c2[1]); }
      cmul(tr, ti, sgr, sgi, Hr, Hi);
      cmul(Hr + pr, Hi + pi, cr, ci, Gr, Gi);
    }
    {
      LAS unsigned char* T = L + B_BASE + (c >> 1) * (2 * B_SLOT) + (c & 1) * 4096;
      const int s32 = ((i15 >> 1) & 1) | ((i15 >> 2) & 2), s8 = (i15 & 1) | ((i15 >> 1) & 2);
#pragma unroll
      for (int t = 0; t < 4; ++t) {
        float orr[4], oi[4];
#pragma unroll
        for (int z = 0; z < 4; ++z) {
          orr[z] = __builtin_fmaf(-qi[z], Gi, __builtin_fmaf(qr[z], Gr, vr[4 * t + z]));
          oi[z] = __builtin_fmaf(-qi[z], Gr, __builtin_fmaf(-qr[z], Gi, vi[4 * t + z]));
        }
        if (t < 3) { float tr, ti; cmul(Gr, Gi, p4r, p4i, tr, ti); Gr = tr; Gi = ti; }
        LAS unsigned char* a = T + i15 * 128 + ((t ^ s32) << 5) + ((q ^ s8) << 3);
        *(LAS v2u*)a = v2u{pk_bf16(orr[0], orr[1]), pk_bf16(orr[2], orr[3])};
        *(LAS v2u*)(a + 2048) = v2u{pk_bf16(oi[0], oi[1]), pk_bf16(oi[2], oi[3])};
      }
      const int qq = i15 >> 2, pp = lane & 3;
      const int so = (ck * NK + 56 + 4 * (cb >> 1) + (cb & 1)) * 4096 + (c >> 1) * 512;
#pragma unroll
      for (int t = 0; t < 4; ++t) {
        v2u part[2];
#pragma unroll
        for (int hh = 0; hh < 2; ++hh)
          part[hh] = lds_rdtr(T + (8 * q + 4 * hh + qq) * 128 + ((t ^ ((qq >> 1) | ((q & 1) << 1))) << 5) + ((pp ^ ((qq & 1) | (hh << 1))) << 3));
        st_sc1(v4u{part[0][0], part[0][1], part[1][0], part[1][1]}, ra2, (lane >> 5) * 8192 + (lane & 31) * 16, so + t * 1024);
      }
    }
    SCHED();
    STAMP(7);
    if (!light) {
      lds_wait(L + LDS_PF + 4 * w);
      b2_read(true, L + w * 16384, lane, b2[2]);
      b2_read(true, L + w * 16384 + 8192, lane, b2[3]);
    }
    wait_vm<0>();
    publish(1);
    __builtin_amdgcn_s_setprio(0);
    STAMP(12);
  }
  kp = kargs();
  pc = p2_ctx(pWd, pCre, pCim, w, N);
  const int d0 = 2 * pc.sh;
  if (light) {
#pragma unroll
    for (int d = 0; d < 4; d += 2)
      if (d == 2 || w >= 4) {
        WAIT_LDS();
        b2_load<0, 0, 8>(pc, vb, d - d0, w * 16384);
        b2_load<8192, 0, 8>(pc, vb, d + 1 - d0, w * 16384);
        wait_vm<0>();
        b2_read(w < 4, L + w * 16384, lane, b2[d]);
        b2_read(w < 4, L + w * 16384 + 8192, lane, b2[d + 1]);
      }
  }
  if (w < 4 || light) {
    WAIT_LDS();
    b2_load<0, 0, 8>(pc, vb, 4 - d0, w * 16384);
    b2_load<8192, 0, 8>(pc, vb, 5 - d0, w * 16384);
    STAMP(13);
  }

  {
    lds_wait4(L + LDS_DONE);
    const int cl = lane & 15, lo = pc.sh ? (pc.g0 - 28) & ~1 : pc.g0 >> 1, hi = pc.sh ? (pc.g0 + pc.nd - 29) | 1 : (pc.g0 + pc.nd - 1) >> 1;
    poll_flags((GAS u64*)(kp->ws + WS_FA2) + ((pc.sh * 16 + (R & 1) * 8 + N) * 16 + (R >> 1)) * 16 + cl, tag64(tag), (cl >= lo && cl <= hi) || cl >= 14, cl < 14);
  }
  STAMP(8);
  rsrc_t ra = mk_rsrc(kp->ws + WS_A2 + (R * NK + 2 * pc.g0) * 4096, pc.nd * 8192);
  const int va = ((lane >> 4) & 1) * 1024 + (lane >> 5) * 256 + (lane & 15) * 16;
  v4u af[8];
  auto ld_a = [&](int n, int o) { af[n] = ld_sc1(ra, va + (n & 1) * 2048 + ((n >> 1) & 1) * 512, o + (n >> 2) * 4096); };
  {
    const int o = p2_ds(pc, 0) * 8192;
#pragma unroll
    for (int n = 0; n < 8; ++n) ld_a(n, o);
  }
  SCHED();
  f32x16 acc[2][2];
#pragma unroll
  for (int e = 0; e < 2; ++e)
#pragma unroll
    for (int tt = 0; tt < 2; ++tt) { acc[e][tt] = f32x16{}; asm volatile("" : "+v"(acc[e][tt])); }
  asm volatile("s_nop 1");
#pragma unroll
  for (int d = 0; d < 7; ++d) {
    if ((d >= 2 && d < 6) || w >= 4) {
      if (d == 3 && w >= 4) { WAIT_LDS(); b2_load<0, 0, 8>(pc, vb, 6, w * 16384); }
      const int o = d + 1 - d0 < pc.nd ? p2_ds(pc, d + 1 - d0) * 8192 : OOB;
#pragma unroll
      for (int n = 0; n < 8; ++n) {
        mfma32t(acc[0][n & 1], b2[d & 3][n >> 2][n & 2], af[n]);
        mfma32t(acc[1][n & 1], b2[d & 3][n >> 2][(n & 2) + 1], af[n]);
        SCHED();
        if (d < 6) ld_a(n, o);
        SCHED();
      }
      if (d == 2 || d == 3) b2_read(w < 4, L + w * 16384 + (d & 1) * 8192, lane, b2[d & 1]);
      if (d == 4) b2_read(false, L + w * 16384, lane, b2[2]);
      SCHED();
    }
  }
  asm volatile("s_nop 15\n\ts_nop 7" : "+v"(acc[0][0]), "+v"(acc[1][0]), "+v"(acc[0][1]), "+v"(acc[1][1]));
  SCHED();
  STAMP(9);
  kp = kargs();
  const int tok = 64 * R + 32 * (w & 1) + 4 * (w >> 1) + (lane >> 4);
  rsrc_t rx = mk_rsrc(kp->x, S_ * D_ * 2), rdk = mk_rsrc(kp->Dsk, D_ * 2), rgi = mk_rsrc(kp->ws + WS_G, S_ * 4);
  const v2u xv0 = __builtin_amdgcn_raw_buffer_load_b64(rx, tok * 1024 + (lane & 15) * 8, N * 128, 0);
  const v2u xv1 = __builtin_amdgcn_raw_buffer_load_b64(rx, tok * 1024 + (lane & 15) * 8, N * 128 + 16384, 0);
  const v2u dv = __builtin_amdgcn_raw_buffer_load_b64(rdk, (lane & 15) * 8, N * 128, 0);
  const float g0 = u2f(__builtin_amdgcn_raw_buffer_load_b32(rgi, tok * 4, 0, AUX_SC1));
  const float g1 = u2f(__builtin_amdgcn_raw_buffer_load_b32(rgi, tok * 4, 64, AUX_SC1));
  {
    const int t3 = lane & 3, g3 = ((t3 & 1) * 3) ^ (t3 >> 1), hi = lane >> 5;
#pragma unroll
    for (int T = 0; T < 2; ++T)
#pragma unroll
      for (int E = 0; E < 2; ++E)
#pragma unroll
        for (int g = 0; g < 4; ++g) {
          const int e = 2 * E + (g >> 1), q = 2 * (g & 1) + hi;
          *(LAS f32x4*)(L + w * 16384 + ((2 * T + ((lane >> 4) & 1)) * 4 + e) * 1024 + ((16 * q + (((((lane >> 2) & 3) ^ q) << 2) | (e ^ g3))) << 4)) =
              f32x4{acc[E][T][4 * g], acc[E][T][4 * g + 1], acc[E][T][4 * g + 2], acc[E][T][4 * g + 3]};
        }
  }
  if (b == 255 && w == 0) {
    rsrc_t rf = mk_rsrc(kp->ws + WS_FA2 + 32768, 256 * 8);
#pragma nounroll
    for (unsigned spins = 0; spins < SPIN_CAP; ++spins) {
      CFENCE();
      const v4u f0 = ld_sc1(rf, lane * 32, 0), f1 = ld_sc1(rf, lane * 32, 16);
      if (wall(((f0[0] ^ tag) | (f0[1] ^ ~tag) | (f0[2] ^ tag) | (f0[3] ^ ~tag) | (f1[0] ^ tag) | (f1[1] ^ ~tag) | (f1[2] ^ tag) | (f1[3] ^ ~tag)) == 0u)) break;
      __builtin_amdgcn_s_sleep(8);
    }
    st_flag(kp->ws, WS_EPOCH, tag, lane == 0 ? 0 : OOB);
  }
  WAIT_LDS();
  BARRIER();
  STAMP(10);
  {
    const int q = lane & 3, e = (lane >> 2) & 3, t3 = lane >> 4;
    const int base = ((w >> 1) * 4 + e) * 1024 + q * 256 + ((e ^ ((t3 & 1) * 3) ^ (t3 >> 1)) << 4);
    rsrc_t ro = mk_rsrc(kp->out, S_ * D_ * 4);
    f32x4 res[2];
#pragma unroll
    for (int hf = 0; hf < 2; ++hf) {
      f32x4 sg = {0.f, 0.f, 0.f, 0.f}, ss = {0.f, 0.f, 0.f, 0.f};
      const int a = base + (((2 * (w & 1) + hf) ^ q) << 6);
#pragma unroll
      for (int ww = 0; ww < 8; ++ww) {
        const f32x4 v = *(LAS f32x4*)(L + ww * 16384 + a);
        if (ww < 4) ss += v; else sg += v;
      }
      const float g = hf ? g1 : g0;
      f32x4 m = ss * (2.f * g) + sg * (1.f - g);
      const v2u xv = hf ? xv1 : xv0;
      m[0] += g * (bf2f(xv[0] & 0xFFFFu) * bf2f(dv[0] & 0xFFFFu)); m[1] += g * (bfhi(xv[0]) * bfhi(dv[0]));
      m[2] += g * (bf2f(xv[1] & 0xFFFFu) * bf2f(dv[1] & 0xFFFFu)); m[3] += g * (bfhi(xv[1]) * bfhi(dv[1]));
      res[hf] = m;
    }
    asm volatile("" : "+v"(res[0]), "+v"(res[1]));
#pragma unroll
    for (int hf = 0; hf < 2; ++hf)
      __builtin_amdgcn_raw_buffer_store_b128(__builtin_bit_cast(v4u, res[hf]), ro, tok * 2048 + (lane & 15) * 16 + hf * 32768 + N * 256, 0, OUT_AUX);
  }
  STAMP(11);
  if (b == PB && w == PW) { const u64 tend = stamp_[PROBE] + 6000 + (EXTRA); for (int z = 0; z < 1000000; ++z) { if (__builtin_amdgcn_s_memrealtime() >= tend) break; __builtin_amdgcn_s_sleep(1); } }
}

#if defined(__HIP_DEVICE_COMPILE__)
#pragma clang attribute pop
#endif

extern "C" void kernel_launch(void* const* d_in, const int* in_sizes, int n_in, void* d_out, int out_size, void* d_ws, size_t ws_size,
                              hipStream_t stream) {
  Params p;
  p.x = (const u16*)d_in[0]; p.Lre = (const u16*)d_in[1]; p.Lim = (const u16*)d_in[2]; p.Bre = (const u16*)d_in[3];
  p.Bim = (const u16*)d_in[4]; p.Cre = (const u16*)d_in[5]; p.Cim = (const u16*)d_in[6]; p.ldt = (const u16*)d_in[7];
  p.Dsk = (const u16*)d_in[8]; p.Wg = (const u16*)d_in[10]; p.Wu = (const u16*)d_in[11]; p.Wd = (const u16*)d_in[12];
  p.gw = (const u16*)d_in[13]; p.out = (float*)d_out; p.ws = (unsigned char*)d_ws;
  (void)hipFuncSetAttribute((const void*)k_fused, hipFuncAttributeMaxDynamicSharedMemorySize, LDS_BYTES);
  k_fused<<<256, 512, LDS_BYTES, stream>>>(p);
}
```

```cpp
#define PRIO2
#include <hip/hip_runtime.h>
#include <stdint.h>

typedef unsigned short u16;
typedef unsigned long long u64;
typedef unsigned v4u __attribute__((ext_vector_type(4)));
typedef unsigned v2u __attribute__((ext_vector_type(2)));
typedef short s16x4 __attribute__((ext_vector_type(4)));
typedef __bf16 bf16x8 __attribute__((ext_vector_type(8)));
typedef __bf16 bf16x2 __attribute__((ext_vector_type(2)));
typedef float f32x2 __attribute__((ext_vector_type(2)));
typedef float f32x4 __attribute__((ext_vector_type(4)));
typedef float f32x16 __attribute__((ext_vector_type(16)));
#define LAS __attribute__((address_space(3)))
#define GAS __attribute__((address_space(1)))
#define RLX_AGENT __ATOMIC_RELAXED, __HIP_MEMORY_SCOPE_AGENT
typedef __amdgpu_buffer_rsrc_t rsrc_t;

constexpr int S_ = 2048, D_ = 512, P_ = 512, F_ = 1792;
constexpr int NK = 88;
constexpr int AUX_SC1 = 16;
#define OUT_AUX 2
constexpr int WS_EPOCH = 0;
constexpr int WS_G = 8192;
constexpr int WS_FA2 = 65536;
constexpr int CTRL_BYTES = 131072;
constexpr int WS_CARRY = 131072;
constexpr int WS_A2 = 524288;
constexpr int A2_BYTES = 32 * NK * 4096;
constexpr int WS_STAMP = 32 << 20;
constexpr int LDS_BYTES = 160 * 1024;
constexpr int X_SLOT = 16384, W_BASE = 65536, W_SLOT = 16384, B_BASE = 131072, B_SLOT = 8192;
constexpr int LDS_DONE = 155648, LDS_PF = LDS_DONE + 32, LDS_MD = LDS_DONE + 48, LDS_TAG = LDS_DONE + 64;
constexpr int LDS_DUMMY = LDS_DONE + 256, LDS_CONST = LDS_DONE + 512;
constexpr int OOB = 1 << 28;
constexpr int CODE_BYTES = 30 * 1024;
constexpr unsigned SPIN_CAP = 1u << 18;

struct Params {
  const u16 *x, *Wg, *Wu, *Bre, *Bim, *Cre, *Cim, *Wd;
  unsigned char* ws;
  const u16 *Lre, *Lim, *ldt, *Dsk, *gw;
  float* out;
};

#if defined(__HIP_DEVICE_COMPILE__)
#pragma clang attribute push(__attribute__((target("no-packed-fp32-ops"))), apply_to = function)
#endif
__device__ __forceinline__ float u2f(unsigned v) { return __builtin_bit_cast(float, v); }
__device__ __forceinline__ unsigned f2u(float v) { return __builtin_bit_cast(unsigned, v); }
__device__ __forceinline__ bool wall(bool p) { return __builtin_amdgcn_ballot_w64(!p) == 0; }
__device__ __forceinline__ bool wany(bool p) { return __builtin_amdgcn_ballot_w64(p) != 0; }
__device__ __forceinline__ int imin(int a, int b) { return a < b ? a : b; }
__device__ __forceinline__ int imax(int a, int b) { return a > b ? a : b; }
template <int CTRL> __device__ __forceinline__ float dpp(float v) { return u2f(__builtin_amdgcn_mov_dpp(f2u(v), CTRL, 0xF, 0xF, true)); }
__device__ __forceinline__ rsrc_t mk_rsrc(const void* p, int bytes) {
  return __builtin_amdgcn_make_buffer_rsrc((void*)p, 0, bytes, 0x00020000);
}
__device__ __forceinline__ float bf2f(unsigned v) { return u2f(v << 16); }
__device__ __forceinline__ float bfhi(unsigned v) { return u2f(v & 0xFFFF0000u); }
__device__ __forceinline__ unsigned pk_bf16(float lo, float hi) {
  f32x2 t = {lo, hi};
  return __builtin_bit_cast(unsigned, __builtin_convertvector(t, bf16x2));
}
__device__ __forceinline__ void mfma16t(f32x4& c, v4u a, v4u b) {
  asm volatile("v_mfma_f32_16x16x32_bf16 %0, %1, %2, %0" : "+v"(c) : "v"(a), "v"(b));
}
__device__ __forceinline__ void mfma32t(f32x16& c, v4u a, v4u b) {
  asm volatile("v_mfma_f32_32x32x16_bf16 %0, %1, %2, %0" : "+v"(c) : "v"(a), "v"(b));
}
__device__ __forceinline__ v4u lds_rd128(LAS unsigned char* p) { return *(LAS v4u*)p; }
__device__ __forceinline__ v2u lds_rdtr(LAS unsigned char* p) {
  return __builtin_bit_cast(v2u, __builtin_amdgcn_ds_read_tr16_b64_v4i16((LAS s16x4*)p));
}
__device__ __forceinline__ v4u mk_rs(const void* p, unsigned bytes) {
  const u64 a = (u64)p;
  unsigned lo = (unsigned)a, hi = (unsigned)(a >> 32) & 0xFFFFu;
  asm volatile("" : "+s"(lo), "+s"(hi));
  return v4u{lo, hi, bytes, 0x00020000u};
}
template <int LI, int SI> __device__ __forceinline__ void glds(v4u rs, int lds, int voff, int soff) {
  int t;
  asm volatile("s_add_u32 m0, %1, %5\n\ts_add_u32 %0, %4, %6\n\tbuffer_load_dwordx4 %2, %3, %0 offen lds"
               : "=&s"(t) : "s"(lds), "v"(voff), "s"(rs), "s"(soff), "i"(LI), "i"(SI) : "memory", "scc");
}
typedef const __attribute__((address_space(4))) Params* KP;
__device__ __forceinline__ KP kargs() {
  KP k = (KP)__builtin_amdgcn_kernarg_segment_ptr();
  asm volatile("" : "+s"(k));
  return k;
}
__device__ __forceinline__ void warm(v4u rs, int voff, int soff) {
  asm volatile("s_mov_b32 m0, %3\n\ts_nop 0\n\tbuffer_load_dword %0, %1, %2 offen lds" : : "v"(voff), "s"(rs), "s"(soff), "i"(LDS_DUMMY) : "memory");
}
__device__ __forceinline__ v4u ld_sc1(rsrc_t rs, int voff, int soff) { return __builtin_amdgcn_raw_buffer_load_b128(rs, voff, soff, AUX_SC1); }
__device__ __forceinline__ void st_sc1(v4u v, rsrc_t rs, int voff, int soff) { __builtin_amdgcn_raw_buffer_store_b128(v, rs, voff + soff, 0, AUX_SC1); }
__device__ __forceinline__ u64 tag64(unsigned tag) { return ((u64)(~tag) << 32) | tag; }
__device__ __forceinline__ void st_flag(unsigned char* ws, int off, unsigned tag, int voff) {
  __builtin_amdgcn_raw_buffer_store_b64(v2u{tag, ~tag}, mk_rsrc(ws, CTRL_BYTES), voff, off, AUX_SC1);
}
__device__ __forceinline__ unsigned lds_word(LAS unsigned char* p) { return __builtin_amdgcn_readfirstlane(*(volatile LAS unsigned*)p); }
#define BARRIER() asm volatile("s_barrier" ::: "memory")
#define SCHED() __builtin_amdgcn_sched_barrier(0)
#define CFENCE() asm volatile("" ::: "memory")
#define WAIT_LDS() asm volatile("s_waitcnt lgkmcnt(0)" ::: "memory")
template <int N> __device__ __forceinline__ void wait_vm() { asm volatile("s_waitcnt vmcnt(%c0)" ::"i"(N) : "memory"); }

#define STAMP(i) do {} while (0)
#define POLL_SLEEP 4
#define POLL_IDLE 16

__device__ __forceinline__ void poll_flags(GAS u64* f, u64 want, bool care, bool late) {
#pragma nounroll
  for (unsigned spins = 0; spins < SPIN_CAP; ++spins) {
    CFENCE();
    const u64 v = __hip_atomic_load(f, RLX_AGENT);
    if (wall(!care || v == want)) break;
    if (wany(late && v == want)) __builtin_amdgcn_s_sleep(POLL_SLEEP); else __builtin_amdgcn_s_sleep(POLL_IDLE);
  }
  CFENCE();
}
__device__ __forceinline__ void csq(float& r, float& i, int n) {
#pragma unroll
  for (int z = 0; z < n; ++z) { const float t = r * r - i * i; i = 2.f * r * i; r = t; }
}

#define NROT 100
#define WARM0 3
#define CONST_AT 2
constexpr int LASTB = 13;
struct Sched {
  int n;
  int type[40], idx[40];
  int lo[LASTB + 1], hi[LASTB + 1];
  int wait[LASTB + 1];
  bool ok;
};
constexpr Sched make_sched(int kind) {
  Sched s{};
  int cnt[40] = {}, need[40] = {}, avail[40] = {};
  int n = 0;
  auto add = [&](int ty, int ix, int nd, int av) {
    if (kind && ty == 2) return;
    s.type[n] = ty; s.idx[n] = ix; cnt[n] = ty == 2 ? 4 : 3; need[n] = nd < LASTB ? nd : LASTB; avail[n] = av; ++n;
  };
  add(0, 0, 0, -1); add(1, 0, 0, -1); add(2, 0, 0, -1); add(2, 1, 0, -1);
  add(0, 1, 1, -1); add(1, 1, 1, -1); add(2, 2, 1, -1); add(2, 3, 2, -1);
  add(0, 2, 3, -1); add(1, 2, 3, -1);
  add(2, 4, 3, 1); add(2, 5, 4, 1);
  for (int b = 2; b < 12; ++b) {
    const int T = b / 2 + 2;
    add(b & 1, T, 2 * T - 1, kind ? (b & ~1) : b);
    add(2, b + 4, b + 3, b);
  }
  s.n = n;
  s.ok = true;
  const int LIM = 8;
  int endpos[40] = {};
  int next = 0, total = 0;
  for (int b = 0; b <= LASTB; ++b) {
    s.lo[b] = next;
    const int start = total;
    for (;;) {
      if (next >= n || avail[next] > b - 1) break;
      int lastneed = 0;
      for (int i = 0; i < next; ++i) if (need[i] <= b) lastneed = endpos[i];
      if (b < 3 && need[next] > b && total + cnt[next] - lastneed > LIM + 2 * b) break;
      if (b >= 3 && need[next] > b + 1 && total + cnt[next] - start > 11) break;
      total += cnt[next]; endpos[next] = total; ++next;
    }
    s.hi[b] = next;
    if (kind == 0 && b >= WARM0 && b < WARM0 + 11) total += 1;
    int lastneed = 0;
    for (int i = 0; i < next; ++i) if (need[i] <= b) lastneed = endpos[i];
    for (int i = next; i < n; ++i) if (need[i] <= b) s.ok = false;
    s.wait[b] = total - lastneed;
  }
  if (next != n || WARM0 + 10 > LASTB) s.ok = false;
  return s;
}
constexpr Sched SCH[2] = {make_sched(0), make_sched(1)};
static_assert(SCH[0].ok && SCH[1].ok, "every ring item must be issued in time");

struct P2Ctx { u64 a0, a1; int so0, sh, g0, nd, rot; unsigned bytes; };
__device__ __forceinline__ P2Ctx p2_ctx(u64 Wd, u64 Cre, u64 Cim, int w, int N) {
  P2Ctx c;
  const int ssm = w < 4;
  const u64 m = 0 - (u64)ssm;
  c.a0 = (Cre & m) | (Wd & ~m); c.a1 = (Cim & m) | (Wd & ~m);
  c.sh = ssm;
  c.so0 = N * 128 + ssm * (N * (65536 - 128) - 14 * 128);
  c.bytes = F_ * D_ * 2u - ssm * (F_ * D_ * 2u - D_ * P_ * 2u);
  c.g0 = 7 * w - 28 + ssm * (56 - 3 * w);
  c.nd = 7 - 3 * ssm;
#define P2ROT 1
  c.rot = P2ROT == 0 ? 0 : (P2ROT == 1 ? (N & 1) * (3 - ssm) : (P2ROT == 2 ? (N & 3) * (2 - ssm) : (N * c.nd) >> 3));
  return c;
}
__device__ __forceinline__ int p2_ds(const P2Ctx& c, int d) { const int t = d + c.rot; return t >= c.nd ? t - c.nd : t; }
__device__ __forceinline__ void b2_src(const P2Ctx& c, int d, v4u& rs, int& so) {
  const int g = c.g0 + p2_ds(c, d);
  const u64 x0 = c.a0, x1 = c.a1;
  rs = mk_rs((const void*)((g & 1) ? x1 : x0), c.bytes);
  so = c.so0 + (c.sh ? (g >> 1) * 128 : g * 65536);
}
template <int LI, int N0, int N1> __device__ __forceinline__ void b2_rows(v4u rs, int lds, int vb, int so) {
  if constexpr (N0 < N1) { glds<LI, N0 * 8192>(rs, lds, (N0 & 1) ? vb ^ 16 : vb, so); b2_rows<LI + 1024, N0 + 1, N1>(rs, lds, vb, so); }
}
template <int LI, int N0, int N1> __device__ __forceinline__ void b2_load(const P2Ctx& c, int vb, int d, int lds) {
  v4u rs; int so;
  b2_src(c, d, rs, so);
  b2_rows<LI, N0, N1>(rs, lds, vb, so);
}
__device__ __forceinline__ void b2_read(bool ssm, LAS unsigned char* img, int lane, v4u (&f)[2][4]) {
  if (ssm) {
    const int i = lane & 31, sw = ((i & 2) << 1) | ((i >> 1) & 2) | ((i >> 3) & 1);
#pragma unroll
    for (int h = 0; h < 2; ++h)
#pragma unroll
      for (int z = 0; z < 4; ++z) f[h][z] = lds_rd128(img + i * 128 + (((4 * h + (z & 2) + (lane >> 5)) ^ sw) << 4) + (z & 1) * 4096);
  } else {
    const int qq = (lane >> 2) & 3, hi = lane >> 5;
    const int c0 = (((lane >> 4) & 1) * 2 + ((lane >> 1) & 1)) ^ (((qq & 2) << 1) | (hi << 1));
#pragma unroll
    for (int h = 0; h < 2; ++h)
#pragma unroll
      for (int z = 0; z < 4; ++z) {
        LAS unsigned char* a = img + (4 * hi + qq) * 128 + ((c0 ^ ((z & 1) << 2) ^ (z >> 1)) << 4) + (lane & 1) * 8 + h * 4096 + (z >> 1) * 1024;
        const v2u lo = lds_rdtr(a), hi2 = lds_rdtr(a + 2048);
        f[h][z] = v4u{lo[0], lo[1], hi2[0], hi2[1]};
      }
  }
}

struct LdCtx { v4u rx, rw, rb; u64 Wd, Cre, Cim; int lw, lb, vx, vw0, vw1, vbb, rot, lane, vb, vwarm, xcd, ch; mutable unsigned sv, lrb, lib, ldb, ep; };
__device__ __forceinline__ void warm_chunk(u64 x0, u64 x1, u64 x2, int xc, int vwarm, int slot) {
  asm volatile("" : "+s"(xc));
  int i = slot < 6 ? slot + xc : slot - 6 + xc;
  if (slot < 6) { i = i >= 12 ? i - 12 : (i >= 6 ? i - 6 : i); } else { i = 6 + (i >= 10 ? i - 10 : (i >= 5 ? i - 5 : i)); }
  const u64 src = i < 7 ? x0 : (i < 9 ? x1 : x2);
  const unsigned bytes = (unsigned)slot > 10u ? 0u : (i < 7 ? F_ * D_ * 2 : D_ * P_ * 2);
  warm(mk_rs((const void*)src, bytes), vwarm, (i < 7 ? i : ((i - 7) & 1)) * 262144);
}

template <int KIND, int I> __device__ __forceinline__ void ld_item(const LdCtx& c) {
  constexpr int ty = SCH[KIND].type[I], t = SCH[KIND].idx[I];
  int rot = c.rot;
  asm volatile("" : "+s"(rot));
  if constexpr (ty < 2) {
    const int k0 = ((t + rot) & 7) * 128;
    glds<(t % 3) * X_SLOT + 2 * ty * 4096, ty * 65536>(c.rx, c.lw * 1024, c.vx, k0);
    glds<(t % 3) * X_SLOT + (2 * ty + 1) * 4096, ty * 65536 + 8192>(c.rx, c.lw * 1024, c.vx, k0);
    glds<B_BASE + (t % 3) * B_SLOT + ty * 2048, ty * 16384>(c.rb, c.lb, c.vbb, k0);
  } else {
    const int k0 = (((t >> 1) + rot) & 7) * (64 * F_ * 2);
    constexpr int LI = W_BASE + (t & 3) * W_SLOT, SI = (t & 1) * 32 * F_ * 2;
    glds<LI, SI>(c.rw, c.lw * 4096, c.vw0, k0);
    glds<LI + 1024, SI + 4 * F_ * 2>(c.rw, c.lw * 4096, c.vw0, k0);
    glds<LI + 2048, SI + 8 * F_ * 2>(c.rw, c.lw * 4096, c.vw1, k0);
    glds<LI + 3072, SI + 12 * F_ * 2>(c.rw, c.lw * 4096, c.vw1, k0);
  }
}
template <int KIND, int LO, int HI> __device__ __forceinline__ void ld_items(const LdCtx& c) {
  if constexpr (LO < HI) { ld_item<KIND, LO>(c); ld_items<KIND, LO + 1, HI>(c); }
}
template <int KIND, int B> __device__ __forceinline__ void loader_steps(const LdCtx& c, LAS unsigned char* L) {
  if constexpr (B == 0) {
    constexpr int FIRST = KIND ? 2 : 4;
    ld_items<KIND, 0, FIRST>(c);
    KP kp = kargs();
    const v4u r0 = mk_rs(kp->Lre, P_ * 2), r1 = mk_rs(kp->Lim, P_ * 2), r2 = mk_rs(kp->ldt, P_ * 2), r3 = mk_rs(kp->ws + WS_EPOCH, c.lw == 3 ? 4 : 0);
    asm volatile("buffer_load_ushort %0, %4, %5, 0 offen\n\tbuffer_load_ushort %1, %4, %6, 0 offen\n\tbuffer_load_ushort %2, %4, %7, 0 offen\n\t"
                 "buffer_load_dword %3, off, %8, 0 sc1"
                 : "=&v"(c.lrb), "=&v"(c.lib), "=&v"(c.ldb), "=&v"(c.ep) : "v"(c.ch * 2), "s"(r0), "s"(r1), "s"(r2), "s"(r3) : "memory");
    ld_items<KIND, FIRST, SCH[KIND].hi[0]>(c);
  } else {
    ld_items<KIND, SCH[KIND].lo[B], SCH[KIND].hi[B]>(c);
  }
  if constexpr (KIND == 0 && B >= WARM0 && B < WARM0 + 11) warm_chunk(c.Wd, c.Cre, c.Cim, c.xcd, c.vwarm, B - WARM0);
  if constexpr (B == CONST_AT) {
    asm volatile("" : "+v"(c.lrb), "+v"(c.lib), "+v"(c.ldb), "+v"(c.ep) : : "memory");
    const float lr = bf2f(c.lrb), li = bf2f(c.lib);
    float dt = __builtin_amdgcn_exp2f(bf2f(c.ldb) * 1.44269504088896341f);
    dt = bf2f(pk_bf16(dt, 0.f) & 0xFFFFu);
    const float rev = __builtin_amdgcn_fractf(dt * li * 0.15915494309189535f);
    const float ex = __builtin_amdgcn_exp2f(dt * lr * 1.44269504088896341f);
    const float ar = ex * __builtin_amdgcn_cosf(rev), ai = ex * __builtin_amdgcn_sinf(rev);
    const float den = __builtin_amdgcn_rcpf(lr * lr + li * li);
    float p16r = ar, p16i = ai;
    csq(p16r, p16i, 4);
    float p32r = p16r, p32i = p16i;
    csq(p32r, p32i, 1);
    LAS f32x4* d = (LAS f32x4*)(L + LDS_CONST + c.lw * 512 + (c.lane & 15) * 32);
    d[0] = f32x4{ar, ai, ((ar - 1.f) * lr + ai * li) * den, (ai * lr - (ar - 1.f) * li) * den};
    d[1] = f32x4{p16r, p16i, p32r, p32i};
    *(volatile LAS unsigned*)(L + LDS_TAG + 4 * (3 - c.lw)) = c.ep + 1u;
    WAIT_LDS();
  }
  SCHED();
  wait_vm<SCH[KIND].wait[B]>();
  BARRIER();
  if constexpr (B < LASTB) loader_steps<KIND, B + 1>(c, L);
}

__device__ __forceinline__ bool all4(LAS unsigned char* p) {
  const v4u d = *(volatile LAS v4u*)p;
  return __builtin_amdgcn_readfirstlane(d[0] & d[1] & d[2] & d[3]) != 0u;
}
__device__ __forceinline__ void lds_wait4(LAS unsigned char* p) {
#pragma nounroll
  for (unsigned spins = 0; spins < (SPIN_CAP << 4); ++spins) {
    if (all4(p)) break;
    __builtin_amdgcn_s_sleep(2);
  }
  CFENCE();
}
__device__ __forceinline__ void lds_wait(LAS unsigned char* word) {
#pragma nounroll
  for (unsigned spins = 0; spins < (SPIN_CAP << 4); ++spins) {
    if (lds_word(word) != 0u) break;
    __builtin_amdgcn_s_sleep(2);
  }
  CFENCE();
}
__device__ __forceinline__ void cmul(float ar, float ai, float br, float bi, float& cr, float& ci) {
  cr = ar * br - ai * bi; ci = ar * bi + ai * br;
}

__global__ __launch_bounds__(512, 2) void k_fused(Params) {
  extern __shared__ __attribute__((aligned(16))) unsigned char lds_[];
  LAS unsigned char* L = (LAS unsigned char*)lds_;
  STAMP(0);
  KP kp = kargs();
  u64 pX = (u64)kp->x, pWg = (u64)kp->Wg, pWu = (u64)kp->Wu, pBre = (u64)kp->Bre, pBim = (u64)kp->Bim, pCre = (u64)kp->Cre, pCim = (u64)kp->Cim, pWd = (u64)kp->Wd;
  const int b = __builtin_amdgcn_workgroup_id_x(), tid = __builtin_amdgcn_workitem_id_x(), lane = tid & 63;
  const int w = __builtin_amdgcn_readfirstlane(tid >> 6);
  const int xcd = b & 7, r = b >> 3;
  warm(mk_rs((const void*)(__builtin_amdgcn_s_getpc() & ~(u64)127), CODE_BYTES), lane < 2 ? lane * 32768 : OOB, (r * 8 + w) * 128);
  const bool light = (r & 7) == 7;
  const int ih = 28 * (xcd & 3) + r - (r >> 3), il = 112 + 4 * (xcd & 3) + (r >> 3);
  const int idx = ih + (il - ih) * (int)light;
  const int pn = 8 * (xcd >> 2) + (idx & 7), cb = idx >> 3;
  const int rot = pn & 7;
  const int R = xcd * 4 + (r & 3), N = r >> 2;
  asm volatile("" : "+s"(pX), "+s"(pWg), "+s"(pWu), "+s"(pBre), "+s"(pBim), "+s"(pCre), "+s"(pCim), "+s"(pWd));
  const int vb = (lane >> 3) * 1024 + (((lane & 7) ^ (((lane >> 2) & 4) | ((lane >> 4) & 2))) << 4);
  v4u b2[4][2][4];
  unsigned tag = 0;
  P2Ctx pc;

  if (__builtin_expect(w >= 4, 1)) {
    LdCtx c;
    c.lw = w - 4; c.rot = rot; c.lane = lane; c.vb = vb; c.sv = 0;
    c.Wd = pWd; c.Cre = pCre; c.Cim = pCim; c.vwarm = lane < 19 ? (lane + ((r - (r >> 3)) * 4 + c.lw) * 19) * 128 : OOB; c.xcd = xcd; c.ch = 32 * cb + 16 * (c.lw >> 1) + (lane & 15);
    const int l3 = lane >> 3, kq = lane >> 4;
    c.rx = mk_rs((const void*)pX, S_ * D_ * 2);
    c.vx = (32 * (c.lw & 1) + 16 * (l3 >> 2) + 4 * (c.lw >> 1) + (l3 & 3)) * 1024 + (((lane & 7) ^ ((4 * c.lw + kq) & 7)) << 4) + pn * 128 * 1024;
    c.rb = mk_rs((const void*)((c.lw >> 1) ? pBim : pBre), P_ * D_ * 2);
    c.vbb = (8 * (c.lw & 1) + l3) * 1024 + (((lane & 7) ^ (4 * (c.lw & 1) + kq)) << 4) + cb * 32 * 1024;
    c.lb = (c.lw >> 1) * 4096 + (c.lw & 1) * 1024;
    c.rw = mk_rs((const void*)((c.lw >> 1) ? pWu : pWg), D_ * F_ * 2);
    c.vw0 = ((c.lw & 1) * 16 + kq) * (F_ * 2) + (((lane & 15) ^ (kq << 2)) << 4) + cb * 256;
    c.vw1 = c.vw0 ^ 32;
    STAMP(1);
    if (__builtin_expect(!light, 1)) loader_steps<0, 0>(c, L);
    else { loader_steps<1, 0>(c, L); BARRIER(); }
    STAMP(2);
    {
      pc = p2_ctx(pWd, pCre, pCim, w, N);
      const int wA = c.lw < 2 ? w : c.lw, wB = wA ^ 4;
      b2_load<0, 0, 8>(pc, vb, 0, wA * 16384);
      b2_load<8192, 0, 8>(pc, vb, 1, wA * 16384);
      const P2Ctx pA = p2_ctx(pWd, pCre, pCim, wA, N), pB = p2_ctx(pWd, pCre, pCim, wB, N);
#pragma unroll
      for (int d = 0; d < 4; ++d) {
        wait_vm<8>();
        b2_read(false, L + wA * 16384 + (d & 1) * 8192, lane, b2[d]);
        WAIT_LDS();
        if (d < 2) b2_load<0, 0, 8>(pc, vb, d + 2, wA * 16384 + (d & 1) * 8192);
        else b2_load<0, 0, 8>(pA, vb, (wA & 4) + (d & 1), wA * 16384 + (d & 1) * 8192);
      }
      STAMP(3);
      lds_wait4(L + LDS_MD);
      b2_load<0, 0, 8>(pB, vb, wB & 4, wB * 16384);
      b2_load<8192, 0, 8>(pB, vb, (wB & 4) + 1, wB * 16384);
      wait_vm<0>();
      *(volatile LAS unsigned*)(L + LDS_PF + 4 * c.lw) = 1u;
      STAMP(4);
    }
    tag = lds_word(L + LDS_TAG);
  } else {
    const int c = w, q = lane >> 4, i15 = lane & 15, G = c & 1;
    const int ch = 32 * cb + 16 * (c >> 1) + i15;
    if (w == 3) {
#pragma unroll
      for (int z = 0; z < 4; ++z) *(volatile LAS v4u*)(L + LDS_DONE + 16 * z) = v4u{0u, 0u, 0u, 0u};
      WAIT_LDS();
    }
    f32x4 k0 = {0.f, 0.f, 0.f, 0.f}, k1 = {0.f, 0.f, 0.f, 0.f};
    unsigned tagv = 0;
    auto rd_const = [&]() {
      k0 = *(LAS f32x4*)(L + LDS_CONST + c * 512 + i15 * 32); k1 = *(LAS f32x4*)(L + LDS_CONST + c * 512 + i15 * 32 + 16);
      tagv = *(volatile LAS unsigned*)(L + LDS_TAG);
    };
    u64 pws = (u64)kp->ws;
    asm volatile("" : "+s"(pws));
    if (w == 2) {
      const int j = lane & 15, g1 = (lane >> 4) & 1, g2 = lane >> 5;
      const int off = j * 65536 + g1 * (WS_A2 + R * NK * 4096) + g2 * (WS_A2 + 2 * pn * NK * 4096) + (j >= 8 - 2 * g1 + 3 * g2) * OOB + g1 * g2 * OOB;
      warm(mk_rs(kp->ws, WS_A2 + A2_BYTES), off, 0);
      warm(mk_rs(kp->out, S_ * D_ * 4), lane < 4 ? lane * 32768 + N * 256 : OOB, R * 131072);
      warm(mk_rs(kp->Dsk, D_ * 2), lane < 1 ? 0 : OOB, 0);
    }
    int xa[2];
#pragma unroll
    for (int ks = 0; ks < 2; ++ks) xa[ks] = i15 * 128 + (((4 * ks + q) ^ ((i15 >> 1) & 7)) << 4);
    asm volatile("" : "+v"(xa[0]), "+v"(xa[1]));
    const int bo = B_BASE + (c >> 1) * 2048;
    f32x4 accs[2][4], acc[4][8];
    v4u bfr[2];
    auto zero_accs = [&]() {
#pragma unroll
      for (int m = 0; m < 2; ++m)
#pragma unroll
        for (int t = 0; t < 4; ++t) { accs[m][t] = f32x4{0.f, 0.f, 0.f, 0.f}; asm volatile("" : "+v"(accs[m][t])); }
    };
    float vr[16], vi[16], t0 = 0.f, t1 = 0.f;
    auto scan_half = [&](int n) {
      const int z = n >> 1;
      if (z == 0) { vr[0] = accs[0][0][0]; vi[0] = accs[1][0][0]; }
      else if ((n & 1) == 0) { t0 = __builtin_fmaf(k0[0], vr[z - 1], accs[0][z >> 2][z & 3]); t1 = __builtin_fmaf(k0[0], vi[z - 1], accs[1][z >> 2][z & 3]); }
      else { vr[z] = __builtin_fmaf(-k0[1], vi[z - 1], t0); vi[z] = __builtin_fmaf(k0[1], vr[z - 1], t1); }
    };
    auto sc = [](int v) { asm volatile("" : "+s"(v)); return v; };

    if (!light) {
      int wa[2];
      {
        const int qq = i15 >> 2, pp = lane & 3;
#pragma unroll
        for (int u = 0; u < 2; ++u)
          wa[u] = W_BASE + (8 * q + qq) * 256 + (((2 * (2 * c + u) + (pp >> 1)) ^ ((qq << 2) | ((q & 1) << 1))) << 4) + 8 * (pp & 1);
      }
      asm volatile("" : "+v"(wa[0]), "+v"(wa[1]));
#pragma unroll
      for (int a = 0; a < 4; ++a)
#pragma unroll
        for (int u = 0; u < 8; ++u) { acc[a][u] = f32x4{0.f, 0.f, 0.f, 0.f}; asm volatile("" : "+v"(acc[a][u])); }
      zero_accs();
      v4u xf[4];
      v2u wf[2][8];
      auto rdw = [&](int s, int z) {
        const int m = z >> 2, u = (z >> 1) & 1, t = z & 1;
        wf[s & 1][z] = lds_rdtr(L + wa[u] + (s & 3) * W_SLOT + m * 8192 + t * 1024);
      };
      STAMP(1);
      BARRIER();
      STAMP(2);
#pragma unroll
      for (int z = 0; z < 8; ++z) rdw(0, z);
#pragma unroll
      for (int z = 0; z < 3; ++z) xf[z] = lds_rd128(L + xa[0] + G * 8192 + z * 2048);
#pragma unroll
      for (int m = 0; m < 2; ++m) bfr[m] = lds_rd128(L + xa[0] + bo + m * 4096);
      auto glu4 = [&](int s, int u, const v4u& x, auto&& between) {
#pragma unroll
        for (int a = 0; a < 4; ++a) {
          const v4u wv = {wf[s & 1][2 * a][0], wf[s & 1][2 * a][1], wf[s & 1][2 * a + 1][0], wf[s & 1][2 * a + 1][1]};
          mfma16t(acc[a][u], wv, x);
          SCHED();
          between(a);
          SCHED();
        }
      };
      int xb = 0, bb = 0;
      auto steps = [&](int n, bool first, const int (&xo)[5]) {
#pragma unroll
        for (int s = 0; s < n; ++s)
#pragma unroll
          for (int u = 0; u < 8; ++u) {
            const int it = 8 * s + u, sn = (it + 3) >> 3, un = (it + 3) & 7;
            SCHED();
            if (u == 0) { if (s > 0 || !first) BARRIER(); SCHED(); }
            glu4(s, u, xf[it & 3], [&](int a) {
              if (a == 0) {
                if ((un & 3) == 0 || it == 0) xb = xa[sn & 1] + sc(xo[sn] + ((un >> 2) ^ G) * 8192);
                xf[(it + 3) & 3] = lds_rd128(L + xb + (un & 3) * 2048);
              }
              if ((a == 1 || a == 2) && u < 4) rdw(s + 1, 2 * u + a - 1);
              if (a == 2 && (u == 4 || u == 5)) {
                if (u == 4) bb = xa[(s + 1) & 1] + sc(bo + (xo[s + 1] >> 1));
                bfr[u - 4] = lds_rd128(L + bb + (u - 4) * 4096);
              }
            });
            if (u < 4) { mfma16t(accs[0][u], xf[it & 3], bfr[0]); mfma16t(accs[1][u], xf[it & 3], bfr[1]); }
          }
      };
      int xo0 = 0;
#pragma nounroll
      for (int o = 0; o < 3; ++o) {
        const int xo1 = xo0 == 2 * X_SLOT ? 0 : xo0 + X_SLOT, xo2 = xo1 == 2 * X_SLOT ? 0 : xo1 + X_SLOT;
        const int xo[5] = {xo0, xo0, xo1, xo1, xo2};
        steps(4, o == 0, xo);
        xo0 = xo2;
      }
      {
        const int xo[5] = {0, 0, X_SLOT, X_SLOT, 0};
        steps(2, false, xo);
      }
      v4u bf2[2];
#pragma unroll
      for (int it = 0; it < 16; ++it) {
        const int s = (it >> 2) & 1, u = (it & 3) + 4 * (it >> 3), nx = it + 3;
        SCHED();
        glu4(s, u, xf[it & 3], [&](int a) {
          if (a == 0 && nx < 16) {
            if ((nx & 3) == 0) xb = xa[(nx >> 2) & 1] + sc(X_SLOT + ((nx >> 3) ^ G) * 8192);
            xf[nx & 3] = lds_rd128(L + xb + (nx & 3) * 2048);
          }
          if (a >= 1 && it < 3 && 3 * it + a - 1 < 8) rdw(3, 3 * it + a - 1);
          if (it == 0 && a == 3) { bb = xa[1] + sc(bo + B_SLOT); bf2[0] = lds_rd128(L + bb); bf2[1] = lds_rd128(L + bb + 4096); }
          if (it == 4 && a == 1) rd_const();
          if (it >= 8) scan_half(4 * (it - 8) + a);
        });
        if (it < 4) { mfma16t(accs[0][u], xf[it & 3], bfr[0]); mfma16t(accs[1][u], xf[it & 3], bfr[1]); }
        else if (it < 8) { mfma16t(accs[0][u], xf[it & 3], bf2[0]); mfma16t(accs[1][u], xf[it & 3], bf2[1]); }
      }
      asm volatile("s_nop 15" : "+v"(acc[0][7]), "+v"(acc[1][7]), "+v"(acc[2][7]), "+v"(acc[3][7]), "+v"(acc[0][6]), "+v"(acc[1][6]), "+v"(acc[2][6]), "+v"(acc[3][6]));
    } else {
      {
      kp = kargs();
      rsrc_t rx = mk_rsrc(kp->x, S_ * D_ * 2), rg = mk_rsrc(kp->gw, D_ * 2), rgo = mk_rsrc(kp->ws + WS_G, S_ * 4);
      const int tk0 = 128 * pn + 64 * (cb - 14) + 16 * c;
      v4u gv[8], xv[2][8];
#pragma unroll
      for (int z = 0; z < 8; ++z) gv[z] = __builtin_amdgcn_raw_buffer_load_b128(rg, (lane & 7) * 16, z * 128, 0);
#pragma unroll
      for (int gi = 0; gi < 2; ++gi)
#pragma unroll
        for (int z = 0; z < 8; ++z) xv[gi][z] = __builtin_amdgcn_raw_buffer_load_b128(rx, (lane >> 3) * 1024 + (lane & 7) * 16, (tk0 + gi * 8) * 1024 + z * 128, 0);
#pragma unroll
      for (int gi = 0; gi < 2; ++gi) {
        float s = 0.f;
#pragma unroll
        for (int z = 0; z < 8; ++z)
#pragma unroll
          for (int y = 0; y < 4; ++y) {
            const unsigned xa_ = xv[gi][z][y], ga = gv[z][y];
            s = __builtin_amdgcn_fdot2_f32_bf16(__builtin_bit_cast(bf16x2, xa_), __builtin_bit_cast(bf16x2, ga), s, false);
          }
        s += dpp<0xB1>(s); s += dpp<0x4E>(s); s += dpp<0x141>(s);
        const float g = __builtin_amdgcn_rcpf(1.f + __builtin_amdgcn_exp2f(s * -1.44269504088896341f));
        __builtin_amdgcn_raw_buffer_store_b32(f2u(g), rgo, (lane & 7) ? OOB : (lane >> 3) * 4, (tk0 + gi * 8) * 4, AUX_SC1);
      }
      }
      STAMP(1);
      zero_accs();
      int xo = 0;
#pragma nounroll
      for (int T = 0; T < 8; ++T) {
#pragma unroll
        for (int ks = 0; ks < 2; ++ks) {
          if (T < 7) BARRIER();
          v4u xf[4];
#pragma unroll
          for (int t = 0; t < 4; ++t) xf[t] = lds_rd128(L + (xa[ks] + xo + G * 8192) + t * 2048);
#pragma unroll
          for (int m = 0; m < 2; ++m) bfr[m] = lds_rd128(L + (xa[ks] + bo + (xo >> 1)) + m * 4096);
#pragma unroll
          for (int t = 0; t < 4; ++t) { mfma16t(accs[0][t], xf[t], bfr[0]); mfma16t(accs[1][t], xf[t], bfr[1]); }
          SCHED();
        }
        xo = xo == 2 * X_SLOT ? 0 : xo + X_SLOT;
      }
      asm volatile("s_nop 15" : "+v"(accs[0][0]), "+v"(accs[1][0]), "+v"(accs[0][1]), "+v"(accs[1][1]), "+v"(accs[0][2]), "+v"(accs[1][2]), "+v"(accs[0][3]), "+v"(accs[1][3]));
      rd_const();
      BARRIER();
#pragma unroll
      for (int n = 0; n < 32; ++n) { scan_half(n); SCHED(); }
    }
    *(volatile LAS unsigned*)(L + LDS_MD + 4 * w) = 1u;
    SCHED();
    STAMP(3);
    tag = __builtin_amdgcn_readfirstlane(tagv);
    const float ar = k0[0], ai = k0[1], cr = k0[2], ci = k0[3], p16r = k1[0], p16i = k1[1], p32r = k1[2], p32i = k1[3];
    __builtin_amdgcn_s_setprio(2);
    rsrc_t ra2 = mk_rsrc((const void*)(pws + WS_A2), A2_BYTES), rcar = mk_rsrc((const void*)(pws + WS_CARRY), 32 * P_ * 16);
    const int ck = 2 * pn + G;
    float pr, pi;
    {
      float tr, ti;
      const auto xr = __builtin_amdgcn_permlane16_swap(f2u(vr[15]), f2u(vr[15]), false, false);
      const auto xi = __builtin_amdgcn_permlane16_swap(f2u(vi[15]), f2u(vi[15]), false, false);
      const float e0r = u2f(xr[0]), e1r = u2f(xr[1]), e0i = u2f(xi[0]), e1i = u2f(xi[1]);
      cmul(e0r, e0i, p16r, p16i, tr, ti); tr += e1r; ti += e1i;
      const auto yr = __builtin_amdgcn_permlane32_swap(f2u(tr), f2u(tr), false, false);
      const auto yi = __builtin_amdgcn_permlane32_swap(f2u(ti), f2u(ti), false, false);
      const float f0r = u2f(yr[0]), f1r = u2f(yr[1]), f0i = u2f(yi[0]), f1i = u2f(yi[1]);
      float Er, Ei;
      cmul(f0r, f0i, p32r, p32i, Er, Ei); Er += f1r; Ei += f1i;
      const float br = (q & 2) ? f0r : 0.f, bi = (q & 2) ? f0i : 0.f;
      cmul(br, bi, p16r, p16i, tr, ti);
      pr = (q & 1) ? tr + e0r : br; pi = (q & 1) ? ti + e0i : bi;
      const v4u gq = {f2u(Er), tag, f2u(Ei), ~tag};
      st_sc1(gq, rcar, q ? OOB : ch * 16, (31 - ck) * (P_ * 16));
    }
    asm volatile("" : "+v"(pr), "+v"(pi) : : "memory");
    SCHED();
    STAMP(4);
    v4u cv[8];
    const int cvo = ch * 16 + (32 - ck + 8 * q) * (P_ * 16);
    auto ld_carry = [&]() {
#pragma unroll
      for (int z = 0; z < 8; ++z) cv[z] = ld_sc1(rcar, cvo, z * (P_ * 16));
    };
    auto publish = [&](int part) {
      *(volatile LAS unsigned*)(L + LDS_DONE + 16 * part + 4 * w) = 1u;
      const bool last = all4(L + LDS_DONE + 16 * part);
      st_flag((unsigned char*)pws, WS_FA2 + part * 32768 + pn * 128 + cb * 8, tag, last && lane < 16 ? lane * 2048 : OOB);
      CFENCE();
    };
    if (!light) {
#pragma unroll
      for (int u = 0; u < 8; ++u) {
        float hv[8];
#pragma unroll
        for (int j = 0; j < 8; ++j) hv[j] = acc[j >> 2][u][j & 3] * -1.44269504088896341f;
        SCHED();
#pragma unroll
        for (int j = 0; j < 8; ++j) hv[j] = __builtin_amdgcn_exp2f(hv[j]);
        SCHED();
#pragma unroll
        for (int j = 0; j < 8; ++j) hv[j] += 1.f;
        SCHED();
#pragma unroll
        for (int j = 0; j < 8; ++j) hv[j] = __builtin_amdgcn_rcpf(hv[j]);
        SCHED();
#pragma unroll
        for (int j = 0; j < 8; ++j) hv[j] *= acc[2 + (j >> 2)][u][j & 3];
        SCHED();
        st_sc1(v4u{pk_bf16(hv[0], hv[1]), pk_bf16(hv[2], hv[3]), pk_bf16(hv[4], hv[5]), pk_bf16(hv[6], hv[7])}, ra2, lane * 16,
               ((2 * pn + ((u >> 2) ^ G)) * NK + 4 * cb + c) * 4096 + (u & 3) * 1024);
        if (u + 1 < 8) asm volatile("" : "+v"(acc[0][u + 1]), "+v"(acc[1][u + 1]) : : "memory");
      }
    }
    CFENCE();
    SCHED();
    STAMP(5);
    ld_carry();
    CFENCE();
    SCHED();
#pragma unroll
    for (int z = 0; z < 16; ++z) {
      const float t = cr * vr[z] - ci * vi[z];
      vi[z] = -cr * vi[z] - ci * vr[z]; vr[z] = t;
    }
    float qr[4], qi[4];
    qr[0] = ar; qi[0] = ai; qr[1] = ar; qi[1] = ai; csq(qr[1], qi[1], 1);
    cmul(qr[1], qi[1], ar, ai, qr[2], qi[2]); qr[3] = qr[1]; qi[3] = qi[1]; csq(qr[3], qi[3], 1);
    const float p4r = qr[3], p4i = qi[3];
    float p64r = p32r, p64i = p32i; csq(p64r, p64i, 1);
    float p512r = p64r, p512i = p64i; csq(p512r, p512i, 3);
    float p1kr = p512r, p1ki = p512i; csq(p1kr, p1ki, 1);
    float sgr, sgi, wgr, wgi;
    cmul((q & 1) ? p16r : 1.f, (q & 1) ? p16i : 0.f, (q & 2) ? p32r : 1.f, (q & 2) ? p32i : 0.f, sgr, sgi);
    cmul((q & 1) ? p512r : 1.f, (q & 1) ? p512i : 0.f, (q & 2) ? p1kr : 1.f, (q & 2) ? p1ki : 0.f, wgr, wgi);
#pragma unroll
    for (int z = 0; z < 16; ++z) asm volatile("" : "+v"(vr[z]), "+v"(vi[z]));
    asm volatile("" : "+v"(sgr), "+v"(sgi), "+v"(wgr), "+v"(wgi), "+v"(p64r), "+v"(p64i));
    SCHED();
    const int want = imin(imax(ck - 8 * q, 0), 8);
    auto carry_ok = [&]() {
      int good = 0;
#pragma unroll
      for (int z = 0; z < 8; ++z) good += ((cv[z][1] ^ tag) | (cv[z][3] ^ ~tag)) == 0u;
      return wall(good == want);
    };
    wait_vm<8>();
    publish(0);
    STAMP(14);
    if (__builtin_expect(!carry_ok(), 0)) {
#pragma nounroll
      for (unsigned spins = 0; spins < SPIN_CAP; ++spins) {
        __builtin_amdgcn_s_sleep(2);
        CFENCE();
        ld_carry();
        if (carry_ok()) break;
      }
    }
    STAMP(6);
    float Gr, Gi;
    {
      float Hr = 0.f, Hi = 0.f, tr, ti;
#pragma unroll
      for (int z = 7; z >= 0; --z) { cmul(Hr, Hi, p64r, p64i, tr, ti); Hr = tr + u2f(cv[z][0]); Hi = ti + u2f(cv[z][2]); }
      cmul(Hr, Hi, wgr, wgi, tr, ti);
      { const auto a = __builtin_amdgcn_permlane16_swap(f2u(tr), f2u(tr), false, false);
        const auto c2 = __builtin_amdgcn_permlane16_swap(f2u(ti), f2u(ti), false, false);
        tr = u2f(a[0]) + u2f(a[1]); ti = u2f(c2[0]) + u2f(c2[1]); }
      { const auto a = __builtin_amdgcn_permlane32_swap(f2u(tr), f2u(tr), false, false);
        const auto c2 = __builtin_amdgcn_permlane32_swap(f2u(ti), f2u(ti), false, false);
        tr = u2f(a[0]) + u2f(a[1]); ti = u2f(c2[0]) + u2f(c2[1]); }
      cmul(tr, ti, sgr, sgi, Hr, Hi);
      cmul(Hr + pr, Hi + pi, cr, ci, Gr, Gi);
    }
    {
      LAS unsigned char* T = L + B_BASE + (c >> 1) * (2 * B_SLOT) + (c & 1) * 4096;
      const int s32 = ((i15 >> 1) & 1) | ((i15 >> 2) & 2), s8 = (i15 & 1) | ((i15 >> 1) & 2);
#pragma unroll
      for (int t = 0; t < 4; ++t) {
        float orr[4], oi[4];
#pragma unroll
        for (int z = 0; z < 4; ++z) {
          orr[z] = __builtin_fmaf(-qi[z], Gi, __builtin_fmaf(qr[z], Gr, vr[4 * t + z]));
          oi[z] = __builtin_fmaf(-qi[z], Gr, __builtin_fmaf(-qr[z], Gi, vi[4 * t + z]));
        }
        if (t < 3) { float tr, ti; cmul(Gr, Gi, p4r, p4i, tr, ti); Gr = tr; Gi = ti; }
        LAS unsigned char* a = T + i15 * 128 + ((t ^ s32) << 5) + ((q ^ s8) << 3);
        *(LAS v2u*)a = v2u{pk_bf16(orr[0], orr[1]), pk_bf16(orr[2], orr[3])};
        *(LAS v2u*)(a + 2048) = v2u{pk_bf16(oi[0], oi[1]), pk_bf16(oi[2], oi[3])};
      }
      const int qq = i15 >> 2, pp = lane & 3;
      const int so = (ck * NK + 56 + 4 * (cb >> 1) + (cb & 1)) * 4096 + (c >> 1) * 512;
#pragma unroll
      for (int t = 0; t < 4; ++t) {
        v2u part[2];
#pragma unroll
        for (int hh = 0; hh < 2; ++hh)
          part[hh] = lds_rdtr(T + (8 * q + 4 * hh + qq) * 128 + ((t ^ ((qq >> 1) | ((q & 1) << 1))) << 5) + ((pp ^ ((qq & 1) | (hh << 1))) << 3));
        st_sc1(v4u{part[0][0], part[0][1], part[1][0], part[1][1]}, ra2, (lane >> 5) * 8192 + (lane & 31) * 16, so + t * 1024);
      }
    }
    SCHED();
    STAMP(7);
    wait_vm<0>();
    publish(1);
    __builtin_amdgcn_s_setprio(0);
    STAMP(12);
  }
  kp = kargs();
  pc = p2_ctx(pWd, pCre, pCim, w, N);
  const int d0 = 2 * pc.sh;
  if (w < 4) {
    lds_wait(L + LDS_PF + 4 * w);
    b2_read(true, L + w * 16384, lane, b2[2]);
    b2_read(true, L + w * 16384 + 8192, lane, b2[3]);
    WAIT_LDS();
    b2_load<0, 0, 8>(pc, vb, 2, w * 16384);
    b2_load<8192, 0, 8>(pc, vb, 3, w * 16384);
    STAMP(13);
  }

  {
    lds_wait4(L + LDS_DONE);
    const int cl = lane & 15, lo = pc.sh ? (pc.g0 - 28) & ~1 : pc.g0 >> 1, hi = pc.sh ? (pc.g0 + pc.nd - 29) | 1 : (pc.g0 + pc.nd - 1) >> 1;
    poll_flags((GAS u64*)(kp->ws + WS_FA2) + ((pc.sh * 16 + (R & 1) * 8 + N) * 16 + (R >> 1)) * 16 + cl, tag64(tag), (cl >= lo && cl <= hi) || cl >= 14, cl < 14);
  }
  STAMP(8);
  if (w >= 4) __builtin_amdgcn_s_setprio(1);
  rsrc_t ra = mk_rsrc(kp->ws + WS_A2 + (R * NK + 2 * pc.g0) * 4096, pc.nd * 8192);
  const int va = ((lane >> 4) & 1) * 1024 + (lane >> 5) * 256 + (lane & 15) * 16;
  v4u af[8];
  auto ld_a = [&](int n, int o) { af[n] = ld_sc1(ra, va + (n & 1) * 2048 + ((n >> 1) & 1) * 512, o + (n >> 2) * 4096); };
  {
    const int o = p2_ds(pc, 0) * 8192;
#pragma unroll
    for (int n = 0; n < 8; ++n) ld_a(n, o);
  }
  SCHED();
  glds<B_BASE + B_SLOT, 0>(mk_rs((const void*)pX, S_ * D_ * 2), w * 1024, ((lane >> 5) * 16 + ((lane >> 3) & 3)) * 1024 + (lane & 7) * 16,
                             (64 * R + 32 * (w & 1) + 4 * (w >> 1)) * 1024 + N * 128);
  const int tok = 64 * R + 32 * (w & 1) + 4 * (w >> 1) + (lane >> 4);
  const v2u dv = __builtin_amdgcn_raw_buffer_load_b64(mk_rsrc(kp->Dsk, D_ * 2), (lane & 15) * 8, N * 128, 0);
  const float ge = u2f(__builtin_amdgcn_raw_buffer_load_b32(mk_rsrc(kp->ws + WS_G, S_ * 4), tok * 4 + (lane & 1) * 64, 0, AUX_SC1));
  SCHED();
  f32x16 acc[2][2];
#pragma unroll
  for (int e = 0; e < 2; ++e)
#pragma unroll
    for (int tt = 0; tt < 2; ++tt) { acc[e][tt] = f32x16{}; asm volatile("" : "+v"(acc[e][tt])); }
  asm volatile("s_nop 1");
#pragma unroll
  for (int d = 0; d < 7; ++d) {
    if ((d >= 2 && d < 6) || w >= 4) {
      if (d == 3 && w >= 4) { WAIT_LDS(); b2_load<0, 0, 8>(pc, vb, 6, w * 16384); }
      const int o = d + 1 - d0 < pc.nd ? p2_ds(pc, d + 1 - d0) * 8192 : OOB;
#pragma unroll
      for (int n = 0; n < 8; ++n) {
        mfma32t(acc[0][n & 1], b2[d & 3][n >> 2][n & 2], af[n]);
        mfma32t(acc[1][n & 1], b2[d & 3][n >> 2][(n & 2) + 1], af[n]);
        SCHED();
        if (d < 6) ld_a(n, o);
        SCHED();
      }
      if (d == 2 || d == 3) b2_read(w < 4, L + w * 16384 + (d & 1) * 8192, lane, b2[d & 1]);
      if (d == 4) b2_read(false, L + w * 16384, lane, b2[2]);
      SCHED();
    }
  }
  asm volatile("s_nop 15\n\ts_nop 7" : "+v"(acc[0][0]), "+v"(acc[1][0]), "+v"(acc[0][1]), "+v"(acc[1][1]));
  SCHED();
  STAMP(9);
  kp = kargs();
  {
    const int t3 = lane & 3, g3 = ((t3 & 1) * 3) ^ (t3 >> 1), hi = lane >> 5;
#pragma unroll
    for (int T = 0; T < 2; ++T)
#pragma unroll
      for (int E = 0; E < 2; ++E)
#pragma unroll
        for (int g = 0; g < 4; ++g) {
          const int e = 2 * E + (g >> 1), q = 2 * (g & 1) + hi;
          *(LAS f32x4*)(L + w * 16384 + ((2 * T + ((lane >> 4) & 1)) * 4 + e) * 1024 + ((16 * q + (((((lane >> 2) & 3) ^ q) << 2) | (e ^ g3))) << 4)) =
              f32x4{acc[E][T][4 * g], acc[E][T][4 * g + 1], acc[E][T][4 * g + 2], acc[E][T][4 * g + 3]};
        }
  }
  if (b == 255 && w == 0) {
    rsrc_t rf = mk_rsrc(kp->ws + WS_FA2 + 32768, 256 * 8);
#pragma nounroll
    for (unsigned spins = 0; spins < SPIN_CAP; ++spins) {
      CFENCE();
      const v4u f0 = ld_sc1(rf, lane * 32, 0), f1 = ld_sc1(rf, lane * 32, 16);
      if (wall(((f0[0] ^ tag) | (f0[1] ^ ~tag) | (f0[2] ^ tag) | (f0[3] ^ ~tag) | (f1[0] ^ tag) | (f1[1] ^ ~tag) | (f1[2] ^ tag) | (f1[3] ^ ~tag)) == 0u)) break;
      __builtin_amdgcn_s_sleep(8);
    }
    st_flag(kp->ws, WS_EPOCH, tag, lane == 0 ? 0 : OOB);
  }
  WAIT_LDS();
  BARRIER();
  STAMP(10);
  {
    const int q = lane & 3, e = (lane >> 2) & 3, t3 = lane >> 4;
    const int base = ((w >> 1) * 4 + e) * 1024 + q * 256 + ((e ^ ((t3 & 1) * 3) ^ (t3 >> 1)) << 4);
    rsrc_t ro = mk_rsrc(kp->out, S_ * D_ * 4);
    const float go = dpp<0xB1>(ge);
    f32x4 res[2];
#pragma unroll
    for (int hf = 0; hf < 2; ++hf) {
      f32x4 sg = {0.f, 0.f, 0.f, 0.f}, ss = {0.f, 0.f, 0.f, 0.f};
      const int a = base + (((2 * (w & 1) + hf) ^ q) << 6);
#pragma unroll
      for (int ww = 0; ww < 8; ++ww) {
        const f32x4 v = *(LAS f32x4*)(L + ww * 16384 + a);
        if (ww < 4) ss += v; else sg += v;
      }
      const float g = (lane & 1) == hf ? ge : go;
      f32x4 m = ss * (2.f * g) + sg * (1.f - g);
      const v2u xv = *(LAS v2u*)(L + B_BASE + B_SLOT + w * 1024 + hf * 512 + lane * 8);
      m[0] += g * (bf2f(xv[0] & 0xFFFFu) * bf2f(dv[0] & 0xFFFFu)); m[1] += g * (bfhi(xv[0]) * bfhi(dv[0]));
      m[2] += g * (bf2f(xv[1] & 0xFFFFu) * bf2f(dv[1] & 0xFFFFu)); m[3] += g * (bfhi(xv[1]) * bfhi(dv[1]));
      res[hf] = m;
    }
    asm volatile("" : "+v"(res[0]), "+v"(res[1]));
#pragma unroll
    for (int hf = 0; hf < 2; ++hf)
      __builtin_amdgcn_raw_buffer_store_b128(__builtin_bit_cast(v4u, res[hf]), ro, tok * 2048 + (lane & 15) * 16 + hf * 32768 + N * 256, 0, OUT_AUX);
  }
  STAMP(11);
}

#if defined(__HIP_DEVICE_COMPILE__)
#pragma clang attribute pop
#endif

extern "C" void kernel_launch(void* const* d_in, const int* in_sizes, int n_in, void* d_out, int out_size, void* d_ws, size_t ws_size,
                              hipStream_t stream) {
  Params p;
  p.x = (const u16*)d_in[0]; p.Lre = (const u16*)d_in[1]; p.Lim = (const u16*)d_in[2]; p.Bre = (const u16*)d_in[3];
  p.Bim = (const u16*)d_in[4]; p.Cre = (const u16*)d_in[5]; p.Cim = (const u16*)d_in[6]; p.ldt = (const u16*)d_in[7];
  p.Dsk = (const u16*)d_in[8]; p.Wg = (const u16*)d_in[10]; p.Wu = (const u16*)d_in[11]; p.Wd = (const u16*)d_in[12];
  p.gw = (const u16*)d_in[13]; p.out = (float*)d_out; p.ws = (unsigned char*)d_ws;
  (void)hipFuncSetAttribute((const void*)k_fused, hipFuncAttributeMaxDynamicSharedMemorySize, LDS_BYTES);
  k_fused<<<256, 512, LDS_BYTES, stream>>>(p);
}
```

```cpp
#define WARMM
#define WARM0 4
#define PRIO2
#include <hip/hip_runtime.h>
#include <stdint.h>

typedef unsigned short u16;
typedef unsigned long long u64;
typedef unsigned v4u __attribute__((ext_vector_type(4)));
typedef unsigned v2u __attribute__((ext_vector_type(2)));
typedef short s16x4 __attribute__((ext_vector_type(4)));
typedef __bf16 bf16x8 __attribute__((ext_vector_type(8)));
typedef __bf16 bf16x2 __attribute__((ext_vector_type(2)));
typedef float f32x2 __attribute__((ext_vector_type(2)));
typedef float f32x4 __attribute__((ext_vector_type(4)));
typedef float f32x16 __attribute__((ext_vector_type(16)));
#define LAS __attribute__((address_space(3)))
#define GAS __attribute__((address_space(1)))
#define RLX_AGENT __ATOMIC_RELAXED, __HIP_MEMORY_SCOPE_AGENT
typedef __amdgpu_buffer_rsrc_t rsrc_t;

constexpr int S_ = 2048, D_ = 512, P_ = 512, F_ = 1792;
constexpr int NK = 88;
constexpr int AUX_SC1 = 16;
#define OUT_AUX 2
constexpr int WS_EPOCH = 0;
constexpr int WS_G = 8192;
constexpr int WS_FA2 = 65536;
constexpr int CTRL_BYTES = 131072;
constexpr int WS_CARRY = 131072;
constexpr int WS_A2 = 524288;
constexpr int A2_BYTES = 32 * NK * 4096;
constexpr int WS_STAMP = 32 << 20;
constexpr int LDS_BYTES = 160 * 1024;
constexpr int X_SLOT = 16384, W_BASE = 65536, W_SLOT = 16384, B_BASE = 131072, B_SLOT = 8192;
constexpr int LDS_DONE = 155648, LDS_PF = LDS_DONE + 32, LDS_MD = LDS_DONE + 48, LDS_TAG = LDS_DONE + 64;
constexpr int LDS_DUMMY = LDS_DONE + 256, LDS_CONST = LDS_DONE + 512;
constexpr int OOB = 1 << 28;
constexpr int CODE_BYTES = 29 * 1024;
constexpr unsigned SPIN_CAP = 1u << 18;

struct Params {
  const u16 *x, *Wg, *Wu, *Bre, *Bim, *Cre, *Cim, *Wd;
  unsigned char* ws;
  const u16 *Lre, *Lim, *ldt, *Dsk, *gw;
  float* out;
};

#if defined(__HIP_DEVICE_COMPILE__)
#pragma clang attribute push(__attribute__((target("no-packed-fp32-ops"))), apply_to = function)
#endif
__device__ __forceinline__ float u2f(unsigned v) { return __builtin_bit_cast(float, v); }
__device__ __forceinline__ unsigned f2u(float v) { return __builtin_bit_cast(unsigned, v); }
__device__ __forceinline__ bool wall(bool p) { return __builtin_amdgcn_ballot_w64(!p) == 0; }
__device__ __forceinline__ bool wany(bool p) { return __builtin_amdgcn_ballot_w64(p) != 0; }
__device__ __forceinline__ int imin(int a, int b) { return a < b ? a : b; }
__device__ __forceinline__ int imax(int a, int b) { return a > b ? a : b; }
template <int CTRL> __device__ __forceinline__ float dpp(float v) { return u2f(__builtin_amdgcn_mov_dpp(f2u(v), CTRL, 0xF, 0xF, true)); }
__device__ __forceinline__ rsrc_t mk_rsrc(const void* p, int bytes) {
  return __builtin_amdgcn_make_buffer_rsrc((void*)p, 0, bytes, 0x00020000);
}
__device__ __forceinline__ float bf2f(unsigned v) { return u2f(v << 16); }
__device__ __forceinline__ float bfhi(unsigned v) { return u2f(v & 0xFFFF0000u); }
__device__ __forceinline__ unsigned pk_bf16(float lo, float hi) {
  f32x2 t = {lo, hi};
  return __builtin_bit_cast(unsigned, __builtin_convertvector(t, bf16x2));
}
__device__ __forceinline__ void mfma16t(f32x4& c, v4u a, v4u b) {
  asm volatile("v_mfma_f32_16x16x32_bf16 %0, %1, %2, %0" : "+v"(c) : "v"(a), "v"(b));
}
__device__ __forceinline__ void mfma32t(f32x16& c, v4u a, v4u b) {
  asm volatile("v_mfma_f32_32x32x16_bf16 %0, %1, %2, %0" : "+v"(c) : "v"(a), "v"(b));
}
__device__ __forceinline__ v4u lds_rd128(LAS unsigned char* p) { return *(LAS v4u*)p; }
__device__ __forceinline__ v2u lds_rdtr(LAS unsigned char* p) {
  return __builtin_bit_cast(v2u, __builtin_amdgcn_ds_read_tr16_b64_v4i16((LAS s16x4*)p));
}
__device__ __forceinline__ v4u mk_rs(const void* p, unsigned bytes) {
  const u64 a = (u64)p;
  unsigned lo = (unsigned)a, hi = (unsigned)(a >> 32) & 0xFFFFu;
  asm volatile("" : "+s"(lo), "+s"(hi));
  return v4u{lo, hi, bytes, 0x00020000u};
}
template <int LI, int SI> __device__ __forceinline__ void glds(v4u rs, int lds, int voff, int soff) {
  int t;
  asm volatile("s_add_u32 m0, %1, %5\n\ts_add_u32 %0, %4, %6\n\tbuffer_load_dwordx4 %2, %3, %0 offen lds"
               : "=&s"(t) : "s"(lds), "v"(voff), "s"(rs), "s"(soff), "i"(LI), "i"(SI) : "memory", "scc");
}
typedef const __attribute__((address_space(4))) Params* KP;
__device__ __forceinline__ KP kargs() {
  KP k = (KP)__builtin_amdgcn_kernarg_segment_ptr();
  asm volatile("" : "+s"(k));
  return k;
}
__device__ __forceinline__ void warm(v4u rs, int voff, int soff) {
  asm volatile("s_mov_b32 m0, %3\n\ts_nop 0\n\tbuffer_load_dword %0, %1, %2 offen lds" : : "v"(voff), "s"(rs), "s"(soff), "i"(LDS_DUMMY) : "memory");
}
__device__ __forceinline__ v4u ld_sc1(rsrc_t rs, int voff, int soff) { return __builtin_amdgcn_raw_buffer_load_b128(rs, voff, soff, AUX_SC1); }
__device__ __forceinline__ void st_sc1(v4u v, rsrc_t rs, int voff, int soff) { __builtin_amdgcn_raw_buffer_store_b128(v, rs, voff + soff, 0, AUX_SC1); }
__device__ __forceinline__ u64 tag64(unsigned tag) { return ((u64)(~tag) << 32) | tag; }
__device__ __forceinline__ void st_flag(unsigned char* ws, int off, unsigned tag, int voff) {
  __builtin_amdgcn_raw_buffer_store_b64(v2u{tag, ~tag}, mk_rsrc(ws, CTRL_BYTES), voff, off, AUX_SC1);
}
__device__ __forceinline__ unsigned lds_word(LAS unsigned char* p) { return __builtin_amdgcn_readfirstlane(*(volatile LAS unsigned*)p); }
#define BARRIER() asm volatile("s_barrier" ::: "memory")
#define SCHED() __builtin_amdgcn_sched_barrier(0)
#define CFENCE() asm volatile("" ::: "memory")
#define WAIT_LDS() asm volatile("s_waitcnt lgkmcnt(0)" ::: "memory")
template <int N> __device__ __forceinline__ void wait_vm() { asm volatile("s_waitcnt vmcnt(%c0)" ::"i"(N) : "memory"); }

#define STAMP(i) do {} while (0)
#define POLL_SLEEP 4
#define POLL_IDLE 16

__device__ __forceinline__ void poll_flags(GAS u64* f, u64 want, bool care, bool late) {
#pragma nounroll
  for (unsigned spins = 0; spins < SPIN_CAP; ++spins) {
    CFENCE();
    const u64 v = __hip_atomic_load(f, RLX_AGENT);
    if (wall(!care || v == want)) break;
    if (wany(late && v == want)) __builtin_amdgcn_s_sleep(POLL_SLEEP); else __builtin_amdgcn_s_sleep(POLL_IDLE);
  }
  CFENCE();
}
__device__ __forceinline__ void csq(float& r, float& i, int n) {
#pragma unroll
  for (int z = 0; z < n; ++z) { const float t = r * r - i * i; i = 2.f * r * i; r = t; }
}

#define NROT 102
#define CONST_AT 2
constexpr int LASTB = 13;
struct Sched {
  int n;
  int type[40], idx[40];
  int lo[LASTB + 1], hi[LASTB + 1];
  int wait[LASTB + 1];
  bool ok;
};
constexpr Sched make_sched(int kind) {
  Sched s{};
  int cnt[40] = {}, need[40] = {}, avail[40] = {};
  int n = 0;
  auto add = [&](int ty, int ix, int nd, int av) {
    if (kind && ty == 2) return;
    s.type[n] = ty; s.idx[n] = ix; cnt[n] = ty == 2 ? 4 : 3; need[n] = nd < LASTB ? nd : LASTB; avail[n] = av; ++n;
  };
  add(0, 0, 0, -1); add(1, 0, 0, -1); add(2, 0, 0, -1); add(2, 1, 0, -1);
  add(0, 1, 1, -1); add(1, 1, 1, -1); add(2, 2, 1, -1); add(2, 3, 2, -1);
  add(0, 2, 3, -1); add(1, 2, 3, -1);
  add(2, 4, 3, 1); add(2, 5, 4, 1);
  for (int b = 2; b < 12; ++b) {
    const int T = b / 2 + 2;
    add(b & 1, T, 2 * T - 1, kind ? (b & ~1) : b);
    add(2, b + 4, b + 3, b);
  }
  s.n = n;
  s.ok = true;
  const int LIM = 8;
  int endpos[40] = {};
  int next = 0, total = 0;
  for (int b = 0; b <= LASTB; ++b) {
    s.lo[b] = next;
    const int start = total;
    for (;;) {
      if (next >= n || avail[next] > b - 1) break;
      int lastneed = 0;
      for (int i = 0; i < next; ++i) if (need[i] <= b) lastneed = endpos[i];
      if (b < 3 && need[next] > b && total + cnt[next] - lastneed > LIM + 2 * b) break;
      if (b >= 3 && need[next] > b + 1 && total + cnt[next] - start > 11) break;
      total += cnt[next]; endpos[next] = total; ++next;
    }
    s.hi[b] = next;
    if (kind == 0 && b >= WARM0 && b < WARM0 + 3) total += 1;
    int lastneed = 0;
    for (int i = 0; i < next; ++i) if (need[i] <= b) lastneed = endpos[i];
    for (int i = next; i < n; ++i) if (need[i] <= b) s.ok = false;
    s.wait[b] = total - lastneed;
  }
  if (next != n) s.ok = false;
  return s;
}
constexpr Sched SCH[2] = {make_sched(0), make_sched(1)};
static_assert(SCH[0].ok && SCH[1].ok, "every ring item must be issued in time");

struct P2Ctx { u64 a0, a1; int so0, sh, g0, nd, rot; unsigned bytes; };
__device__ __forceinline__ P2Ctx p2_ctx(u64 Wd, u64 Cre, u64 Cim, int w, int N) {
  P2Ctx c;
  const int ssm = w < 4;
  const u64 m = 0 - (u64)ssm;
  c.a0 = (Cre & m) | (Wd & ~m); c.a1 = (Cim & m) | (Wd & ~m);
  c.sh = ssm;
  c.so0 = N * 128 + ssm * (N * (65536 - 128) - 14 * 128);
  c.bytes = F_ * D_ * 2u - ssm * (F_ * D_ * 2u - D_ * P_ * 2u);
  c.g0 = 7 * w - 28 + ssm * (56 - 3 * w);
  c.nd = 7 - 3 * ssm;
#define P2ROT 1
  c.rot = P2ROT == 0 ? 0 : (P2ROT == 1 ? (N & 1) * (3 - ssm) : (P2ROT == 2 ? (N & 3) * (2 - ssm) : (N * c.nd) >> 3));
  return c;
}
__device__ __forceinline__ int p2_ds(const P2Ctx& c, int d) { const int t = d + c.rot; return t >= c.nd ? t - c.nd : t; }
__device__ __forceinline__ void b2_src(const P2Ctx& c, int d, v4u& rs, int& so) {
  const int g = c.g0 + p2_ds(c, d);
  const u64 x0 = c.a0, x1 = c.a1;
  rs = mk_rs((const void*)((g & 1) ? x1 : x0), c.bytes);
  so = c.so0 + (c.sh ? (g >> 1) * 128 : g * 65536);
}
template <int LI, int N0, int N1> __device__ __forceinline__ void b2_rows(v4u rs, int lds, int vb, int so) {
  if constexpr (N0 < N1) { glds<LI, N0 * 8192>(rs, lds, (N0 & 1) ? vb ^ 16 : vb, so); b2_rows<LI + 1024, N0 + 1, N1>(rs, lds, vb, so); }
}
template <int LI, int N0, int N1> __device__ __forceinline__ void b2_load(const P2Ctx& c, int vb, int d, int lds) {
  v4u rs; int so;
  b2_src(c, d, rs, so);
  b2_rows<LI, N0, N1>(rs, lds, vb, so);
}
__device__ __forceinline__ void b2_read(bool ssm, LAS unsigned char* img, int lane, v4u (&f)[2][4]) {
  if (ssm) {
    const int i = lane & 31, sw = ((i & 2) << 1) | ((i >> 1) & 2) | ((i >> 3) & 1);
#pragma unroll
    for (int h = 0; h < 2; ++h)
#pragma unroll
      for (int z = 0; z < 4; ++z) f[h][z] = lds_rd128(img + i * 128 + (((4 * h + (z & 2) + (lane >> 5)) ^ sw) << 4) + (z & 1) * 4096);
  } else {
    const int qq = (lane >> 2) & 3, hi = lane >> 5;
    const int c0 = (((lane >> 4) & 1) * 2 + ((lane >> 1) & 1)) ^ (((qq & 2) << 1) | (hi << 1));
#pragma unroll
    for (int h = 0; h < 2; ++h)
#pragma unroll
      for (int z = 0; z < 4; ++z) {
        LAS unsigned char* a = img + (4 * hi + qq) * 128 + ((c0 ^ ((z & 1) << 2) ^ (z >> 1)) << 4) + (lane & 1) * 8 + h * 4096 + (z >> 1) * 1024;
        const v2u lo = lds_rdtr(a), hi2 = lds_rdtr(a + 2048);
        f[h][z] = v4u{lo[0], lo[1], hi2[0], hi2[1]};
      }
  }
}

struct LdCtx { v4u rx, rw, rb; u64 Wd, Cre, Cim; int lw, lb, vx, vw0, vw1, vbb, rot, lane, vb, vwarm, xcd, ch; mutable unsigned sv, lrb, lib, ldb, ep; };
__device__ __forceinline__ void warm_chunk(u64 x0, u64 x1, u64 x2, int xc, int vwarm, int slot) {
  asm volatile("" : "+s"(xc));
  int i = slot < 6 ? slot + xc : slot - 6 + xc;
  if (slot < 6) { i = i >= 12 ? i - 12 : (i >= 6 ? i - 6 : i); } else { i = 6 + (i >= 10 ? i - 10 : (i >= 5 ? i - 5 : i)); }
  const u64 src = i < 7 ? x0 : (i < 9 ? x1 : x2);
  const unsigned bytes = (unsigned)slot > 10u ? 0u : (i < 7 ? F_ * D_ * 2 : D_ * P_ * 2);
  warm(mk_rs((const void*)src, bytes), vwarm, (i < 7 ? i : ((i - 7) & 1)) * 262144);
}

template <int KIND, int I> __device__ __forceinline__ void ld_item(const LdCtx& c) {
  constexpr int ty = SCH[KIND].type[I], t = SCH[KIND].idx[I];
  int rot = c.rot;
  asm volatile("" : "+s"(rot));
  if constexpr (ty < 2) {
    const int k0 = ((t + rot) & 7) * 128;
    glds<(t % 3) * X_SLOT + 2 * ty * 4096, ty * 65536>(c.rx, c.lw * 1024, c.vx, k0);
    glds<(t % 3) * X_SLOT + (2 * ty + 1) * 4096, ty * 65536 + 8192>(c.rx, c.lw * 1024, c.vx, k0);
    glds<B_BASE + (t % 3) * B_SLOT + ty * 2048, ty * 16384>(c.rb, c.lb, c.vbb, k0);
  } else {
    const int k0 = (((t >> 1) + rot) & 7) * (64 * F_ * 2);
    constexpr int LI = W_BASE + (t & 3) * W_SLOT, SI = (t & 1) * 32 * F_ * 2;
    glds<LI, SI>(c.rw, c.lw * 4096, c.vw0, k0);
    glds<LI + 1024, SI + 4 * F_ * 2>(c.rw, c.lw * 4096, c.vw0, k0);
    glds<LI + 2048, SI + 8 * F_ * 2>(c.rw, c.lw * 4096, c.vw1, k0);
    glds<LI + 3072, SI + 12 * F_ * 2>(c.rw, c.lw * 4096, c.vw1, k0);
  }
}
template <int KIND, int LO, int HI> __device__ __forceinline__ void ld_items(const LdCtx& c) {
  if constexpr (LO < HI) { ld_item<KIND, LO>(c); ld_items<KIND, LO + 1, HI>(c); }
}
template <int KIND, int B> __device__ __forceinline__ void loader_steps(const LdCtx& c, LAS unsigned char* L) {
  if constexpr (B == 0) {
    constexpr int FIRST = KIND ? 2 : 4;
    ld_items<KIND, 0, FIRST>(c);
    KP kp = kargs();
    const v4u r0 = mk_rs(kp->Lre, P_ * 2), r1 = mk_rs(kp->Lim, P_ * 2), r2 = mk_rs(kp->ldt, P_ * 2), r3 = mk_rs(kp->ws + WS_EPOCH, c.lw == 3 ? 4 : 0);
    asm volatile("buffer_load_ushort %0, %4, %5, 0 offen\n\tbuffer_load_ushort %1, %4, %6, 0 offen\n\tbuffer_load_ushort %2, %4, %7, 0 offen\n\t"
                 "buffer_load_dword %3, off, %8, 0 sc1"
                 : "=&v"(c.lrb), "=&v"(c.lib), "=&v"(c.ldb), "=&v"(c.ep) : "v"(c.ch * 2), "s"(r0), "s"(r1), "s"(r2), "s"(r3) : "memory");
    ld_items<KIND, FIRST, SCH[KIND].hi[0]>(c);
  } else {
    ld_items<KIND, SCH[KIND].lo[B], SCH[KIND].hi[B]>(c);
  }
  if constexpr (KIND == 0 && B == WARM0) warm(mk_rs((const void*)c.Wd, F_ * D_ * 2), c.lane < 16 ? (c.vwarm * 16 + c.lane) * 128 : OOB, 0);
  if constexpr (KIND == 0 && B == WARM0 + 1) warm(mk_rs((const void*)c.Cre, D_ * P_ * 2), c.lane < 5 ? (c.vwarm * 5 + c.lane) * 128 : OOB, 0);
  if constexpr (KIND == 0 && B == WARM0 + 2) warm(mk_rs((const void*)c.Cim, D_ * P_ * 2), c.lane < 5 ? (c.vwarm * 5 + c.lane) * 128 : OOB, 0);
  if constexpr (B == CONST_AT) {
    asm volatile("" : "+v"(c.lrb), "+v"(c.lib), "+v"(c.ldb), "+v"(c.ep) : : "memory");
    const float lr = bf2f(c.lrb), li = bf2f(c.lib);
    float dt = __builtin_amdgcn_exp2f(bf2f(c.ldb) * 1.44269504088896341f);
    dt = bf2f(pk_bf16(dt, 0.f) & 0xFFFFu);
    const float rev = __builtin_amdgcn_fractf(dt * li * 0.15915494309189535f);
    const float ex = __builtin_amdgcn_exp2f(dt * lr * 1.44269504088896341f);
    const float ar = ex * __builtin_amdgcn_cosf(rev), ai = ex * __builtin_amdgcn_sinf(rev);
    const float den = __builtin_amdgcn_rcpf(lr * lr + li * li);
    float p16r = ar, p16i = ai;
    csq(p16r, p16i, 4);
    float p32r = p16r, p32i = p16i;
    csq(p32r, p32i, 1);
    LAS f32x4* d = (LAS f32x4*)(L + LDS_CONST + c.lw * 512 + (c.lane & 15) * 32);
    d[0] = f32x4{ar, ai, ((ar - 1.f) * lr + ai * li) * den, (ai * lr - (ar - 1.f) * li) * den};
    d[1] = f32x4{p16r, p16i, p32r, p32i};
    *(volatile LAS unsigned*)(L + LDS_TAG + 4 * (3 - c.lw)) = c.ep + 1u;
    WAIT_LDS();
  }
  SCHED();
  wait_vm<SCH[KIND].wait[B]>();
  BARRIER();
  if constexpr (B < LASTB) loader_steps<KIND, B + 1>(c, L);
}

__device__ __forceinline__ bool all4(LAS unsigned char* p) {
  const v4u d = *(volatile LAS v4u*)p;
  return __builtin_amdgcn_readfirstlane(d[0] & d[1] & d[2] & d[3]) != 0u;
}
__device__ __forceinline__ void lds_wait4(LAS unsigned char* p) {
#pragma nounroll
  for (unsigned spins = 0; spins < (SPIN_CAP << 4); ++spins) {
    if (all4(p)) break;
    __builtin_amdgcn_s_sleep(2);
  }
  CFENCE();
}
__device__ __forceinline__ void lds_wait(LAS unsigned char* word) {
#pragma nounroll
  for (unsigned spins = 0; spins < (SPIN_CAP << 4); ++spins) {
    if (lds_word(word) != 0u) break;
    __builtin_amdgcn_s_sleep(2);
  }
  CFENCE();
}
__device__ __forceinline__ void cmul(float ar, float ai, float br, float bi, float& cr, float& ci) {
  cr = ar * br - ai * bi; ci = ar * bi + ai * br;
}

__global__ __launch_bounds__(512, 2) void k_fused(Params) {
  extern __shared__ __attribute__((aligned(16))) unsigned char lds_[];
  LAS unsigned char* L = (LAS unsigned char*)lds_;
  STAMP(0);
  KP kp = kargs();
  u64 pX = (u64)kp->x, pWg = (u64)kp->Wg, pWu = (u64)kp->Wu, pBre = (u64)kp->Bre, pBim = (u64)kp->Bim, pCre = (u64)kp->Cre, pCim = (u64)kp->Cim, pWd = (u64)kp->Wd;
  const int b = __builtin_amdgcn_workgroup_id_x(), tid = __builtin_amdgcn_workitem_id_x(), lane = tid & 63;
  const int w = __builtin_amdgcn_readfirstlane(tid >> 6);
  const int xcd = b & 7, r = b >> 3;
  warm(mk_rs((const void*)(__builtin_amdgcn_s_getpc() & ~(u64)127), CODE_BYTES), lane < 2 ? lane * 32768 : OOB, (r * 8 + w) * 128);
  const bool light = (r & 7) == 7;
  const int ih = 28 * (xcd & 3) + r - (r >> 3), il = 112 + 4 * (xcd & 3) + (r >> 3);
  const int idx = ih + (il - ih) * (int)light;
  const int pn = 8 * (xcd >> 2) + (idx & 7), cb = idx >> 3;
  const int rot = pn & 6;
  const int R = xcd * 4 + (r & 3), N = r >> 2;
  asm volatile("" : "+s"(pX), "+s"(pWg), "+s"(pWu), "+s"(pBre), "+s"(pBim), "+s"(pCre), "+s"(pCim), "+s"(pWd));
  const int vb = (lane >> 3) * 1024 + (((lane & 7) ^ (((lane >> 2) & 4) | ((lane >> 4) & 2))) << 4);
  v4u b2[4][2][4];
  unsigned tag = 0;
  P2Ctx pc;

  if (__builtin_expect(w >= 4, 1)) {
    LdCtx c;
    c.lw = w - 4; c.rot = rot; c.lane = lane; c.vb = vb; c.sv = 0;
    c.Wd = pWd; c.Cre = pCre; c.Cim = pCim;
    c.vwarm = ((r - (r >> 3)) * 8 + xcd) * 4 + c.lw;
    c.xcd = xcd; c.ch = 32 * cb + 16 * (c.lw >> 1) + (lane & 15);
    const int l3 = lane >> 3, kq = lane >> 4;
    c.rx = mk_rs((const void*)pX, S_ * D_ * 2);
    c.vx = (32 * (c.lw & 1) + 16 * (l3 >> 2) + 4 * (c.lw >> 1) + (l3 & 3)) * 1024 + (((lane & 7) ^ ((4 * c.lw + kq) & 7)) << 4) + pn * 128 * 1024;
    c.rb = mk_rs((const void*)((c.lw >> 1) ? pBim : pBre), P_ * D_ * 2);
    c.vbb = (8 * (c.lw & 1) + l3) * 1024 + (((lane & 7) ^ (4 * (c.lw & 1) + kq)) << 4) + cb * 32 * 1024;
    c.lb = (c.lw >> 1) * 4096 + (c.lw & 1) * 1024;
    c.rw = mk_rs((const void*)((c.lw >> 1) ? pWu : pWg), D_ * F_ * 2);
    c.vw0 = ((c.lw & 1) * 16 + kq) * (F_ * 2) + (((lane & 15) ^ (kq << 2)) << 4) + cb * 256;
    c.vw1 = c.vw0 ^ 32;
    STAMP(1);
    if (__builtin_expect(!light, 1)) loader_steps<0, 0>(c, L);
    else { loader_steps<1, 0>(c, L); BARRIER(); }
    STAMP(2);
    {
      pc = p2_ctx(pWd, pCre, pCim, w, N);
      const int wA = c.lw < 2 ? w : c.lw, wB = wA ^ 4;
      b2_load<0, 0, 8>(pc, vb, 0, wA * 16384);
      b2_load<8192, 0, 8>(pc, vb, 1, wA * 16384);
      const P2Ctx pA = p2_ctx(pWd, pCre, pCim, wA, N), pB = p2_ctx(pWd, pCre, pCim, wB, N);
#pragma unroll
      for (int d = 0; d < 4; ++d) {
        wait_vm<8>();
        b2_read(false, L + wA * 16384 + (d & 1) * 8192, lane, b2[d]);
        WAIT_LDS();
        if (d < 2) b2_load<0, 0, 8>(pc, vb, d + 2, wA * 16384 + (d & 1) * 8192);
        else b2_load<0, 0, 8>(pA, vb, (wA & 4) + (d & 1), wA * 16384 + (d & 1) * 8192);
      }
      STAMP(3);
      lds_wait4(L + LDS_MD);
      b2_load<0, 0, 8>(pB, vb, wB & 4, wB * 16384);
      b2_load<8192, 0, 8>(pB, vb, (wB & 4) + 1, wB * 16384);
      wait_vm<0>();
      *(volatile LAS unsigned*)(L + LDS_PF + 4 * c.lw) = 1u;
      STAMP(4);
    }
    tag = lds_word(L + LDS_TAG);
  } else {
    const int c = w, q = lane >> 4, i15 = lane & 15, G = c & 1;
    const int ch = 32 * cb + 16 * (c >> 1) + i15;
    if (w == 3) {
#pragma unroll
      for (int z = 0; z < 4; ++z) *(volatile LAS v4u*)(L + LDS_DONE + 16 * z) = v4u{0u, 0u, 0u, 0u};
      WAIT_LDS();
    }
    f32x4 k0 = {0.f, 0.f, 0.f, 0.f}, k1 = {0.f, 0.f, 0.f, 0.f};
    unsigned tagv = 0;
    auto rd_const = [&]() {
      k0 = *(LAS f32x4*)(L + LDS_CONST + c * 512 + i15 * 32); k1 = *(LAS f32x4*)(L + LDS_CONST + c * 512 + i15 * 32 + 16);
      tagv = *(volatile LAS unsigned*)(L + LDS_TAG);
    };
    u64 pws = (u64)kp->ws;
    asm volatile("" : "+s"(pws));
    if (w == 2) {
      const int j = lane & 15, g1 = (lane >> 4) & 1, g2 = lane >> 5;
      const int off = j * 65536 + g1 * (WS_A2 + R * NK * 4096) + g2 * (WS_A2 + 2 * pn * NK * 4096) + (j >= 8 - 2 * g1 + 3 * g2) * OOB + g1 * g2 * OOB;
      warm(mk_rs(kp->ws, WS_A2 + A2_BYTES), off, 0);
      warm(mk_rs(kp->out, S_ * D_ * 4), lane < 4 ? lane * 32768 + N * 256 : OOB, R * 131072);
      warm(mk_rs(kp->Dsk, D_ * 2), lane < 1 ? 0 : OOB, 0);
    }
    int xa[2];
#pragma unroll
    for (int ks = 0; ks < 2; ++ks) xa[ks] = i15 * 128 + (((4 * ks + q) ^ ((i15 >> 1) & 7)) << 4);
    asm volatile("" : "+v"(xa[0]), "+v"(xa[1]));
    const int bo = B_BASE + (c >> 1) * 2048;
    f32x4 accs[2][4], acc[4][8];
    v4u bfr[2];
    auto zero_accs = [&]() {
#pragma unroll
      for (int m = 0; m < 2; ++m)
#pragma unroll
        for (int t = 0; t < 4; ++t) { accs[m][t] = f32x4{0.f, 0.f, 0.f, 0.f}; asm volatile("" : "+v"(accs[m][t])); }
    };
    float vr[16], vi[16], t0 = 0.f, t1 = 0.f;
    auto scan_half = [&](int n) {
      const int z = n >> 1;
      if (z == 0) { vr[0] = accs[0][0][0]; vi[0] = accs[1][0][0]; }
      else if ((n & 1) == 0) { t0 = __builtin_fmaf(k0[0], vr[z - 1], accs[0][z >> 2][z & 3]); t1 = __builtin_fmaf(k0[0], vi[z - 1], accs[1][z >> 2][z & 3]); }
      else { vr[z] = __builtin_fmaf(-k0[1], vi[z - 1], t0); vi[z] = __builtin_fmaf(k0[1], vr[z - 1], t1); }
    };
    auto sc = [](int v) { asm volatile("" : "+s"(v)); return v; };

    if (!light) {
      int wa[2];
      {
        const int qq = i15 >> 2, pp = lane & 3;
#pragma unroll
        for (int u = 0; u < 2; ++u)
          wa[u] = W_BASE + (8 * q + qq) * 256 + (((2 * (2 * c + u) + (pp >> 1)) ^ ((qq << 2) | ((q & 1) << 1))) << 4) + 8 * (pp & 1);
      }
      asm volatile("" : "+v"(wa[0]), "+v"(wa[1]));
#pragma unroll
      for (int a = 0; a < 4; ++a)
#pragma unroll
        for (int u = 0; u < 8; ++u) { acc[a][u] = f32x4{0.f, 0.f, 0.f, 0.f}; asm volatile("" : "+v"(acc[a][u])); }
      zero_accs();
      v4u xf[4];
      v2u wf[2][8];
      auto rdw = [&](int s, int z) {
        const int m = z >> 2, u = (z >> 1) & 1, t = z & 1;
        wf[s & 1][z] = lds_rdtr(L + wa[u] + (s & 3) * W_SLOT + m * 8192 + t * 1024);
      };
      STAMP(1);
      BARRIER();
      STAMP(2);
#pragma unroll
      for (int z = 0; z < 8; ++z) rdw(0, z);
#pragma unroll
      for (int z = 0; z < 3; ++z) xf[z] = lds_rd128(L + xa[0] + G * 8192 + z * 2048);
#pragma unroll
      for (int m = 0; m < 2; ++m) bfr[m] = lds_rd128(L + xa[0] + bo + m * 4096);
      auto glu4 = [&](int s, int u, const v4u& x, auto&& between) {
#pragma unroll
        for (int a = 0; a < 4; ++a) {
          const v4u wv = {wf[s & 1][2 * a][0], wf[s & 1][2 * a][1], wf[s & 1][2 * a + 1][0], wf[s & 1][2 * a + 1][1]};
          mfma16t(acc[a][u], wv, x);
          SCHED();
          between(a);
          SCHED();
        }
      };
      int xb = 0, bb = 0;
      auto steps = [&](int n, bool first, const int (&xo)[5]) {
#pragma unroll
        for (int s = 0; s < n; ++s)
#pragma unroll
          for (int u = 0; u < 8; ++u) {
            const int it = 8 * s + u, sn = (it + 3) >> 3, un = (it + 3) & 7;
            SCHED();
            if (u == 0) { if (s > 0 || !first) BARRIER(); SCHED(); }
            glu4(s, u, xf[it & 3], [&](int a) {
              if (a == 0) {
                if ((un & 3) == 0 || it == 0) xb = xa[sn & 1] + sc(xo[sn] + ((un >> 2) ^ G) * 8192);
                xf[(it + 3) & 3] = lds_rd128(L + xb + (un & 3) * 2048);
              }
              if ((a == 1 || a == 2) && u < 4) rdw(s + 1, 2 * u + a - 1);
              if (a == 2 && (u == 4 || u == 5)) {
                if (u == 4) bb = xa[(s + 1) & 1] + sc(bo + (xo[s + 1] >> 1));
                bfr[u - 4] = lds_rd128(L + bb + (u - 4) * 4096);
              }
            });
            if (u < 4) { mfma16t(accs[0][u], xf[it & 3], bfr[0]); mfma16t(accs[1][u], xf[it & 3], bfr[1]); }
          }
      };
      int xo0 = 0;
#pragma nounroll
      for (int o = 0; o < 3; ++o) {
        const int xo1 = xo0 == 2 * X_SLOT ? 0 : xo0 + X_SLOT, xo2 = xo1 == 2 * X_SLOT ? 0 : xo1 + X_SLOT;
        const int xo[5] = {xo0, xo0, xo1, xo1, xo2};
        steps(4, o == 0, xo);
        xo0 = xo2;
      }
      {
        const int xo[5] = {0, 0, X_SLOT, X_SLOT, 0};
        steps(2, false, xo);
      }
      v4u bf2[2];
#pragma unroll
      for (int it = 0; it < 16; ++it) {
        const int s = (it >> 2) & 1, u = (it & 3) + 4 * (it >> 3), nx = it + 3;
        SCHED();
        glu4(s, u, xf[it & 3], [&](int a) {
          if (a == 0 && nx < 16) {
            if ((nx & 3) == 0) xb = xa[(nx >> 2) & 1] + sc(X_SLOT + ((nx >> 3) ^ G) * 8192);
            xf[nx & 3] = lds_rd128(L + xb + (nx & 3) * 2048);
          }
          if (a >= 1 && it < 3 && 3 * it + a - 1 < 8) rdw(3, 3 * it + a - 1);
          if (it == 0 && a == 3) { bb = xa[1] + sc(bo + B_SLOT); bf2[0] = lds_rd128(L + bb); bf2[1] = lds_rd128(L + bb + 4096); }
          if (it == 4 && a == 1) rd_const();
          if (it >= 8) scan_half(4 * (it - 8) + a);
        });
        if (it < 4) { mfma16t(accs[0][u], xf[it & 3], bfr[0]); mfma16t(accs[1][u], xf[it & 3], bfr[1]); }
        else if (it < 8) { mfma16t(accs[0][u], xf[it & 3], bf2[0]); mfma16t(accs[1][u], xf[it & 3], bf2[1]); }
      }
      asm volatile("s_nop 15" : "+v"(acc[0][7]), "+v"(acc[1][7]), "+v"(acc[2][7]), "+v"(acc[3][7]), "+v"(acc[0][6]), "+v"(acc[1][6]), "+v"(acc[2][6]), "+v"(acc[3][6]));
    } else {
      {
      kp = kargs();
      rsrc_t rx = mk_rsrc(kp->x, S_ * D_ * 2), rg = mk_rsrc(kp->gw, D_ * 2), rgo = mk_rsrc(kp->ws + WS_G, S_ * 4);
      const int tk0 = 128 * pn + 64 * (cb - 14) + 16 * c;
      v4u gv[8], xv[2][8];
#pragma unroll
      for (int z = 0; z < 8; ++z) gv[z] = __builtin_amdgcn_raw_buffer_load_b128(rg, (lane & 7) * 16, z * 128, 0);
#pragma unroll
      for (int gi = 0; gi < 2; ++gi)
#pragma unroll
        for (int z = 0; z < 8; ++z) xv[gi][z] = __builtin_amdgcn_raw_buffer_load_b128(rx, (lane >> 3) * 1024 + (lane & 7) * 16, (tk0 + gi * 8) * 1024 + z * 128, 0);
#pragma unroll
      for (int gi = 0; gi < 2; ++gi) {
        float s = 0.f;
#pragma unroll
        for (int z = 0; z < 8; ++z)
#pragma unroll
          for (int y = 0; y < 4; ++y) {
            const unsigned xa_ = xv[gi][z][y], ga = gv[z][y];
            s = __builtin_amdgcn_fdot2_f32_bf16(__builtin_bit_cast(bf16x2, xa_), __builtin_bit_cast(bf16x2, ga), s, false);
          }
        s += dpp<0xB1>(s); s += dpp<0x4E>(s); s += dpp<0x141>(s);
        const float g = __builtin_amdgcn_rcpf(1.f + __builtin_amdgcn_exp2f(s * -1.44269504088896341f));
        __builtin_amdgcn_raw_buffer_store_b32(f2u(g), rgo, (lane & 7) ? OOB : (lane >> 3) * 4, (tk0 + gi * 8) * 4, AUX_SC1);
      }
      }
      STAMP(1);
      zero_accs();
      int xo = 0;
#pragma nounroll
      for (int T = 0; T < 8; ++T) {
#pragma unroll
        for (int ks = 0; ks < 2; ++ks) {
          if (T < 7) BARRIER();
          v4u xf[4];
#pragma unroll
          for (int t = 0; t < 4; ++t) xf[t] = lds_rd128(L + (xa[ks] + xo + G * 8192) + t * 2048);
#pragma unroll
          for (int m = 0; m < 2; ++m) bfr[m] = lds_rd128(L + (xa[ks] + bo + (xo >> 1)) + m * 4096);
#pragma unroll
          for (int t = 0; t < 4; ++t) { mfma16t(accs[0][t], xf[t], bfr[0]); mfma16t(accs[1][t], xf[t], bfr[1]); }
          SCHED();
        }
        xo = xo == 2 * X_SLOT ? 0 : xo + X_SLOT;
      }
      asm volatile("s_nop 15" : "+v"(accs[0][0]), "+v"(accs[1][0]), "+v"(accs[0][1]), "+v"(accs[1][1]), "+v"(accs[0][2]), "+v"(accs[1][2]), "+v"(accs[0][3]), "+v"(accs[1][3]));
      rd_const();
      BARRIER();
#pragma unroll
      for (int n = 0; n < 32; ++n) { scan_half(n); SCHED(); }
    }
    *(volatile LAS unsigned*)(L + LDS_MD + 4 * w) = 1u;
    SCHED();
    STAMP(3);
    tag = __builtin_amdgcn_readfirstlane(tagv);
    const float ar = k0[0], ai = k0[1], cr = k0[2], ci = k0[3], p16r = k1[0], p16i = k1[1], p32r = k1[2], p32i = k1[3];
    __builtin_amdgcn_s_setprio(2);
    rsrc_t ra2 = mk_rsrc((const void*)(pws + WS_A2), A2_BYTES), rcar = mk_rsrc((const void*)(pws + WS_CARRY), 32 * P_ * 16);
    const int ck = 2 * pn + G;
    float pr, pi;
    {
      float tr, ti;
      const auto xr = __builtin_amdgcn_permlane16_swap(f2u(vr[15]), f2u(vr[15]), false, false);
      const auto xi = __builtin_amdgcn_permlane16_swap(f2u(vi[15]), f2u(vi[15]), false, false);
      const float e0r = u2f(xr[0]), e1r = u2f(xr[1]), e0i = u2f(xi[0]), e1i = u2f(xi[1]);
      cmul(e0r, e0i, p16r, p16i, tr, ti); tr += e1r; ti += e1i;
      const auto yr = __builtin_amdgcn_permlane32_swap(f2u(tr), f2u(tr), false, false);
      const auto yi = __builtin_amdgcn_permlane32_swap(f2u(ti), f2u(ti), false, false);
      const float f0r = u2f(yr[0]), f1r = u2f(yr[1]), f0i = u2f(yi[0]), f1i = u2f(yi[1]);
      float Er, Ei;
      cmul(f0r, f0i, p32r, p32i, Er, Ei); Er += f1r; Ei += f1i;
      const float br = (q & 2) ? f0r : 0.f, bi = (q & 2) ? f0i : 0.f;
      cmul(br, bi, p16r, p16i, tr, ti);
      pr = (q & 1) ? tr + e0r : br; pi = (q & 1) ? ti + e0i : bi;
      const v4u gq = {f2u(Er), tag, f2u(Ei), ~tag};
      st_sc1(gq, rcar, q ? OOB : ch * 16, (31 - ck) * (P_ * 16));
    }
    asm volatile("" : "+v"(pr), "+v"(pi) : : "memory");
    SCHED();
    STAMP(4);
    v4u cv[8];
    const int cvo = ch * 16 + (32 - ck + 8 * q) * (P_ * 16);
    auto ld_carry = [&]() {
#pragma unroll
      for (int z = 0; z < 8; ++z) cv[z] = ld_sc1(rcar, cvo, z * (P_ * 16));
    };
    auto publish = [&](int part) {
      *(volatile LAS unsigned*)(L + LDS_DONE + 16 * part + 4 * w) = 1u;
      const bool last = all4(L + LDS_DONE + 16 * part);
      st_flag((unsigned char*)pws, WS_FA2 + part * 32768 + pn * 128 + cb * 8, tag, last && lane < 16 ? lane * 2048 : OOB);
      CFENCE();
    };
    if (!light) {
#pragma unroll
      for (int u = 0; u < 8; ++u) {
        float hv[8];
#pragma unroll
        for (int j = 0; j < 8; ++j) hv[j] = acc[j >> 2][u][j & 3] * -1.44269504088896341f;
        SCHED();
#pragma unroll
        for (int j = 0; j < 8; ++j) hv[j] = __builtin_amdgcn_exp2f(hv[j]);
        SCHED();
#pragma unroll
        for (int j = 0; j < 8; ++j) hv[j] += 1.f;
        SCHED();
#pragma unroll
        for (int j = 0; j < 8; ++j) hv[j] = __builtin_amdgcn_rcpf(hv[j]);
        SCHED();
#pragma unroll
        for (int j = 0; j < 8; ++j) hv[j] *= acc[2 + (j >> 2)][u][j & 3];
        SCHED();
        st_sc1(v4u{pk_bf16(hv[0], hv[1]), pk_bf16(hv[2], hv[3]), pk_bf16(hv[4], hv[5]), pk_bf16(hv[6], hv[7])}, ra2, lane * 16,
               ((2 * pn + ((u >> 2) ^ G)) * NK + 4 * cb + c) * 4096 + (u & 3) * 1024);
        if (u + 1 < 8) asm volatile("" : "+v"(acc[0][u + 1]), "+v"(acc[1][u + 1]) : : "memory");
      }
    }
    CFENCE();
    SCHED();
    STAMP(5);
    ld_carry();
    CFENCE();
    SCHED();
#pragma unroll
    for (int z = 0; z < 16; ++z) {
      const float t = cr * vr[z] - ci * vi[z];
      vi[z] = -cr * vi[z] - ci * vr[z]; vr[z] = t;
    }
    float qr[4], qi[4];
    qr[0] = ar; qi[0] = ai; qr[1] = ar; qi[1] = ai; csq(qr[1], qi[1], 1);
    cmul(qr[1], qi[1], ar, ai, qr[2], qi[2]); qr[3] = qr[1]; qi[3] = qi[1]; csq(qr[3], qi[3], 1);
    const float p4r = qr[3], p4i = qi[3];
    float p64r = p32r, p64i = p32i; csq(p64r, p64i, 1);
    float p512r = p64r, p512i = p64i; csq(p512r, p512i, 3);
    float p1kr = p512r, p1ki = p512i; csq(p1kr, p1ki, 1);
    float sgr, sgi, wgr, wgi;
    cmul((q & 1) ? p16r : 1.f, (q & 1) ? p16i : 0.f, (q & 2) ? p32r : 1.f, (q & 2) ? p32i : 0.f, sgr, sgi);
    cmul((q & 1) ? p512r : 1.f, (q & 1) ? p512i : 0.f, (q & 2) ? p1kr : 1.f, (q & 2) ? p1ki : 0.f, wgr, wgi);
#pragma unroll
    for (int z = 0; z < 16; ++z) asm volatile("" : "+v"(vr[z]), "+v"(vi[z]));
    asm volatile("" : "+v"(sgr), "+v"(sgi), "+v"(wgr), "+v"(wgi), "+v"(p64r), "+v"(p64i));
    SCHED();
    const int want = imin(imax(ck - 8 * q, 0), 8);
    auto carry_ok = [&]() {
      int good = 0;
#pragma unroll
      for (int z = 0; z < 8; ++z) good += ((cv[z][1] ^ tag) | (cv[z][3] ^ ~tag)) == 0u;
      return wall(good == want);
    };
    wait_vm<8>();
    publish(0);
    STAMP(14);
    if (__builtin_expect(!carry_ok(), 0)) {
#pragma nounroll
      for (unsigned spins = 0; spins < SPIN_CAP; ++spins) {
        __builtin_amdgcn_s_sleep(2);
        CFENCE();
        ld_carry();
        if (carry_ok()) break;
      }
    }
    STAMP(6);
    float Gr, Gi;
    {
      float Hr = 0.f, Hi = 0.f, tr, ti;
#pragma unroll
      for (int z = 7; z >= 0; --z) { cmul(Hr, Hi, p64r, p64i, tr, ti); Hr = tr + u2f(cv[z][0]); Hi = ti + u2f(cv[z][2]); }
      cmul(Hr, Hi, wgr, wgi, tr, ti);
      { const auto a = __builtin_amdgcn_permlane16_swap(f2u(tr), f2u(tr), false, false);
        const auto c2 = __builtin_amdgcn_permlane16_swap(f2u(ti), f2u(ti), false, false);
        tr = u2f(a[0]) + u2f(a[1]); ti = u2f(c2[0]) + u2f(c2[1]); }
      { const auto a = __builtin_amdgcn_permlane32_swap(f2u(tr), f2u(tr), false, false);
        const auto c2 = __builtin_amdgcn_permlane32_swap(f2u(ti), f2u(ti), false, false);
        tr = u2f(a[0]) + u2f(a[1]); ti = u2f(c2[0]) + u2f(c2[1]); }
      cmul(tr, ti, sgr, sgi, Hr, Hi);
      cmul(Hr + pr, Hi + pi, cr, ci, Gr, Gi);
    }
    {
      LAS unsigned char* T = L + B_BASE + (c >> 1) * (2 * B_SLOT) + (c & 1) * 4096;
      const int s32 = ((i15 >> 1) & 1) | ((i15 >> 2) & 2), s8 = (i15 & 1) | ((i15 >> 1) & 2);
#pragma unroll
      for (int t = 0; t < 4; ++t) {
        float orr[4], oi[4];
#pragma unroll
        for (int z = 0; z < 4; ++z) {
          orr[z] = __builtin_fmaf(-qi[z], Gi, __builtin_fmaf(qr[z], Gr, vr[4 * t + z]));
          oi[z] = __builtin_fmaf(-qi[z], Gr, __builtin_fmaf(-qr[z], Gi, vi[4 * t + z]));
        }
        if (t < 3) { float tr, ti; cmul(Gr, Gi, p4r, p4i, tr, ti); Gr = tr; Gi = ti; }
        LAS unsigned char* a = T + i15 * 128 + ((t ^ s32) << 5) + ((q ^ s8) << 3);
        *(LAS v2u*)a = v2u{pk_bf16(orr[0], orr[1]), pk_bf16(orr[2], orr[3])};
        *(LAS v2u*)(a + 2048) = v2u{pk_bf16(oi[0], oi[1]), pk_bf16(oi[2], oi[3])};
      }
      const int qq = i15 >> 2, pp = lane & 3;
      const int so = (ck * NK + 56 + 4 * (cb >> 1) + (cb & 1)) * 4096 + (c >> 1) * 512;
#pragma unroll
      for (int t = 0; t < 4; ++t) {
        v2u part[2];
#pragma unroll
        for (int hh = 0; hh < 2; ++hh)
          part[hh] = lds_rdtr(T + (8 * q + 4 * hh + qq) * 128 + ((t ^ ((qq >> 1) | ((q & 1) << 1))) << 5) + ((pp ^ ((qq & 1) | (hh << 1))) << 3));
        st_sc1(v4u{part[0][0], part[0][1], part[1][0], part[1][1]}, ra2, (lane >> 5) * 8192 + (lane & 31) * 16, so + t * 1024);
      }
    }
    SCHED();
    STAMP(7);
    wait_vm<0>();
    publish(1);
    __builtin_amdgcn_s_setprio(0);
    STAMP(12);
  }
  kp = kargs();
  pc = p2_ctx(pWd, pCre, pCim, w, N);
  const int d0 = 2 * pc.sh;
  if (w < 4) {
    lds_wait(L + LDS_PF + 4 * w);
    b2_read(true, L + w * 16384, lane, b2[2]);
    b2_read(true, L + w * 16384 + 8192, lane, b2[3]);
    WAIT_LDS();
    b2_load<0, 0, 8>(pc, vb, 2, w * 16384);
    b2_load<8192, 0, 8>(pc, vb, 3, w * 16384);
    STAMP(13);
  }

  {
    lds_wait4(L + LDS_DONE);
    const int cl = lane & 15, lo = pc.sh ? (pc.g0 - 28) & ~1 : pc.g0 >> 1, hi = pc.sh ? (pc.g0 + pc.nd - 29) | 1 : (pc.g0 + pc.nd - 1) >> 1;
    poll_flags((GAS u64*)(kp->ws + WS_FA2) + ((pc.sh * 16 + (R & 1) * 8 + N) * 16 + (R >> 1)) * 16 + cl, tag64(tag), (cl >= lo && cl <= hi) || cl >= 14, cl < 14);
  }
  STAMP(8);
  if (w >= 4) __builtin_amdgcn_s_setprio(1);
  rsrc_t ra = mk_rsrc(kp->ws + WS_A2 + (R * NK + 2 * pc.g0) * 4096, pc.nd * 8192);
  const int va = ((lane >> 4) & 1) * 1024 + (lane >> 5) * 256 + (lane & 15) * 16;
  v4u af[8];
  auto ld_a = [&](int n, int o) { af[n] = ld_sc1(ra, va + (n & 1) * 2048 + ((n >> 1) & 1) * 512, o + (n >> 2) * 4096); };
  {
    const int o = p2_ds(pc, 0) * 8192;
#pragma unroll
    for (int n = 0; n < 8; ++n) ld_a(n, o);
  }
  SCHED();
  glds<B_BASE + B_SLOT, 0>(mk_rs((const void*)pX, S_ * D_ * 2), w * 1024, ((lane >> 5) * 16 + ((lane >> 3) & 3)) * 1024 + (lane & 7) * 16,
                             (64 * R + 32 * (w & 1) + 4 * (w >> 1)) * 1024 + N * 128);
  const int tok = 64 * R + 32 * (w & 1) + 4 * (w >> 1) + (lane >> 4);
  const v2u dv = __builtin_amdgcn_raw_buffer_load_b64(mk_rsrc(kp->Dsk, D_ * 2), (lane & 15) * 8, N * 128, 0);
  const float ge = u2f(__builtin_amdgcn_raw_buffer_load_b32(mk_rsrc(kp->ws + WS_G, S_ * 4), tok * 4 + (lane & 1) * 64, 0, AUX_SC1));
  SCHED();
  f32x16 acc[2][2];
#pragma unroll
  for (int e = 0; e < 2; ++e)
#pragma unroll
    for (int tt = 0; tt < 2; ++tt) { acc[e][tt] = f32x16{}; asm volatile("" : "+v"(acc[e][tt])); }
  asm volatile("s_nop 1");
#pragma unroll
  for (int d = 0; d < 7; ++d) {
    if ((d >= 2 && d < 6) || w >= 4) {
      v4u rs6; int so6 = 0;
      if (d == 3) { WAIT_LDS(); b2_src(pc, 6 - 3 * pc.sh, rs6, so6); rs6[2] = pc.sh ? 0u : rs6[2]; }
      const int o = d + 1 - d0 < pc.nd ? p2_ds(pc, d + 1 - d0) * 8192 : OOB;
#pragma unroll
      for (int n = 0; n < 8; ++n) {
        mfma32t(acc[0][n & 1], b2[d & 3][n >> 2][n & 2], af[n]);
        mfma32t(acc[1][n & 1], b2[d & 3][n >> 2][(n & 2) + 1], af[n]);
        SCHED();
        if (d == 3) glds<0, 0>(rs6, w * 16384 + n * 1024, (n & 1) ? vb ^ 16 : vb, so6 + n * 8192);
        if (d < 6) ld_a(n, o);
        SCHED();
      }
      if (d == 2 || d == 3) b2_read(w < 4, L + w * 16384 + (d & 1) * 8192, lane, b2[d & 1]);
      if (d == 4) b2_read(false, L + w * 16384, lane, b2[2]);
      SCHED();
    }
  }
  asm volatile("s_nop 15\n\ts_nop 7" : "+v"(acc[0][0]), "+v"(acc[1][0]), "+v"(acc[0][1]), "+v"(acc[1][1]));
  SCHED();
  STAMP(9);
  kp = kargs();
  {
    const int t3 = lane & 3, g3 = ((t3 & 1) * 3) ^ (t3 >> 1), hi = lane >> 5;
#pragma unroll
    for (int T = 0; T < 2; ++T)
#pragma unroll
      for (int E = 0; E < 2; ++E)
#pragma unroll
        for (int g = 0; g < 4; ++g) {
          const int e = 2 * E + (g >> 1), q = 2 * (g & 1) + hi;
          *(LAS f32x4*)(L + w * 16384 + ((2 * T + ((lane >> 4) & 1)) * 4 + e) * 1024 + ((16 * q + (((((lane >> 2) & 3) ^ q) << 2) | (e ^ g3))) << 4)) =
              f32x4{acc[E][T][4 * g], acc[E][T][4 * g + 1], acc[E][T][4 * g + 2], acc[E][T][4 * g + 3]};
        }
  }
  if (b == 255 && w == 0) {
    rsrc_t rf = mk_rsrc(kp->ws + WS_FA2 + 32768, 256 * 8);
#pragma nounroll
    for (unsigned spins = 0; spins < SPIN_CAP; ++spins) {
      CFENCE();
      const v4u f0 = ld_sc1(rf, lane * 32, 0), f1 = ld_sc1(rf, lane * 32, 16);
      if (wall(((f0[0] ^ tag) | (f0[1] ^ ~tag) | (f0[2] ^ tag) | (f0[3] ^ ~tag) | (f1[0] ^ tag) | (f1[1] ^ ~tag) | (f1[2] ^ tag) | (f1[3] ^ ~tag)) == 0u)) break;
      __builtin_amdgcn_s_sleep(8);
    }
    st_flag(kp->ws, WS_EPOCH, tag, lane == 0 ? 0 : OOB);
  }
  WAIT_LDS();
  BARRIER();
  STAMP(10);
  {
    const int q = lane & 3, e = (lane >> 2) & 3, t3 = lane >> 4;
    const int base = ((w >> 1) * 4 + e) * 1024 + q * 256 + ((e ^ ((t3 & 1) * 3) ^ (t3 >> 1)) << 4);
    rsrc_t ro = mk_rsrc(kp->out, S_ * D_ * 4);
    const float go = dpp<0xB1>(ge);
    f32x4 res[2];
#pragma unroll
    for (int hf = 0; hf < 2; ++hf) {
      f32x4 sg = {0.f, 0.f, 0.f, 0.f}, ss = {0.f, 0.f, 0.f, 0.f};
      const int a = base + (((2 * (w & 1) + hf) ^ q) << 6);
#pragma unroll
      for (int ww = 0; ww < 8; ++ww) {
        const f32x4 v = *(LAS f32x4*)(L + ww * 16384 + a);
        if (ww < 4) ss += v; else sg += v;
      }
      const float g = (lane & 1) == hf ? ge : go;
      f32x4 m = ss * (2.f * g) + sg * (1.f - g);
      const v2u xv = *(LAS v2u*)(L + B_BASE + B_SLOT + w * 1024 + hf * 512 + lane * 8);
      m[0] += g * (bf2f(xv[0] & 0xFFFFu) * bf2f(dv[0] & 0xFFFFu)); m[1] += g * (bfhi(xv[0]) * bfhi(dv[0]));
      m[2] += g * (bf2f(xv[1] & 0xFFFFu) * bf2f(dv[1] & 0xFFFFu)); m[3] += g * (bfhi(xv[1]) * bfhi(dv[1]));
      res[hf] = m;
    }
    asm volatile("" : "+v"(res[0]), "+v"(res[1]));
#pragma unroll
    for (int hf = 0; hf < 2; ++hf)
      __builtin_amdgcn_raw_buffer_store_b128(__builtin_bit_cast(v4u, res[hf]), ro, tok * 2048 + (lane & 15) * 16 + hf * 32768 + N * 256, 0, OUT_AUX);
  }
  STAMP(11);
}

#if defined(__HIP_DEVICE_COMPILE__)
#pragma clang attribute pop
#endif

extern "C" void kernel_launch(void* const* d_in, const int* in_sizes, int n_in, void* d_out, int out_size, void* d_ws, size_t ws_size,
                              hipStream_t stream) {
  Params p;
  p.x = (const u16*)d_in[0]; p.Lre = (const u16*)d_in[1]; p.Lim = (const u16*)d_in[2]; p.Bre = (const u16*)d_in[3];
  p.Bim = (const u16*)d_in[4]; p.Cre = (const u16*)d_in[5]; p.Cim = (const u16*)d_in[6]; p.ldt = (const u16*)d_in[7];
  p.Dsk = (const u16*)d_in[8]; p.Wg = (const u16*)d_in[10]; p.Wu = (const u16*)d_in[11]; p.Wd = (const u16*)d_in[12];
  p.gw = (const u16*)d_in[13]; p.out = (float*)d_out; p.ws = (unsigned char*)d_ws;
  (void)hipFuncSetAttribute((const void*)k_fused, hipFuncAttributeMaxDynamicSharedMemorySize, LDS_BYTES);
  k_fused<<<256, 512, LDS_BYTES, stream>>>(p);
}
```

```cpp
#define WARMM
#define WARM0 4
#define PRIO2
#include <hip/hip_runtime.h>
#include <stdint.h>

typedef unsigned short u16;
typedef unsigned long long u64;
typedef unsigned v4u __attribute__((ext_vector_type(4)));
typedef unsigned v2u __attribute__((ext_vector_type(2)));
typedef short s16x4 __attribute__((ext_vector_type(4)));
typedef __bf16 bf16x8 __attribute__((ext_vector_type(8)));
typedef __bf16 bf16x2 __attribute__((ext_vector_type(2)));
typedef float f32x2 __attribute__((ext_vector_type(2)));
typedef float f32x4 __attribute__((ext_vector_type(4)));
typedef float f32x16 __attribute__((ext_vector_type(16)));
#define LAS __attribute__((address_space(3)))
#define GAS __attribute__((address_space(1)))
#define RLX_AGENT __ATOMIC_RELAXED, __HIP_MEMORY_SCOPE_AGENT
typedef __amdgpu_buffer_rsrc_t rsrc_t;

constexpr int S_ = 2048, D_ = 512, P_ = 512, F_ = 1792;
constexpr int NK = 88;
constexpr int AUX_SC1 = 16;
#define OUT_AUX 2
constexpr int WS_EPOCH = 0;
constexpr int WS_G = 8192;
constexpr int WS_FA2 = 65536;
constexpr int CTRL_BYTES = 131072;
constexpr int WS_CARRY = 131072;
constexpr int WS_A2 = 524288;
constexpr int A2_BYTES = 32 * NK * 4096;
constexpr int WS_STAMP = 32 << 20;
constexpr int LDS_BYTES = 160 * 1024;
constexpr int X_SLOT = 16384, W_BASE = 65536, W_SLOT = 16384, B_BASE = 131072, B_SLOT = 8192;
constexpr int LDS_DONE = 155648, LDS_PF = LDS_DONE + 32, LDS_MD = LDS_DONE + 48, LDS_TAG = LDS_DONE + 64;
constexpr int LDS_DUMMY = LDS_DONE + 256, LDS_CONST = LDS_DONE + 512;
constexpr int OOB = 1 << 28;
constexpr int CODE_BYTES = 29 * 1024;
constexpr unsigned SPIN_CAP = 1u << 18;

struct Params {
  const u16 *x, *Wg, *Wu, *Bre, *Bim, *Cre, *Cim, *Wd;
  unsigned char* ws;
  const u16 *Lre, *Lim, *ldt, *Dsk, *gw;
  float* out;
};

#if defined(__HIP_DEVICE_COMPILE__)
#pragma clang attribute push(__attribute__((target("no-packed-fp32-ops"))), apply_to = function)
#endif
__device__ __forceinline__ float u2f(unsigned v) { return __builtin_bit_cast(float, v); }
__device__ __forceinline__ unsigned f2u(float v) { return __builtin_bit_cast(unsigned, v); }
__device__ __forceinline__ bool wall(bool p) { return __builtin_amdgcn_ballot_w64(!p) == 0; }
__device__ __forceinline__ bool wany(bool p) { return __builtin_amdgcn_ballot_w64(p) != 0; }
__device__ __forceinline__ int imin(int a, int b) { return a < b ? a : b; }
__device__ __forceinline__ int imax(int a, int b) { return a > b ? a : b; }
template <int CTRL> __device__ __forceinline__ float dpp(float v) { return u2f(__builtin_amdgcn_mov_dpp(f2u(v), CTRL, 0xF, 0xF, true)); }
__device__ __forceinline__ rsrc_t mk_rsrc(const void* p, int bytes) {
  return __builtin_amdgcn_make_buffer_rsrc((void*)p, 0, bytes, 0x00020000);
}
__device__ __forceinline__ float bf2f(unsigned v) { return u2f(v << 16); }
__device__ __forceinline__ float bfhi(unsigned v) { return u2f(v & 0xFFFF0000u); }
__device__ __forceinline__ unsigned pk_bf16(float lo, float hi) {
  f32x2 t = {lo, hi};
  return __builtin_bit_cast(unsigned, __builtin_convertvector(t, bf16x2));
}
__device__ __forceinline__ void mfma16t(f32x4& c, v4u a, v4u b) {
  asm volatile("v_mfma_f32_16x16x32_bf16 %0, %1, %2, %0" : "+v"(c) : "v"(a), "v"(b));
}
__device__ __forceinline__ void mfma32t(f32x16& c, v4u a, v4u b) {
  asm volatile("v_mfma_f32_32x32x16_bf16 %0, %1, %2, %0" : "+v"(c) : "v"(a), "v"(b));
}
__device__ __forceinline__ v4u lds_rd128(LAS unsigned char* p) { return *(LAS v4u*)p; }
__device__ __forceinline__ v2u lds_rdtr(LAS unsigned char* p) {
  return __builtin_bit_cast(v2u, __builtin_amdgcn_ds_read_tr16_b64_v4i16((LAS s16x4*)p));
}
__device__ __forceinline__ v4u mk_rs(const void* p, unsigned bytes) {
  const u64 a = (u64)p;
  unsigned lo = (unsigned)a, hi = (unsigned)(a >> 32) & 0xFFFFu;
  asm volatile("" : "+s"(lo), "+s"(hi));
  return v4u{lo, hi, bytes, 0x00020000u};
}
template <int LI, int SI> __device__ __forceinline__ void glds(v4u rs, int lds, int voff, int soff) {
  int t;
  asm volatile("s_add_u32 m0, %1, %5\n\ts_add_u32 %0, %4, %6\n\tbuffer_load_dwordx4 %2, %3, %0 offen lds"
               : "=&s"(t) : "s"(lds), "v"(voff), "s"(rs), "s"(soff), "i"(LI), "i"(SI) : "memory", "scc");
}
typedef const __attribute__((address_space(4))) Params* KP;
__device__ __forceinline__ KP kargs() {
  KP k = (KP)__builtin_amdgcn_kernarg_segment_ptr();
  asm volatile("" : "+s"(k));
  return k;
}
__device__ __forceinline__ void warm(v4u rs, int voff, int soff) {
  asm volatile("s_mov_b32 m0, %3\n\ts_nop 0\n\tbuffer_load_dword %0, %1, %2 offen lds" : : "v"(voff), "s"(rs), "s"(soff), "i"(LDS_DUMMY) : "memory");
}
__device__ __forceinline__ v4u ld_sc1(rsrc_t rs, int voff, int soff) { return __builtin_amdgcn_raw_buffer_load_b128(rs, voff, soff, AUX_SC1); }
__device__ __forceinline__ void st_sc1(v4u v, rsrc_t rs, int voff, int soff) { __builtin_amdgcn_raw_buffer_store_b128(v, rs, voff + soff, 0, AUX_SC1); }
__device__ __forceinline__ u64 tag64(unsigned tag) { return ((u64)(~tag) << 32) | tag; }
__device__ __forceinline__ void st_flag(unsigned char* ws, int off, unsigned tag, int voff) {
  __builtin_amdgcn_raw_buffer_store_b64(v2u{tag, ~tag}, mk_rsrc(ws, CTRL_BYTES), voff, off, AUX_SC1);
}
__device__ __forceinline__ unsigned lds_word(LAS unsigned char* p) { return __builtin_amdgcn_readfirstlane(*(volatile LAS unsigned*)p); }
#define BARRIER() asm volatile("s_barrier" ::: "memory")
#define SCHED() __builtin_amdgcn_sched_barrier(0)
#define CFENCE() asm volatile("" ::: "memory")
#define WAIT_LDS() asm volatile("s_waitcnt lgkmcnt(0)" ::: "memory")
template <int N> __device__ __forceinline__ void wait_vm() { asm volatile("s_waitcnt vmcnt(%c0)" ::"i"(N) : "memory"); }

#define STAMP(i) do {} while (0)
#define POLL_SLEEP 4
#define POLL_IDLE 16

__device__ __forceinline__ void poll_flags(GAS u64* f, u64 want, bool care, bool late) {
#pragma nounroll
  for (unsigned spins = 0; spins < SPIN_CAP; ++spins) {
    CFENCE();
    const u64 v = __hip_atomic_load(f, RLX_AGENT);
    if (wall(!care || v == want)) break;
    if (wany(late && v == want)) __builtin_amdgcn_s_sleep(POLL_SLEEP); else __builtin_amdgcn_s_sleep(POLL_IDLE);
  }
  CFENCE();
}
__device__ __forceinline__ void csq(float& r, float& i, int n) {
#pragma unroll
  for (int z = 0; z < n; ++z) { const float t = r * r - i * i; i = 2.f * r * i; r = t; }
}

#define NROT 102
#define CONST_AT 2
constexpr int LASTB = 13;
struct Sched {
  int n;
  int type[40], idx[40];
  int lo[LASTB + 1], hi[LASTB + 1];
  int wait[LASTB + 1];
  bool ok;
};
constexpr Sched make_sched(int kind) {
  Sched s{};
  int cnt[40] = {}, need[40] = {}, avail[40] = {};
  int n = 0;
  auto add = [&](int ty, int ix, int nd, int av) {
    if (kind && ty == 2) return;
    s.type[n] = ty; s.idx[n] = ix; cnt[n] = ty == 2 ? 4 : 3; need[n] = nd < LASTB ? nd : LASTB; avail[n] = av; ++n;
  };
  add(0, 0, 0, -1); add(1, 0, 0, -1); add(2, 0, 0, -1); add(2, 1, 0, -1);
  add(0, 1, 1, -1); add(1, 1, 1, -1); add(2, 2, 1, -1); add(2, 3, 2, -1);
  add(0, 2, 3, -1); add(1, 2, 3, -1);
  add(2, 4, 3, 1); add(2, 5, 4, 1);
  for (int b = 2; b < 12; ++b) {
    const int T = b / 2 + 2;
    add(b & 1, T, 2 * T - 1, kind ? (b & ~1) : b);
    add(2, b + 4, b + 3, b);
  }
  s.n = n;
  s.ok = true;
  const int LIM = 8;
  int endpos[40] = {};
  int next = 0, total = 0;
  for (int b = 0; b <= LASTB; ++b) {
    s.lo[b] = next;
    const int start = total;
    for (;;) {
      if (next >= n || avail[next] > b - 1) break;
      int lastneed = 0;
      for (int i = 0; i < next; ++i) if (need[i] <= b) lastneed = endpos[i];
      if (b < 3 && need[next] > b && total + cnt[next] - lastneed > LIM + 2 * b) break;
      if (b >= 3 && need[next] > b + 1 && total + cnt[next] - start > 11) break;
      total += cnt[next]; endpos[next] = total; ++next;
    }
    s.hi[b] = next;
    if (kind == 0 && b >= WARM0 && b < WARM0 + 3) total += 1;
    int lastneed = 0;
    for (int i = 0; i < next; ++i) if (need[i] <= b) lastneed = endpos[i];
    for (int i = next; i < n; ++i) if (need[i] <= b) s.ok = false;
    s.wait[b] = total - lastneed;
  }
  if (next != n) s.ok = false;
  return s;
}
constexpr Sched SCH[2] = {make_sched(0), make_sched(1)};
static_assert(SCH[0].ok && SCH[1].ok, "every ring item must be issued in time");

struct P2Ctx { u64 a0, a1; int so0, sh, g0, nd, rot; unsigned bytes; };
__device__ __forceinline__ P2Ctx p2_ctx(u64 Wd, u64 Cre, u64 Cim, int w, int N) {
  P2Ctx c;
  const int ssm = w < 4;
  const u64 m = 0 - (u64)ssm;
  c.a0 = (Cre & m) | (Wd & ~m); c.a1 = (Cim & m) | (Wd & ~m);
  c.sh = ssm;
  c.so0 = N * 128 + ssm * (N * (65536 - 128) - 14 * 128);
  c.bytes = F_ * D_ * 2u - ssm * (F_ * D_ * 2u - D_ * P_ * 2u);
  c.g0 = 7 * w - 28 + ssm * (56 - 3 * w);
  c.nd = 7 - 3 * ssm;
#define P2ROT 1
  c.rot = P2ROT == 0 ? 0 : (P2ROT == 1 ? (N & 1) * (3 - ssm) : (P2ROT == 2 ? (N & 3) * (2 - ssm) : (N * c.nd) >> 3));
  return c;
}
__device__ __forceinline__ int p2_ds(const P2Ctx& c, int d) { const int t = d + c.rot; return t >= c.nd ? t - c.nd : t; }
__device__ __forceinline__ void b2_src(const P2Ctx& c, int d, v4u& rs, int& so) {
  const int g = c.g0 + p2_ds(c, d);
  const u64 x0 = c.a0, x1 = c.a1;
  rs = mk_rs((const void*)((g & 1) ? x1 : x0), c.bytes);
  so = c.so0 + (c.sh ? (g >> 1) * 128 : g * 65536);
}
template <int LI, int N0, int N1> __device__ __forceinline__ void b2_rows(v4u rs, int lds, int vb, int so) {
  if constexpr (N0 < N1) { glds<LI, N0 * 8192>(rs, lds, (N0 & 1) ? vb ^ 16 : vb, so); b2_rows<LI + 1024, N0 + 1, N1>(rs, lds, vb, so); }
}
template <int LI, int N0, int N1> __device__ __forceinline__ void b2_load(const P2Ctx& c, int vb, int d, int lds) {
  v4u rs; int so;
  b2_src(c, d, rs, so);
  b2_rows<LI, N0, N1>(rs, lds, vb, so);
}
__device__ __forceinline__ void b2_read(bool ssm, LAS unsigned char* img, int lane, v4u (&f)[2][4]) {
  if (ssm) {
    const int i = lane & 31, sw = ((i & 2) << 1) | ((i >> 1) & 2) | ((i >> 3) & 1);
#pragma unroll
    for (int h = 0; h < 2; ++h)
#pragma unroll
      for (int z = 0; z < 4; ++z) f[h][z] = lds_rd128(img + i * 128 + (((4 * h + (z & 2) + (lane >> 5)) ^ sw) << 4) + (z & 1) * 4096);
  } else {
    const int qq = (lane >> 2) & 3, hi = lane >> 5;
    const int c0 = (((lane >> 4) & 1) * 2 + ((lane >> 1) & 1)) ^ (((qq & 2) << 1) | (hi << 1));
#pragma unroll
    for (int h = 0; h < 2; ++h)
#pragma unroll
      for (int z = 0; z < 4; ++z) {
        LAS unsigned char* a = img + (4 * hi + qq) * 128 + ((c0 ^ ((z & 1) << 2) ^ (z >> 1)) << 4) + (lane & 1) * 8 + h * 4096 + (z >> 1) * 1024;
        const v2u lo = lds_rdtr(a), hi2 = lds_rdtr(a + 2048);
        f[h][z] = v4u{lo[0], lo[1], hi2[0], hi2[1]};
      }
  }
}

struct LdCtx { v4u rx, rw, rb; u64 Wd, Cre, Cim; int lw, lb, vx, vw0, vw1, vbb, rot, lane, vb, vwarm, xcd, ch; mutable unsigned sv, lrb, lib, ldb, ep; };
__device__ __forceinline__ void warm_chunk(u64 x0, u64 x1, u64 x2, int xc, int vwarm, int slot) {
  asm volatile("" : "+s"(xc));
  int i = slot < 6 ? slot + xc : slot - 6 + xc;
  if (slot < 6) { i = i >= 12 ? i - 12 : (i >= 6 ? i - 6 : i); } else { i = 6 + (i >= 10 ? i - 10 : (i >= 5 ? i - 5 : i)); }
  const u64 src = i < 7 ? x0 : (i < 9 ? x1 : x2);
  const unsigned bytes = (unsigned)slot > 10u ? 0u : (i < 7 ? F_ * D_ * 2 : D_ * P_ * 2);
  warm(mk_rs((const void*)src, bytes), vwarm, (i < 7 ? i : ((i - 7) & 1)) * 262144);
}

template <int KIND, int I> __device__ __forceinline__ void ld_item(const LdCtx& c) {
  constexpr int ty = SCH[KIND].type[I], t = SCH[KIND].idx[I];
  int rot = c.rot;
  asm volatile("" : "+s"(rot));
  if constexpr (ty < 2) {
    const int k0 = ((t + rot) & 7) * 128;
    glds<(t % 3) * X_SLOT + 2 * ty * 4096, ty * 65536>(c.rx, c.lw * 1024, c.vx, k0);
    glds<(t % 3) * X_SLOT + (2 * ty + 1) * 4096, ty * 65536 + 8192>(c.rx, c.lw * 1024, c.vx, k0);
    glds<B_BASE + (t % 3) * B_SLOT + ty * 2048, ty * 16384>(c.rb, c.lb, c.vbb, k0);
  } else {
    const int k0 = (((t >> 1) + rot) & 7) * (64 * F_ * 2);
    constexpr int LI = W_BASE + (t & 3) * W_SLOT, SI = (t & 1) * 32 * F_ * 2;
    glds<LI, SI>(c.rw, c.lw * 4096, c.vw0, k0);
    glds<LI + 1024, SI + 4 * F_ * 2>(c.rw, c.lw * 4096, c.vw0, k0);
    glds<LI + 2048, SI + 8 * F_ * 2>(c.rw, c.lw * 4096, c.vw1, k0);
    glds<LI + 3072, SI + 12 * F_ * 2>(c.rw, c.lw * 4096, c.vw1, k0);
  }
}
template <int KIND, int LO, int HI> __device__ __forceinline__ void ld_items(const LdCtx& c) {
  if constexpr (LO < HI) { ld_item<KIND, LO>(c); ld_items<KIND, LO + 1, HI>(c); }
}
template <int KIND, int B> __device__ __forceinline__ void loader_steps(const LdCtx& c, LAS unsigned char* L) {
  if constexpr (B == 0) {
    constexpr int FIRST = KIND ? 2 : 3;
    ld_items<KIND, 0, FIRST>(c);
    KP kp = kargs();
    const v4u r0 = mk_rs(kp->Lre, P_ * 2), r1 = mk_rs(kp->Lim, P_ * 2), r2 = mk_rs(kp->ldt, P_ * 2), r3 = mk_rs(kp->ws + WS_EPOCH, c.lw == 3 ? 4 : 0);
    asm volatile("buffer_load_ushort %0, %4, %5, 0 offen\n\tbuffer_load_ushort %1, %4, %6, 0 offen\n\tbuffer_load_ushort %2, %4, %7, 0 offen\n\t"
                 "buffer_load_dword %3, off, %8, 0 sc1"
                 : "=&v"(c.lrb), "=&v"(c.lib), "=&v"(c.ldb), "=&v"(c.ep) : "v"(c.ch * 2), "s"(r0), "s"(r1), "s"(r2), "s"(r3) : "memory");
    if constexpr (KIND == 0) {
      ld_items<0, 3, 5>(c);
      wait_vm<7>();
      BARRIER();
      ld_items<0, 5, SCH[0].hi[0]>(c);
    } else {
      ld_items<KIND, FIRST, SCH[KIND].hi[0]>(c);
    }
  } else {
    ld_items<KIND, SCH[KIND].lo[B], SCH[KIND].hi[B]>(c);
  }
  if constexpr (KIND == 0 && B == WARM0) warm(mk_rs((const void*)c.Wd, F_ * D_ * 2), c.lane < 16 ? (c.vwarm * 16 + c.lane) * 128 : OOB, 0);
  if constexpr (KIND == 0 && B == WARM0 + 1) warm(mk_rs((const void*)c.Cre, D_ * P_ * 2), c.lane < 5 ? (c.vwarm * 5 + c.lane) * 128 : OOB, 0);
  if constexpr (KIND == 0 && B == WARM0 + 2) warm(mk_rs((const void*)c.Cim, D_ * P_ * 2), c.lane < 5 ? (c.vwarm * 5 + c.lane) * 128 : OOB, 0);
  if constexpr (B == CONST_AT) {
    asm volatile("" : "+v"(c.lrb), "+v"(c.lib), "+v"(c.ldb), "+v"(c.ep) : : "memory");
    const float lr = bf2f(c.lrb), li = bf2f(c.lib);
    float dt = __builtin_amdgcn_exp2f(bf2f(c.ldb) * 1.44269504088896341f);
    dt = bf2f(pk_bf16(dt, 0.f) & 0xFFFFu);
    const float rev = __builtin_amdgcn_fractf(dt * li * 0.15915494309189535f);
    const float ex = __builtin_amdgcn_exp2f(dt * lr * 1.44269504088896341f);
    const float ar = ex * __builtin_amdgcn_cosf(rev), ai = ex * __builtin_amdgcn_sinf(rev);
    const float den = __builtin_amdgcn_rcpf(lr * lr + li * li);
    float p16r = ar, p16i = ai;
    csq(p16r, p16i, 4);
    float p32r = p16r, p32i = p16i;
    csq(p32r, p32i, 1);
    LAS f32x4* d = (LAS f32x4*)(L + LDS_CONST + c.lw * 512 + (c.lane & 15) * 32);
    d[0] = f32x4{ar, ai, ((ar - 1.f) * lr + ai * li) * den, (ai * lr - (ar - 1.f) * li) * den};
    d[1] = f32x4{p16r, p16i, p32r, p32i};
    *(volatile LAS unsigned*)(L + LDS_TAG + 4 * (3 - c.lw)) = c.ep + 1u;
    WAIT_LDS();
  }
  SCHED();
  wait_vm<SCH[KIND].wait[B]>();
  BARRIER();
  if constexpr (B < LASTB) loader_steps<KIND, B + 1>(c, L);
}

__device__ __forceinline__ bool all4(LAS unsigned char* p) {
  const v4u d = *(volatile LAS v4u*)p;
  return __builtin_amdgcn_readfirstlane(d[0] & d[1] & d[2] & d[3]) != 0u;
}
__device__ __forceinline__ void lds_wait4(LAS unsigned char* p) {
#pragma nounroll
  for (unsigned spins = 0; spins < (SPIN_CAP << 4); ++spins) {
    if (all4(p)) break;
    __builtin_amdgcn_s_sleep(2);
  }
  CFENCE();
}
__device__ __forceinline__ void lds_wait(LAS unsigned char* word) {
#pragma nounroll
  for (unsigned spins = 0; spins < (SPIN_CAP << 4); ++spins) {
    if (lds_word(word) != 0u) break;
    __builtin_amdgcn_s_sleep(2);
  }
  CFENCE();
}
__device__ __forceinline__ void cmul(float ar, float ai, float br, float bi, float& cr, float& ci) {
  cr = ar * br - ai * bi; ci = ar * bi + ai * br;
}

template <int V> struct IC { static constexpr int value = V; };
template <int n> __device__ __forceinline__ void p2_first(v4u (&af)[8], v4u ra, int va, int o0, int o1) {
  asm volatile("buffer_load_dwordx4 %0, %1, %2, %3 offen offset:%c4 sc1" : "=v"(af[n]) : "v"(va), "s"(ra), "s"(n < 4 ? o0 : o1), "i"((n & 1) * 2048 + (n & 2) * 256) : "memory");
  if constexpr (n < 7) p2_first<n + 1>(af, ra, va, o0, o1);
}
template <int d, int n>
__device__ __forceinline__ void p2_step(f32x16 (&acc)[2][2], const v4u (&bb)[2][4], v4u (&af)[8], v4u ra, int va, int o0, int o1, int w, v4u rs6, int vb,
                                        int so6) {
  constexpr int WG = d == 3 ? 7 + n : (d == 4 ? 14 - n : (d == 6 ? 7 - n : 7)), WS = d == 5 ? 7 - n : 7;
  if constexpr (d >= 3 && d <= 5 && WS < WG)
    asm volatile("s_waitcnt vmcnt(%c2)\n\ts_cmp_gt_u32 %1, 3\n\ts_cbranch_scc1 1f\n\ts_waitcnt vmcnt(%c3)\n1:" : "+v"(af[n]) : "s"(w), "i"(WG), "i"(WS) : "memory", "scc");
  else
    asm volatile("s_waitcnt vmcnt(%c1)" : "+v"(af[n]) : "i"(WG) : "memory");
  mfma32t(acc[0][n & 1], bb[n >> 2][n & 2], af[n]);
  mfma32t(acc[1][n & 1], bb[n >> 2][(n & 2) + 1], af[n]);
  if constexpr (d == 3) {
    int t;
    asm volatile("s_cmp_lt_u32 %1, 4\n\ts_cbranch_scc1 1f\n\ts_lshl_b32 %0, %1, 14\n\ts_add_u32 m0, %0, %5\n\ts_add_u32 %0, %4, %6\n\t"
                 "buffer_load_dwordx4 %2, %3, %0 offen lds\n1:"
                 : "=&s"(t) : "s"(w), "v"((n & 1) ? vb ^ 16 : vb), "s"(rs6), "s"(so6), "i"(n * 1024), "i"(n * 8192) : "memory", "scc");
  }
  if constexpr (d < 5)
    asm volatile("buffer_load_dwordx4 %0, %1, %2, %3 offen offset:%c4 sc1" : "+v"(af[n]) : "v"(va), "s"(ra), "s"(n < 4 ? o0 : o1), "i"((n & 1) * 2048 + (n & 2) * 256) : "memory");
  if constexpr (d == 5)
    asm volatile("s_cmp_lt_u32 %5, 4\n\ts_cbranch_scc1 1f\n\tbuffer_load_dwordx4 %0, %1, %2, %3 offen offset:%c4 sc1\n1:"
                 : "+v"(af[n]) : "v"(va), "s"(ra), "s"(n < 4 ? o0 : o1), "i"((n & 1) * 2048 + (n & 2) * 256), "s"(w) : "memory", "scc");
  if constexpr (n < 7) p2_step<d, n + 1>(acc, bb, af, ra, va, o0, o1, w, rs6, vb, so6);
}

__global__ __launch_bounds__(512, 2) void k_fused(Params) {
  extern __shared__ __attribute__((aligned(16))) unsigned char lds_[];
  LAS unsigned char* L = (LAS unsigned char*)lds_;
  STAMP(0);
  KP kp = kargs();
  u64 pX = (u64)kp->x, pWg = (u64)kp->Wg, pWu = (u64)kp->Wu, pBre = (u64)kp->Bre, pBim = (u64)kp->Bim, pCre = (u64)kp->Cre, pCim = (u64)kp->Cim, pWd = (u64)kp->Wd;
  const int b = __builtin_amdgcn_workgroup_id_x(), tid = __builtin_amdgcn_workitem_id_x(), lane = tid & 63;
  const int w = __builtin_amdgcn_readfirstlane(tid >> 6);
  const int xcd = b & 7, r = b >> 3;
  warm(mk_rs((const void*)(__builtin_amdgcn_s_getpc() & ~(u64)127), CODE_BYTES), lane < 2 ? lane * 32768 : OOB, (r * 8 + w) * 128);
  const bool light = (r & 7) == 7;
  const int ih = 28 * (xcd & 3) + r - (r >> 3), il = 112 + 4 * (xcd & 3) + (r >> 3);
  const int idx = ih + (il - ih) * (int)light;
  const int pn = 8 * (xcd >> 2) + (idx & 7), cb = idx >> 3;
  const int rot = pn & 6;
  const int R = xcd * 4 + (r & 3), N = r >> 2;
  asm volatile("" : "+s"(pX), "+s"(pWg), "+s"(pWu), "+s"(pBre), "+s"(pBim), "+s"(pCre), "+s"(pCim), "+s"(pWd));
  const int vb = (lane >> 3) * 1024 + (((lane & 7) ^ (((lane >> 2) & 4) | ((lane >> 4) & 2))) << 4);
  v4u b2[4][2][4];
  unsigned tag = 0;
  P2Ctx pc;

  if (__builtin_expect(w >= 4, 1)) {
    LdCtx c;
    c.lw = w - 4; c.rot = rot; c.lane = lane; c.vb = vb; c.sv = 0;
    c.Wd = pWd; c.Cre = pCre; c.Cim = pCim;
    c.vwarm = ((r - (r >> 3)) * 8 + xcd) * 4 + c.lw;
    c.xcd = xcd; c.ch = 32 * cb + 16 * (c.lw >> 1) + (lane & 15);
    const int l3 = lane >> 3, kq = lane >> 4;
    c.rx = mk_rs((const void*)pX, S_ * D_ * 2);
    c.vx = (32 * (c.lw & 1) + 16 * (l3 >> 2) + 4 * (c.lw >> 1) + (l3 & 3)) * 1024 + (((lane & 7) ^ ((4 * c.lw + kq) & 7)) << 4) + pn * 128 * 1024;
    c.rb = mk_rs((const void*)((c.lw >> 1) ? pBim : pBre), P_ * D_ * 2);
    c.vbb = (8 * (c.lw & 1) + l3) * 1024 + (((lane & 7) ^ (4 * (c.lw & 1) + kq)) << 4) + cb * 32 * 1024;
    c.lb = (c.lw >> 1) * 4096 + (c.lw & 1) * 1024;
    c.rw = mk_rs((const void*)((c.lw >> 1) ? pWu : pWg), D_ * F_ * 2);
    c.vw0 = ((c.lw & 1) * 16 + kq) * (F_ * 2) + (((lane & 15) ^ (kq << 2)) << 4) + cb * 256;
    c.vw1 = c.vw0 ^ 32;
    STAMP(1);
    if (__builtin_expect(!light, 1)) loader_steps<0, 0>(c, L);
    else { loader_steps<1, 0>(c, L); BARRIER(); }
    STAMP(2);
    {
      pc = p2_ctx(pWd, pCre, pCim, w, N);
      const int wA = c.lw < 2 ? w : c.lw, wB = wA ^ 4;
      b2_load<0, 0, 8>(pc, vb, 0, wA * 16384);
      b2_load<8192, 0, 8>(pc, vb, 1, wA * 16384);
      const P2Ctx pA = p2_ctx(pWd, pCre, pCim, wA, N), pB = p2_ctx(pWd, pCre, pCim, wB, N);
#pragma unroll
      for (int d = 0; d < 4; ++d) {
        wait_vm<8>();
        b2_read(false, L + wA * 16384 + (d & 1) * 8192, lane, b2[d]);
        WAIT_LDS();
        if (d < 2) b2_load<0, 0, 8>(pc, vb, d + 2, wA * 16384 + (d & 1) * 8192);
        else b2_load<0, 0, 8>(pA, vb, (wA & 4) + (d & 1), wA * 16384 + (d & 1) * 8192);
      }
      STAMP(3);
      lds_wait4(L + LDS_MD);
      b2_load<0, 0, 8>(pB, vb, wB & 4, wB * 16384);
      b2_load<8192, 0, 8>(pB, vb, (wB & 4) + 1, wB * 16384);
      wait_vm<0>();
      *(volatile LAS unsigned*)(L + LDS_PF + 4 * c.lw) = 1u;
      STAMP(4);
    }
    tag = lds_word(L + LDS_TAG);
  } else {
    const int c = w, q = lane >> 4, i15 = lane & 15, G = c & 1;
    const int ch = 32 * cb + 16 * (c >> 1) + i15;
    if (w == 3) {
#pragma unroll
      for (int z = 0; z < 4; ++z) *(volatile LAS v4u*)(L + LDS_DONE + 16 * z) = v4u{0u, 0u, 0u, 0u};
      WAIT_LDS();
    }
    f32x4 k0 = {0.f, 0.f, 0.f, 0.f}, k1 = {0.f, 0.f, 0.f, 0.f};
    unsigned tagv = 0;
    auto rd_const = [&]() {
      k0 = *(LAS f32x4*)(L + LDS_CONST + c * 512 + i15 * 32); k1 = *(LAS f32x4*)(L + LDS_CONST + c * 512 + i15 * 32 + 16);
      tagv = *(volatile LAS unsigned*)(L + LDS_TAG);
    };
    u64 pws = (u64)kp->ws;
    asm volatile("" : "+s"(pws));
    if (w == 2) {
      const int j = lane & 15, g1 = (lane >> 4) & 1, g2 = lane >> 5;
      const int off = j * 65536 + g1 * (WS_A2 + R * NK * 4096) + g2 * (WS_A2 + 2 * pn * NK * 4096) + (j >= 8 - 2 * g1 + 3 * g2) * OOB + g1 * g2 * OOB;
      warm(mk_rs(kp->ws, WS_A2 + A2_BYTES), off, 0);
      warm(mk_rs(kp->out, S_ * D_ * 4), lane < 4 ? lane * 32768 + N * 256 : OOB, R * 131072);
      warm(mk_rs(kp->Dsk, D_ * 2), lane < 1 ? 0 : OOB, 0);
    }
    int xa[2];
#pragma unroll
    for (int ks = 0; ks < 2; ++ks) xa[ks] = i15 * 128 + (((4 * ks + q) ^ ((i15 >> 1) & 7)) << 4);
    asm volatile("" : "+v"(xa[0]), "+v"(xa[1]));
    const int bo = B_BASE + (c >> 1) * 2048;
    f32x4 accs[2][4], acc[4][8];
    v4u bfr[2];
    auto zero_accs = [&]() {
#pragma unroll
      for (int m = 0; m < 2; ++m)
#pragma unroll
        for (int t = 0; t < 4; ++t) { accs[m][t] = f32x4{0.f, 0.f, 0.f, 0.f}; asm volatile("" : "+v"(accs[m][t])); }
    };
    float vr[16], vi[16], t0 = 0.f, t1 = 0.f;
    auto scan_half = [&](int n) {
      const int z = n >> 1;
      if (z == 0) { vr[0] = accs[0][0][0]; vi[0] = accs[1][0][0]; }
      else if ((n & 1) == 0) { t0 = __builtin_fmaf(k0[0], vr[z - 1], accs[0][z >> 2][z & 3]); t1 = __builtin_fmaf(k0[0], vi[z - 1], accs[1][z >> 2][z & 3]); }
      else { vr[z] = __builtin_fmaf(-k0[1], vi[z - 1], t0); vi[z] = __builtin_fmaf(k0[1], vr[z - 1], t1); }
    };
    auto sc = [](int v) { asm volatile("" : "+s"(v)); return v; };

    if (!light) {
      int wa[2];
      {
        const int qq = i15 >> 2, pp = lane & 3;
#pragma unroll
        for (int u = 0; u < 2; ++u)
          wa[u] = W_BASE + (8 * q + qq) * 256 + (((2 * (2 * c + u) + (pp >> 1)) ^ ((qq << 2) | ((q & 1) << 1))) << 4) + 8 * (pp & 1);
      }
      asm volatile("" : "+v"(wa[0]), "+v"(wa[1]));
#pragma unroll
      for (int a = 0; a < 4; ++a)
#pragma unroll
        for (int u = 0; u < 8; ++u) { acc[a][u] = f32x4{0.f, 0.f, 0.f, 0.f}; asm volatile("" : "+v"(acc[a][u])); }
      zero_accs();
      v4u xf[4];
      v2u wf[2][8];
      auto rdw = [&](int s, int z) {
        const int m = z >> 2, u = (z >> 1) & 1, t = z & 1;
        wf[s & 1][z] = lds_rdtr(L + wa[u] + (s & 3) * W_SLOT + m * 8192 + t * 1024);
      };
      STAMP(1);
      BARRIER();
      STAMP(2);
#pragma unroll
      for (int z = 0; z < 8; ++z) rdw(0, z);
#pragma unroll
      for (int z = 0; z < 3; ++z) xf[z] = lds_rd128(L + xa[0] + G * 8192 + z * 2048);
#pragma unroll
      for (int m = 0; m < 2; ++m) bfr[m] = lds_rd128(L + xa[0] + bo + m * 4096);
      auto glu4 = [&](int s, int u, const v4u& x, auto&& between) {
#pragma unroll
        for (int a = 0; a < 4; ++a) {
          const v4u wv = {wf[s & 1][2 * a][0], wf[s & 1][2 * a][1], wf[s & 1][2 * a + 1][0], wf[s & 1][2 * a + 1][1]};
          mfma16t(acc[a][u], wv, x);
          SCHED();
          between(a);
          SCHED();
        }
      };
      int xb = 0, bb = 0;
      auto steps = [&](int s0, int n, const int (&xo)[5]) {
#pragma unroll
        for (int j = 0; j < n; ++j)
#pragma unroll
          for (int u = 0; u < 8; ++u) {
            const int s = s0 + j, jn = j + ((u + 3) >> 3), un = (u + 3) & 7, uw = s == 0 ? u - 4 : u;
            SCHED();
            if (u == (s == 0 ? 4 : 0)) { BARRIER(); SCHED(); }
            glu4(s, u, xf[u & 3], [&](int a) {
              if (a == 0) {
                if ((un & 3) == 0 || (j == 0 && u == 0)) xb = xa[(s0 + jn) & 1] + sc(xo[jn] + ((un >> 2) ^ G) * 8192);
                xf[(u + 3) & 3] = lds_rd128(L + xb + (un & 3) * 2048);
              }
              if ((a == 1 || a == 2) && uw >= 0 && uw < 4) rdw(s + 1, 2 * uw + a - 1);
              if (a == 3 && (u == 4 || u == 5)) {
                if (u == 4) bb = xa[(s + 1) & 1] + sc(bo + (xo[j + 1] >> 1));
                bfr[u - 4] = lds_rd128(L + bb + (u - 4) * 4096);
              }
            });
            if (u < 4) { mfma16t(accs[0][u], xf[u & 3], bfr[0]); mfma16t(accs[1][u], xf[u & 3], bfr[1]); }
          }
      };
      {
        const int xo[5] = {0, 0, 0, 0, 0};
        steps(0, 1, xo);
      }
      int xo0 = 0;
#pragma nounroll
      for (int o = 0; o < 3; ++o) {
        const int xo1 = xo0 == 2 * X_SLOT ? 0 : xo0 + X_SLOT, xo2 = xo1 == 2 * X_SLOT ? 0 : xo1 + X_SLOT;
        const int xo[5] = {xo0, xo1, xo1, xo2, xo2};
        steps(1, 4, xo);
        xo0 = xo2;
      }
      {
        const int xo[5] = {0, X_SLOT, 0, 0, 0};
        steps(1, 1, xo);
      }
      v4u bf2[2];
#pragma unroll
      for (int it = 0; it < 16; ++it) {
        const int s = (it >> 2) & 1, u = (it & 3) + 4 * (it >> 3), nx = it + 3;
        SCHED();
        glu4(s, u, xf[it & 3], [&](int a) {
          if (a == 0 && nx < 16) {
            if ((nx & 3) == 0) xb = xa[(nx >> 2) & 1] + sc(X_SLOT + ((nx >> 3) ^ G) * 8192);
            xf[nx & 3] = lds_rd128(L + xb + (nx & 3) * 2048);
          }
          if (a >= 1 && it < 3 && 3 * it + a - 1 < 8) rdw(3, 3 * it + a - 1);
          if (it == 0 && a == 3) { bb = xa[1] + sc(bo + B_SLOT); bf2[0] = lds_rd128(L + bb); bf2[1] = lds_rd128(L + bb + 4096); }
          if (it == 4 && a == 1) rd_const();
          if (it >= 8) scan_half(4 * (it - 8) + a);
        });
        if (it < 4) { mfma16t(accs[0][u], xf[it & 3], bfr[0]); mfma16t(accs[1][u], xf[it & 3], bfr[1]); }
        else if (it < 8) { mfma16t(accs[0][u], xf[it & 3], bf2[0]); mfma16t(accs[1][u], xf[it & 3], bf2[1]); }
      }
      asm volatile("s_nop 15" : "+v"(acc[0][7]), "+v"(acc[1][7]), "+v"(acc[2][7]), "+v"(acc[3][7]), "+v"(acc[0][6]), "+v"(acc[1][6]), "+v"(acc[2][6]), "+v"(acc[3][6]));
    } else {
      {
      kp = kargs();
      rsrc_t rx = mk_rsrc(kp->x, S_ * D_ * 2), rg = mk_rsrc(kp->gw, D_ * 2), rgo = mk_rsrc(kp->ws + WS_G, S_ * 4);
      const int tk0 = 128 * pn + 64 * (cb - 14) + 16 * c;
      v4u gv[8], xv[2][8];
#pragma unroll
      for (int z = 0; z < 8; ++z) gv[z] = __builtin_amdgcn_raw_buffer_load_b128(rg, (lane & 7) * 16, z * 128, 0);
#pragma unroll
      for (int gi = 0; gi < 2; ++gi)
#pragma unroll
        for (int z = 0; z < 8; ++z) xv[gi][z] = __builtin_amdgcn_raw_buffer_load_b128(rx, (lane >> 3) * 1024 + (lane & 7) * 16, (tk0 + gi * 8) * 1024 + z * 128, 0);
#pragma unroll
      for (int gi = 0; gi < 2; ++gi) {
        float s = 0.f;
#pragma unroll
        for (int z = 0; z < 8; ++z)
#pragma unroll
          for (int y = 0; y < 4; ++y) {
            const unsigned xa_ = xv[gi][z][y], ga = gv[z][y];
            s = __builtin_amdgcn_fdot2_f32_bf16(__builtin_bit_cast(bf16x2, xa_), __builtin_bit_cast(bf16x2, ga), s, false);
          }
        s += dpp<0xB1>(s); s += dpp<0x4E>(s); s += dpp<0x141>(s);
        const float g = __builtin_amdgcn_rcpf(1.f + __builtin_amdgcn_exp2f(s * -1.44269504088896341f));
        __builtin_amdgcn_raw_buffer_store_b32(f2u(g), rgo, (lane & 7) ? OOB : (lane >> 3) * 4, (tk0 + gi * 8) * 4, AUX_SC1);
      }
      }
      STAMP(1);
      zero_accs();
      int xo = 0;
#pragma nounroll
      for (int T = 0; T < 8; ++T) {
#pragma unroll
        for (int ks = 0; ks < 2; ++ks) {
          if (T < 7) BARRIER();
          v4u xf[4];
#pragma unroll
          for (int t = 0; t < 4; ++t) xf[t] = lds_rd128(L + (xa[ks] + xo + G * 8192) + t * 2048);
#pragma unroll
          for (int m = 0; m < 2; ++m) bfr[m] = lds_rd128(L + (xa[ks] + bo + (xo >> 1)) + m * 4096);
#pragma unroll
          for (int t = 0; t < 4; ++t) { mfma16t(accs[0][t], xf[t], bfr[0]); mfma16t(accs[1][t], xf[t], bfr[1]); }
          SCHED();
        }
        xo = xo == 2 * X_SLOT ? 0 : xo + X_SLOT;
      }
      asm volatile("s_nop 15" : "+v"(accs[0][0]), "+v"(accs[1][0]), "+v"(accs[0][1]), "+v"(accs[1][1]), "+v"(accs[0][2]), "+v"(accs[1][2]), "+v"(accs[0][3]), "+v"(accs[1][3]));
      rd_const();
      BARRIER();
#pragma unroll
      for (int n = 0; n < 32; ++n) { scan_half(n); SCHED(); }
    }
    *(volatile LAS unsigned*)(L + LDS_MD + 4 * w) = 1u;
    SCHED();
    STAMP(3);
    tag = __builtin_amdgcn_readfirstlane(tagv);
    const float ar = k0[0], ai = k0[1], cr = k0[2], ci = k0[3], p16r = k1[0], p16i = k1[1], p32r = k1[2], p32i = k1[3];
    __builtin_amdgcn_s_setprio(2);
    rsrc_t ra2 = mk_rsrc((const void*)(pws + WS_A2), A2_BYTES), rcar = mk_rsrc((const void*)(pws + WS_CARRY), 32 * P_ * 16);
    const int ck = 2 * pn + G;
    float pr, pi;
    {
      float tr, ti;
      const auto xr = __builtin_amdgcn_permlane16_swap(f2u(vr[15]), f2u(vr[15]), false, false);
      const auto xi = __builtin_amdgcn_permlane16_swap(f2u(vi[15]), f2u(vi[15]), false, false);
      const float e0r = u2f(xr[0]), e1r = u2f(xr[1]), e0i = u2f(xi[0]), e1i = u2f(xi[1]);
      cmul(e0r, e0i, p16r, p16i, tr, ti); tr += e1r; ti += e1i;
      const auto yr = __builtin_amdgcn_permlane32_swap(f2u(tr), f2u(tr), false, false);
      const auto yi = __builtin_amdgcn_permlane32_swap(f2u(ti), f2u(ti), false, false);
      const float f0r = u2f(yr[0]), f1r = u2f(yr[1]), f0i = u2f(yi[0]), f1i = u2f(yi[1]);
      float Er, Ei;
      cmul(f0r, f0i, p32r, p32i, Er, Ei); Er += f1r; Ei += f1i;
      const float br = (q & 2) ? f0r : 0.f, bi = (q & 2) ? f0i : 0.f;
      cmul(br, bi, p16r, p16i, tr, ti);
      pr = (q & 1) ? tr + e0r : br; pi = (q & 1) ? ti + e0i : bi;
      const v4u gq = {f2u(Er), tag, f2u(Ei), ~tag};
      st_sc1(gq, rcar, q ? OOB : ch * 16, (31 - ck) * (P_ * 16));
    }
    asm volatile("" : "+v"(pr), "+v"(pi) : : "memory");
    SCHED();
    STAMP(4);
    v4u cv[8];
    const int cvo = ch * 16 + (32 - ck + 8 * q) * (P_ * 16);
    auto ld_carry = [&]() {
#pragma unroll
      for (int z = 0; z < 8; ++z) cv[z] = ld_sc1(rcar, cvo, z * (P_ * 16));
    };
    auto publish = [&](int part) {
      *(volatile LAS unsigned*)(L + LDS_DONE + 16 * part + 4 * w) = 1u;
      const bool last = all4(L + LDS_DONE + 16 * part);
      st_flag((unsigned char*)pws, WS_FA2 + part * 32768 + pn * 128 + cb * 8, tag, last && lane < 16 ? lane * 2048 : OOB);
      CFENCE();
    };
    if (!light) {
#pragma unroll
      for (int u = 0; u < 8; ++u) {
        float hv[8];
#pragma unroll
        for (int j = 0; j < 8; ++j) hv[j] = acc[j >> 2][u][j & 3] * -1.44269504088896341f;
        SCHED();
#pragma unroll
        for (int j = 0; j < 8; ++j) hv[j] = __builtin_amdgcn_exp2f(hv[j]);
        SCHED();
#pragma unroll
        for (int j = 0; j < 8; ++j) hv[j] += 1.f;
        SCHED();
#pragma unroll
        for (int j = 0; j < 8; ++j) hv[j] = __builtin_amdgcn_rcpf(hv[j]);
        SCHED();
#pragma unroll
        for (int j = 0; j < 8; ++j) hv[j] *= acc[2 + (j >> 2)][u][j & 3];
        SCHED();
        st_sc1(v4u{pk_bf16(hv[0], hv[1]), pk_bf16(hv[2], hv[3]), pk_bf16(hv[4], hv[5]), pk_bf16(hv[6], hv[7])}, ra2, lane * 16,
               ((2 * pn + ((u >> 2) ^ G)) * NK + 4 * cb + c) * 4096 + (u & 3) * 1024);
        if (u + 1 < 8) asm volatile("" : "+v"(acc[0][u + 1]), "+v"(acc[1][u + 1]) : : "memory");
      }
    }
    CFENCE();
    SCHED();
    STAMP(5);
    ld_carry();
    CFENCE();
    SCHED();
#pragma unroll
    for (int z = 0; z < 16; ++z) {
      const float t = cr * vr[z] - ci * vi[z];
      vi[z] = -cr * vi[z] - ci * vr[z]; vr[z] = t;
    }
    float qr[4], qi[4];
    qr[0] = ar; qi[0] = ai; qr[1] = ar; qi[1] = ai; csq(qr[1], qi[1], 1);
    cmul(qr[1], qi[1], ar, ai, qr[2], qi[2]); qr[3] = qr[1]; qi[3] = qi[1]; csq(qr[3], qi[3], 1);
    const float p4r = qr[3], p4i = qi[3];
    float p64r = p32r, p64i = p32i; csq(p64r, p64i, 1);
    float p512r = p64r, p512i = p64i; csq(p512r, p512i, 3);
    float p1kr = p512r, p1ki = p512i; csq(p1kr, p1ki, 1);
    float sgr, sgi, wgr, wgi;
    cmul((q & 1) ? p16r : 1.f, (q & 1) ? p16i : 0.f, (q & 2) ? p32r : 1.f, (q & 2) ? p32i : 0.f, sgr, sgi);
    cmul((q & 1) ? p512r : 1.f, (q & 1) ? p512i : 0.f, (q & 2) ? p1kr : 1.f, (q & 2) ? p1ki : 0.f, wgr, wgi);
#pragma unroll
    for (int z = 0; z < 16; ++z) asm volatile("" : "+v"(vr[z]), "+v"(vi[z]));
    asm volatile("" : "+v"(sgr), "+v"(sgi), "+v"(wgr), "+v"(wgi), "+v"(p64r), "+v"(p64i));
    SCHED();
    const int want = imin(imax(ck - 8 * q, 0), 8);
    auto carry_ok = [&]() {
      int good = 0;
#pragma unroll
      for (int z = 0; z < 8; ++z) good += ((cv[z][1] ^ tag) | (cv[z][3] ^ ~tag)) == 0u;
      return wall(good == want);
    };
    wait_vm<8>();
    publish(0);
    STAMP(14);
    if (__builtin_expect(!carry_ok(), 0)) {
#pragma nounroll
      for (unsigned spins = 0; spins < SPIN_CAP; ++spins) {
        __builtin_amdgcn_s_sleep(2);
        CFENCE();
        ld_carry();
        if (carry_ok()) break;
      }
    }
    STAMP(6);
    float Gr, Gi;
    {
      float Hr = 0.f, Hi = 0.f, tr, ti;
#pragma unroll
      for (int z = 7; z >= 0; --z) { cmul(Hr, Hi, p64r, p64i, tr, ti); Hr = tr + u2f(cv[z][0]); Hi = ti + u2f(cv[z][2]); }
      cmul(Hr, Hi, wgr, wgi, tr, ti);
      { const auto a = __builtin_amdgcn_permlane16_swap(f2u(tr), f2u(tr), false, false);
        const auto c2 = __builtin_amdgcn_permlane16_swap(f2u(ti), f2u(ti), false, false);
        tr = u2f(a[0]) + u2f(a[1]); ti = u2f(c2[0]) + u2f(c2[1]); }
      { const auto a = __builtin_amdgcn_permlane32_swap(f2u(tr), f2u(tr), false, false);
        const auto c2 = __builtin_amdgcn_permlane32_swap(f2u(ti), f2u(ti), false, false);
        tr = u2f(a[0]) + u2f(a[1]); ti = u2f(c2[0]) + u2f(c2[1]); }
      cmul(tr, ti, sgr, sgi, Hr, Hi);
      cmul(Hr + pr, Hi + pi, cr, ci, Gr, Gi);
    }
    {
      LAS unsigned char* T = L + B_BASE + (c >> 1) * (2 * B_SLOT) + (c & 1) * 4096;
      const int s32 = ((i15 >> 1) & 1) | ((i15 >> 2) & 2), s8 = (i15 & 1) | ((i15 >> 1) & 2);
#pragma unroll
      for (int t = 0; t < 4; ++t) {
        float orr[4], oi[4];
#pragma unroll
        for (int z = 0; z < 4; ++z) {
          orr[z] = __builtin_fmaf(-qi[z], Gi, __builtin_fmaf(qr[z], Gr, vr[4 * t + z]));
          oi[z] = __builtin_fmaf(-qi[z], Gr, __builtin_fmaf(-qr[z], Gi, vi[4 * t + z]));
        }
        if (t < 3) { float tr, ti; cmul(Gr, Gi, p4r, p4i, tr, ti); Gr = tr; Gi = ti; }
        LAS unsigned char* a = T + i15 * 128 + ((t ^ s32) << 5) + ((q ^ s8) << 3);
        *(LAS v2u*)a = v2u{pk_bf16(orr[0], orr[1]), pk_bf16(orr[2], orr[3])};
        *(LAS v2u*)(a + 2048) = v2u{pk_bf16(oi[0], oi[1]), pk_bf16(oi[2], oi[3])};
      }
      const int qq = i15 >> 2, pp = lane & 3;
      const int so = (ck * NK + 56 + 4 * (cb >> 1) + (cb & 1)) * 4096 + (c >> 1) * 512;
#pragma unroll
      for (int t = 0; t < 4; ++t) {
        v2u part[2];
#pragma unroll
        for (int hh = 0; hh < 2; ++hh)
          part[hh] = lds_rdtr(T + (8 * q + 4 * hh + qq) * 128 + ((t ^ ((qq >> 1) | ((q & 1) << 1))) << 5) + ((pp ^ ((qq & 1) | (hh << 1))) << 3));
        st_sc1(v4u{part[0][0], part[0][1], part[1][0], part[1][1]}, ra2, (lane >> 5) * 8192 + (lane & 31) * 16, so + t * 1024);
      }
    }
    SCHED();
    STAMP(7);
    wait_vm<0>();
    publish(1);
    STAMP(12);
  }
  kp = kargs();
  pc = p2_ctx(pWd, pCre, pCim, w, N);
  const int d0 = 2 * pc.sh;
  if (w < 4) {
    lds_wait(L + LDS_PF + 4 * w);
    b2_read(true, L + w * 16384, lane, b2[2]);
    b2_read(true, L + w * 16384 + 8192, lane, b2[3]);
    WAIT_LDS();
    b2_load<0, 0, 8>(pc, vb, 2, w * 16384);
    b2_load<8192, 0, 8>(pc, vb, 3, w * 16384);
    STAMP(13);
  }

  {
    lds_wait4(L + LDS_DONE);
    const int cl = lane & 15, lo = pc.sh ? (pc.g0 - 28) & ~1 : pc.g0 >> 1, hi = pc.sh ? (pc.g0 + pc.nd - 29) | 1 : (pc.g0 + pc.nd - 1) >> 1;
    poll_flags((GAS u64*)(kp->ws + WS_FA2) + ((pc.sh * 16 + (R & 1) * 8 + N) * 16 + (R >> 1)) * 16 + cl, tag64(tag), (cl >= lo && cl <= hi) || cl >= 14, cl < 14);
  }
  STAMP(8);
  if (w >= 4) __builtin_amdgcn_s_setprio(1);
  glds<B_BASE + B_SLOT, 0>(mk_rs((const void*)pX, S_ * D_ * 2), w * 1024, ((lane >> 5) * 16 + ((lane >> 3) & 3)) * 1024 + (lane & 7) * 16,
                             (64 * R + 32 * (w & 1) + 4 * (w >> 1)) * 1024 + N * 128);
  const int tok = 64 * R + 32 * (w & 1) + 4 * (w >> 1) + (lane >> 4);
  const v2u dv = __builtin_amdgcn_raw_buffer_load_b64(mk_rsrc(kp->Dsk, D_ * 2), (lane & 15) * 8, N * 128, 0);
  const float ge = u2f(__builtin_amdgcn_raw_buffer_load_b32(mk_rsrc(kp->ws + WS_G, S_ * 4), tok * 4 + (lane & 1) * 64, 0, AUX_SC1));
  SCHED();
  const v4u ra = mk_rs(kp->ws + WS_A2 + (R * NK + 2 * pc.g0) * 4096, pc.nd * 8192);
  const int va = ((lane >> 4) & 1) * 1024 + (lane >> 5) * 256 + (lane & 15) * 16;
  v4u af[8];
  {
    const int o = p2_ds(pc, 0) * 8192;
    p2_first<0>(af, ra, va, o, o + 4096);
  }
  SCHED();
#define CPRIO 0
  if (w < 4) __builtin_amdgcn_s_setprio(CPRIO);
  f32x16 acc[2][2];
#pragma unroll
  for (int e = 0; e < 2; ++e)
#pragma unroll
    for (int tt = 0; tt < 2; ++tt) { acc[e][tt] = f32x16{}; asm volatile("" : "+v"(acc[e][tt])); }
  asm volatile("s_nop 1");
  auto step = [&](auto Dc) {
    constexpr int d = decltype(Dc)::value;
    v4u rs6 = ra; int so6 = 0;
    if (d == 3) { WAIT_LDS(); b2_src(pc, 6 - 3 * pc.sh, rs6, so6); }
    const int o = p2_ds(pc, d + 1 - d0) * 8192;
    p2_step<d, 0>(acc, b2[d & 3], af, ra, va, o, o + 4096, w, rs6, vb, so6);
    if (d == 2 || d == 3) b2_read(w < 4, L + w * 16384 + (d & 1) * 8192, lane, b2[d & 1]);
    if (d == 4 && w >= 4) b2_read(false, L + w * 16384, lane, b2[2]);
    SCHED();
  };
  if (w >= 4) { step(IC<0>{}); step(IC<1>{}); }
  step(IC<2>{}); step(IC<3>{}); step(IC<4>{}); step(IC<5>{});
  if (w >= 4) step(IC<6>{});
  asm volatile("s_nop 15\n\ts_nop 7" : "+v"(acc[0][0]), "+v"(acc[1][0]), "+v"(acc[0][1]), "+v"(acc[1][1]));
  SCHED();
  STAMP(9);
  kp = kargs();
  {
    const int t3 = lane & 3, g3 = ((t3 & 1) * 3) ^ (t3 >> 1), hi = lane >> 5;
#pragma unroll
    for (int T = 0; T < 2; ++T)
#pragma unroll
      for (int E = 0; E < 2; ++E)
#pragma unroll
        for (int g = 0; g < 4; ++g) {
          const int e = 2 * E + (g >> 1), q = 2 * (g & 1) + hi;
          *(LAS f32x4*)(L + w * 16384 + ((2 * T + ((lane >> 4) & 1)) * 4 + e) * 1024 + ((16 * q + (((((lane >> 2) & 3) ^ q) << 2) | (e ^ g3))) << 4)) =
              f32x4{acc[E][T][4 * g], acc[E][T][4 * g + 1], acc[E][T][4 * g + 2], acc[E][T][4 * g + 3]};
        }
  }
  if (b == 255 && w == 0) {
    rsrc_t rf = mk_rsrc(kp->ws + WS_FA2 + 32768, 256 * 8);
#pragma nounroll
    for (unsigned spins = 0; spins < SPIN_CAP; ++spins) {
      CFENCE();
      const v4u f0 = ld_sc1(rf, lane * 32, 0), f1 = ld_sc1(rf, lane * 32, 16);
      if (wall(((f0[0] ^ tag) | (f0[1] ^ ~tag) | (f0[2] ^ tag) | (f0[3] ^ ~tag) | (f1[0] ^ tag) | (f1[1] ^ ~tag) | (f1[2] ^ tag) | (f1[3] ^ ~tag)) == 0u)) break;
      __builtin_amdgcn_s_sleep(8);
    }
    st_flag(kp->ws, WS_EPOCH, tag, lane == 0 ? 0 : OOB);
  }
  WAIT_LDS();
  BARRIER();
  STAMP(10);
  {
    const int q = lane & 3, e = (lane >> 2) & 3, t3 = lane >> 4;
    const int base = ((w >> 1) * 4 + e) * 1024 + q * 256 + ((e ^ ((t3 & 1) * 3) ^ (t3 >> 1)) << 4);
    rsrc_t ro = mk_rsrc(kp->out, S_ * D_ * 4);
    const float go = dpp<0xB1>(ge);
    f32x4 res[2];
#pragma unroll
    for (int hf = 0; hf < 2; ++hf) {
      f32x4 sg = {0.f, 0.f, 0.f, 0.f}, ss = {0.f, 0.f, 0.f, 0.f};
      const int a = base + (((2 * (w & 1) + hf) ^ q) << 6);
#pragma unroll
      for (int ww = 0; ww < 8; ++ww) {
        const f32x4 v = *(LAS f32x4*)(L + ww * 16384 + a);
        if (ww < 4) ss += v; else sg += v;
      }
      const float g = (lane & 1) == hf ? ge : go;
      f32x4 m = ss * (2.f * g) + sg * (1.f - g);
      const v2u xv = *(LAS v2u*)(L + B_BASE + B_SLOT + w * 1024 + hf * 512 + lane * 8);
      m[0] += g * (bf2f(xv[0] & 0xFFFFu) * bf2f(dv[0] & 0xFFFFu)); m[1] += g * (bfhi(xv[0]) * bfhi(dv[0]));
      m[2] += g * (bf2f(xv[1] & 0xFFFFu) * bf2f(dv[1] & 0xFFFFu)); m[3] += g * (bfhi(xv[1]) * bfhi(dv[1]));
      res[hf] = m;
    }
    asm volatile("" : "+v"(res[0]), "+v"(res[1]));
#pragma unroll
    for (int hf = 0; hf < 2; ++hf)
      __builtin_amdgcn_raw_buffer_store_b128(__builtin_bit_cast(v4u, res[hf]), ro, tok * 2048 + (lane & 15) * 16 + hf * 32768 + N * 256, 0, OUT_AUX);
  }
  STAMP(11);
}

#if defined(__HIP_DEVICE_COMPILE__)
#pragma clang attribute pop
#endif

extern "C" void kernel_launch(void* const* d_in, const int* in_sizes, int n_in, void* d_out, int out_size, void* d_ws, size_t ws_size,
                              hipStream_t stream) {
  Params p;
  p.x = (const u16*)d_in[0]; p.Lre = (const u16*)d_in[1]; p.Lim = (const u16*)d_in[2]; p.Bre = (const u16*)d_in[3];
  p.Bim = (const u16*)d_in[4]; p.Cre = (const u16*)d_in[5]; p.Cim = (const u16*)d_in[6]; p.ldt = (const u16*)d_in[7];
  p.Dsk = (const u16*)d_in[8]; p.Wg = (const u16*)d_in[10]; p.Wu = (const u16*)d_in[11]; p.Wd = (const u16*)d_in[12];
  p.gw = (const u16*)d_in[13]; p.out = (float*)d_out; p.ws = (unsigned char*)d_ws;
  (void)hipFuncSetAttribute((const void*)k_fused, hipFuncAttributeMaxDynamicSharedMemorySize, LDS_BYTES);
  k_fused<<<256, 512, LDS_BYTES, stream>>>(p);
}
```

```cpp
#define PROBE 0
#define PB 130
#define PW 0
#define EXTRA 0
#define WARMM
#define WARM0 4
#define PRIO2
#include <hip/hip_runtime.h>
#include <stdint.h>

typedef unsigned short u16;
typedef unsigned long long u64;
typedef unsigned v4u __attribute__((ext_vector_type(4)));
typedef unsigned v2u __attribute__((ext_vector_type(2)));
typedef short s16x4 __attribute__((ext_vector_type(4)));
typedef __bf16 bf16x8 __attribute__((ext_vector_type(8)));
typedef __bf16 bf16x2 __attribute__((ext_vector_type(2)));
typedef float f32x2 __attribute__((ext_vector_type(2)));
typedef float f32x4 __attribute__((ext_vector_type(4)));
typedef float f32x16 __attribute__((ext_vector_type(16)));
#define LAS __attribute__((address_space(3)))
#define GAS __attribute__((address_space(1)))
#define RLX_AGENT __ATOMIC_RELAXED, __HIP_MEMORY_SCOPE_AGENT
typedef __amdgpu_buffer_rsrc_t rsrc_t;

constexpr int S_ = 2048, D_ = 512, P_ = 512, F_ = 1792;
constexpr int NK = 88;
constexpr int AUX_SC1 = 16;
#define OUT_AUX 2
constexpr int WS_EPOCH = 0;
constexpr int WS_G = 8192;
constexpr int WS_FA2 = 65536;
constexpr int CTRL_BYTES = 131072;
constexpr int WS_CARRY = 131072;
constexpr int WS_A2 = 524288;
constexpr int A2_BYTES = 32 * NK * 4096;
constexpr int WS_STAMP = 32 << 20;
constexpr int LDS_BYTES = 160 * 1024;
constexpr int X_SLOT = 16384, W_BASE = 65536, W_SLOT = 16384, B_BASE = 131072, B_SLOT = 8192;
constexpr int LDS_DONE = 155648, LDS_PF = LDS_DONE + 32, LDS_MD = LDS_DONE + 48, LDS_TAG = LDS_DONE + 64;
constexpr int LDS_DUMMY = LDS_DONE + 256, LDS_CONST = LDS_DONE + 512;
constexpr int OOB = 1 << 28;
constexpr int CODE_BYTES = 29 * 1024;
constexpr unsigned SPIN_CAP = 1u << 18;

struct Params {
  const u16 *x, *Wg, *Wu, *Bre, *Bim, *Cre, *Cim, *Wd;
  unsigned char* ws;
  const u16 *Lre, *Lim, *ldt, *Dsk, *gw;
  float* out;
};

#if defined(__HIP_DEVICE_COMPILE__)
#pragma clang attribute push(__attribute__((target("no-packed-fp32-ops"))), apply_to = function)
#endif
__device__ __forceinline__ float u2f(unsigned v) { return __builtin_bit_cast(float, v); }
__device__ __forceinline__ unsigned f2u(float v) { return __builtin_bit_cast(unsigned, v); }
__device__ __forceinline__ bool wall(bool p) { return __builtin_amdgcn_ballot_w64(!p) == 0; }
__device__ __forceinline__ bool wany(bool p) { return __builtin_amdgcn_ballot_w64(p) != 0; }
__device__ __forceinline__ int imin(int a, int b) { return a < b ? a : b; }
__device__ __forceinline__ int imax(int a, int b) { return a > b ? a : b; }
template <int CTRL> __device__ __forceinline__ float dpp(float v) { return u2f(__builtin_amdgcn_mov_dpp(f2u(v), CTRL, 0xF, 0xF, true)); }
__device__ __forceinline__ rsrc_t mk_rsrc(const void* p, int bytes) {
  return __builtin_amdgcn_make_buffer_rsrc((void*)p, 0, bytes, 0x00020000);
}
__device__ __forceinline__ float bf2f(unsigned v) { return u2f(v << 16); }
__device__ __forceinline__ float bfhi(unsigned v) { return u2f(v & 0xFFFF0000u); }
__device__ __forceinline__ unsigned pk_bf16(float lo, float hi) {
  f32x2 t = {lo, hi};
  return __builtin_bit_cast(unsigned, __builtin_convertvector(t, bf16x2));
}
__device__ __forceinline__ void mfma16t(f32x4& c, v4u a, v4u b) {
  asm volatile("v_mfma_f32_16x16x32_bf16 %0, %1, %2, %0" : "+v"(c) : "v"(a), "v"(b));
}
__device__ __forceinline__ void mfma32t(f32x16& c, v4u a, v4u b) {
  asm volatile("v_mfma_f32_32x32x16_bf16 %0, %1, %2, %0" : "+v"(c) : "v"(a), "v"(b));
}
__device__ __forceinline__ v4u lds_rd128(LAS unsigned char* p) { return *(LAS v4u*)p; }
__device__ __forceinline__ v2u lds_rdtr(LAS unsigned char* p) {
  return __builtin_bit_cast(v2u, __builtin_amdgcn_ds_read_tr16_b64_v4i16((LAS s16x4*)p));
}
__device__ __forceinline__ v4u mk_rs(const void* p, unsigned bytes) {
  const u64 a = (u64)p;
  unsigned lo = (unsigned)a, hi = (unsigned)(a >> 32) & 0xFFFFu;
  asm volatile("" : "+s"(lo), "+s"(hi));
  return v4u{lo, hi, bytes, 0x00020000u};
}
template <int LI, int SI> __device__ __forceinline__ void glds(v4u rs, int lds, int voff, int soff) {
  int t;
  asm volatile("s_add_u32 m0, %1, %5\n\ts_add_u32 %0, %4, %6\n\tbuffer_load_dwordx4 %2, %3, %0 offen lds"
               : "=&s"(t) : "s"(lds), "v"(voff), "s"(rs), "s"(soff), "i"(LI), "i"(SI) : "memory", "scc");
}
typedef const __attribute__((address_space(4))) Params* KP;
__device__ __forceinline__ KP kargs() {
  KP k = (KP)__builtin_amdgcn_kernarg_segment_ptr();
  asm volatile("" : "+s"(k));
  return k;
}
__device__ __forceinline__ void warm(v4u rs, int voff, int soff) {
  asm volatile("s_mov_b32 m0, %3\n\ts_nop 0\n\tbuffer_load_dword %0, %1, %2 offen lds" : : "v"(voff), "s"(rs), "s"(soff), "i"(LDS_DUMMY) : "memory");
}
__device__ __forceinline__ v4u ld_sc1(rsrc_t rs, int voff, int soff) { return __builtin_amdgcn_raw_buffer_load_b128(rs, voff, soff, AUX_SC1); }
__device__ __forceinline__ void st_sc1(v4u v, rsrc_t rs, int voff, int soff) { __builtin_amdgcn_raw_buffer_store_b128(v, rs, voff + soff, 0, AUX_SC1); }
__device__ __forceinline__ u64 tag64(unsigned tag) { return ((u64)(~tag) << 32) | tag; }
__device__ __forceinline__ void st_flag(unsigned char* ws, int off, unsigned tag, int voff) {
  __builtin_amdgcn_raw_buffer_store_b64(v2u{tag, ~tag}, mk_rsrc(ws, CTRL_BYTES), voff, off, AUX_SC1);
}
__device__ __forceinline__ unsigned lds_word(LAS unsigned char* p) { return __builtin_amdgcn_readfirstlane(*(volatile LAS unsigned*)p); }
#define BARRIER() asm volatile("s_barrier" ::: "memory")
#define SCHED() __builtin_amdgcn_sched_barrier(0)
#define CFENCE() asm volatile("" ::: "memory")
#define WAIT_LDS() asm volatile("s_waitcnt lgkmcnt(0)" ::: "memory")
template <int N> __device__ __forceinline__ void wait_vm() { asm volatile("s_waitcnt vmcnt(%c0)" ::"i"(N) : "memory"); }

#define STAMP(i) do { if ((i) == PROBE) stamp_[i] = __builtin_amdgcn_s_memrealtime(); } while (0)
#define POLL_SLEEP 4
#define POLL_IDLE 16

__device__ __forceinline__ void poll_flags(GAS u64* f, u64 want, bool care, bool late) {
#pragma nounroll
  for (unsigned spins = 0; spins < SPIN_CAP; ++spins) {
    CFENCE();
    const u64 v = __hip_atomic_load(f, RLX_AGENT);
    if (wall(!care || v == want)) break;
    if (wany(late && v == want)) __builtin_amdgcn_s_sleep(POLL_SLEEP); else __builtin_amdgcn_s_sleep(POLL_IDLE);
  }
  CFENCE();
}
__device__ __forceinline__ void csq(float& r, float& i, int n) {
#pragma unroll
  for (int z = 0; z < n; ++z) { const float t = r * r - i * i; i = 2.f * r * i; r = t; }
}

#define NROT 102
#define CONST_AT 2
constexpr int LASTB = 13;
struct Sched {
  int n;
  int type[40], idx[40];
  int lo[LASTB + 1], hi[LASTB + 1];
  int wait[LASTB + 1];
  bool ok;
};
constexpr Sched make_sched(int kind) {
  Sched s{};
  int cnt[40] = {}, need[40] = {}, avail[40] = {};
  int n = 0;
  auto add = [&](int ty, int ix, int nd, int av) {
    if (kind && ty == 2) return;
    s.type[n] = ty; s.idx[n] = ix; cnt[n] = ty == 2 ? 4 : 3; need[n] = nd < LASTB ? nd : LASTB; avail[n] = av; ++n;
  };
  add(0, 0, 0, -1); add(1, 0, 0, -1); add(2, 0, 0, -1); add(2, 1, 0, -1);
  add(0, 1, 1, -1); add(1, 1, 1, -1); add(2, 2, 1, -1); add(2, 3, 2, -1);
  add(0, 2, 3, -1); add(1, 2, 3, -1);
  add(2, 4, 3, 1); add(2, 5, 4, 1);
  for (int b = 2; b < 12; ++b) {
    const int T = b / 2 + 2;
    add(b & 1, T, 2 * T - 1, kind ? (b & ~1) : b);
    add(2, b + 4, b + 3, b);
  }
  s.n = n;
  s.ok = true;
  const int LIM = 8;
  int endpos[40] = {};
  int next = 0, total = 0;
  for (int b = 0; b <= LASTB; ++b) {
    s.lo[b] = next;
    const int start = total;
    for (;;) {
      if (next >= n || avail[next] > b - 1) break;
      int lastneed = 0;
      for (int i = 0; i < next; ++i) if (need[i] <= b) lastneed = endpos[i];
      if (b < 3 && need[next] > b && total + cnt[next] - lastneed > LIM + 2 * b) break;
      if (b >= 3 && need[next] > b + 1 && total + cnt[next] - start > 11) break;
      total += cnt[next]; endpos[next] = total; ++next;
    }
    s.hi[b] = next;
    if (kind == 0 && b >= WARM0 && b < WARM0 + 3) total += 1;
    int lastneed = 0;
    for (int i = 0; i < next; ++i) if (need[i] <= b) lastneed = endpos[i];
    for (int i = next; i < n; ++i) if (need[i] <= b) s.ok = false;
    s.wait[b] = total - lastneed;
  }
  if (next != n) s.ok = false;
  return s;
}
constexpr Sched SCH[2] = {make_sched(0), make_sched(1)};
static_assert(SCH[0].ok && SCH[1].ok, "every ring item must be issued in time");

struct P2Ctx { u64 a0, a1; int so0, sh, g0, nd, rot; unsigned bytes; };
__device__ __forceinline__ P2Ctx p2_ctx(u64 Wd, u64 Cre, u64 Cim, int w, int N) {
  P2Ctx c;
  const int ssm = w < 4;
  const u64 m = 0 - (u64)ssm;
  c.a0 = (Cre & m) | (Wd & ~m); c.a1 = (Cim & m) | (Wd & ~m);
  c.sh = ssm;
  c.so0 = N * 128 + ssm * (N * (65536 - 128) - 14 * 128);
  c.bytes = F_ * D_ * 2u - ssm * (F_ * D_ * 2u - D_ * P_ * 2u);
  c.g0 = 7 * w - 28 + ssm * (56 - 3 * w);
  c.nd = 7 - 3 * ssm;
#define P2ROT 1
  c.rot = P2ROT == 0 ? 0 : (P2ROT == 1 ? (N & 1) * (3 - ssm) : (P2ROT == 2 ? (N & 3) * (2 - ssm) : (N * c.nd) >> 3));
  return c;
}
__device__ __forceinline__ int p2_ds(const P2Ctx& c, int d) { const int t = d + c.rot; return t >= c.nd ? t - c.nd : t; }
__device__ __forceinline__ void b2_src(const P2Ctx& c, int d, v4u& rs, int& so) {
  const int g = c.g0 + p2_ds(c, d);
  const u64 x0 = c.a0, x1 = c.a1;
  rs = mk_rs((const void*)((g & 1) ? x1 : x0), c.bytes);
  so = c.so0 + (c.sh ? (g >> 1) * 128 : g * 65536);
}
template <int LI, int N0, int N1> __device__ __forceinline__ void b2_rows(v4u rs, int lds, int vb, int so) {
  if constexpr (N0 < N1) { glds<LI, N0 * 8192>(rs, lds, (N0 & 1) ? vb ^ 16 : vb, so); b2_rows<LI + 1024, N0 + 1, N1>(rs, lds, vb, so); }
}
template <int LI, int N0, int N1> __device__ __forceinline__ void b2_load(const P2Ctx& c, int vb, int d, int lds) {
  v4u rs; int so;
  b2_src(c, d, rs, so);
  b2_rows<LI, N0, N1>(rs, lds, vb, so);
}
__device__ __forceinline__ void b2_read(bool ssm, LAS unsigned char* img, int lane, v4u (&f)[2][4]) {
  if (ssm) {
    const int i = lane & 31, sw = ((i & 2) << 1) | ((i >> 1) & 2) | ((i >> 3) & 1);
#pragma unroll
    for (int h = 0; h < 2; ++h)
#pragma unroll
      for (int z = 0; z < 4; ++z) f[h][z] = lds_rd128(img + i * 128 + (((4 * h + (z & 2) + (lane >> 5)) ^ sw) << 4) + (z & 1) * 4096);
  } else {
    const int qq = (lane >> 2) & 3, hi = lane >> 5;
    const int c0 = (((lane >> 4) & 1) * 2 + ((lane >> 1) & 1)) ^ (((qq & 2) << 1) | (hi << 1));
#pragma unroll
    for (int h = 0; h < 2; ++h)
#pragma unroll
      for (int z = 0; z < 4; ++z) {
        LAS unsigned char* a = img + (4 * hi + qq) * 128 + ((c0 ^ ((z & 1) << 2) ^ (z >> 1)) << 4) + (lane & 1) * 8 + h * 4096 + (z >> 1) * 1024;
        const v2u lo = lds_rdtr(a), hi2 = lds_rdtr(a + 2048);
        f[h][z] = v4u{lo[0], lo[1], hi2[0], hi2[1]};
      }
  }
}

struct LdCtx { v4u rx, rw, rb; u64 Wd, Cre, Cim; int lw, lb, vx, vw0, vw1, vbb, rot, lane, vb, vwarm, xcd, ch; mutable unsigned sv, lrb, lib, ldb, ep; };
__device__ __forceinline__ void warm_chunk(u64 x0, u64 x1, u64 x2, int xc, int vwarm, int slot) {
  asm volatile("" : "+s"(xc));
  int i = slot < 6 ? slot + xc : slot - 6 + xc;
  if (slot < 6) { i = i >= 12 ? i - 12 : (i >= 6 ? i - 6 : i); } else { i = 6 + (i >= 10 ? i - 10 : (i >= 5 ? i - 5 : i)); }
  const u64 src = i < 7 ? x0 : (i < 9 ? x1 : x2);
  const unsigned bytes = (unsigned)slot > 10u ? 0u : (i < 7 ? F_ * D_ * 2 : D_ * P_ * 2);
  warm(mk_rs((const void*)src, bytes), vwarm, (i < 7 ? i : ((i - 7) & 1)) * 262144);
}

template <int KIND, int I> __device__ __forceinline__ void ld_item(const LdCtx& c) {
  constexpr int ty = SCH[KIND].type[I], t = SCH[KIND].idx[I];
  int rot = c.rot;
  asm volatile("" : "+s"(rot));
  if constexpr (ty < 2) {
    const int k0 = ((t + rot) & 7) * 128;
    glds<(t % 3) * X_SLOT + 2 * ty * 4096, ty * 65536>(c.rx, c.lw * 1024, c.vx, k0);
    glds<(t % 3) * X_SLOT + (2 * ty + 1) * 4096, ty * 65536 + 8192>(c.rx, c.lw * 1024, c.vx, k0);
    glds<B_BASE + (t % 3) * B_SLOT + ty * 2048, ty * 16384>(c.rb, c.lb, c.vbb, k0);
  } else {
    const int k0 = (((t >> 1) + rot) & 7) * (64 * F_ * 2);
    constexpr int LI = W_BASE + (t & 3) * W_SLOT, SI = (t & 1) * 32 * F_ * 2;
    glds<LI, SI>(c.rw, c.lw * 4096, c.vw0, k0);
    glds<LI + 1024, SI + 4 * F_ * 2>(c.rw, c.lw * 4096, c.vw0, k0);
    glds<LI + 2048, SI + 8 * F_ * 2>(c.rw, c.lw * 4096, c.vw1, k0);
    glds<LI + 3072, SI + 12 * F_ * 2>(c.rw, c.lw * 4096, c.vw1, k0);
  }
}
template <int KIND, int LO, int HI> __device__ __forceinline__ void ld_items(const LdCtx& c) {
  if constexpr (LO < HI) { ld_item<KIND, LO>(c); ld_items<KIND, LO + 1, HI>(c); }
}
template <int KIND, int B> __device__ __forceinline__ void loader_steps(const LdCtx& c, LAS unsigned char* L) {
  if constexpr (B == 0) {
    constexpr int FIRST = KIND ? 2 : 3;
    ld_items<KIND, 0, FIRST>(c);
    KP kp = kargs();
    const v4u r0 = mk_rs(kp->Lre, P_ * 2), r1 = mk_rs(kp->Lim, P_ * 2), r2 = mk_rs(kp->ldt, P_ * 2), r3 = mk_rs(kp->ws + WS_EPOCH, c.lw == 3 ? 4 : 0);
    asm volatile("buffer_load_ushort %0, %4, %5, 0 offen\n\tbuffer_load_ushort %1, %4, %6, 0 offen\n\tbuffer_load_ushort %2, %4, %7, 0 offen\n\t"
                 "buffer_load_dword %3, off, %8, 0 sc1"
                 : "=&v"(c.lrb), "=&v"(c.lib), "=&v"(c.ldb), "=&v"(c.ep) : "v"(c.ch * 2), "s"(r0), "s"(r1), "s"(r2), "s"(r3) : "memory");
    if constexpr (KIND == 0) {
      ld_items<0, 3, 5>(c);
      wait_vm<7>();
      BARRIER();
      ld_items<0, 5, SCH[0].hi[0]>(c);
    } else {
      ld_items<KIND, FIRST, SCH[KIND].hi[0]>(c);
    }
  } else {
    ld_items<KIND, SCH[KIND].lo[B], SCH[KIND].hi[B]>(c);
  }
  if constexpr (KIND == 0 && B == WARM0) warm(mk_rs((const void*)c.Wd, F_ * D_ * 2), c.lane < 16 ? (c.vwarm * 16 + c.lane) * 128 : OOB, 0);
  if constexpr (KIND == 0 && B == WARM0 + 1) warm(mk_rs((const void*)c.Cre, D_ * P_ * 2), c.lane < 5 ? (c.vwarm * 5 + c.lane) * 128 : OOB, 0);
  if constexpr (KIND == 0 && B == WARM0 + 2) warm(mk_rs((const void*)c.Cim, D_ * P_ * 2), c.lane < 5 ? (c.vwarm * 5 + c.lane) * 128 : OOB, 0);
  if constexpr (B == CONST_AT) {
    asm volatile("" : "+v"(c.lrb), "+v"(c.lib), "+v"(c.ldb), "+v"(c.ep) : : "memory");
    const float lr = bf2f(c.lrb), li = bf2f(c.lib);
    float dt = __builtin_amdgcn_exp2f(bf2f(c.ldb) * 1.44269504088896341f);
    dt = bf2f(pk_bf16(dt, 0.f) & 0xFFFFu);
    const float rev = __builtin_amdgcn_fractf(dt * li * 0.15915494309189535f);
    const float ex = __builtin_amdgcn_exp2f(dt * lr * 1.44269504088896341f);
    const float ar = ex * __builtin_amdgcn_cosf(rev), ai = ex * __builtin_amdgcn_sinf(rev);
    const float den = __builtin_amdgcn_rcpf(lr * lr + li * li);
    float p16r = ar, p16i = ai;
    csq(p16r, p16i, 4);
    float p32r = p16r, p32i = p16i;
    csq(p32r, p32i, 1);
    LAS f32x4* d = (LAS f32x4*)(L + LDS_CONST + c.lw * 512 + (c.lane & 15) * 32);
    d[0] = f32x4{ar, ai, ((ar - 1.f) * lr + ai * li) * den, (ai * lr - (ar - 1.f) * li) * den};
    d[1] = f32x4{p16r, p16i, p32r, p32i};
    *(volatile LAS unsigned*)(L + LDS_TAG + 4 * (3 - c.lw)) = c.ep + 1u;
    WAIT_LDS();
  }
  SCHED();
  wait_vm<SCH[KIND].wait[B]>();
  BARRIER();
  if constexpr (B < LASTB) loader_steps<KIND, B + 1>(c, L);
}

__device__ __forceinline__ bool all4(LAS unsigned char* p) {
  const v4u d = *(volatile LAS v4u*)p;
  return __builtin_amdgcn_readfirstlane(d[0] & d[1] & d[2] & d[3]) != 0u;
}
__device__ __forceinline__ void lds_wait4(LAS unsigned char* p) {
#pragma nounroll
  for (unsigned spins = 0; spins < (SPIN_CAP << 4); ++spins) {
    if (all4(p)) break;
    __builtin_amdgcn_s_sleep(2);
  }
  CFENCE();
}
__device__ __forceinline__ void lds_wait(LAS unsigned char* word) {
#pragma nounroll
  for (unsigned spins = 0; spins < (SPIN_CAP << 4); ++spins) {
    if (lds_word(word) != 0u) break;
    __builtin_amdgcn_s_sleep(2);
  }
  CFENCE();
}
__device__ __forceinline__ void cmul(float ar, float ai, float br, float bi, float& cr, float& ci) {
  cr = ar * br - ai * bi; ci = ar * bi + ai * br;
}

template <int V> struct IC { static constexpr int value = V; };
template <int n> __device__ __forceinline__ void p2_first(v4u (&af)[8], v4u ra, int va, int o0, int o1) {
  asm volatile("buffer_load_dwordx4 %0, %1, %2, %3 offen offset:%c4 sc1" : "=v"(af[n]) : "v"(va), "s"(ra), "s"(n < 4 ? o0 : o1), "i"((n & 1) * 2048 + (n & 2) * 256) : "memory");
  if constexpr (n < 7) p2_first<n + 1>(af, ra, va, o0, o1);
}
template <int d, int n>
__device__ __forceinline__ void p2_step(f32x16 (&acc)[2][2], const v4u (&bb)[2][4], v4u (&af)[8], v4u ra, int va, int o0, int o1, int w, v4u rs6, int vb,
                                        int so6) {
  constexpr int WG = d == 3 ? 7 + n : (d == 4 ? 14 - n : (d == 6 ? 7 - n : 7)), WS = d == 5 ? 7 - n : 7;
  if constexpr (d >= 3 && d <= 5 && WS < WG)
    asm volatile("s_waitcnt vmcnt(%c2)\n\ts_cmp_gt_u32 %1, 3\n\ts_cbranch_scc1 1f\n\ts_waitcnt vmcnt(%c3)\n1:" : "+v"(af[n]) : "s"(w), "i"(WG), "i"(WS) : "memory", "scc");
  else
    asm volatile("s_waitcnt vmcnt(%c1)" : "+v"(af[n]) : "i"(WG) : "memory");
  mfma32t(acc[0][n & 1], bb[n >> 2][n & 2], af[n]);
  mfma32t(acc[1][n & 1], bb[n >> 2][(n & 2) + 1], af[n]);
  if constexpr (d == 3) {
    int t;
    asm volatile("s_cmp_lt_u32 %1, 4\n\ts_cbranch_scc1 1f\n\ts_lshl_b32 %0, %1, 14\n\ts_add_u32 m0, %0, %5\n\ts_add_u32 %0, %4, %6\n\t"
                 "buffer_load_dwordx4 %2, %3, %0 offen lds\n1:"
                 : "=&s"(t) : "s"(w), "v"((n & 1) ? vb ^ 16 : vb), "s"(rs6), "s"(so6), "i"(n * 1024), "i"(n * 8192) : "memory", "scc");
  }
  if constexpr (d < 5)
    asm volatile("buffer_load_dwordx4 %0, %1, %2, %3 offen offset:%c4 sc1" : "+v"(af[n]) : "v"(va), "s"(ra), "s"(n < 4 ? o0 : o1), "i"((n & 1) * 2048 + (n & 2) * 256) : "memory");
  if constexpr (d == 5)
    asm volatile("s_cmp_lt_u32 %5, 4\n\ts_cbranch_scc1 1f\n\tbuffer_load_dwordx4 %0, %1, %2, %3 offen offset:%c4 sc1\n1:"
                 : "+v"(af[n]) : "v"(va), "s"(ra), "s"(n < 4 ? o0 : o1), "i"((n & 1) * 2048 + (n & 2) * 256), "s"(w) : "memory", "scc");
  if constexpr (n < 7) p2_step<d, n + 1>(acc, bb, af, ra, va, o0, o1, w, rs6, vb, so6);
}

__global__ __launch_bounds__(512, 2) void k_fused(Params) {
  extern __shared__ __attribute__((aligned(16))) unsigned char lds_[];
  LAS unsigned char* L = (LAS unsigned char*)lds_;
  u64 stamp_[16] = {};
  STAMP(0);
  KP kp = kargs();
  u64 pX = (u64)kp->x, pWg = (u64)kp->Wg, pWu = (u64)kp->Wu, pBre = (u64)kp->Bre, pBim = (u64)kp->Bim, pCre = (u64)kp->Cre, pCim = (u64)kp->Cim, pWd = (u64)kp->Wd;
  const int b = __builtin_amdgcn_workgroup_id_x(), tid = __builtin_amdgcn_workitem_id_x(), lane = tid & 63;
  const int w = __builtin_amdgcn_readfirstlane(tid >> 6);
  const int xcd = b & 7, r = b >> 3;
  warm(mk_rs((const void*)(__builtin_amdgcn_s_getpc() & ~(u64)127), CODE_BYTES), lane < 2 ? lane * 32768 : OOB, (r * 8 + w) * 128);
  const bool light = (r & 7) == 7;
  const int ih = 28 * (xcd & 3) + r - (r >> 3), il = 112 + 4 * (xcd & 3) + (r >> 3);
  const int idx = ih + (il - ih) * (int)light;
  const int pn = 8 * (xcd >> 2) + (idx & 7), cb = idx >> 3;
  const int rot = pn & 6;
  const int R = xcd * 4 + (r & 3), N = r >> 2;
  asm volatile("" : "+s"(pX), "+s"(pWg), "+s"(pWu), "+s"(pBre), "+s"(pBim), "+s"(pCre), "+s"(pCim), "+s"(pWd));
  const int vb = (lane >> 3) * 1024 + (((lane & 7) ^ (((lane >> 2) & 4) | ((lane >> 4) & 2))) << 4);
  v4u b2[4][2][4];
  unsigned tag = 0;
  P2Ctx pc;

  if (__builtin_expect(w >= 4, 1)) {
    LdCtx c;
    c.lw = w - 4; c.rot = rot; c.lane = lane; c.vb = vb; c.sv = 0;
    c.Wd = pWd; c.Cre = pCre; c.Cim = pCim;
    c.vwarm = ((r - (r >> 3)) * 8 + xcd) * 4 + c.lw;
    c.xcd = xcd; c.ch = 32 * cb + 16 * (c.lw >> 1) + (lane & 15);
    const int l3 = lane >> 3, kq = lane >> 4;
    c.rx = mk_rs((const void*)pX, S_ * D_ * 2);
    c.vx = (32 * (c.lw & 1) + 16 * (l3 >> 2) + 4 * (c.lw >> 1) + (l3 & 3)) * 1024 + (((lane & 7) ^ ((4 * c.lw + kq) & 7)) << 4) + pn * 128 * 1024;
    c.rb = mk_rs((const void*)((c.lw >> 1) ? pBim : pBre), P_ * D_ * 2);
    c.vbb = (8 * (c.lw & 1) + l3) * 1024 + (((lane & 7) ^ (4 * (c.lw & 1) + kq)) << 4) + cb * 32 * 1024;
    c.lb = (c.lw >> 1) * 4096 + (c.lw & 1) * 1024;
    c.rw = mk_rs((const void*)((c.lw >> 1) ? pWu : pWg), D_ * F_ * 2);
    c.vw0 = ((c.lw & 1) * 16 + kq) * (F_ * 2) + (((lane & 15) ^ (kq << 2)) << 4) + cb * 256;
    c.vw1 = c.vw0 ^ 32;
    STAMP(1);
    if (__builtin_expect(!light, 1)) loader_steps<0, 0>(c, L);
    else { loader_steps<1, 0>(c, L); BARRIER(); }
    STAMP(2);
    {
      pc = p2_ctx(pWd, pCre, pCim, w, N);
      const int wA = c.lw < 2 ? w : c.lw, wB = wA ^ 4;
      b2_load<0, 0, 8>(pc, vb, 0, wA * 16384);
      b2_load<8192, 0, 8>(pc, vb, 1, wA * 16384);
      const P2Ctx pA = p2_ctx(pWd, pCre, pCim, wA, N), pB = p2_ctx(pWd, pCre, pCim, wB, N);
#pragma unroll
      for (int d = 0; d < 4; ++d) {
        wait_vm<8>();
        b2_read(false, L + wA * 16384 + (d & 1) * 8192, lane, b2[d]);
        WAIT_LDS();
        if (d < 2) b2_load<0, 0, 8>(pc, vb, d + 2, wA * 16384 + (d & 1) * 8192);
        else b2_load<0, 0, 8>(pA, vb, (wA & 4) + (d & 1), wA * 16384 + (d & 1) * 8192);
      }
      STAMP(3);
      lds_wait4(L + LDS_MD);
      b2_load<0, 0, 8>(pB, vb, wB & 4, wB * 16384);
      b2_load<8192, 0, 8>(pB, vb, (wB & 4) + 1, wB * 16384);
      wait_vm<0>();
      *(volatile LAS unsigned*)(L + LDS_PF + 4 * c.lw) = 1u;
      STAMP(4);
    }
    tag = lds_word(L + LDS_TAG);
  } else {
    const int c = w, q = lane >> 4, i15 = lane & 15, G = c & 1;
    const int ch = 32 * cb + 16 * (c >> 1) + i15;
    if (w == 3) {
#pragma unroll
      for (int z = 0; z < 4; ++z) *(volatile LAS v4u*)(L + LDS_DONE + 16 * z) = v4u{0u, 0u, 0u, 0u};
      WAIT_LDS();
    }
    f32x4 k0 = {0.f, 0.f, 0.f, 0.f}, k1 = {0.f, 0.f, 0.f, 0.f};
    unsigned tagv = 0;
    auto rd_const = [&]() {
      k0 = *(LAS f32x4*)(L + LDS_CONST + c * 512 + i15 * 32); k1 = *(LAS f32x4*)(L + LDS_CONST + c * 512 + i15 * 32 + 16);
      tagv = *(volatile LAS unsigned*)(L + LDS_TAG);
    };
    u64 pws = (u64)kp->ws;
    asm volatile("" : "+s"(pws));
    if (w == 2) {
      const int j = lane & 15, g1 = (lane >> 4) & 1, g2 = lane >> 5;
      const int off = j * 65536 + g1 * (WS_A2 + R * NK * 4096) + g2 * (WS_A2 + 2 * pn * NK * 4096) + (j >= 8 - 2 * g1 + 3 * g2) * OOB + g1 * g2 * OOB;
      warm(mk_rs(kp->ws, WS_A2 + A2_BYTES), off, 0);
      warm(mk_rs(kp->out, S_ * D_ * 4), lane < 4 ? lane * 32768 + N * 256 : OOB, R * 131072);
      warm(mk_rs(kp->Dsk, D_ * 2), lane < 1 ? 0 : OOB, 0);
    }
    int xa[2];
#pragma unroll
    for (int ks = 0; ks < 2; ++ks) xa[ks] = i15 * 128 + (((4 * ks + q) ^ ((i15 >> 1) & 7)) << 4);
    asm volatile("" : "+v"(xa[0]), "+v"(xa[1]));
    const int bo = B_BASE + (c >> 1) * 2048;
    f32x4 accs[2][4], acc[4][8];
    v4u bfr[2];
    auto zero_accs = [&]() {
#pragma unroll
      for (int m = 0; m < 2; ++m)
#pragma unroll
        for (int t = 0; t < 4; ++t) { accs[m][t] = f32x4{0.f, 0.f, 0.f, 0.f}; asm volatile("" : "+v"(accs[m][t])); }
    };
    float vr[16], vi[16], t0 = 0.f, t1 = 0.f;
    auto scan_half = [&](int n) {
      const int z = n >> 1;
      if (z == 0) { vr[0] = accs[0][0][0]; vi[0] = accs[1][0][0]; }
      else if ((n & 1) == 0) { t0 = __builtin_fmaf(k0[0], vr[z - 1], accs[0][z >> 2][z & 3]); t1 = __builtin_fmaf(k0[0], vi[z - 1], accs[1][z >> 2][z & 3]); }
      else { vr[z] = __builtin_fmaf(-k0[1], vi[z - 1], t0); vi[z] = __builtin_fmaf(k0[1], vr[z - 1], t1); }
    };
    auto sc = [](int v) { asm volatile("" : "+s"(v)); return v; };

    if (!light) {
      int wa[2];
      {
        const int qq = i15 >> 2, pp = lane & 3;
#pragma unroll
        for (int u = 0; u < 2; ++u)
          wa[u] = W_BASE + (8 * q + qq) * 256 + (((2 * (2 * c + u) + (pp >> 1)) ^ ((qq << 2) | ((q & 1) << 1))) << 4) + 8 * (pp & 1);
      }
      asm volatile("" : "+v"(wa[0]), "+v"(wa[1]));
#pragma unroll
      for (int a = 0; a < 4; ++a)
#pragma unroll
        for (int u = 0; u < 8; ++u) { acc[a][u] = f32x4{0.f, 0.f, 0.f, 0.f}; asm volatile("" : "+v"(acc[a][u])); }
      zero_accs();
      v4u xf[4];
      v2u wf[2][8];
      auto rdw = [&](int s, int z) {
        const int m = z >> 2, u = (z >> 1) & 1, t = z & 1;
        wf[s & 1][z] = lds_rdtr(L + wa[u] + (s & 3) * W_SLOT + m * 8192 + t * 1024);
      };
      STAMP(1);
      BARRIER();
      STAMP(2);
#pragma unroll
      for (int z = 0; z < 8; ++z) rdw(0, z);
#pragma unroll
      for (int z = 0; z < 3; ++z) xf[z] = lds_rd128(L + xa[0] + G * 8192 + z * 2048);
#pragma unroll
      for (int m = 0; m < 2; ++m) bfr[m] = lds_rd128(L + xa[0] + bo + m * 4096);
      auto glu4 = [&](int s, int u, const v4u& x, auto&& between) {
#pragma unroll
        for (int a = 0; a < 4; ++a) {
          const v4u wv = {wf[s & 1][2 * a][0], wf[s & 1][2 * a][1], wf[s & 1][2 * a + 1][0], wf[s & 1][2 * a + 1][1]};
          mfma16t(acc[a][u], wv, x);
          SCHED();
          between(a);
          SCHED();
        }
      };
      int xb = 0, bb = 0;
      auto steps = [&](int s0, int n, const int (&xo)[5]) {
#pragma unroll
        for (int j = 0; j < n; ++j)
#pragma unroll
          for (int u = 0; u < 8; ++u) {
            const int s = s0 + j, jn = j + ((u + 3) >> 3), un = (u + 3) & 7, uw = s == 0 ? u - 4 : u;
            SCHED();
            if (u == (s == 0 ? 4 : 0)) { BARRIER(); SCHED(); }
            glu4(s, u, xf[u & 3], [&](int a) {
              if (a == 0) {
                if ((un & 3) == 0 || (j == 0 && u == 0)) xb = xa[(s0 + jn) & 1] + sc(xo[jn] + ((un >> 2) ^ G) * 8192);
                xf[(u + 3) & 3] = lds_rd128(L + xb + (un & 3) * 2048);
              }
              if ((a == 1 || a == 2) && uw >= 0 && uw < 4) rdw(s + 1, 2 * uw + a - 1);
              if (a == 3 && (u == 4 || u == 5)) {
                if (u == 4) bb = xa[(s + 1) & 1] + sc(bo + (xo[j + 1] >> 1));
                bfr[u - 4] = lds_rd128(L + bb + (u - 4) * 4096);
              }
            });
            if (u < 4) { mfma16t(accs[0][u], xf[u & 3], bfr[0]); mfma16t(accs[1][u], xf[u & 3], bfr[1]); }
          }
      };
      {
        const int xo[5] = {0, 0, 0, 0, 0};
        steps(0, 1, xo);
      }
      int xo0 = 0;
#pragma nounroll
      for (int o = 0; o < 3; ++o) {
        const int xo1 = xo0 == 2 * X_SLOT ? 0 : xo0 + X_SLOT, xo2 = xo1 == 2 * X_SLOT ? 0 : xo1 + X_SLOT;
        const int xo[5] = {xo0, xo1, xo1, xo2, xo2};
        steps(1, 4, xo);
        xo0 = xo2;
      }
      {
        const int xo[5] = {0, X_SLOT, 0, 0, 0};
        steps(1, 1, xo);
      }
      v4u bf2[2];
#pragma unroll
      for (int it = 0; it < 16; ++it) {
        const int s = (it >> 2) & 1, u = (it & 3) + 4 * (it >> 3), nx = it + 3;
        SCHED();
        glu4(s, u, xf[it & 3], [&](int a) {
          if (a == 0 && nx < 16) {
            if ((nx & 3) == 0) xb = xa[(nx >> 2) & 1] + sc(X_SLOT + ((nx >> 3) ^ G) * 8192);
            xf[nx & 3] = lds_rd128(L + xb + (nx & 3) * 2048);
          }
          if (a >= 1 && it < 3 && 3 * it + a - 1 < 8) rdw(3, 3 * it + a - 1);
          if (it == 0 && a == 3) { bb = xa[1] + sc(bo + B_SLOT); bf2[0] = lds_rd128(L + bb); bf2[1] = lds_rd128(L + bb + 4096); }
          if (it == 4 && a == 1) rd_const();
          if (it >= 8) scan_half(4 * (it - 8) + a);
        });
        if (it < 4) { mfma16t(accs[0][u], xf[it & 3], bfr[0]); mfma16t(accs[1][u], xf[it & 3], bfr[1]); }
        else if (it < 8) { mfma16t(accs[0][u], xf[it & 3], bf2[0]); mfma16t(accs[1][u], xf[it & 3], bf2[1]); }
      }
      asm volatile("s_nop 15" : "+v"(acc[0][7]), "+v"(acc[1][7]), "+v"(acc[2][7]), "+v"(acc[3][7]), "+v"(acc[0][6]), "+v"(acc[1][6]), "+v"(acc[2][6]), "+v"(acc[3][6]));
    } else {
      {
      kp = kargs();
      rsrc_t rx = mk_rsrc(kp->x, S_ * D_ * 2), rg = mk_rsrc(kp->gw, D_ * 2), rgo = mk_rsrc(kp->ws + WS_G, S_ * 4);
      const int tk0 = 128 * pn + 64 * (cb - 14) + 16 * c;
      v4u gv[8], xv[2][8];
#pragma unroll
      for (int z = 0; z < 8; ++z) gv[z] = __builtin_amdgcn_raw_buffer_load_b128(rg, (lane & 7) * 16, z * 128, 0);
#pragma unroll
      for (int gi = 0; gi < 2; ++gi)
#pragma unroll
        for (int z = 0; z < 8; ++z) xv[gi][z] = __builtin_amdgcn_raw_buffer_load_b128(rx, (lane >> 3) * 1024 + (lane & 7) * 16, (tk0 + gi * 8) * 1024 + z * 128, 0);
#pragma unroll
      for (int gi = 0; gi < 2; ++gi) {
        float s = 0.f;
#pragma unroll
        for (int z = 0; z < 8; ++z)
#pragma unroll
          for (int y = 0; y < 4; ++y) {
            const unsigned xa_ = xv[gi][z][y], ga = gv[z][y];
            s = __builtin_amdgcn_fdot2_f32_bf16(__builtin_bit_cast(bf16x2, xa_), __builtin_bit_cast(bf16x2, ga), s, false);
          }
        s += dpp<0xB1>(s); s += dpp<0x4E>(s); s += dpp<0x141>(s);
        const float g = __builtin_amdgcn_rcpf(1.f + __builtin_amdgcn_exp2f(s * -1.44269504088896341f));
        __builtin_amdgcn_raw_buffer_store_b32(f2u(g), rgo, (lane & 7) ? OOB : (lane >> 3) * 4, (tk0 + gi * 8) * 4, AUX_SC1);
      }
      }
      STAMP(1);
      zero_accs();
      int xo = 0;
#pragma nounroll
      for (int T = 0; T < 8; ++T) {
#pragma unroll
        for (int ks = 0; ks < 2; ++ks) {
          if (T < 7) BARRIER();
          v4u xf[4];
#pragma unroll
          for (int t = 0; t < 4; ++t) xf[t] = lds_rd128(L + (xa[ks] + xo + G * 8192) + t * 2048);
#pragma unroll
          for (int m = 0; m < 2; ++m) bfr[m] = lds_rd128(L + (xa[ks] + bo + (xo >> 1)) + m * 4096);
#pragma unroll
          for (int t = 0; t < 4; ++t) { mfma16t(accs[0][t], xf[t], bfr[0]); mfma16t(accs[1][t], xf[t], bfr[1]); }
          SCHED();
        }
        xo = xo == 2 * X_SLOT ? 0 : xo + X_SLOT;
      }
      asm volatile("s_nop 15" : "+v"(accs[0][0]), "+v"(accs[1][0]), "+v"(accs[0][1]), "+v"(accs[1][1]), "+v"(accs[0][2]), "+v"(accs[1][2]), "+v"(accs[0][3]), "+v"(accs[1][3]));
      rd_const();
      BARRIER();
#pragma unroll
      for (int n = 0; n < 32; ++n) { scan_half(n); SCHED(); }
    }
    *(volatile LAS unsigned*)(L + LDS_MD + 4 * w) = 1u;
    SCHED();
    STAMP(3);
    tag = __builtin_amdgcn_readfirstlane(tagv);
    const float ar = k0[0], ai = k0[1], cr = k0[2], ci = k0[3], p16r = k1[0], p16i = k1[1], p32r = k1[2], p32i = k1[3];
    __builtin_amdgcn_s_setprio(2);
    rsrc_t ra2 = mk_rsrc((const void*)(pws + WS_A2), A2_BYTES), rcar = mk_rsrc((const void*)(pws + WS_CARRY), 32 * P_ * 16);
    const int ck = 2 * pn + G;
    float pr, pi;
    {
      float tr, ti;
      const auto xr = __builtin_amdgcn_permlane16_swap(f2u(vr[15]), f2u(vr[15]), false, false);
      const auto xi = __builtin_amdgcn_permlane16_swap(f2u(vi[15]), f2u(vi[15]), false, false);
      const float e0r = u2f(xr[0]), e1r = u2f(xr[1]), e0i = u2f(xi[0]), e1i = u2f(xi[1]);
      cmul(e0r, e0i, p16r, p16i, tr, ti); tr += e1r; ti += e1i;
      const auto yr = __builtin_amdgcn_permlane32_swap(f2u(tr), f2u(tr), false, false);
      const auto yi = __builtin_amdgcn_permlane32_swap(f2u(ti), f2u(ti), false, false);
      const float f0r = u2f(yr[0]), f1r = u2f(yr[1]), f0i = u2f(yi[0]), f1i = u2f(yi[1]);
      float Er, Ei;
      cmul(f0r, f0i, p32r, p32i, Er, Ei); Er += f1r; Ei += f1i;
      const float br = (q & 2) ? f0r : 0.f, bi = (q & 2) ? f0i : 0.f;
      cmul(br, bi, p16r, p16i, tr, ti);
      pr = (q & 1) ? tr + e0r : br; pi = (q & 1) ? ti + e0i : bi;
      const v4u gq = {f2u(Er), tag, f2u(Ei), ~tag};
      st_sc1(gq, rcar, q ? OOB : ch * 16, (31 - ck) * (P_ * 16));
    }
    asm volatile("" : "+v"(pr), "+v"(pi) : : "memory");
    SCHED();
    STAMP(4);
    v4u cv[8];
    const int cvo = ch * 16 + (32 - ck + 8 * q) * (P_ * 16);
    auto ld_carry = [&]() {
#pragma unroll
      for (int z = 0; z < 8; ++z) cv[z] = ld_sc1(rcar, cvo, z * (P_ * 16));
    };
    auto publish = [&](int part) {
      *(volatile LAS unsigned*)(L + LDS_DONE + 16 * part + 4 * w) = 1u;
      const bool last = all4(L + LDS_DONE + 16 * part);
      st_flag((unsigned char*)pws, WS_FA2 + part * 32768 + pn * 128 + cb * 8, tag, last && lane < 16 ? lane * 2048 : OOB);
      CFENCE();
    };
    if (!light) {
#pragma unroll
      for (int u = 0; u < 8; ++u) {
        float hv[8];
#pragma unroll
        for (int j = 0; j < 8; ++j) hv[j] = acc[j >> 2][u][j & 3] * -1.44269504088896341f;
        SCHED();
#pragma unroll
        for (int j = 0; j < 8; ++j) hv[j] = __builtin_amdgcn_exp2f(hv[j]);
        SCHED();
#pragma unroll
        for (int j = 0; j < 8; ++j) hv[j] += 1.f;
        SCHED();
#pragma unroll
        for (int j = 0; j < 8; ++j) hv[j] = __builtin_amdgcn_rcpf(hv[j]);
        SCHED();
#pragma unroll
        for (int j = 0; j < 8; ++j) hv[j] *= acc[2 + (j >> 2)][u][j & 3];
        SCHED();
        st_sc1(v4u{pk_bf16(hv[0], hv[1]), pk_bf16(hv[2], hv[3]), pk_bf16(hv[4], hv[5]), pk_bf16(hv[6], hv[7])}, ra2, lane * 16,
               ((2 * pn + ((u >> 2) ^ G)) * NK + 4 * cb + c) * 4096 + (u & 3) * 1024);
        if (u + 1 < 8) asm volatile("" : "+v"(acc[0][u + 1]), "+v"(acc[1][u + 1]) : : "memory");
      }
    }
    CFENCE();
    SCHED();
    STAMP(5);
    ld_carry();
    CFENCE();
    SCHED();
#pragma unroll
    for (int z = 0; z < 16; ++z) {
      const float t = cr * vr[z] - ci * vi[z];
      vi[z] = -cr * vi[z] - ci * vr[z]; vr[z] = t;
    }
    float qr[4], qi[4];
    qr[0] = ar; qi[0] = ai; qr[1] = ar; qi[1] = ai; csq(qr[1], qi[1], 1);
    cmul(qr[1], qi[1], ar, ai, qr[2], qi[2]); qr[3] = qr[1]; qi[3] = qi[1]; csq(qr[3], qi[3], 1);
    const float p4r = qr[3], p4i = qi[3];
    float p64r = p32r, p64i = p32i; csq(p64r, p64i, 1);
    float p512r = p64r, p512i = p64i; csq(p512r, p512i, 3);
    float p1kr = p512r, p1ki = p512i; csq(p1kr, p1ki, 1);
    float sgr, sgi, wgr, wgi;
    cmul((q & 1) ? p16r : 1.f, (q & 1) ? p16i : 0.f, (q & 2) ? p32r : 1.f, (q & 2) ? p32i : 0.f, sgr, sgi);
    cmul((q & 1) ? p512r : 1.f, (q & 1) ? p512i : 0.f, (q & 2) ? p1kr : 1.f, (q & 2) ? p1ki : 0.f, wgr, wgi);
#pragma unroll
    for (int z = 0; z < 16; ++z) asm volatile("" : "+v"(vr[z]), "+v"(vi[z]));
    asm volatile("" : "+v"(sgr), "+v"(sgi), "+v"(wgr), "+v"(wgi), "+v"(p64r), "+v"(p64i));
    SCHED();
    const int want = imin(imax(ck - 8 * q, 0), 8);
    auto carry_ok = [&]() {
      int good = 0;
#pragma unroll
      for (int z = 0; z < 8; ++z) good += ((cv[z][1] ^ tag) | (cv[z][3] ^ ~tag)) == 0u;
      return wall(good == want);
    };
    wait_vm<8>();
    publish(0);
    STAMP(14);
    if (__builtin_expect(!carry_ok(), 0)) {
#pragma nounroll
      for (unsigned spins = 0; spins < SPIN_CAP; ++spins) {
        __builtin_amdgcn_s_sleep(2);
        CFENCE();
        ld_carry();
        if (carry_ok()) break;
      }
    }
    STAMP(6);
    float Gr, Gi;
    {
      float Hr = 0.f, Hi = 0.f, tr, ti;
#pragma unroll
      for (int z = 7; z >= 0; --z) { cmul(Hr, Hi, p64r, p64i, tr, ti); Hr = tr + u2f(cv[z][0]); Hi = ti + u2f(cv[z][2]); }
      cmul(Hr, Hi, wgr, wgi, tr, ti);
      { const auto a = __builtin_amdgcn_permlane16_swap(f2u(tr), f2u(tr), false, false);
        const auto c2 = __builtin_amdgcn_permlane16_swap(f2u(ti), f2u(ti), false, false);
        tr = u2f(a[0]) + u2f(a[1]); ti = u2f(c2[0]) + u2f(c2[1]); }
      { const auto a = __builtin_amdgcn_permlane32_swap(f2u(tr), f2u(tr), false, false);
        const auto c2 = __builtin_amdgcn_permlane32_swap(f2u(ti), f2u(ti), false, false);
        tr = u2f(a[0]) + u2f(a[1]); ti = u2f(c2[0]) + u2f(c2[1]); }
      cmul(tr, ti, sgr, sgi, Hr, Hi);
      cmul(Hr + pr, Hi + pi, cr, ci, Gr, Gi);
    }
    {
      LAS unsigned char* T = L + B_BASE + (c >> 1) * (2 * B_SLOT) + (c & 1) * 4096;
      const int s32 = ((i15 >> 1) & 1) | ((i15 >> 2) & 2), s8 = (i15 & 1) | ((i15 >> 1) & 2);
#pragma unroll
      for (int t = 0; t < 4; ++t) {
        float orr[4], oi[4];
#pragma unroll
        for (int z = 0; z < 4; ++z) {
          orr[z] = __builtin_fmaf(-qi[z], Gi, __builtin_fmaf(qr[z], Gr, vr[4 * t + z]));
          oi[z] = __builtin_fmaf(-qi[z], Gr, __builtin_fmaf(-qr[z], Gi, vi[4 * t + z]));
        }
        if (t < 3) { float tr, ti; cmul(Gr, Gi, p4r, p4i, tr, ti); Gr = tr; Gi = ti; }
        LAS unsigned char* a = T + i15 * 128 + ((t ^ s32) << 5) + ((q ^ s8) << 3);
        *(LAS v2u*)a = v2u{pk_bf16(orr[0], orr[1]), pk_bf16(orr[2], orr[3])};
        *(LAS v2u*)(a + 2048) = v2u{pk_bf16(oi[0], oi[1]), pk_bf16(oi[2], oi[3])};
      }
      const int qq = i15 >> 2, pp = lane & 3;
      const int so = (ck * NK + 56 + 4 * (cb >> 1) + (cb & 1)) * 4096 + (c >> 1) * 512;
#pragma unroll
      for (int t = 0; t < 4; ++t) {
        v2u part[2];
#pragma unroll
        for (int hh = 0; hh < 2; ++hh)
          part[hh] = lds_rdtr(T + (8 * q + 4 * hh + qq) * 128 + ((t ^ ((qq >> 1) | ((q & 1) << 1))) << 5) + ((pp ^ ((qq & 1) | (hh << 1))) << 3));
        st_sc1(v4u{part[0][0], part[0][1], part[1][0], part[1][1]}, ra2, (lane >> 5) * 8192 + (lane & 31) * 16, so + t * 1024);
      }
    }
    SCHED();
    STAMP(7);
    wait_vm<0>();
    publish(1);
    STAMP(12);
  }
  kp = kargs();
  pc = p2_ctx(pWd, pCre, pCim, w, N);
  const int d0 = 2 * pc.sh;
  if (w < 4) {
    lds_wait(L + LDS_PF + 4 * w);
    b2_read(true, L + w * 16384, lane, b2[2]);
    b2_read(true, L + w * 16384 + 8192, lane, b2[3]);
    WAIT_LDS();
    b2_load<0, 0, 8>(pc, vb, 2, w * 16384);
    b2_load<8192, 0, 8>(pc, vb, 3, w * 16384);
    STAMP(13);
  }

  {
    lds_wait4(L + LDS_DONE);
    const int cl = lane & 15, lo = pc.sh ? (pc.g0 - 28) & ~1 : pc.g0 >> 1, hi = pc.sh ? (pc.g0 + pc.nd - 29) | 1 : (pc.g0 + pc.nd - 1) >> 1;
    poll_flags((GAS u64*)(kp->ws + WS_FA2) + ((pc.sh * 16 + (R & 1) * 8 + N) * 16 + (R >> 1)) * 16 + cl, tag64(tag), (cl >= lo && cl <= hi) || cl >= 14, cl < 14);
  }
  STAMP(8);
  if (w >= 4) __builtin_amdgcn_s_setprio(1);
  glds<B_BASE + B_SLOT, 0>(mk_rs((const void*)pX, S_ * D_ * 2), w * 1024, ((lane >> 5) * 16 + ((lane >> 3) & 3)) * 1024 + (lane & 7) * 16,
                             (64 * R + 32 * (w & 1) + 4 * (w >> 1)) * 1024 + N * 128);
  const int tok = 64 * R + 32 * (w & 1) + 4 * (w >> 1) + (lane >> 4);
  const v2u dv = __builtin_amdgcn_raw_buffer_load_b64(mk_rsrc(kp->Dsk, D_ * 2), (lane & 15) * 8, N * 128, 0);
  const float ge = u2f(__builtin_amdgcn_raw_buffer_load_b32(mk_rsrc(kp->ws + WS_G, S_ * 4), tok * 4 + (lane & 1) * 64, 0, AUX_SC1));
  SCHED();
  const v4u ra = mk_rs(kp->ws + WS_A2 + (R * NK + 2 * pc.g0) * 4096, pc.nd * 8192);
  const int va = ((lane >> 4) & 1) * 1024 + (lane >> 5) * 256 + (lane & 15) * 16;
  v4u af[8];
  {
    const int o = p2_ds(pc, 0) * 8192;
    p2_first<0>(af, ra, va, o, o + 4096);
  }
  SCHED();
#define CPRIO 0
  if (w < 4) __builtin_amdgcn_s_setprio(CPRIO);
  f32x16 acc[2][2];
#pragma unroll
  for (int e = 0; e < 2; ++e)
#pragma unroll
    for (int tt = 0; tt < 2; ++tt) { acc[e][tt] = f32x16{}; asm volatile("" : "+v"(acc[e][tt])); }
  asm volatile("s_nop 1");
  auto step = [&](auto Dc) {
    constexpr int d = decltype(Dc)::value;
    v4u rs6 = ra; int so6 = 0;
    if (d == 3) { WAIT_LDS(); b2_src(pc, 6 - 3 * pc.sh, rs6, so6); }
    const int o = p2_ds(pc, d + 1 - d0) * 8192;
    p2_step<d, 0>(acc, b2[d & 3], af, ra, va, o, o + 4096, w, rs6, vb, so6);
    if (d == 2 || d == 3) b2_read(w < 4, L + w * 16384 + (d & 1) * 8192, lane, b2[d & 1]);
    if (d == 4 && w >= 4) b2_read(false, L + w * 16384, lane, b2[2]);
    SCHED();
  };
  if (w >= 4) { step(IC<0>{}); step(IC<1>{}); }
  step(IC<2>{}); step(IC<3>{}); step(IC<4>{}); step(IC<5>{});
  if (w >= 4) step(IC<6>{});
  asm volatile("s_nop 15\n\ts_nop 7" : "+v"(acc[0][0]), "+v"(acc[1][0]), "+v"(acc[0][1]), "+v"(acc[1][1]));
  SCHED();
  STAMP(9);
  kp = kargs();
  {
    const int t3 = lane & 3, g3 = ((t3 & 1) * 3) ^ (t3 >> 1), hi = lane >> 5;
#pragma unroll
    for (int T = 0; T < 2; ++T)
#pragma unroll
      for (int E = 0; E < 2; ++E)
#pragma unroll
        for (int g = 0; g < 4; ++g) {
          const int e = 2 * E + (g >> 1), q = 2 * (g & 1) + hi;
          *(LAS f32x4*)(L + w * 16384 + ((2 * T + ((lane >> 4) & 1)) * 4 + e) * 1024 + ((16 * q + (((((lane >> 2) & 3) ^ q) << 2) | (e ^ g3))) << 4)) =
              f32x4{acc[E][T][4 * g], acc[E][T][4 * g + 1], acc[E][T][4 * g + 2], acc[E][T][4 * g + 3]};
        }
  }
  if (b == 255 && w == 0) {
    rsrc_t rf = mk_rsrc(kp->ws + WS_FA2 + 32768, 256 * 8);
#pragma nounroll
    for (unsigned spins = 0; spins < SPIN_CAP; ++spins) {
      CFENCE();
      const v4u f0 = ld_sc1(rf, lane * 32, 0), f1 = ld_sc1(rf, lane * 32, 16);
      if (wall(((f0[0] ^ tag) | (f0[1] ^ ~tag) | (f0[2] ^ tag) | (f0[3] ^ ~tag) | (f1[0] ^ tag) | (f1[1] ^ ~tag) | (f1[2] ^ tag) | (f1[3] ^ ~tag)) == 0u)) break;
      __builtin_amdgcn_s_sleep(8);
    }
    st_flag(kp->ws, WS_EPOCH, tag, lane == 0 ? 0 : OOB);
  }
  WAIT_LDS();
  BARRIER();
  STAMP(10);
  {
    const int q = lane & 3, e = (lane >> 2) & 3, t3 = lane >> 4;
    const int base = ((w >> 1) * 4 + e) * 1024 + q * 256 + ((e ^ ((t3 & 1) * 3) ^ (t3 >> 1)) << 4);
    rsrc_t ro = mk_rsrc(kp->out, S_ * D_ * 4);
    const float go = dpp<0xB1>(ge);
    f32x4 res[2];
#pragma unroll
    for (int hf = 0; hf < 2; ++hf) {
      f32x4 sg = {0.f, 0.f, 0.f, 0.f}, ss = {0.f, 0.f, 0.f, 0.f};
      const int a = base + (((2 * (w & 1) + hf) ^ q) << 6);
#pragma unroll
      for (int ww = 0; ww < 8; ++ww) {
        const f32x4 v = *(LAS f32x4*)(L + ww * 16384 + a);
        if (ww < 4) ss += v; else sg += v;
      }
      const float g = (lane & 1) == hf ? ge : go;
      f32x4 m = ss * (2.f * g) + sg * (1.f - g);
      const v2u xv = *(LAS v2u*)(L + B_BASE + B_SLOT + w * 1024 + hf * 512 + lane * 8);
      m[0] += g * (bf2f(xv[0] & 0xFFFFu) * bf2f(dv[0] & 0xFFFFu)); m[1] += g * (bfhi(xv[0]) * bfhi(dv[0]));
      m[2] += g * (bf2f(xv[1] & 0xFFFFu) * bf2f(dv[1] & 0xFFFFu)); m[3] += g * (bfhi(xv[1]) * bfhi(dv[1]));
      res[hf] = m;
    }
    asm volatile("" : "+v"(res[0]), "+v"(res[1]));
#pragma unroll
    for (int hf = 0; hf < 2; ++hf)
      __builtin_amdgcn_raw_buffer_store_b128(__builtin_bit_cast(v4u, res[hf]), ro, tok * 2048 + (lane & 15) * 16 + hf * 32768 + N * 256, 0, OUT_AUX);
  }
  STAMP(11);
  if (b == PB && w == PW) { const u64 tend = stamp_[PROBE] + 6000 + (EXTRA); for (int z = 0; z < 1000000; ++z) { if (__builtin_amdgcn_s_memrealtime() >= tend) break; __builtin_amdgcn_s_sleep(1); } }
}

#if defined(__HIP_DEVICE_COMPILE__)
#pragma clang attribute pop
#endif

extern "C" void kernel_launch(void* const* d_in, const int* in_sizes, int n_in, void* d_out, int out_size, void* d_ws, size_t ws_size,
                              hipStream_t stream) {
  Params p;
  p.x = (const u16*)d_in[0]; p.Lre = (const u16*)d_in[1]; p.Lim = (const u16*)d_in[2]; p.Bre = (const u16*)d_in[3];
  p.Bim = (const u16*)d_in[4]; p.Cre = (const u16*)d_in[5]; p.Cim = (const u16*)d_in[6]; p.ldt = (const u16*)d_in[7];
  p.Dsk = (const u16*)d_in[8]; p.Wg = (const u16*)d_in[10]; p.Wu = (const u16*)d_in[11]; p.Wd = (const u16*)d_in[12];
  p.gw = (const u16*)d_in[13]; p.out = (float*)d_out; p.ws = (unsigned char*)d_ws;
  (void)hipFuncSetAttribute((const void*)k_fused, hipFuncAttributeMaxDynamicSharedMemorySize, LDS_BYTES);
  k_fused<<<256, 512, LDS_BYTES, stream>>>(p);
}
```

```cpp
#define WARMM
#define WARM0 4
#define PRIO2
#include <hip/hip_runtime.h>
#include <stdint.h>

typedef unsigned short u16;
typedef unsigned long long u64;
typedef unsigned v4u __attribute__((ext_vector_type(4)));
typedef unsigned v2u __attribute__((ext_vector_type(2)));
typedef short s16x4 __attribute__((ext_vector_type(4)));
typedef __bf16 bf16x8 __attribute__((ext_vector_type(8)));
typedef __bf16 bf16x2 __attribute__((ext_vector_type(2)));
typedef float f32x2 __attribute__((ext_vector_type(2)));
typedef float f32x4 __attribute__((ext_vector_type(4)));
typedef float f32x16 __attribute__((ext_vector_type(16)));
#define LAS __attribute__((address_space(3)))
#define GAS __attribute__((address_space(1)))
#define RLX_AGENT __ATOMIC_RELAXED, __HIP_MEMORY_SCOPE_AGENT
typedef __amdgpu_buffer_rsrc_t rsrc_t;

constexpr int S_ = 2048, D_ = 512, P_ = 512, F_ = 1792;
constexpr int NK = 88;
constexpr int AUX_SC1 = 16;
#define OUT_AUX 2
constexpr int WS_EPOCH = 0;
constexpr int WS_G = 8192;
constexpr int WS_FA2 = 65536;
constexpr int CTRL_BYTES = 131072;
constexpr int WS_CARRY = 131072;
constexpr int WS_A2 = 524288;
constexpr int A2_BYTES = 32 * NK * 4096;
constexpr int WS_STAMP = 32 << 20;
constexpr int LDS_BYTES = 160 * 1024;
constexpr int X_SLOT = 16384, W_BASE = 65536, W_SLOT = 16384, B_BASE = 131072, B_SLOT = 8192;
constexpr int LDS_DONE = 155648, LDS_PF = LDS_DONE + 32, LDS_MD = LDS_DONE + 48, LDS_TAG = LDS_DONE + 64;
constexpr int LDS_DUMMY = LDS_DONE + 256, LDS_CONST = LDS_DONE + 512;
constexpr int OOB = 1 << 28;
constexpr int CODE_BYTES = 29 * 1024;
constexpr unsigned SPIN_CAP = 1u << 18;

struct Params {
  const u16 *x, *Wg, *Wu, *Bre, *Bim, *Cre, *Cim, *Wd;
  unsigned char* ws;
  const u16 *Lre, *Lim, *ldt, *Dsk, *gw;
  float* out;
};

#if defined(__HIP_DEVICE_COMPILE__)
#pragma clang attribute push(__attribute__((target("no-packed-fp32-ops"))), apply_to = function)
#endif
__device__ __forceinline__ float u2f(unsigned v) { return __builtin_bit_cast(float, v); }
__device__ __forceinline__ unsigned f2u(float v) { return __builtin_bit_cast(unsigned, v); }
__device__ __forceinline__ bool wall(bool p) { return __builtin_amdgcn_ballot_w64(!p) == 0; }
__device__ __forceinline__ bool wany(bool p) { return __builtin_amdgcn_ballot_w64(p) != 0; }
__device__ __forceinline__ int imin(int a, int b) { return a < b ? a : b; }
__device__ __forceinline__ int imax(int a, int b) { return a > b ? a : b; }
template <int CTRL> __device__ __forceinline__ float dpp(float v) { return u2f(__builtin_amdgcn_mov_dpp(f2u(v), CTRL, 0xF, 0xF, true)); }
__device__ __forceinline__ rsrc_t mk_rsrc(const void* p, int bytes) {
  return __builtin_amdgcn_make_buffer_rsrc((void*)p, 0, bytes, 0x00020000);
}
__device__ __forceinline__ float bf2f(unsigned v) { return u2f(v << 16); }
__device__ __forceinline__ float bfhi(unsigned v) { return u2f(v & 0xFFFF0000u); }
__device__ __forceinline__ unsigned pk_bf16(float lo, float hi) {
  f32x2 t = {lo, hi};
  return __builtin_bit_cast(unsigned, __builtin_convertvector(t, bf16x2));
}
__device__ __forceinline__ void mfma16t(f32x4& c, v4u a, v4u b) {
  asm volatile("v_mfma_f32_16x16x32_bf16 %0, %1, %2, %0" : "+v"(c) : "v"(a), "v"(b));
}
__device__ __forceinline__ void mfma32t(f32x16& c, v4u a, v4u b) {
  asm volatile("v_mfma_f32_32x32x16_bf16 %0, %1, %2, %0" : "+v"(c) : "v"(a), "v"(b));
}
__device__ __forceinline__ v4u lds_rd128(LAS unsigned char* p) { return *(LAS v4u*)p; }
__device__ __forceinline__ v2u lds_rdtr(LAS unsigned char* p) {
  return __builtin_bit_cast(v2u, __builtin_amdgcn_ds_read_tr16_b64_v4i16((LAS s16x4*)p));
}
__device__ __forceinline__ v4u mk_rs(const void* p, unsigned bytes) {
  const u64 a = (u64)p;
  unsigned lo = (unsigned)a, hi = (unsigned)(a >> 32) & 0xFFFFu;
  asm volatile("" : "+s"(lo), "+s"(hi));
  return v4u{lo, hi, bytes, 0x00020000u};
}
template <int LI, int SI> __device__ __forceinline__ void glds(v4u rs, int lds, int voff, int soff) {
  int t;
  asm volatile("s_add_u32 m0, %1, %5\n\ts_add_u32 %0, %4, %6\n\tbuffer_load_dwordx4 %2, %3, %0 offen lds"
               : "=&s"(t) : "s"(lds), "v"(voff), "s"(rs), "s"(soff), "i"(LI), "i"(SI) : "memory", "scc");
}
typedef const __attribute__((address_space(4))) Params* KP;
__device__ __forceinline__ KP kargs() {
  KP k = (KP)__builtin_amdgcn_kernarg_segment_ptr();
  asm volatile("" : "+s"(k));
  return k;
}
__device__ __forceinline__ void warm(v4u rs, int voff, int soff) {
  asm volatile("s_mov_b32 m0, %3\n\ts_nop 0\n\tbuffer_load_dword %0, %1, %2 offen lds" : : "v"(voff), "s"(rs), "s"(soff), "i"(LDS_DUMMY) : "memory");
}
__device__ __forceinline__ v4u ld_sc1(rsrc_t rs, int voff, int soff) { return __builtin_amdgcn_raw_buffer_load_b128(rs, voff, soff, AUX_SC1); }
__device__ __forceinline__ void st_sc1(v4u v, rsrc_t rs, int voff, int soff) { __builtin_amdgcn_raw_buffer_store_b128(v, rs, voff + soff, 0, AUX_SC1); }
__device__ __forceinline__ u64 tag64(unsigned tag) { return ((u64)(~tag) << 32) | tag; }
__device__ __forceinline__ void st_flag(unsigned char* ws, int off, unsigned tag, int voff) {
  __builtin_amdgcn_raw_buffer_store_b64(v2u{tag, ~tag}, mk_rsrc(ws, CTRL_BYTES), voff, off, AUX_SC1);
}
__device__ __forceinline__ unsigned lds_word(LAS unsigned char* p) { return __builtin_amdgcn_readfirstlane(*(volatile LAS unsigned*)p); }
#define BARRIER() asm volatile("s_barrier" ::: "memory")
#define SCHED() __builtin_amdgcn_sched_barrier(0)
#define CFENCE() asm volatile("" ::: "memory")
#define WAIT_LDS() asm volatile("s_waitcnt lgkmcnt(0)" ::: "memory")
template <int N> __device__ __forceinline__ void wait_vm() { asm volatile("s_waitcnt vmcnt(%c0)" ::"i"(N) : "memory"); }

#define STAMP(i) do {} while (0)
#define POLL_SLEEP 4
#define POLL_IDLE 16

__device__ __forceinline__ void poll_flags(GAS u64* f, u64 want, bool care, bool late) {
#pragma nounroll
  for (unsigned spins = 0; spins < SPIN_CAP; ++spins) {
    CFENCE();
    const u64 v = __hip_atomic_load(f, RLX_AGENT);
    if (wall(!care || v == want)) break;
    if (wany(late && v == want)) __builtin_amdgcn_s_sleep(POLL_SLEEP); else __builtin_amdgcn_s_sleep(POLL_IDLE);
  }
  CFENCE();
}
__device__ __forceinline__ void csq(float& r, float& i, int n) {
#pragma unroll
  for (int z = 0; z < n; ++z) { const float t = r * r - i * i; i = 2.f * r * i; r = t; }
}

#define NROT 102
#define CONST_AT 2
constexpr int LASTB = 13;
struct Sched {
  int n;
  int type[40], idx[40];
  int lo[LASTB + 1], hi[LASTB + 1];
  int wait[LASTB + 1];
  bool ok;
};
constexpr Sched make_sched(int kind) {
  Sched s{};
  int cnt[40] = {}, need[40] = {}, avail[40] = {};
  int n = 0;
  auto add = [&](int ty, int ix, int nd, int av) {
    if (kind && ty == 2) return;
    s.type[n] = ty; s.idx[n] = ix; cnt[n] = ty == 2 ? 4 : 3; need[n] = nd < LASTB ? nd : LASTB; avail[n] = av; ++n;
  };
  add(0, 0, 0, -1); add(1, 0, 0, -1); add(2, 0, 0, -1); add(2, 1, 0, -1);
  add(0, 1, 1, -1); add(1, 1, 1, -1); add(2, 2, 1, -1); add(2, 3, 2, -1);
  add(0, 2, 3, -1); add(1, 2, 3, -1);
  add(2, 4, 3, 1); add(2, 5, 4, 1);
  for (int b = 2; b < 12; ++b) {
    const int T = b / 2 + 2;
    add(b & 1, T, 2 * T - 1, kind ? (b & ~1) : b);
    add(2, b + 4, b + 3, b);
  }
  s.n = n;
  s.ok = true;
  const int LIM = 8;
  int endpos[40] = {};
  int next = 0, total = 0;
  for (int b = 0; b <= LASTB; ++b) {
    s.lo[b] = next;
    const int start = total;
    for (;;) {
      if (next >= n || avail[next] > b - 1) break;
      int lastneed = 0;
      for (int i = 0; i < next; ++i) if (need[i] <= b) lastneed = endpos[i];
      if (b < 3 && need[next] > b && total + cnt[next] - lastneed > LIM + 2 * b) break;
      if (b >= 3 && need[next] > b + 1 && total + cnt[next] - start > 11) break;
      total += cnt[next]; endpos[next] = total; ++next;
    }
    s.hi[b] = next;
    if (kind == 0 && b >= WARM0 && b < WARM0 + 3) total += 1;
    int lastneed = 0;
    for (int i = 0; i < next; ++i) if (need[i] <= b) lastneed = endpos[i];
    for (int i = next; i < n; ++i) if (need[i] <= b) s.ok = false;
    s.wait[b] = total - lastneed;
  }
  if (next != n) s.ok = false;
  return s;
}
constexpr Sched SCH[2] = {make_sched(0), make_sched(1)};
static_assert(SCH[0].ok && SCH[1].ok, "every ring item must be issued in time");

struct P2Ctx { u64 a0, a1; int so0, sh, g0, nd, rot; unsigned bytes; };
__device__ __forceinline__ P2Ctx p2_ctx(u64 Wd, u64 Cre, u64 Cim, int w, int N) {
  P2Ctx c;
  const int ssm = w < 4;
  const u64 m = 0 - (u64)ssm;
  c.a0 = (Cre & m) | (Wd & ~m); c.a1 = (Cim & m) | (Wd & ~m);
  c.sh = ssm;
  c.so0 = N * 128 + ssm * (N * (65536 - 128) - 14 * 128);
  c.bytes = F_ * D_ * 2u - ssm * (F_ * D_ * 2u - D_ * P_ * 2u);
  c.g0 = 7 * w - 28 + ssm * (56 - 3 * w);
  c.nd = 7 - 3 * ssm;
#define P2ROT 1
  c.rot = P2ROT == 0 ? 0 : (P2ROT == 1 ? (N & 1) * (3 - ssm) : (P2ROT == 2 ? (N & 3) * (2 - ssm) : (N * c.nd) >> 3));
  return c;
}
__device__ __forceinline__ int p2_ds(const P2Ctx& c, int d) { const int t = d + c.rot; return t >= c.nd ? t - c.nd : t; }
__device__ __forceinline__ void b2_src(const P2Ctx& c, int d, v4u& rs, int& so) {
  const int g = c.g0 + p2_ds(c, d);
  const u64 x0 = c.a0, x1 = c.a1;
  rs = mk_rs((const void*)((g & 1) ? x1 : x0), c.bytes);
  so = c.so0 + (c.sh ? (g >> 1) * 128 : g * 65536);
}
template <int LI, int N0, int N1> __device__ __forceinline__ void b2_rows(v4u rs, int lds, int vb, int so) {
  if constexpr (N0 < N1) { glds<LI, N0 * 8192>(rs, lds, (N0 & 1) ? vb ^ 16 : vb, so); b2_rows<LI + 1024, N0 + 1, N1>(rs, lds, vb, so); }
}
template <int LI, int N0, int N1> __device__ __forceinline__ void b2_load(const P2Ctx& c, int vb, int d, int lds) {
  v4u rs; int so;
  b2_src(c, d, rs, so);
  b2_rows<LI, N0, N1>(rs, lds, vb, so);
}
__device__ __forceinline__ void b2_read(bool ssm, LAS unsigned char* img, int lane, v4u (&f)[2][4]) {
  if (ssm) {
    const int i = lane & 31, sw = ((i & 2) << 1) | ((i >> 1) & 2) | ((i >> 3) & 1);
#pragma unroll
    for (int h = 0; h < 2; ++h)
#pragma unroll
      for (int z = 0; z < 4; ++z) f[h][z] = lds_rd128(img + i * 128 + (((4 * h + (z & 2) + (lane >> 5)) ^ sw) << 4) + (z & 1) * 4096);
  } else {
    const int qq = (lane >> 2) & 3, hi = lane >> 5;
    const int c0 = (((lane >> 4) & 1) * 2 + ((lane >> 1) & 1)) ^ (((qq & 2) << 1) | (hi << 1));
#pragma unroll
    for (int h = 0; h < 2; ++h)
#pragma unroll
      for (int z = 0; z < 4; ++z) {
        LAS unsigned char* a = img + (4 * hi + qq) * 128 + ((c0 ^ ((z & 1) << 2) ^ (z >> 1)) << 4) + (lane & 1) * 8 + h * 4096 + (z >> 1) * 1024;
        const v2u lo = lds_rdtr(a), hi2 = lds_rdtr(a + 2048);
        f[h][z] = v4u{lo[0], lo[1], hi2[0], hi2[1]};
      }
  }
}

struct LdCtx { v4u rx, rw, rb; u64 Wd, Cre, Cim; int lw, lb, vx, vw0, vw1, vbb, rot, lane, vb, vwarm, xcd, ch; mutable unsigned sv, lrb, lib, ldb, ep; };
__device__ __forceinline__ void warm_chunk(u64 x0, u64 x1, u64 x2, int xc, int vwarm, int slot) {
  asm volatile("" : "+s"(xc));
  int i = slot < 6 ? slot + xc : slot - 6 + xc;
  if (slot < 6) { i = i >= 12 ? i - 12 : (i >= 6 ? i - 6 : i); } else { i = 6 + (i >= 10 ? i - 10 : (i >= 5 ? i - 5 : i)); }
  const u64 src = i < 7 ? x0 : (i < 9 ? x1 : x2);
  const unsigned bytes = (unsigned)slot > 10u ? 0u : (i < 7 ? F_ * D_ * 2 : D_ * P_ * 2);
  warm(mk_rs((const void*)src, bytes), vwarm, (i < 7 ? i : ((i - 7) & 1)) * 262144);
}

template <int KIND, int I> __device__ __forceinline__ void ld_item(const LdCtx& c) {
  constexpr int ty = SCH[KIND].type[I], t = SCH[KIND].idx[I];
  int rot = c.rot;
  asm volatile("" : "+s"(rot));
  if constexpr (ty < 2) {
    const int k0 = ((t + rot) & 7) * 128;
    glds<(t % 3) * X_SLOT + 2 * ty * 4096, ty * 65536>(c.rx, c.lw * 1024, c.vx, k0);
    glds<(t % 3) * X_SLOT + (2 * ty + 1) * 4096, ty * 65536 + 8192>(c.rx, c.lw * 1024, c.vx, k0);
    glds<B_BASE + (t % 3) * B_SLOT + ty * 2048, ty * 16384>(c.rb, c.lb, c.vbb, k0);
  } else {
    const int k0 = (((t >> 1) + rot) & 7) * (64 * F_ * 2);
    constexpr int LI = W_BASE + (t & 3) * W_SLOT, SI = (t & 1) * 32 * F_ * 2;
    glds<LI, SI>(c.rw, c.lw * 4096, c.vw0, k0);
    glds<LI + 1024, SI + 4 * F_ * 2>(c.rw, c.lw * 4096, c.vw0, k0);
    glds<LI + 2048, SI + 8 * F_ * 2>(c.rw, c.lw * 4096, c.vw1, k0);
    glds<LI + 3072, SI + 12 * F_ * 2>(c.rw, c.lw * 4096, c.vw1, k0);
  }
}
template <int KIND, int LO, int HI> __device__ __forceinline__ void ld_items(const LdCtx& c) {
  if constexpr (LO < HI) { ld_item<KIND, LO>(c); ld_items<KIND, LO + 1, HI>(c); }
}
template <int KIND, int B> __device__ __forceinline__ void loader_steps(const LdCtx& c, LAS unsigned char* L) {
  if constexpr (B == 0) {
    constexpr int FIRST = KIND ? 2 : 3;
    ld_items<KIND, 0, FIRST>(c);
    KP kp = kargs();
    const v4u r0 = mk_rs(kp->Lre, P_ * 2), r1 = mk_rs(kp->Lim, P_ * 2), r2 = mk_rs(kp->ldt, P_ * 2), r3 = mk_rs(kp->ws + WS_EPOCH, c.lw == 3 ? 4 : 0);
    asm volatile("buffer_load_ushort %0, %4, %5, 0 offen\n\tbuffer_load_ushort %1, %4, %6, 0 offen\n\tbuffer_load_ushort %2, %4, %7, 0 offen\n\t"
                 "buffer_load_dword %3, off, %8, 0 sc1"
                 : "=&v"(c.lrb), "=&v"(c.lib), "=&v"(c.ldb), "=&v"(c.ep) : "v"(c.ch * 2), "s"(r0), "s"(r1), "s"(r2), "s"(r3) : "memory");
    if constexpr (KIND == 0) {
      ld_items<0, 3, 5>(c);
      wait_vm<7>();
      BARRIER();
      ld_items<0, 5, SCH[0].hi[0]>(c);
    } else {
      ld_items<KIND, FIRST, SCH[KIND].hi[0]>(c);
    }
  } else {
    ld_items<KIND, SCH[KIND].lo[B], SCH[KIND].hi[B]>(c);
  }
  if constexpr (KIND == 0 && B == WARM0) warm(mk_rs((const void*)c.Wd, F_ * D_ * 2), c.lane < 16 ? (c.vwarm * 16 + c.lane) * 128 : OOB, 0);
  if constexpr (KIND == 0 && B == WARM0 + 1) warm(mk_rs((const void*)c.Cre, D_ * P_ * 2), c.lane < 5 ? (c.vwarm * 5 + c.lane) * 128 : OOB, 0);
  if constexpr (KIND == 0 && B == WARM0 + 2) warm(mk_rs((const void*)c.Cim, D_ * P_ * 2), c.lane < 5 ? (c.vwarm * 5 + c.lane) * 128 : OOB, 0);
  if constexpr (B == CONST_AT) {
    asm volatile("" : "+v"(c.lrb), "+v"(c.lib), "+v"(c.ldb), "+v"(c.ep) : : "memory");
    const float lr = bf2f(c.lrb), li = bf2f(c.lib);
    float dt = __builtin_amdgcn_exp2f(bf2f(c.ldb) * 1.44269504088896341f);
    dt = bf2f(pk_bf16(dt, 0.f) & 0xFFFFu);
    const float rev = __builtin_amdgcn_fractf(dt * li * 0.15915494309189535f);
    const float ex = __builtin_amdgcn_exp2f(dt * lr * 1.44269504088896341f);
    const float ar = ex * __builtin_amdgcn_cosf(rev), ai = ex * __builtin_amdgcn_sinf(rev);
    const float den = __builtin_amdgcn_rcpf(lr * lr + li * li);
    float p16r = ar, p16i = ai;
    csq(p16r, p16i, 4);
    float p32r = p16r, p32i = p16i;
    csq(p32r, p32i, 1);
    LAS f32x4* d = (LAS f32x4*)(L + LDS_CONST + c.lw * 512 + (c.lane & 15) * 32);
    d[0] = f32x4{ar, ai, ((ar - 1.f) * lr + ai * li) * den, (ai * lr - (ar - 1.f) * li) * den};
    d[1] = f32x4{p16r, p16i, p32r, p32i};
    *(volatile LAS unsigned*)(L + LDS_TAG + 4 * (3 - c.lw)) = c.ep + 1u;
    WAIT_LDS();
  }
  SCHED();
  wait_vm<SCH[KIND].wait[B]>();
  BARRIER();
  if constexpr (B < LASTB) loader_steps<KIND, B + 1>(c, L);
}

__device__ __forceinline__ bool all4(LAS unsigned char* p) {
  const v4u d = *(volatile LAS v4u*)p;
  return __builtin_amdgcn_readfirstlane(d[0] & d[1] & d[2] & d[3]) != 0u;
}
__device__ __forceinline__ void lds_wait4(LAS unsigned char* p) {
#pragma nounroll
  for (unsigned spins = 0; spins < (SPIN_CAP << 4); ++spins) {
    if (all4(p)) break;
    __builtin_amdgcn_s_sleep(2);
  }
  CFENCE();
}
__device__ __forceinline__ void lds_wait(LAS unsigned char* word) {
#pragma nounroll
  for (unsigned spins = 0; spins < (SPIN_CAP << 4); ++spins) {
    if (lds_word(word) != 0u) break;
    __builtin_amdgcn_s_sleep(2);
  }
  CFENCE();
}
__device__ __forceinline__ void cmul(float ar, float ai, float br, float bi, float& cr, float& ci) {
  cr = ar * br - ai * bi; ci = ar * bi + ai * br;
}

template <int V> struct IC { static constexpr int value = V; };
template <int n> __device__ __forceinline__ void p2_first(v4u (&af)[8], v4u ra, int va, int o0, int o1) {
  asm volatile("buffer_load_dwordx4 %0, %1, %2, %3 offen offset:%c4 sc1" : "=v"(af[n]) : "v"(va), "s"(ra), "s"(n < 4 ? o0 : o1), "i"((n & 1) * 2048 + (n & 2) * 256) : "memory");
  if constexpr (n < 7) p2_first<n + 1>(af, ra, va, o0, o1);
}
template <int d, int n>
__device__ __forceinline__ void p2_step(f32x16 (&acc)[2][2], const v4u (&bb)[2][4], v4u (&af)[8], v4u ra, int va, int o0, int o1, int w, v4u rs6, int vb,
                                        int so6) {
  constexpr int WG = d == 3 ? 7 + n : (d == 4 ? 14 - n : (d == 6 ? 7 - n : 7)), WS = d == 5 ? 7 - n : 7;
  if constexpr (d >= 3 && d <= 5 && WS < WG)
    asm volatile("s_waitcnt vmcnt(%c2)\n\ts_cmp_gt_u32 %1, 3\n\ts_cbranch_scc1 1f\n\ts_waitcnt vmcnt(%c3)\n1:" : "+v"(af[n]) : "s"(w), "i"(WG), "i"(WS) : "memory", "scc");
  else
    asm volatile("s_waitcnt vmcnt(%c1)" : "+v"(af[n]) : "i"(WG) : "memory");
  mfma32t(acc[0][n & 1], bb[n >> 2][n & 2], af[n]);
  mfma32t(acc[1][n & 1], bb[n >> 2][(n & 2) + 1], af[n]);
  if constexpr (d == 3) {
    int t;
    asm volatile("s_cmp_lt_u32 %1, 4\n\ts_cbranch_scc1 1f\n\ts_lshl_b32 %0, %1, 14\n\ts_add_u32 m0, %0, %5\n\ts_add_u32 %0, %4, %6\n\t"
                 "buffer_load_dwordx4 %2, %3, %0 offen lds\n1:"
                 : "=&s"(t) : "s"(w), "v"((n & 1) ? vb ^ 16 : vb), "s"(rs6), "s"(so6), "i"(n * 1024), "i"(n * 8192) : "memory", "scc");
  }
  if constexpr (d < 5)
    asm volatile("buffer_load_dwordx4 %0, %1, %2, %3 offen offset:%c4 sc1" : "+v"(af[n]) : "v"(va), "s"(ra), "s"(n < 4 ? o0 : o1), "i"((n & 1) * 2048 + (n & 2) * 256) : "memory");
  if constexpr (d == 5)
    asm volatile("s_cmp_lt_u32 %5, 4\n\ts_cbranch_scc1 1f\n\tbuffer_load_dwordx4 %0, %1, %2, %3 offen offset:%c4 sc1\n1:"
                 : "+v"(af[n]) : "v"(va), "s"(ra), "s"(n < 4 ? o0 : o1), "i"((n & 1) * 2048 + (n & 2) * 256), "s"(w) : "memory", "scc");
  if constexpr (n < 7) p2_step<d, n + 1>(acc, bb, af, ra, va, o0, o1, w, rs6, vb, so6);
}

__global__ __launch_bounds__(512, 2) void k_fused(Params) {
  extern __shared__ __attribute__((aligned(16))) unsigned char lds_[];
  LAS unsigned char* L = (LAS unsigned char*)lds_;
  STAMP(0);
  KP kp = kargs();
  u64 pX = (u64)kp->x, pWg = (u64)kp->Wg, pWu = (u64)kp->Wu, pBre = (u64)kp->Bre, pBim = (u64)kp->Bim, pCre = (u64)kp->Cre, pCim = (u64)kp->Cim, pWd = (u64)kp->Wd;
  const int b = __builtin_amdgcn_workgroup_id_x(), tid = __builtin_amdgcn_workitem_id_x(), lane = tid & 63;
  const int w = __builtin_amdgcn_readfirstlane(tid >> 6);
  const int xcd = b & 7, r = b >> 3;
  warm(mk_rs((const void*)(__builtin_amdgcn_s_getpc() & ~(u64)127), CODE_BYTES), lane < 2 ? lane * 32768 : OOB, (r * 8 + w) * 128);
  const bool light = (r & 7) == 7;
  const int ih = 28 * (xcd & 3) + r - (r >> 3), il = 112 + 4 * (xcd & 3) + (r >> 3);
  const int idx = ih + (il - ih) * (int)light;
  const int pn = 8 * (xcd >> 2) + (idx & 7), cb = idx >> 3;
  const int rot = pn & 6;
  const int R = xcd * 4 + (r & 3), N = r >> 2;
  asm volatile("" : "+s"(pX), "+s"(pWg), "+s"(pWu), "+s"(pBre), "+s"(pBim), "+s"(pCre), "+s"(pCim), "+s"(pWd));
  const int vb = (lane >> 3) * 1024 + (((lane & 7) ^ (((lane >> 2) & 4) | ((lane >> 4) & 2))) << 4);
  v4u b2[4][2][4];
  unsigned tag = 0;
  P2Ctx pc;

  if (__builtin_expect(w >= 4, 1)) {
    LdCtx c;
    c.lw = w - 4; c.rot = rot; c.lane = lane; c.vb = vb; c.sv = 0;
    c.Wd = pWd; c.Cre = pCre; c.Cim = pCim;
    c.vwarm = ((r - (r >> 3)) * 8 + xcd) * 4 + c.lw;
    c.xcd = xcd; c.ch = 32 * cb + 16 * (c.lw >> 1) + (lane & 15);
    const int l3 = lane >> 3, kq = lane >> 4;
    c.rx = mk_rs((const void*)pX, S_ * D_ * 2);
    c.vx = (32 * (c.lw & 1) + 16 * (l3 >> 2) + 4 * (c.lw >> 1) + (l3 & 3)) * 1024 + (((lane & 7) ^ ((4 * c.lw + kq) & 7)) << 4) + pn * 128 * 1024;
    c.rb = mk_rs((const void*)((c.lw >> 1) ? pBim : pBre), P_ * D_ * 2);
    c.vbb = (8 * (c.lw & 1) + l3) * 1024 + (((lane & 7) ^ (4 * (c.lw & 1) + kq)) << 4) + cb * 32 * 1024;
    c.lb = (c.lw >> 1) * 4096 + (c.lw & 1) * 1024;
    c.rw = mk_rs((const void*)((c.lw >> 1) ? pWu : pWg), D_ * F_ * 2);
    c.vw0 = ((c.lw & 1) * 16 + kq) * (F_ * 2) + (((lane & 15) ^ (kq << 2)) << 4) + cb * 256;
    c.vw1 = c.vw0 ^ 32;
    STAMP(1);
    if (__builtin_expect(!light, 1)) loader_steps<0, 0>(c, L);
    else { loader_steps<1, 0>(c, L); BARRIER(); }
    STAMP(2);
    {
      pc = p2_ctx(pWd, pCre, pCim, w, N);
      const int wA = c.lw < 2 ? w : c.lw, wB = wA ^ 4;
      b2_load<0, 0, 8>(pc, vb, 0, wA * 16384);
      b2_load<8192, 0, 8>(pc, vb, 1, wA * 16384);
      const P2Ctx pA = p2_ctx(pWd, pCre, pCim, wA, N), pB = p2_ctx(pWd, pCre, pCim, wB, N);
#pragma unroll
      for (int d = 0; d < 4; ++d) {
        wait_vm<8>();
        b2_read(false, L + wA * 16384 + (d & 1) * 8192, lane, b2[d]);
        WAIT_LDS();
        if (d < 2) b2_load<0, 0, 8>(pc, vb, d + 2, wA * 16384 + (d & 1) * 8192);
        else b2_load<0, 0, 8>(pA, vb, (wA & 4) + (d & 1), wA * 16384 + (d & 1) * 8192);
      }
      STAMP(3);
      lds_wait4(L + LDS_MD);
      b2_load<0, 0, 8>(pB, vb, wB & 4, wB * 16384);
      b2_load<8192, 0, 8>(pB, vb, (wB & 4) + 1, wB * 16384);
      wait_vm<0>();
      *(volatile LAS unsigned*)(L + LDS_PF + 4 * c.lw) = 1u;
      STAMP(4);
    }
    tag = lds_word(L + LDS_TAG);
  } else {
    const int c = w, q = lane >> 4, i15 = lane & 15, G = c & 1;
    const int ch = 32 * cb + 16 * (c >> 1) + i15;
    if (w == 3) {
#pragma unroll
      for (int z = 0; z < 4; ++z) *(volatile LAS v4u*)(L + LDS_DONE + 16 * z) = v4u{0u, 0u, 0u, 0u};
      WAIT_LDS();
    }
    f32x4 k0 = {0.f, 0.f, 0.f, 0.f}, k1 = {0.f, 0.f, 0.f, 0.f};
    unsigned tagv = 0;
    auto rd_const = [&]() {
      k0 = *(LAS f32x4*)(L + LDS_CONST + c * 512 + i15 * 32); k1 = *(LAS f32x4*)(L + LDS_CONST + c * 512 + i15 * 32 + 16);
      tagv = *(volatile LAS unsigned*)(L + LDS_TAG);
    };
    u64 pws = (u64)kp->ws;
    asm volatile("" : "+s"(pws));
    int xa[2];
#pragma unroll
    for (int ks = 0; ks < 2; ++ks) xa[ks] = i15 * 128 + (((4 * ks + q) ^ ((i15 >> 1) & 7)) << 4);
    asm volatile("" : "+v"(xa[0]), "+v"(xa[1]));
    const int bo = B_BASE + (c >> 1) * 2048;
    f32x4 accs[2][4], acc[4][8];
    v4u bfr[2];
    auto zero_accs = [&]() {
#pragma unroll
      for (int m = 0; m < 2; ++m)
#pragma unroll
        for (int t = 0; t < 4; ++t) { accs[m][t] = f32x4{0.f, 0.f, 0.f, 0.f}; asm volatile("" : "+v"(accs[m][t])); }
    };
    float vr[16], vi[16], t0 = 0.f, t1 = 0.f;
    auto scan_half = [&](int n) {
      const int z = n >> 1;
      if (z == 0) { vr[0] = accs[0][0][0]; vi[0] = accs[1][0][0]; }
      else if ((n & 1) == 0) { t0 = __builtin_fmaf(k0[0], vr[z - 1], accs[0][z >> 2][z & 3]); t1 = __builtin_fmaf(k0[0], vi[z - 1], accs[1][z >> 2][z & 3]); }
      else { vr[z] = __builtin_fmaf(-k0[1], vi[z - 1], t0); vi[z] = __builtin_fmaf(k0[1], vr[z - 1], t1); }
    };
    auto sc = [](int v) { asm volatile("" : "+s"(v)); return v; };

    if (!light) {
      int wa[2];
      {
        const int qq = i15 >> 2, pp = lane & 3;
#pragma unroll
        for (int u = 0; u < 2; ++u)
          wa[u] = W_BASE + (8 * q + qq) * 256 + (((2 * (2 * c + u) + (pp >> 1)) ^ ((qq << 2) | ((q & 1) << 1))) << 4) + 8 * (pp & 1);
      }
      asm volatile("" : "+v"(wa[0]), "+v"(wa[1]));
#pragma unroll
      for (int a = 0; a < 4; ++a)
#pragma unroll
        for (int u = 0; u < 8; ++u) { acc[a][u] = f32x4{0.f, 0.f, 0.f, 0.f}; asm volatile("" : "+v"(acc[a][u])); }
      zero_accs();
      v4u xf[4];
      v2u wf[2][8];
      auto rdw = [&](int s, int z) {
        const int m = z >> 2, u = (z >> 1) & 1, t = z & 1;
        wf[s & 1][z] = lds_rdtr(L + wa[u] + (s & 3) * W_SLOT + m * 8192 + t * 1024);
      };
      STAMP(1);
      BARRIER();
      STAMP(2);
#pragma unroll
      for (int z = 0; z < 8; ++z) rdw(0, z);
#pragma unroll
      for (int z = 0; z < 3; ++z) xf[z] = lds_rd128(L + xa[0] + G * 8192 + z * 2048);
#pragma unroll
      for (int m = 0; m < 2; ++m) bfr[m] = lds_rd128(L + xa[0] + bo + m * 4096);
      auto glu4 = [&](int s, int u, const v4u& x, auto&& between) {
#pragma unroll
        for (int a = 0; a < 4; ++a) {
          const v4u wv = {wf[s & 1][2 * a][0], wf[s & 1][2 * a][1], wf[s & 1][2 * a + 1][0], wf[s & 1][2 * a + 1][1]};
          mfma16t(acc[a][u], wv, x);
          SCHED();
          between(a);
          SCHED();
        }
      };
      int xb = 0, bb = 0;
      auto steps = [&](int s0, int n, const int (&xo)[5]) {
#pragma unroll
        for (int j = 0; j < n; ++j)
#pragma unroll
          for (int u = 0; u < 8; ++u) {
            const int s = s0 + j, jn = j + ((u + 3) >> 3), un = (u + 3) & 7, uw = s == 0 ? u - 4 : u;
            SCHED();
            if (u == (s == 0 ? 4 : 0)) { BARRIER(); SCHED(); }
            glu4(s, u, xf[u & 3], [&](int a) {
              if (a == 0) {
                if ((un & 3) == 0 || (j == 0 && u == 0)) xb = xa[(s0 + jn) & 1] + sc(xo[jn] + ((un >> 2) ^ G) * 8192);
                xf[(u + 3) & 3] = lds_rd128(L + xb + (un & 3) * 2048);
              }
              if ((a == 1 || a == 2) && uw >= 0 && uw < 4) rdw(s + 1, 2 * uw + a - 1);
              if (a == 3 && (u == 4 || u == 5)) {
                if (u == 4) bb = xa[(s + 1) & 1] + sc(bo + (xo[j + 1] >> 1));
                bfr[u - 4] = lds_rd128(L + bb + (u - 4) * 4096);
              }
            });
            if (u < 4) { mfma16t(accs[0][u], xf[u & 3], bfr[0]); mfma16t(accs[1][u], xf[u & 3], bfr[1]); }
          }
      };
      {
        const int xo[5] = {0, 0, 0, 0, 0};
        steps(0, 1, xo);
      }
      int xo0 = 0;
#pragma nounroll
      for (int o = 0; o < 3; ++o) {
        const int xo1 = xo0 == 2 * X_SLOT ? 0 : xo0 + X_SLOT, xo2 = xo1 == 2 * X_SLOT ? 0 : xo1 + X_SLOT;
        const int xo[5] = {xo0, xo1, xo1, xo2, xo2};
        steps(1, 4, xo);
        xo0 = xo2;
      }
      {
        const int xo[5] = {0, X_SLOT, 0, 0, 0};
        steps(1, 1, xo);
      }
      v4u bf2[2];
#pragma unroll
      for (int it = 0; it < 16; ++it) {
        const int s = (it >> 2) & 1, u = (it & 3) + 4 * (it >> 3), nx = it + 3;
        SCHED();
        glu4(s, u, xf[it & 3], [&](int a) {
          if (a == 0 && nx < 16) {
            if ((nx & 3) == 0) xb = xa[(nx >> 2) & 1] + sc(X_SLOT + ((nx >> 3) ^ G) * 8192);
            xf[nx & 3] = lds_rd128(L + xb + (nx & 3) * 2048);
          }
          if (a >= 1 && it < 3 && 3 * it + a - 1 < 8) rdw(3, 3 * it + a - 1);
          if (it == 0 && a == 3) { bb = xa[1] + sc(bo + B_SLOT); bf2[0] = lds_rd128(L + bb); bf2[1] = lds_rd128(L + bb + 4096); }
          if (it == 4 && a == 1) rd_const();
          if (it >= 8) scan_half(4 * (it - 8) + a);
        });
        if (it < 4) { mfma16t(accs[0][u], xf[it & 3], bfr[0]); mfma16t(accs[1][u], xf[it & 3], bfr[1]); }
        else if (it < 8) { mfma16t(accs[0][u], xf[it & 3], bf2[0]); mfma16t(accs[1][u], xf[it & 3], bf2[1]); }
      }
      asm volatile("s_nop 15" : "+v"(acc[0][7]), "+v"(acc[1][7]), "+v"(acc[2][7]), "+v"(acc[3][7]), "+v"(acc[0][6]), "+v"(acc[1][6]), "+v"(acc[2][6]), "+v"(acc[3][6]));
    } else {
      {
      kp = kargs();
      rsrc_t rx = mk_rsrc(kp->x, S_ * D_ * 2), rg = mk_rsrc(kp->gw, D_ * 2), rgo = mk_rsrc(kp->ws + WS_G, S_ * 4);
      const int tk0 = 128 * pn + 64 * (cb - 14) + 16 * c;
      v4u gv[8], xv[2][8];
#pragma unroll
      for (int z = 0; z < 8; ++z) gv[z] = __builtin_amdgcn_raw_buffer_load_b128(rg, (lane & 7) * 16, z * 128, 0);
#pragma unroll
      for (int gi = 0; gi < 2; ++gi)
#pragma unroll
        for (int z = 0; z < 8; ++z) xv[gi][z] = __builtin_amdgcn_raw_buffer_load_b128(rx, (lane >> 3) * 1024 + (lane & 7) * 16, (tk0 + gi * 8) * 1024 + z * 128, 0);
#pragma unroll
      for (int gi = 0; gi < 2; ++gi) {
        float s = 0.f;
#pragma unroll
        for (int z = 0; z < 8; ++z)
#pragma unroll
          for (int y = 0; y < 4; ++y) {
            const unsigned xa_ = xv[gi][z][y], ga = gv[z][y];
            s = __builtin_amdgcn_fdot2_f32_bf16(__builtin_bit_cast(bf16x2, xa_), __builtin_bit_cast(bf16x2, ga), s, false);
          }
        s += dpp<0xB1>(s); s += dpp<0x4E>(s); s += dpp<0x141>(s);
        const float g = __builtin_amdgcn_rcpf(1.f + __builtin_amdgcn_exp2f(s * -1.44269504088896341f));
        __builtin_amdgcn_raw_buffer_store_b32(f2u(g), rgo, (lane & 7) ? OOB : (lane >> 3) * 4, (tk0 + gi * 8) * 4, AUX_SC1);
      }
      }
      STAMP(1);
      zero_accs();
      int xo = 0;
#pragma nounroll
      for (int T = 0; T < 8; ++T) {
#pragma unroll
        for (int ks = 0; ks < 2; ++ks) {
          if (T < 7) BARRIER();
          v4u xf[4];
#pragma unroll
          for (int t = 0; t < 4; ++t) xf[t] = lds_rd128(L + (xa[ks] + xo + G * 8192) + t * 2048);
#pragma unroll
          for (int m = 0; m < 2; ++m) bfr[m] = lds_rd128(L + (xa[ks] + bo + (xo >> 1)) + m * 4096);
#pragma unroll
          for (int t = 0; t < 4; ++t) { mfma16t(accs[0][t], xf[t], bfr[0]); mfma16t(accs[1][t], xf[t], bfr[1]); }
          SCHED();
        }
        xo = xo == 2 * X_SLOT ? 0 : xo + X_SLOT;
      }
      asm volatile("s_nop 15" : "+v"(accs[0][0]), "+v"(accs[1][0]), "+v"(accs[0][1]), "+v"(accs[1][1]), "+v"(accs[0][2]), "+v"(accs[1][2]), "+v"(accs[0][3]), "+v"(accs[1][3]));
      rd_const();
      BARRIER();
#pragma unroll
      for (int n = 0; n < 32; ++n) { scan_half(n); SCHED(); }
    }
    *(volatile LAS unsigned*)(L + LDS_MD + 4 * w) = 1u;
    SCHED();
    STAMP(3);
    tag = __builtin_amdgcn_readfirstlane(tagv);
    const float ar = k0[0], ai = k0[1], cr = k0[2], ci = k0[3], p16r = k1[0], p16i = k1[1], p32r = k1[2], p32i = k1[3];
    __builtin_amdgcn_s_setprio(2);
    rsrc_t ra2 = mk_rsrc((const void*)(pws + WS_A2), A2_BYTES), rcar = mk_rsrc((const void*)(pws + WS_CARRY), 32 * P_ * 16);
    const int ck = 2 * pn + G;
    float pr, pi;
    {
      float tr, ti;
      const auto xr = __builtin_amdgcn_permlane16_swap(f2u(vr[15]), f2u(vr[15]), false, false);
      const auto xi = __builtin_amdgcn_permlane16_swap(f2u(vi[15]), f2u(vi[15]), false, false);
      const float e0r = u2f(xr[0]), e1r = u2f(xr[1]), e0i = u2f(xi[0]), e1i = u2f(xi[1]);
      cmul(e0r, e0i, p16r, p16i, tr, ti); tr += e1r; ti += e1i;
      const auto yr = __builtin_amdgcn_permlane32_swap(f2u(tr), f2u(tr), false, false);
      const auto yi = __builtin_amdgcn_permlane32_swap(f2u(ti), f2u(ti), false, false);
      const float f0r = u2f(yr[0]), f1r = u2f(yr[1]), f0i = u2f(yi[0]), f1i = u2f(yi[1]);
      float Er, Ei;
      cmul(f0r, f0i, p32r, p32i, Er, Ei); Er += f1r; Ei += f1i;
      const float br = (q & 2) ? f0r : 0.f, bi = (q & 2) ? f0i : 0.f;
      cmul(br, bi, p16r, p16i, tr, ti);
      pr = (q & 1) ? tr + e0r : br; pi = (q & 1) ? ti + e0i : bi;
      const v4u gq = {f2u(Er), tag, f2u(Ei), ~tag};
      st_sc1(gq, rcar, q ? OOB : ch * 16, (31 - ck) * (P_ * 16));
    }
    asm volatile("" : "+v"(pr), "+v"(pi) : : "memory");
    SCHED();
    STAMP(4);
    v4u cv[8];
    const int cvo = ch * 16 + (32 - ck + 8 * q) * (P_ * 16);
    auto ld_carry = [&]() {
#pragma unroll
      for (int z = 0; z < 8; ++z) cv[z] = ld_sc1(rcar, cvo, z * (P_ * 16));
    };
    auto publish = [&](int part) {
      *(volatile LAS unsigned*)(L + LDS_DONE + 16 * part + 4 * w) = 1u;
      const bool last = all4(L + LDS_DONE + 16 * part);
      st_flag((unsigned char*)pws, WS_FA2 + part * 32768 + pn * 128 + cb * 8, tag, last && lane < 16 ? lane * 2048 : OOB);
      CFENCE();
    };
    if (!light) {
#pragma unroll
      for (int u = 0; u < 8; ++u) {
        float hv[8];
#pragma unroll
        for (int j = 0; j < 8; ++j) hv[j] = acc[j >> 2][u][j & 3] * -1.44269504088896341f;
        SCHED();
#pragma unroll
        for (int j = 0; j < 8; ++j) hv[j] = __builtin_amdgcn_exp2f(hv[j]);
        SCHED();
#pragma unroll
        for (int j = 0; j < 8; ++j) hv[j] += 1.f;
        SCHED();
#pragma unroll
        for (int j = 0; j < 8; ++j) hv[j] = __builtin_amdgcn_rcpf(hv[j]);
        SCHED();
#pragma unroll
        for (int j = 0; j < 8; ++j) hv[j] *= acc[2 + (j >> 2)][u][j & 3];
        SCHED();
        st_sc1(v4u{pk_bf16(hv[0], hv[1]), pk_bf16(hv[2], hv[3]), pk_bf16(hv[4], hv[5]), pk_bf16(hv[6], hv[7])}, ra2, lane * 16,
               ((2 * pn + ((u >> 2) ^ G)) * NK + 4 * cb + c) * 4096 + (u & 3) * 1024);
        if (u + 1 < 8) asm volatile("" : "+v"(acc[0][u + 1]), "+v"(acc[1][u + 1]) : : "memory");
      }
    }
    CFENCE();
    SCHED();
    STAMP(5);
    ld_carry();
    CFENCE();
    SCHED();
#pragma unroll
    for (int z = 0; z < 16; ++z) {
      const float t = cr * vr[z] - ci * vi[z];
      vi[z] = -cr * vi[z] - ci * vr[z]; vr[z] = t;
    }
    float qr[4], qi[4];
    qr[0] = ar; qi[0] = ai; qr[1] = ar; qi[1] = ai; csq(qr[1], qi[1], 1);
    cmul(qr[1], qi[1], ar, ai, qr[2], qi[2]); qr[3] = qr[1]; qi[3] = qi[1]; csq(qr[3], qi[3], 1);
    const float p4r = qr[3], p4i = qi[3];
    float p64r = p32r, p64i = p32i; csq(p64r, p64i, 1);
    float p512r = p64r, p512i = p64i; csq(p512r, p512i, 3);
    float p1kr = p512r, p1ki = p512i; csq(p1kr, p1ki, 1);
    float sgr, sgi, wgr, wgi;
    cmul((q & 1) ? p16r : 1.f, (q & 1) ? p16i : 0.f, (q & 2) ? p32r : 1.f, (q & 2) ? p32i : 0.f, sgr, sgi);
    cmul((q & 1) ? p512r : 1.f, (q & 1) ? p512i : 0.f, (q & 2) ? p1kr : 1.f, (q & 2) ? p1ki : 0.f, wgr, wgi);
#pragma unroll
    for (int z = 0; z < 16; ++z) asm volatile("" : "+v"(vr[z]), "+v"(vi[z]));
    asm volatile("" : "+v"(sgr), "+v"(sgi), "+v"(wgr), "+v"(wgi), "+v"(p64r), "+v"(p64i));
    SCHED();
    const int want = imin(imax(ck - 8 * q, 0), 8);
    auto carry_ok = [&]() {
      int good = 0;
#pragma unroll
      for (int z = 0; z < 8; ++z) good += ((cv[z][1] ^ tag) | (cv[z][3] ^ ~tag)) == 0u;
      return wall(good == want);
    };
    wait_vm<8>();
    publish(0);
    STAMP(14);
    if (__builtin_expect(!carry_ok(), 0)) {
#pragma nounroll
      for (unsigned spins = 0; spins < SPIN_CAP; ++spins) {
        __builtin_amdgcn_s_sleep(2);
        CFENCE();
        ld_carry();
        if (carry_ok()) break;
      }
    }
    STAMP(6);
    float Gr, Gi;
    {
      float Hr = 0.f, Hi = 0.f, tr, ti;
#pragma unroll
      for (int z = 7; z >= 0; --z) { cmul(Hr, Hi, p64r, p64i, tr, ti); Hr = tr + u2f(cv[z][0]); Hi = ti + u2f(cv[z][2]); }
      cmul(Hr, Hi, wgr, wgi, tr, ti);
      { const auto a = __builtin_amdgcn_permlane16_swap(f2u(tr), f2u(tr), false, false);
        const auto c2 = __builtin_amdgcn_permlane16_swap(f2u(ti), f2u(ti), false, false);
        tr = u2f(a[0]) + u2f(a[1]); ti = u2f(c2[0]) + u2f(c2[1]); }
      { const auto a = __builtin_amdgcn_permlane32_swap(f2u(tr), f2u(tr), false, false);
        const auto c2 = __builtin_amdgcn_permlane32_swap(f2u(ti), f2u(ti), false, false);
        tr = u2f(a[0]) + u2f(a[1]); ti = u2f(c2[0]) + u2f(c2[1]); }
      cmul(tr, ti, sgr, sgi, Hr, Hi);
      cmul(Hr + pr, Hi + pi, cr, ci, Gr, Gi);
    }
    {
      LAS unsigned char* T = L + B_BASE + (c >> 1) * (2 * B_SLOT) + (c & 1) * 4096;
      const int s32 = ((i15 >> 1) & 1) | ((i15 >> 2) & 2), s8 = (i15 & 1) | ((i15 >> 1) & 2);
#pragma unroll
      for (int t = 0; t < 4; ++t) {
        float orr[4], oi[4];
#pragma unroll
        for (int z = 0; z < 4; ++z) {
          orr[z] = __builtin_fmaf(-qi[z], Gi, __builtin_fmaf(qr[z], Gr, vr[4 * t + z]));
          oi[z] = __builtin_fmaf(-qi[z], Gr, __builtin_fmaf(-qr[z], Gi, vi[4 * t + z]));
        }
        if (t < 3) { float tr, ti; cmul(Gr, Gi, p4r, p4i, tr, ti); Gr = tr; Gi = ti; }
        LAS unsigned char* a = T + i15 * 128 + ((t ^ s32) << 5) + ((q ^ s8) << 3);
        *(LAS v2u*)a = v2u{pk_bf16(orr[0], orr[1]), pk_bf16(orr[2], orr[3])};
        *(LAS v2u*)(a + 2048) = v2u{pk_bf16(oi[0], oi[1]), pk_bf16(oi[2], oi[3])};
      }
      const int qq = i15 >> 2, pp = lane & 3;
      const int so = (ck * NK + 56 + 4 * (cb >> 1) + (cb & 1)) * 4096 + (c >> 1) * 512;
#pragma unroll
      for (int t = 0; t < 4; ++t) {
        v2u part[2];
#pragma unroll
        for (int hh = 0; hh < 2; ++hh)
          part[hh] = lds_rdtr(T + (8 * q + 4 * hh + qq) * 128 + ((t ^ ((qq >> 1) | ((q & 1) << 1))) << 5) + ((pp ^ ((qq & 1) | (hh << 1))) << 3));
        st_sc1(v4u{part[0][0], part[0][1], part[1][0], part[1][1]}, ra2, (lane >> 5) * 8192 + (lane & 31) * 16, so + t * 1024);
      }
    }
    SCHED();
    STAMP(7);
    wait_vm<0>();
    publish(1);
    STAMP(12);
  }
  kp = kargs();
  pc = p2_ctx(pWd, pCre, pCim, w, N);
  const int d0 = 2 * pc.sh;
  if (w < 4) {
    lds_wait(L + LDS_PF + 4 * w);
    b2_read(true, L + w * 16384, lane, b2[2]);
    b2_read(true, L + w * 16384 + 8192, lane, b2[3]);
    WAIT_LDS();
    b2_load<0, 0, 8>(pc, vb, 2, w * 16384);
    b2_load<8192, 0, 8>(pc, vb, 3, w * 16384);
    STAMP(13);
  }

  {
    lds_wait4(L + LDS_DONE);
    const int cl = lane & 15, lo = pc.sh ? (pc.g0 - 28) & ~1 : pc.g0 >> 1, hi = pc.sh ? (pc.g0 + pc.nd - 29) | 1 : (pc.g0 + pc.nd - 1) >> 1;
    poll_flags((GAS u64*)(kp->ws + WS_FA2) + ((pc.sh * 16 + (R & 1) * 8 + N) * 16 + (R >> 1)) * 16 + cl, tag64(tag), (cl >= lo && cl <= hi) || cl >= 14, cl < 14);
  }
  STAMP(8);
  if (w >= 4) __builtin_amdgcn_s_setprio(1);
  glds<B_BASE + B_SLOT, 0>(mk_rs((const void*)pX, S_ * D_ * 2), w * 1024, ((lane >> 5) * 16 + ((lane >> 3) & 3)) * 1024 + (lane & 7) * 16,
                             (64 * R + 32 * (w & 1) + 4 * (w >> 1)) * 1024 + N * 128);
  const int tok = 64 * R + 32 * (w & 1) + 4 * (w >> 1) + (lane >> 4);
  const v2u dv = __builtin_amdgcn_raw_buffer_load_b64(mk_rsrc(kp->Dsk, D_ * 2), (lane & 15) * 8, N * 128, 0);
  const float ge = u2f(__builtin_amdgcn_raw_buffer_load_b32(mk_rsrc(kp->ws + WS_G, S_ * 4), tok * 4 + (lane & 1) * 64, 0, AUX_SC1));
  SCHED();
  const v4u ra = mk_rs(kp->ws + WS_A2 + (R * NK + 2 * pc.g0) * 4096, pc.nd * 8192);
  const int va = ((lane >> 4) & 1) * 1024 + (lane >> 5) * 256 + (lane & 15) * 16;
  v4u af[8];
  {
    const int o = p2_ds(pc, 0) * 8192;
    p2_first<0>(af, ra, va, o, o + 4096);
  }
  SCHED();
#define CPRIO 0
  if (w < 4) __builtin_amdgcn_s_setprio(CPRIO);
  f32x16 acc[2][2];
#pragma unroll
  for (int e = 0; e < 2; ++e)
#pragma unroll
    for (int tt = 0; tt < 2; ++tt) { acc[e][tt] = f32x16{}; asm volatile("" : "+v"(acc[e][tt])); }
  asm volatile("s_nop 1");
  auto step = [&](auto Dc) {
    constexpr int d = decltype(Dc)::value;
    v4u rs6 = ra; int so6 = 0;
    if (d == 3) { WAIT_LDS(); b2_src(pc, 6 - 3 * pc.sh, rs6, so6); }
    const int o = p2_ds(pc, d + 1 - d0) * 8192;
    p2_step<d, 0>(acc, b2[d & 3], af, ra, va, o, o + 4096, w, rs6, vb, so6);
    if (d == 2 || d == 3) b2_read(w < 4, L + w * 16384 + (d & 1) * 8192, lane, b2[d & 1]);
    if (d == 4 && w >= 4) b2_read(false, L + w * 16384, lane, b2[2]);
    SCHED();
  };
  if (w >= 4) { step(IC<0>{}); step(IC<1>{}); }
  step(IC<2>{}); step(IC<3>{}); step(IC<4>{}); step(IC<5>{});
  if (w >= 4) step(IC<6>{});
  asm volatile("s_nop 15\n\ts_nop 7" : "+v"(acc[0][0]), "+v"(acc[1][0]), "+v"(acc[0][1]), "+v"(acc[1][1]));
  SCHED();
  STAMP(9);
  kp = kargs();
  {
    const int t3 = lane & 3, g3 = ((t3 & 1) * 3) ^ (t3 >> 1), hi = lane >> 5;
#pragma unroll
    for (int T = 0; T < 2; ++T)
#pragma unroll
      for (int E = 0; E < 2; ++E)
#pragma unroll
        for (int g = 0; g < 4; ++g) {
          const int e = 2 * E + (g >> 1), q = 2 * (g & 1) + hi;
          *(LAS f32x4*)(L + w * 16384 + ((2 * T + ((lane >> 4) & 1)) * 4 + e) * 1024 + ((16 * q + (((((lane >> 2) & 3) ^ q) << 2) | (e ^ g3))) << 4)) =
              f32x4{acc[E][T][4 * g], acc[E][T][4 * g + 1], acc[E][T][4 * g + 2], acc[E][T][4 * g + 3]};
        }
  }
  if (b == 255 && w == 0) {
    rsrc_t rf = mk_rsrc(kp->ws + WS_FA2 + 32768, 256 * 8);
#pragma nounroll
    for (unsigned spins = 0; spins < SPIN_CAP; ++spins) {
      CFENCE();
      const v4u f0 = ld_sc1(rf, lane * 32, 0), f1 = ld_sc1(rf, lane * 32, 16);
      if (wall(((f0[0] ^ tag) | (f0[1] ^ ~tag) | (f0[2] ^ tag) | (f0[3] ^ ~tag) | (f1[0] ^ tag) | (f1[1] ^ ~tag) | (f1[2] ^ tag) | (f1[3] ^ ~tag)) == 0u)) break;
      __builtin_amdgcn_s_sleep(8);
    }
    st_flag(kp->ws, WS_EPOCH, tag, lane == 0 ? 0 : OOB);
  }
  WAIT_LDS();
  BARRIER();
  STAMP(10);
  {
    const int q = lane & 3, e = (lane >> 2) & 3, t3 = lane >> 4;
    const int base = ((w >> 1) * 4 + e) * 1024 + q * 256 + ((e ^ ((t3 & 1) * 3) ^ (t3 >> 1)) << 4);
    rsrc_t ro = mk_rsrc(kp->out, S_ * D_ * 4);
    const float go = dpp<0xB1>(ge);
    f32x4 res[2];
#pragma unroll
    for (int hf = 0; hf < 2; ++hf) {
      f32x4 sg = {0.f, 0.f, 0.f, 0.f}, ss = {0.f, 0.f, 0.f, 0.f};
      const int a = base + (((2 * (w & 1) + hf) ^ q) << 6);
#pragma unroll
      for (int ww = 0; ww < 8; ++ww) {
        const f32x4 v = *(LAS f32x4*)(L + ww * 16384 + a);
        if (ww < 4) ss += v; else sg += v;
      }
      const float g = (lane & 1) == hf ? ge : go;
      f32x4 m = ss * (2.f * g) + sg * (1.f - g);
      const v2u xv = *(LAS v2u*)(L + B_BASE + B_SLOT + w * 1024 + hf * 512 + lane * 8);
      m[0] += g * (bf2f(xv[0] & 0xFFFFu) * bf2f(dv[0] & 0xFFFFu)); m[1] += g * (bfhi(xv[0]) * bfhi(dv[0]));
      m[2] += g * (bf2f(xv[1] & 0xFFFFu) * bf2f(dv[1] & 0xFFFFu)); m[3] += g * (bfhi(xv[1]) * bfhi(dv[1]));
      res[hf] = m;
    }
    asm volatile("" : "+v"(res[0]), "+v"(res[1]));
#pragma unroll
    for (int hf = 0; hf < 2; ++hf)
      __builtin_amdgcn_raw_buffer_store_b128(__builtin_bit_cast(v4u, res[hf]), ro, tok * 2048 + (lane & 15) * 16 + hf * 32768 + N * 256, 0, OUT_AUX);
  }
  STAMP(11);
}

#if defined(__HIP_DEVICE_COMPILE__)
#pragma clang attribute pop
#endif

extern "C" void kernel_launch(void* const* d_in, const int* in_sizes, int n_in, void* d_out, int out_size, void* d_ws, size_t ws_size,
                              hipStream_t stream) {
  Params p;
  p.x = (const u16*)d_in[0]; p.Lre = (const u16*)d_in[1]; p.Lim = (const u16*)d_in[2]; p.Bre = (const u16*)d_in[3];
  p.Bim = (const u16*)d_in[4]; p.Cre = (const u16*)d_in[5]; p.Cim = (const u16*)d_in[6]; p.ldt = (const u16*)d_in[7];
  p.Dsk = (const u16*)d_in[8]; p.Wg = (const u16*)d_in[10]; p.Wu = (const u16*)d_in[11]; p.Wd = (const u16*)d_in[12];
  p.gw = (const u16*)d_in[13]; p.out = (float*)d_out; p.ws = (unsigned char*)d_ws;
  (void)hipFuncSetAttribute((const void*)k_fused, hipFuncAttributeMaxDynamicSharedMemorySize, LDS_BYTES);
  k_fused<<<256, 512, LDS_BYTES, stream>>>(p);
}
```

```cpp
#define SP_FIRST 8
#define WARMM
#define WARM0 2
#define PRIO2
#include <hip/hip_runtime.h>
#include <stdint.h>

typedef unsigned short u16;
typedef unsigned long long u64;
typedef unsigned v4u __attribute__((ext_vector_type(4)));
typedef unsigned v2u __attribute__((ext_vector_type(2)));
typedef short s16x4 __attribute__((ext_vector_type(4)));
typedef __bf16 bf16x8 __attribute__((ext_vector_type(8)));
typedef __bf16 bf16x2 __attribute__((ext_vector_type(2)));
typedef float f32x2 __attribute__((ext_vector_type(2)));
typedef float f32x4 __attribute__((ext_vector_type(4)));
typedef float f32x16 __attribute__((ext_vector_type(16)));
#define LAS __attribute__((address_space(3)))
#define GAS __attribute__((address_space(1)))
#define RLX_AGENT __ATOMIC_RELAXED, __HIP_MEMORY_SCOPE_AGENT
typedef __amdgpu_buffer_rsrc_t rsrc_t;

constexpr int S_ = 2048, D_ = 512, P_ = 512, F_ = 1792;
constexpr int NK = 88;
constexpr int AUX_SC1 = 16;
#define OUT_AUX 2
constexpr int WS_EPOCH = 0;
constexpr int WS_G = 8192;
constexpr int WS_FA2 = 65536;
constexpr int CTRL_BYTES = 131072;
constexpr int WS_CARRY = 131072;
constexpr int WS_A2 = 524288;
constexpr int A2_BYTES = 32 * NK * 4096;
constexpr int WS_STAMP = 32 << 20;
constexpr int LDS_BYTES = 160 * 1024;
constexpr int X_SLOT = 16384, W_BASE = 65536, W_SLOT = 16384, B_BASE = 131072, B_SLOT = 8192;
constexpr int LDS_DONE = 155648, LDS_PF = LDS_DONE + 32, LDS_MD = LDS_DONE + 48, LDS_CO = LDS_DONE + 64, LDS_TAG = LDS_DONE + 80;
constexpr int LDS_RDY = LDS_DONE + 96;
constexpr int LDS_DUMMY = LDS_DONE + 256, LDS_CONST = LDS_DONE + 512;
constexpr int OOB = 1 << 28;
constexpr int CODE_BYTES = 29 * 1024;
constexpr unsigned SPIN_CAP = 1u << 18;

struct Params {
  const u16 *x, *Wg, *Wu, *Bre, *Bim, *Cre, *Cim, *Wd;
  unsigned char* ws;
  const u16 *Lre, *Lim, *ldt, *Dsk, *gw;
  float* out;
};

#if defined(__HIP_DEVICE_COMPILE__)
#pragma clang attribute push(__attribute__((target("no-packed-fp32-ops"))), apply_to = function)
#endif
__device__ __forceinline__ float u2f(unsigned v) { return __builtin_bit_cast(float, v); }
__device__ __forceinline__ unsigned f2u(float v) { return __builtin_bit_cast(unsigned, v); }
__device__ __forceinline__ bool wall(bool p) { return __builtin_amdgcn_ballot_w64(!p) == 0; }
__device__ __forceinline__ bool wany(bool p) { return __builtin_amdgcn_ballot_w64(p) != 0; }
__device__ __forceinline__ int imin(int a, int b) { return a < b ? a : b; }
__device__ __forceinline__ int imax(int a, int b) { return a > b ? a : b; }
template <int CTRL> __device__ __forceinline__ float dpp(float v) { return u2f(__builtin_amdgcn_mov_dpp(f2u(v), CTRL, 0xF, 0xF, true)); }
__device__ __forceinline__ rsrc_t mk_rsrc(const void* p, int bytes) {
  return __builtin_amdgcn_make_buffer_rsrc((void*)p, 0, bytes, 0x00020000);
}
__device__ __forceinline__ float bf2f(unsigned v) { return u2f(v << 16); }
__device__ __forceinline__ float bfhi(unsigned v) { return u2f(v & 0xFFFF0000u); }
__device__ __forceinline__ unsigned pk_bf16(float lo, float hi) {
  f32x2 t = {lo, hi};
  return __builtin_bit_cast(unsigned, __builtin_convertvector(t, bf16x2));
}
__device__ __forceinline__ void mfma16t(f32x4& c, v4u a, v4u b) {
  asm volatile("v_mfma_f32_16x16x32_bf16 %0, %1, %2, %0" : "+v"(c) : "v"(a), "v"(b));
}
__device__ __forceinline__ void mfma32t(f32x16& c, v4u a, v4u b) {
  asm volatile("v_mfma_f32_32x32x16_bf16 %0, %1, %2, %0" : "+v"(c) : "v"(a), "v"(b));
}
__device__ __forceinline__ v4u lds_rd128(LAS unsigned char* p) { return *(LAS v4u*)p; }
__device__ __forceinline__ v2u lds_rdtr(LAS unsigned char* p) {
  return __builtin_bit_cast(v2u, __builtin_amdgcn_ds_read_tr16_b64_v4i16((LAS s16x4*)p));
}
__device__ __forceinline__ v4u mk_rs(const void* p, unsigned bytes) {
  const u64 a = (u64)p;
  unsigned lo = (unsigned)a, hi = (unsigned)(a >> 32) & 0xFFFFu;
  asm volatile("" : "+s"(lo), "+s"(hi));
  return v4u{lo, hi, bytes, 0x00020000u};
}
template <int LI, int SI> __device__ __forceinline__ void glds(v4u rs, int lds, int voff, int soff) {
  int t;
  asm volatile("s_add_u32 m0, %1, %5\n\ts_add_u32 %0, %4, %6\n\tbuffer_load_dwordx4 %2, %3, %0 offen lds"
               : "=&s"(t) : "s"(lds), "v"(voff), "s"(rs), "s"(soff), "i"(LI), "i"(SI) : "memory", "scc");
}
typedef const __attribute__((address_space(4))) Params* KP;
__device__ __forceinline__ KP kargs() {
  KP k = (KP)__builtin_amdgcn_kernarg_segment_ptr();
  asm volatile("" : "+s"(k));
  return k;
}
__device__ __forceinline__ void warm(v4u rs, int voff, int soff) {
  asm volatile("s_mov_b32 m0, %3\n\ts_nop 0\n\tbuffer_load_dword %0, %1, %2 offen lds" : : "v"(voff), "s"(rs), "s"(soff), "i"(LDS_DUMMY) : "memory");
}
__device__ __forceinline__ v4u ld_sc1(rsrc_t rs, int voff, int soff) { return __builtin_amdgcn_raw_buffer_load_b128(rs, voff, soff, AUX_SC1); }
__device__ __forceinline__ void st_sc1(v4u v, rsrc_t rs, int voff, int soff) { __builtin_amdgcn_raw_buffer_store_b128(v, rs, voff + soff, 0, AUX_SC1); }
__device__ __forceinline__ u64 tag64(unsigned tag) { return ((u64)(~tag) << 32) | tag; }
__device__ __forceinline__ void st_flag(unsigned char* ws, int off, unsigned tag, int voff) {
  __builtin_amdgcn_raw_buffer_store_b64(v2u{tag, ~tag}, mk_rsrc(ws, CTRL_BYTES), voff, off, AUX_SC1);
}
__device__ __forceinline__ unsigned lds_word(LAS unsigned char* p) { return __builtin_amdgcn_readfirstlane(*(volatile LAS unsigned*)p); }
#define BARRIER() asm volatile("s_barrier" ::: "memory")
#define SCHED() __builtin_amdgcn_sched_barrier(0)
#define CFENCE() asm volatile("" ::: "memory")
#define WAIT_LDS() asm volatile("s_waitcnt lgkmcnt(0)" ::: "memory")
template <int N> __device__ __forceinline__ void wait_vm() { asm volatile("s_waitcnt vmcnt(%c0)" ::"i"(N) : "memory"); }

#define STAMP(i) do {} while (0)
#define PICK 0
#define SP_NEXT 8
#define PK6 3
#define POLL_SLEEP 4
#define POLL_IDLE 16

__device__ __forceinline__ void poll_flags(GAS u64* f, u64 want, bool care, bool late) {
#pragma nounroll
  for (unsigned spins = 0; spins < SPIN_CAP; ++spins) {
    CFENCE();
    const u64 v = __hip_atomic_load(f, RLX_AGENT);
    if (wall(!care || v == want)) break;
    if (wany(late && v == want)) __builtin_amdgcn_s_sleep(POLL_SLEEP); else __builtin_amdgcn_s_sleep(POLL_IDLE);
  }
  CFENCE();
}
__device__ __forceinline__ void csq(float& r, float& i, int n) {
#pragma unroll
  for (int z = 0; z < n; ++z) { const float t = r * r - i * i; i = 2.f * r * i; r = t; }
}

#define NROT 102
#define CONST_AT 2
constexpr int LASTB = 13;
#define RAWN 2
#define NRAWD 2
constexpr int raw_lo(int b) { const int n = (b - (LASTB + 1)) * RAWN + 8 * NRAWD; return n < 0 ? 0 : n; }
struct Sched {
  int n;
  int type[40], idx[40];
  int lo[LASTB + 1], hi[LASTB + 1];
  int wait[LASTB + 1];
  bool ok;
};
constexpr Sched make_sched(int kind) {
  Sched s{};
  int cnt[40] = {}, need[40] = {}, avail[40] = {};
  int n = 0;
  auto add = [&](int ty, int ix, int nd, int av) {
    if (kind && ty == 2) return;
    s.type[n] = ty; s.idx[n] = ix; cnt[n] = ty == 2 ? 4 : 3; need[n] = nd < LASTB ? nd : LASTB; avail[n] = av; ++n;
  };
  add(0, 0, 0, -1); add(1, 0, 0, -1); add(2, 0, 0, -1); add(2, 1, 0, -1);
  add(0, 1, 1, -1); add(1, 1, 1, -1); add(2, 2, 1, -1); add(2, 3, 2, -1);
  add(0, 2, 3, -1); add(1, 2, 3, -1);
  add(2, 4, 3, 1); add(2, 5, 4, 1);
  for (int b = 2; b < 12; ++b) {
    const int T = b / 2 + 2;
    add(b & 1, T, 2 * T - 1, kind ? (b & ~1) : b);
    add(2, b + 4, b + 3, b);
  }
  s.n = n;
  s.ok = true;
  const int LIM = 8;
  int endpos[40] = {};
  int next = 0, total = 0;
  for (int b = 0; b <= LASTB; ++b) {
    s.lo[b] = next;
    const int start = total;
    for (;;) {
      if (next >= n || avail[next] > b - 1) break;
      int lastneed = 0;
      for (int i = 0; i < next; ++i) if (need[i] <= b) lastneed = endpos[i];
      if (b < 3 && need[next] > b && total + cnt[next] - lastneed > LIM + 2 * b) break;
      if (b >= 3 && need[next] > b + 1 && total + cnt[next] - start > 11) break;
      total += cnt[next]; endpos[next] = total; ++next;
    }
    s.hi[b] = next;
    if (kind == 0) total += raw_lo(b + 1) - raw_lo(b);
    if (kind == 0 && b >= WARM0 && b < WARM0 + 3) total += 1;
    int lastneed = 0;
    for (int i = 0; i < next; ++i) if (need[i] <= b) lastneed = endpos[i];
    for (int i = next; i < n; ++i) if (need[i] <= b) s.ok = false;
    s.wait[b] = total - lastneed;
  }
  if (next != n) s.ok = false;
  return s;
}
constexpr Sched SCH[2] = {make_sched(0), make_sched(1)};
static_assert(SCH[0].ok && SCH[1].ok, "every ring item must be issued in time");

struct P2Ctx { u64 a0, a1; int so0, sh, g0, nd, rot; unsigned bytes; };
__device__ __forceinline__ P2Ctx p2_ctx(u64 Wd, u64 Cre, u64 Cim, int w, int N) {
  P2Ctx c;
  const int ssm = w < 4;
  const u64 m = 0 - (u64)ssm;
  c.a0 = (Cre & m) | (Wd & ~m); c.a1 = (Cim & m) | (Wd & ~m);
  c.sh = ssm;
  c.so0 = N * 128 + ssm * (N * (65536 - 128) - 14 * 128);
  c.bytes = F_ * D_ * 2u - ssm * (F_ * D_ * 2u - D_ * P_ * 2u);
  c.g0 = 7 * w - 28 + ssm * (56 - 3 * w);
  c.nd = 7 - 3 * ssm;
#define P2ROT 1
  c.rot = P2ROT == 0 ? 0 : (P2ROT == 1 ? (N & 1) * (3 - ssm) : (P2ROT == 2 ? (N & 3) * (2 - ssm) : (N * c.nd) >> 3));
  return c;
}
__device__ __forceinline__ int p2_ds(const P2Ctx& c, int d) { const int t = d + c.rot; return t >= c.nd ? t - c.nd : t; }
__device__ __forceinline__ void b2_src(const P2Ctx& c, int d, v4u& rs, int& so) {
  const int g = c.g0 + p2_ds(c, d);
  const u64 x0 = c.a0, x1 = c.a1;
  rs = mk_rs((const void*)((g & 1) ? x1 : x0), c.bytes);
  so = c.so0 + (c.sh ? (g >> 1) * 128 : g * 65536);
}
template <int LI, int N0, int N1> __device__ __forceinline__ void b2_rows(v4u rs, int lds, int vb, int so) {
  if constexpr (N0 < N1) { glds<LI, N0 * 8192>(rs, lds, (N0 & 1) ? vb ^ 16 : vb, so); b2_rows<LI + 1024, N0 + 1, N1>(rs, lds, vb, so); }
}
template <int LI, int N0, int N1> __device__ __forceinline__ void b2_load(const P2Ctx& c, int vb, int d, int lds) {
  v4u rs; int so;
  b2_src(c, d, rs, so);
  b2_rows<LI, N0, N1>(rs, lds, vb, __builtin_amdgcn_readfirstlane(so));
}
__device__ __forceinline__ void b2_read(bool ssm, LAS unsigned char* img, int lane, v4u (&f)[2][4]) {
  if (ssm) {
    const int i = lane & 31, sw = ((i & 2) << 1) | ((i >> 1) & 2) | ((i >> 3) & 1);
#pragma unroll
    for (int h = 0; h < 2; ++h)
#pragma unroll
      for (int z = 0; z < 4; ++z) f[h][z] = lds_rd128(img + i * 128 + (((4 * h + (z & 2) + (lane >> 5)) ^ sw) << 4) + (z & 1) * 4096);
  } else {
    const int qq = (lane >> 2) & 3, hi = lane >> 5;
    const int c0 = (((lane >> 4) & 1) * 2 + ((lane >> 1) & 1)) ^ (((qq & 2) << 1) | (hi << 1));
#pragma unroll
    for (int h = 0; h < 2; ++h)
#pragma unroll
      for (int z = 0; z < 4; ++z) {
        LAS unsigned char* a = img + (4 * hi + qq) * 128 + ((c0 ^ ((z & 1) << 2) ^ (z >> 1)) << 4) + (lane & 1) * 8 + h * 4096 + (z >> 1) * 1024;
        const v2u lo = lds_rdtr(a), hi2 = lds_rdtr(a + 2048);
        f[h][z] = v4u{lo[0], lo[1], hi2[0], hi2[1]};
      }
  }
}

struct LdCtx { v4u rx, rw, rb; u64 Wd, Cre, Cim; int lw, lb, vx, vw0, vw1, vbb, rot, lane, vb, vwarm, xcd, ch; mutable unsigned sv, lrb, lib, ldb, ep; };
__device__ __forceinline__ void warm_chunk(u64 x0, u64 x1, u64 x2, int xc, int vwarm, int slot) {
  asm volatile("" : "+s"(xc));
  int i = slot < 6 ? slot + xc : slot - 6 + xc;
  if (slot < 6) { i = i >= 12 ? i - 12 : (i >= 6 ? i - 6 : i); } else { i = 6 + (i >= 10 ? i - 10 : (i >= 5 ? i - 5 : i)); }
  const u64 src = i < 7 ? x0 : (i < 9 ? x1 : x2);
  const unsigned bytes = (unsigned)slot > 10u ? 0u : (i < 7 ? F_ * D_ * 2 : D_ * P_ * 2);
  warm(mk_rs((const void*)src, bytes), vwarm, (i < 7 ? i : ((i - 7) & 1)) * 262144);
}

template <int KIND, int I> __device__ __forceinline__ void ld_item(const LdCtx& c) {
  constexpr int ty = SCH[KIND].type[I], t = SCH[KIND].idx[I];
  int rot = c.rot;
  asm volatile("" : "+s"(rot));
  if constexpr (ty < 2) {
    const int k0 = ((t + rot) & 7) * 128;
    glds<(t % 3) * X_SLOT + 2 * ty * 4096, ty * 65536>(c.rx, c.lw * 1024, c.vx, k0);
    glds<(t % 3) * X_SLOT + (2 * ty + 1) * 4096, ty * 65536 + 8192>(c.rx, c.lw * 1024, c.vx, k0);
    glds<B_BASE + (t % 3) * B_SLOT + ty * 2048, ty * 16384>(c.rb, c.lb, c.vbb, k0);
  } else {
    const int k0 = (((t >> 1) + rot) & 7) * (64 * F_ * 2);
    constexpr int LI = W_BASE + (t & 3) * W_SLOT, SI = (t & 1) * 32 * F_ * 2;
    glds<LI, SI>(c.rw, c.lw * 4096, c.vw0, k0);
    glds<LI + 1024, SI + 4 * F_ * 2>(c.rw, c.lw * 4096, c.vw0, k0);
    glds<LI + 2048, SI + 8 * F_ * 2>(c.rw, c.lw * 4096, c.vw1, k0);
    glds<LI + 3072, SI + 12 * F_ * 2>(c.rw, c.lw * 4096, c.vw1, k0);
  }
}
template <int KIND, int LO, int HI> __device__ __forceinline__ void ld_items(const LdCtx& c) {
  if constexpr (LO < HI) { ld_item<KIND, LO>(c); ld_items<KIND, LO + 1, HI>(c); }
}
template <int A, int E> __device__ __forceinline__ void raw_loads(const P2Ctx& pc, int vb, v4u (&raw)[8 * NRAWD]) {
  if constexpr (A < E) {
    v4u rs; int so;
    b2_src(pc, 4 + (A >> 3), rs, so);
    asm volatile("buffer_load_dwordx4 %0, %1, %2, %3 offen" : "=v"(raw[A]) : "v"((A & 1) ? vb ^ 16 : vb), "s"(rs), "s"(so + (A & 7) * 8192) : "memory");
    raw_loads<A + 1, E>(pc, vb, raw);
  }
}
template <int KIND, int B> __device__ __forceinline__ void loader_steps(const LdCtx& c, LAS unsigned char* L, const P2Ctx& pc, v4u (&raw)[8 * NRAWD]) {
  if constexpr (B == 0) {
    constexpr int FIRST = KIND ? 2 : 3;
    ld_items<KIND, 0, FIRST>(c);
    KP kp = kargs();
    const v4u r0 = mk_rs(kp->Lre, P_ * 2), r1 = mk_rs(kp->Lim, P_ * 2), r2 = mk_rs(kp->ldt, P_ * 2), r3 = mk_rs(kp->ws + WS_EPOCH, c.lw == 3 ? 4 : 0);
    asm volatile("buffer_load_ushort %0, %4, %5, 0 offen\n\tbuffer_load_ushort %1, %4, %6, 0 offen\n\tbuffer_load_ushort %2, %4, %7, 0 offen\n\t"
                 "buffer_load_dword %3, off, %8, 0 sc1"
                 : "=&v"(c.lrb), "=&v"(c.lib), "=&v"(c.ldb), "=&v"(c.ep) : "v"(c.ch * 2), "s"(r0), "s"(r1), "s"(r2), "s"(r3) : "memory");
    if constexpr (KIND == 0) {
      ld_items<0, 3, 5>(c);
      wait_vm<7>();
      BARRIER();
      ld_items<0, 5, SCH[0].hi[0]>(c);
    } else {
      ld_items<KIND, FIRST, SCH[KIND].hi[0]>(c);
    }
  } else {
    ld_items<KIND, SCH[KIND].lo[B], SCH[KIND].hi[B]>(c);
  }
  if constexpr (KIND == 0) raw_loads<raw_lo(B), raw_lo(B + 1)>(pc, c.vb, raw);
  if constexpr (KIND == 0 && B == WARM0) warm(mk_rs((const void*)c.Wd, F_ * D_ * 2), c.lane < 16 ? (c.vwarm * 16 + c.lane) * 128 : OOB, 0);
  if constexpr (KIND == 0 && B == WARM0 + 1) warm(mk_rs((const void*)c.Cre, D_ * P_ * 2), c.lane < 5 ? (c.vwarm * 5 + c.lane) * 128 : OOB, 0);
  if constexpr (KIND == 0 && B == WARM0 + 2) warm(mk_rs((const void*)c.Cim, D_ * P_ * 2), c.lane < 5 ? (c.vwarm * 5 + c.lane) * 128 : OOB, 0);
  if constexpr (B == CONST_AT) {
    asm volatile("" : "+v"(c.lrb), "+v"(c.lib), "+v"(c.ldb), "+v"(c.ep) : : "memory");
    const float lr = bf2f(c.lrb), li = bf2f(c.lib);
    float dt = __builtin_amdgcn_exp2f(bf2f(c.ldb) * 1.44269504088896341f);
    dt = bf2f(pk_bf16(dt, 0.f) & 0xFFFFu);
    const float rev = __builtin_amdgcn_fractf(dt * li * 0.15915494309189535f);
    const float ex = __builtin_amdgcn_exp2f(dt * lr * 1.44269504088896341f);
    const float ar = ex * __builtin_amdgcn_cosf(rev), ai = ex * __builtin_amdgcn_sinf(rev);
    const float den = __builtin_amdgcn_rcpf(lr * lr + li * li);
    float p16r = ar, p16i = ai;
    csq(p16r, p16i, 4);
    float p32r = p16r, p32i = p16i;
    csq(p32r, p32i, 1);
    LAS f32x4* d = (LAS f32x4*)(L + LDS_CONST + c.lw * 512 + (c.lane & 15) * 32);
    d[0] = f32x4{ar, ai, ((ar - 1.f) * lr + ai * li) * den, (ai * lr - (ar - 1.f) * li) * den};
    d[1] = f32x4{p16r, p16i, p32r, p32i};
    *(volatile LAS unsigned*)(L + LDS_TAG + 4 * (3 - c.lw)) = c.ep + 1u;
    WAIT_LDS();
  }
  SCHED();
  wait_vm<SCH[KIND].wait[B]>();
  BARRIER();
  if constexpr (B < LASTB) loader_steps<KIND, B + 1>(c, L, pc, raw);
}

__device__ __forceinline__ bool all4(LAS unsigned char* p) {
  const v4u d = *(volatile LAS v4u*)p;
  return __builtin_amdgcn_readfirstlane(d[0] & d[1] & d[2] & d[3]) != 0u;
}
__device__ __forceinline__ void lds_wait4(LAS unsigned char* p) {
#pragma nounroll
  for (unsigned spins = 0; spins < (SPIN_CAP << 4); ++spins) {
    if (all4(p)) break;
    __builtin_amdgcn_s_sleep(2);
  }
  CFENCE();
}
__device__ __forceinline__ void lds_wait(LAS unsigned char* word) {
#pragma nounroll
  for (unsigned spins = 0; spins < (SPIN_CAP << 4); ++spins) {
    if (lds_word(word) != 0u) break;
    __builtin_amdgcn_s_sleep(2);
  }
  CFENCE();
}
__device__ __forceinline__ void lds_wait_eq(LAS unsigned char* word, unsigned want) {
#pragma nounroll
  for (unsigned spins = 0; spins < (SPIN_CAP << 4); ++spins) {
    if (lds_word(word) == want) break;
    __builtin_amdgcn_s_sleep(2);
  }
  CFENCE();
}
__device__ __forceinline__ void cmul(float ar, float ai, float br, float bi, float& cr, float& ci) {
  cr = ar * br - ai * bi; ci = ar * bi + ai * br;
}

template <int V> struct IC { static constexpr int value = V; };
template <int n> __device__ __forceinline__ void p2_first(v4u (&af)[8], v4u ra, int va, int o0, int o1) {
  asm volatile("buffer_load_dwordx4 %0, %1, %2, %3 offen offset:%c4 sc1" : "=v"(af[n]) : "v"(va), "s"(ra), "s"(n < 4 ? o0 : o1), "i"((n & 1) * 2048 + (n & 2) * 256) : "memory");
  if constexpr (n < 7) p2_first<n + 1>(af, ra, va, o0, o1);
}
template <int d, int n>
__device__ __forceinline__ void p2_step(f32x16 (&acc)[2][2], const v4u (&bb)[2][4], v4u (&af)[8], v4u ra, int va, int o0, int o1, int w) {
  constexpr int WG = d == 6 ? 7 - n : 7, WS = d == 5 ? 7 - n : 7;
  if constexpr (d >= 3 && d <= 5 && WS < WG)
    asm volatile("s_waitcnt vmcnt(%c2)\n\ts_cmp_gt_u32 %1, 3\n\ts_cbranch_scc1 1f\n\ts_waitcnt vmcnt(%c3)\n1:" : "+v"(af[n]) : "s"(w), "i"(WG), "i"(WS) : "memory", "scc");
  else
    asm volatile("s_waitcnt vmcnt(%c1)" : "+v"(af[n]) : "i"(WG) : "memory");
  mfma32t(acc[0][n & 1], bb[n >> 2][n & 2], af[n]);
  mfma32t(acc[1][n & 1], bb[n >> 2][(n & 2) + 1], af[n]);
  if constexpr (d < 5)
    asm volatile("buffer_load_dwordx4 %0, %1, %2, %3 offen offset:%c4 sc1" : "+v"(af[n]) : "v"(va), "s"(ra), "s"(n < 4 ? o0 : o1), "i"((n & 1) * 2048 + (n & 2) * 256) : "memory");
  if constexpr (d == 5)
    asm volatile("s_cmp_lt_u32 %5, 4\n\ts_cbranch_scc1 1f\n\tbuffer_load_dwordx4 %0, %1, %2, %3 offen offset:%c4 sc1\n1:"
                 : "+v"(af[n]) : "v"(va), "s"(ra), "s"(n < 4 ? o0 : o1), "i"((n & 1) * 2048 + (n & 2) * 256), "s"(w) : "memory", "scc");
  if constexpr (n < 7) p2_step<d, n + 1>(acc, bb, af, ra, va, o0, o1, w);
}

__global__ __launch_bounds__(512, 2) void k_fused(Params) {
  extern __shared__ __attribute__((aligned(16))) unsigned char lds_[];
  LAS unsigned char* L = (LAS unsigned char*)lds_;
  STAMP(0);
  KP kp = kargs();
  u64 pX = (u64)kp->x, pWg = (u64)kp->Wg, pWu = (u64)kp->Wu, pBre = (u64)kp->Bre, pBim = (u64)kp->Bim, pCre = (u64)kp->Cre, pCim = (u64)kp->Cim, pWd = (u64)kp->Wd;
  const int b = __builtin_amdgcn_workgroup_id_x(), tid = __builtin_amdgcn_workitem_id_x(), lane = tid & 63;
  const int w = __builtin_amdgcn_readfirstlane(tid >> 6);
  const int xcd = b & 7, r = b >> 3;
  warm(mk_rs((const void*)(__builtin_amdgcn_s_getpc() & ~(u64)127), CODE_BYTES), lane < 2 ? lane * 32768 : OOB, (r * 8 + w) * 128);
  const bool light = (r & 7) == 7;
  const int ih = 28 * (xcd & 3) + r - (r >> 3), il = 112 + 4 * (xcd & 3) + (r >> 3);
  const int idx = ih + (il - ih) * (int)light;
  const int pn = 8 * (xcd >> 2) + (idx & 7), cb = idx >> 3;
  const int rot = pn & 6;
  int R = xcd * 4 + (r & 3), N = r >> 2;
  asm volatile("" : "+s"(pX), "+s"(pWg), "+s"(pWu), "+s"(pBre), "+s"(pBim), "+s"(pCre), "+s"(pCim), "+s"(pWd));
  const int vb = (lane >> 3) * 1024 + (((lane & 7) ^ (((lane >> 2) & 4) | ((lane >> 4) & 2))) << 4);
  v4u b2[4][2][4];
  unsigned tag = 0;
  P2Ctx pc;

  if (__builtin_expect(w >= 4, 1)) {
    LdCtx c;
    c.lw = w - 4; c.rot = rot; c.lane = lane; c.vb = vb; c.sv = 0;
    c.Wd = pWd; c.Cre = pCre; c.Cim = pCim;
    c.vwarm = ((r - (r >> 3)) * 8 + xcd) * 4 + c.lw;
    c.xcd = xcd; c.ch = 32 * cb + 16 * (c.lw >> 1) + (lane & 15);
    const int l3 = lane >> 3, kq = lane >> 4;
    c.rx = mk_rs((const void*)pX, S_ * D_ * 2);
    c.vx = (32 * (c.lw & 1) + 16 * (l3 >> 2) + 4 * (c.lw >> 1) + (l3 & 3)) * 1024 + (((lane & 7) ^ ((4 * c.lw + kq) & 7)) << 4) + pn * 128 * 1024;
    c.rb = mk_rs((const void*)((c.lw >> 1) ? pBim : pBre), P_ * D_ * 2);
    c.vbb = (8 * (c.lw & 1) + l3) * 1024 + (((lane & 7) ^ (4 * (c.lw & 1) + kq)) << 4) + cb * 32 * 1024;
    c.lb = (c.lw >> 1) * 4096 + (c.lw & 1) * 1024;
    c.rw = mk_rs((const void*)((c.lw >> 1) ? pWu : pWg), D_ * F_ * 2);
    c.vw0 = ((c.lw & 1) * 16 + kq) * (F_ * 2) + (((lane & 15) ^ (kq << 2)) << 4) + cb * 256;
    c.vw1 = c.vw0 ^ 32;
    STAMP(1);
    pc = p2_ctx(pWd, pCre, pCim, w, N);
    v4u raw[8 * NRAWD];
    if (__builtin_expect(!light, 1)) loader_steps<0, 0>(c, L, pc, raw);
    else { loader_steps<1, 0>(c, L, pc, raw); BARRIER(); raw_loads<0, 8 * NRAWD>(pc, vb, raw); }
    STAMP(2);
    {
      const int wA = c.lw < 2 ? w : c.lw;
      b2_load<0, 0, 8>(pc, vb, 0, wA * 16384);
      b2_load<8192, 0, 8>(pc, vb, 1, wA * 16384);
      const P2Ctx pp = p2_ctx(pWd, pCre, pCim, c.lw, N);
#pragma unroll
      for (int d = 0; d < 4; ++d) {
        wait_vm<8>();
        b2_read(false, L + wA * 16384 + (d & 1) * 8192, lane, b2[d]);
        WAIT_LDS();
        if (d == 2) lds_wait4(L + LDS_MD);
        if (d < 2) b2_load<0, 0, 8>(pc, vb, d + 2, wA * 16384 + (d & 1) * 8192);
        else b2_load<0, 0, 8>(pp, vb, d & 1, c.lw * 16384 + (d & 1) * 8192);
      }
      STAMP(3);
#pragma unroll
      for (int z = 0; z < 16; z += 8)
        asm volatile("" : "+v"(raw[z]), "+v"(raw[z + 1]), "+v"(raw[z + 2]), "+v"(raw[z + 3]), "+v"(raw[z + 4]), "+v"(raw[z + 5]), "+v"(raw[z + 6]), "+v"(raw[z + 7]));
#pragma unroll
      for (int n = 0; n < 16; ++n) *(LAS v4u*)(L + w * 16384 + n * 1024 + lane * 16) = raw[n];
      wait_vm<0>();
      *(volatile LAS unsigned*)(L + LDS_PF + 4 * c.lw) = 1u;
      STAMP(4);
      lds_wait(L + LDS_CO + 4 * c.lw);
      b2_load<0, 0, 8>(pp, vb, 2, c.lw * 16384);
      b2_load<8192, 0, 8>(pp, vb, 3, c.lw * 16384);
      wait_vm<0>();
      *(volatile LAS unsigned*)(L + LDS_PF + 4 * c.lw) = 2u;
      STAMP(5);
    }
    tag = lds_word(L + LDS_TAG);
  } else {
    const int c = w, q = lane >> 4, i15 = lane & 15, G = c & 1;
    const int ch = 32 * cb + 16 * (c >> 1) + i15;
    if (w == 3) {
#pragma unroll
      for (int z = 0; z < 5; ++z) *(volatile LAS v4u*)(L + LDS_DONE + 16 * z) = v4u{0u, 0u, 0u, 0u};
      *(volatile LAS unsigned*)(L + LDS_RDY) = 0u;
      WAIT_LDS();
    }
    f32x4 k0 = {0.f, 0.f, 0.f, 0.f}, k1 = {0.f, 0.f, 0.f, 0.f};
    unsigned tagv = 0;
    auto rd_const = [&]() {
      k0 = *(LAS f32x4*)(L + LDS_CONST + c * 512 + i15 * 32); k1 = *(LAS f32x4*)(L + LDS_CONST + c * 512 + i15 * 32 + 16);
      tagv = *(volatile LAS unsigned*)(L + LDS_TAG);
    };
    u64 pws = (u64)kp->ws;
    asm volatile("" : "+s"(pws));
    int xa[2];
#pragma unroll
    for (int ks = 0; ks < 2; ++ks) xa[ks] = i15 * 128 + (((4 * ks + q) ^ ((i15 >> 1) & 7)) << 4);
    asm volatile("" : "+v"(xa[0]), "+v"(xa[1]));
    const int bo = B_BASE + (c >> 1) * 2048;
    f32x4 accs[2][4], acc[4][8];
    v4u bfr[2];
    auto zero_accs = [&]() {
#pragma unroll
      for (int m = 0; m < 2; ++m)
#pragma unroll
        for (int t = 0; t < 4; ++t) { accs[m][t] = f32x4{0.f, 0.f, 0.f, 0.f}; asm volatile("" : "+v"(accs[m][t])); }
    };
    float vr[16], vi[16], t0 = 0.f, t1 = 0.f;
    auto scan_half = [&](int n) {
      const int z = n >> 1;
      if (z == 0) { vr[0] = accs[0][0][0]; vi[0] = accs[1][0][0]; }
      else if ((n & 1) == 0) { t0 = __builtin_fmaf(k0[0], vr[z - 1], accs[0][z >> 2][z & 3]); t1 = __builtin_fmaf(k0[0], vi[z - 1], accs[1][z >> 2][z & 3]); }
      else { vr[z] = __builtin_fmaf(-k0[1], vi[z - 1], t0); vi[z] = __builtin_fmaf(k0[1], vr[z - 1], t1); }
    };
    auto sc = [](int v) { asm volatile("" : "+s"(v)); return v; };

    if (!light) {
      int wa[2];
      {
        const int qq = i15 >> 2, pp = lane & 3;
#pragma unroll
        for (int u = 0; u < 2; ++u)
          wa[u] = W_BASE + (8 * q + qq) * 256 + (((2 * (2 * c + u) + (pp >> 1)) ^ ((qq << 2) | ((q & 1) << 1))) << 4) + 8 * (pp & 1);
      }
      asm volatile("" : "+v"(wa[0]), "+v"(wa[1]));
#pragma unroll
      for (int a = 0; a < 4; ++a)
#pragma unroll
        for (int u = 0; u < 8; ++u) { acc[a][u] = f32x4{0.f, 0.f, 0.f, 0.f}; asm volatile("" : "+v"(acc[a][u])); }
      zero_accs();
      v4u xf[4];
      v2u wf[2][8];
      auto rdw = [&](int s, int z) {
        const int m = z >> 2, u = (z >> 1) & 1, t = z & 1;
        wf[s & 1][z] = lds_rdtr(L + wa[u] + (s & 3) * W_SLOT + m * 8192 + t * 1024);
      };
      STAMP(1);
      BARRIER();
      STAMP(2);
#pragma unroll
      for (int z = 0; z < 8; ++z) rdw(0, z);
#pragma unroll
      for (int z = 0; z < 3; ++z) xf[z] = lds_rd128(L + xa[0] + G * 8192 + z * 2048);
#pragma unroll
      for (int m = 0; m < 2; ++m) bfr[m] = lds_rd128(L + xa[0] + bo + m * 4096);
      auto glu4 = [&](int s, int u, const v4u& x, auto&& between) {
#pragma unroll
        for (int a = 0; a < 4; ++a) {
          const v4u wv = {wf[s & 1][2 * a][0], wf[s & 1][2 * a][1], wf[s & 1][2 * a + 1][0], wf[s & 1][2 * a + 1][1]};
          mfma16t(acc[a][u], wv, x);
          SCHED();
          between(a);
          SCHED();
        }
      };
      int xb = 0, bb = 0;
      auto steps = [&](int s0, int n, const int (&xo)[5]) {
#pragma unroll
        for (int j = 0; j < n; ++j)
#pragma unroll
          for (int u = 0; u < 8; ++u) {
            const int s = s0 + j, jn = j + ((u + 3) >> 3), un = (u + 3) & 7, uw = s == 0 ? u - 4 : u;
            SCHED();
            if (u == (s == 0 ? 4 : 0)) { BARRIER(); SCHED(); }
            glu4(s, u, xf[u & 3], [&](int a) {
              if (a == 0) {
                if ((un & 3) == 0 || (j == 0 && u == 0)) xb = xa[(s0 + jn) & 1] + sc(xo[jn] + ((un >> 2) ^ G) * 8192);
                xf[(u + 3) & 3] = lds_rd128(L + xb + (un & 3) * 2048);
              }
              if ((a == 1 || a == 2) && uw >= 0 && uw < 4) rdw(s + 1, 2 * uw + a - 1);
              if (a == 3 && (u == 4 || u == 5)) {
                if (u == 4) bb = xa[(s + 1) & 1] + sc(bo + (xo[j + 1] >> 1));
                bfr[u - 4] = lds_rd128(L + bb + (u - 4) * 4096);
              }
            });
            if (u < 4) { mfma16t(accs[0][u], xf[u & 3], bfr[0]); mfma16t(accs[1][u], xf[u & 3], bfr[1]); }
          }
      };
      {
        const int xo[5] = {0, 0, 0, 0, 0};
        steps(0, 1, xo);
      }
      int xo0 = 0;
#pragma nounroll
      for (int o = 0; o < 3; ++o) {
        const int xo1 = xo0 == 2 * X_SLOT ? 0 : xo0 + X_SLOT, xo2 = xo1 == 2 * X_SLOT ? 0 : xo1 + X_SLOT;
        const int xo[5] = {xo0, xo1, xo1, xo2, xo2};
        steps(1, 4, xo);
        xo0 = xo2;
      }
      {
        const int xo[5] = {0, X_SLOT, 0, 0, 0};
        steps(1, 1, xo);
      }
      v4u bf2[2];
#pragma unroll
      for (int it = 0; it < 16; ++it) {
        const int s = (it >> 2) & 1, u = (it & 3) + 4 * (it >> 3), nx = it + 3;
        SCHED();
        glu4(s, u, xf[it & 3], [&](int a) {
          if (a == 0 && nx < 16) {
            if ((nx & 3) == 0) xb = xa[(nx >> 2) & 1] + sc(X_SLOT + ((nx >> 3) ^ G) * 8192);
            xf[nx & 3] = lds_rd128(L + xb + (nx & 3) * 2048);
          }
          if (a >= 1 && it < 3 && 3 * it + a - 1 < 8) rdw(3, 3 * it + a - 1);
          if (it == 0 && a == 3) { bb = xa[1] + sc(bo + B_SLOT); bf2[0] = lds_rd128(L + bb); bf2[1] = lds_rd128(L + bb + 4096); }
          if (it == 4 && a == 1) rd_const();
          if (it >= 8) scan_half(4 * (it - 8) + a);
        });
        if (it < 4) { mfma16t(accs[0][u], xf[it & 3], bfr[0]); mfma16t(accs[1][u], xf[it & 3], bfr[1]); }
        else if (it < 8) { mfma16t(accs[0][u], xf[it & 3], bf2[0]); mfma16t(accs[1][u], xf[it & 3], bf2[1]); }
      }
      asm volatile("s_nop 15" : "+v"(acc[0][7]), "+v"(acc[1][7]), "+v"(acc[2][7]), "+v"(acc[3][7]), "+v"(acc[0][6]), "+v"(acc[1][6]), "+v"(acc[2][6]), "+v"(acc[3][6]));
    } else {
      {
      kp = kargs();
      rsrc_t rx = mk_rsrc(kp->x, S_ * D_ * 2), rg = mk_rsrc(kp->gw, D_ * 2), rgo = mk_rsrc(kp->ws + WS_G, S_ * 4);
      const int tk0 = 128 * pn + 64 * (cb - 14) + 16 * c;
      v4u gv[8], xv[2][8];
#pragma unroll
      for (int z = 0; z < 8; ++z) gv[z] = __builtin_amdgcn_raw_buffer_load_b128(rg, (lane & 7) * 16, z * 128, 0);
#pragma unroll
      for (int gi = 0; gi < 2; ++gi)
#pragma unroll
        for (int z = 0; z < 8; ++z) xv[gi][z] = __builtin_amdgcn_raw_buffer_load_b128(rx, (lane >> 3) * 1024 + (lane & 7) * 16, (tk0 + gi * 8) * 1024 + z * 128, 0);
#pragma unroll
      for (int gi = 0; gi < 2; ++gi) {
        float s = 0.f;
#pragma unroll
        for (int z = 0; z < 8; ++z)
#pragma unroll
          for (int y = 0; y < 4; ++y) {
            const unsigned xa_ = xv[gi][z][y], ga = gv[z][y];
            s = __builtin_amdgcn_fdot2_f32_bf16(__builtin_bit_cast(bf16x2, xa_), __builtin_bit_cast(bf16x2, ga), s, false);
          }
        s += dpp<0xB1>(s); s += dpp<0x4E>(s); s += dpp<0x141>(s);
        const float g = __builtin_amdgcn_rcpf(1.f + __builtin_amdgcn_exp2f(s * -1.44269504088896341f));
        __builtin_amdgcn_raw_buffer_store_b32(f2u(g), rgo, (lane & 7) ? OOB : (lane >> 3) * 4, (tk0 + gi * 8) * 4, AUX_SC1);
      }
      }
      STAMP(1);
      zero_accs();
      int xo = 0;
#pragma nounroll
      for (int T = 0; T < 8; ++T) {
#pragma unroll
        for (int ks = 0; ks < 2; ++ks) {
          if (T < 7) BARRIER();
          v4u xf[4];
#pragma unroll
          for (int t = 0; t < 4; ++t) xf[t] = lds_rd128(L + (xa[ks] + xo + G * 8192) + t * 2048);
#pragma unroll
          for (int m = 0; m < 2; ++m) bfr[m] = lds_rd128(L + (xa[ks] + bo + (xo >> 1)) + m * 4096);
#pragma unroll
          for (int t = 0; t < 4; ++t) { mfma16t(accs[0][t], xf[t], bfr[0]); mfma16t(accs[1][t], xf[t], bfr[1]); }
          SCHED();
        }
        xo = xo == 2 * X_SLOT ? 0 : xo + X_SLOT;
      }
      asm volatile("s_nop 15" : "+v"(accs[0][0]), "+v"(accs[1][0]), "+v"(accs[0][1]), "+v"(accs[1][1]), "+v"(accs[0][2]), "+v"(accs[1][2]), "+v"(accs[0][3]), "+v"(accs[1][3]));
      rd_const();
      BARRIER();
#pragma unroll
      for (int n = 0; n < 32; ++n) { scan_half(n); SCHED(); }
    }
    *(volatile LAS unsigned*)(L + LDS_MD + 4 * w) = 1u;
    SCHED();
    STAMP(3);
    tag = __builtin_amdgcn_readfirstlane(tagv);
    const float ar = k0[0], ai = k0[1], cr = k0[2], ci = k0[3], p16r = k1[0], p16i = k1[1], p32r = k1[2], p32i = k1[3];
    __builtin_amdgcn_s_setprio(2);
    rsrc_t ra2 = mk_rsrc((const void*)(pws + WS_A2), A2_BYTES), rcar = mk_rsrc((const void*)(pws + WS_CARRY), 32 * P_ * 16);
    const int ck = 2 * pn + G;
    float pr, pi;
    {
      float tr, ti;
      const auto xr = __builtin_amdgcn_permlane16_swap(f2u(vr[15]), f2u(vr[15]), false, false);
      const auto xi = __builtin_amdgcn_permlane16_swap(f2u(vi[15]), f2u(vi[15]), false, false);
      const float e0r = u2f(xr[0]), e1r = u2f(xr[1]), e0i = u2f(xi[0]), e1i = u2f(xi[1]);
      cmul(e0r, e0i, p16r, p16i, tr, ti); tr += e1r; ti += e1i;
      const auto yr = __builtin_amdgcn_permlane32_swap(f2u(tr), f2u(tr), false, false);
      const auto yi = __builtin_amdgcn_permlane32_swap(f2u(ti), f2u(ti), false, false);
      const float f0r = u2f(yr[0]), f1r = u2f(yr[1]), f0i = u2f(yi[0]), f1i = u2f(yi[1]);
      float Er, Ei;
      cmul(f0r, f0i, p32r, p32i, Er, Ei); Er += f1r; Ei += f1i;
      const float br = (q & 2) ? f0r : 0.f, bi = (q & 2) ? f0i : 0.f;
      cmul(br, bi, p16r, p16i, tr, ti);
      pr = (q & 1) ? tr + e0r : br; pi = (q & 1) ? ti + e0i : bi;
      const v4u gq = {f2u(Er), tag, f2u(Ei), ~tag};
      st_sc1(gq, rcar, q ? OOB : ch * 16, (31 - ck) * (P_ * 16));
    }
    asm volatile("" : "+v"(pr), "+v"(pi) : : "memory");
    SCHED();
    STAMP(4);
    v4u cv[8];
    const int cvo = ch * 16 + (32 - ck + 8 * q) * (P_ * 16);
    auto ld_carry = [&]() {
#pragma unroll
      for (int z = 0; z < 8; ++z) cv[z] = ld_sc1(rcar, cvo, z * (P_ * 16));
    };
    auto publish = [&](int part) {
      *(volatile LAS unsigned*)(L + LDS_DONE + 16 * part + 4 * w) = 1u;
      const bool last = all4(L + LDS_DONE + 16 * part);
      st_flag((unsigned char*)pws, WS_FA2 + part * 32768 + pn * 128 + cb * 8, tag, last && lane < 16 ? lane * 2048 : OOB);
      CFENCE();
    };
    if (!light) {
#pragma unroll
      for (int u = 0; u < 8; ++u) {
        float hv[8];
#pragma unroll
        for (int j = 0; j < 8; ++j) hv[j] = acc[j >> 2][u][j & 3] * -1.44269504088896341f;
        SCHED();
#pragma unroll
        for (int j = 0; j < 8; ++j) hv[j] = __builtin_amdgcn_exp2f(hv[j]);
        SCHED();
#pragma unroll
        for (int j = 0; j < 8; ++j) hv[j] += 1.f;
        SCHED();
#pragma unroll
        for (int j = 0; j < 8; ++j) hv[j] = __builtin_amdgcn_rcpf(hv[j]);
        SCHED();
#pragma unroll
        for (int j = 0; j < 8; ++j) hv[j] *= acc[2 + (j >> 2)][u][j & 3];
        SCHED();
        st_sc1(v4u{pk_bf16(hv[0], hv[1]), pk_bf16(hv[2], hv[3]), pk_bf16(hv[4], hv[5]), pk_bf16(hv[6], hv[7])}, ra2, lane * 16,
               ((2 * pn + ((u >> 2) ^ G)) * NK + 4 * cb + c) * 4096 + (u & 3) * 1024);
        if (u + 1 < 8) asm volatile("" : "+v"(acc[0][u + 1]), "+v"(acc[1][u + 1]) : : "memory");
      }
    }
    CFENCE();
    SCHED();
    STAMP(5);
    ld_carry();
    CFENCE();
    SCHED();
#pragma unroll
    for (int z = 0; z < 16; ++z) {
      const float t = cr * vr[z] - ci * vi[z];
      vi[z] = -cr * vi[z] - ci * vr[z]; vr[z] = t;
    }
    float qr[4], qi[4];
    qr[0] = ar; qi[0] = ai; qr[1] = ar; qi[1] = ai; csq(qr[1], qi[1], 1);
    cmul(qr[1], qi[1], ar, ai, qr[2], qi[2]); qr[3] = qr[1]; qi[3] = qi[1]; csq(qr[3], qi[3], 1);
    const float p4r = qr[3], p4i = qi[3];
    float p64r = p32r, p64i = p32i; csq(p64r, p64i, 1);
    float p512r = p64r, p512i = p64i; csq(p512r, p512i, 3);
    float p1kr = p512r, p1ki = p512i; csq(p1kr, p1ki, 1);
    float sgr, sgi, wgr, wgi;
    cmul((q & 1) ? p16r : 1.f, (q & 1) ? p16i : 0.f, (q & 2) ? p32r : 1.f, (q & 2) ? p32i : 0.f, sgr, sgi);
    cmul((q & 1) ? p512r : 1.f, (q & 1) ? p512i : 0.f, (q & 2) ? p1kr : 1.f, (q & 2) ? p1ki : 0.f, wgr, wgi);
#pragma unroll
    for (int z = 0; z < 16; ++z) asm volatile("" : "+v"(vr[z]), "+v"(vi[z]));
    asm volatile("" : "+v"(sgr), "+v"(sgi), "+v"(wgr), "+v"(wgi), "+v"(p64r), "+v"(p64i));
    SCHED();
    const int want = imin(imax(ck - 8 * q, 0), 8);
    auto carry_ok = [&]() {
      int good = 0;
#pragma unroll
      for (int z = 0; z < 8; ++z) good += ((cv[z][1] ^ tag) | (cv[z][3] ^ ~tag)) == 0u;
      return wall(good == want);
    };
    {
      lds_wait(L + LDS_PF + 4 * w);
      b2_read(true, L + w * 16384, lane, b2[2]);
      b2_read(true, L + w * 16384 + 8192, lane, b2[3]);
      CFENCE();
      *(volatile LAS unsigned*)(L + LDS_CO + 4 * w) = 1u;
      CFENCE();
    }
    wait_vm<8>();
    publish(0);
    STAMP(14);
    if (__builtin_expect(!carry_ok(), 0)) {
#pragma nounroll
      for (unsigned spins = 0; spins < SPIN_CAP; ++spins) {
        __builtin_amdgcn_s_sleep(2);
        CFENCE();
        ld_carry();
        if (carry_ok()) break;
      }
    }
    STAMP(6);
    float Gr, Gi;
    {
      float Hr = 0.f, Hi = 0.f, tr, ti;
#pragma unroll
      for (int z = 7; z >= 0; --z) { cmul(Hr, Hi, p64r, p64i, tr, ti); Hr = tr + u2f(cv[z][0]); Hi = ti + u2f(cv[z][2]); }
      cmul(Hr, Hi, wgr, wgi, tr, ti);
      { const auto a = __builtin_amdgcn_permlane16_swap(f2u(tr), f2u(tr), false, false);
        const auto c2 = __builtin_amdgcn_permlane16_swap(f2u(ti), f2u(ti), false, false);
        tr = u2f(a[0]) + u2f(a[1]); ti = u2f(c2[0]) + u2f(c2[1]); }
      { const auto a = __builtin_amdgcn_permlane32_swap(f2u(tr), f2u(tr), false, false);
        const auto c2 = __builtin_amdgcn_permlane32_swap(f2u(ti), f2u(ti), false, false);
        tr = u2f(a[0]) + u2f(a[1]); ti = u2f(c2[0]) + u2f(c2[1]); }
      cmul(tr, ti, sgr, sgi, Hr, Hi);
      cmul(Hr + pr, Hi + pi, cr, ci, Gr, Gi);
    }
    {
      LAS unsigned char* T = L + B_BASE + (c >> 1) * (2 * B_SLOT) + (c & 1) * 4096;
      const int s32 = ((i15 >> 1) & 1) | ((i15 >> 2) & 2), s8 = (i15 & 1) | ((i15 >> 1) & 2);
#pragma unroll
      for (int t = 0; t < 4; ++t) {
        float orr[4], oi[4];
#pragma unroll
        for (int z = 0; z < 4; ++z) {
          orr[z] = __builtin_fmaf(-qi[z], Gi, __builtin_fmaf(qr[z], Gr, vr[4 * t + z]));
          oi[z] = __builtin_fmaf(-qi[z], Gr, __builtin_fmaf(-qr[z], Gi, vi[4 * t + z]));
        }
        if (t < 3) { float tr, ti; cmul(Gr, Gi, p4r, p4i, tr, ti); Gr = tr; Gi = ti; }
        LAS unsigned char* a = T + i15 * 128 + ((t ^ s32) << 5) + ((q ^ s8) << 3);
        *(LAS v2u*)a = v2u{pk_bf16(orr[0], orr[1]), pk_bf16(orr[2], orr[3])};
        *(LAS v2u*)(a + 2048) = v2u{pk_bf16(oi[0], oi[1]), pk_bf16(oi[2], oi[3])};
      }
      const int qq = i15 >> 2, pp = lane & 3;
      const int so = (ck * NK + 56 + 4 * (cb >> 1) + (cb & 1)) * 4096 + (c >> 1) * 512;
#pragma unroll
      for (int t = 0; t < 4; ++t) {
        v2u part[2];
#pragma unroll
        for (int hh = 0; hh < 2; ++hh)
          part[hh] = lds_rdtr(T + (8 * q + 4 * hh + qq) * 128 + ((t ^ ((qq >> 1) | ((q & 1) << 1))) << 5) + ((pp ^ ((qq & 1) | (hh << 1))) << 3));
        st_sc1(v4u{part[0][0], part[0][1], part[1][0], part[1][1]}, ra2, (lane >> 5) * 8192 + (lane & 31) * 16, so + t * 1024);
      }
    }
    SCHED();
    STAMP(7);
    wait_vm<0>();
    publish(1);
    STAMP(12);
  }
  kp = kargs();
  asm volatile("" : "+s"(R), "+s"(N));
  pc = p2_ctx(pWd, pCre, pCim, w, N);
  const int d0 = 2 * pc.sh;
  if (w < 4) {
    lds_wait_eq(L + LDS_PF + 4 * w, 2u);
    b2_read(true, L + w * 16384, lane, b2[0]);
    b2_read(true, L + w * 16384 + 8192, lane, b2[1]);
    WAIT_LDS();
    b2_load<0, 0, 8>(p2_ctx(pWd, pCre, pCim, w + 4, N), vb, 6, w * 16384);
    wait_vm<0>();
    *(volatile LAS unsigned*)(L + LDS_CO + 4 * w) = 2u;
    STAMP(13);
  }

  {
    lds_wait4(L + LDS_DONE);
    const int cl = lane & 15, lo = pc.sh ? (pc.g0 - 28) & ~1 : pc.g0 >> 1, hi = pc.sh ? (pc.g0 + pc.nd - 29) | 1 : (pc.g0 + pc.nd - 1) >> 1;
    GAS u64* fl = (GAS u64*)(kp->ws + WS_FA2) + ((pc.sh * 16 + (R & 1) * 8 + N) * 16 + (R >> 1)) * 16 + cl;
    if (w >= 4) {
      poll_flags(fl, tag64(tag), (cl >= lo && cl <= hi) || cl >= 14, cl < 14);
    } else if (w == 3) {
      lds_wait4(L + LDS_DONE + 16);
      __builtin_amdgcn_s_sleep(SP_FIRST);
#pragma nounroll
      for (unsigned spins = 0; spins < SPIN_CAP; ++spins) {
        CFENCE();
        const u64 v = __hip_atomic_load(fl, RLX_AGENT);
        const unsigned up = (unsigned)__builtin_amdgcn_ballot_w64(v == tag64(tag)) & 0xFFFFu;
        CFENCE();
        *(volatile LAS unsigned*)(L + LDS_RDY) = up;
        if (up == 0xFFFFu) break;
        if (up & 0x3FFFu) __builtin_amdgcn_s_sleep(SP_NEXT); else __builtin_amdgcn_s_sleep(32);
      }
      CFENCE();
    } else {
      const unsigned need = (0xFu << lo) | 0xC000u;
#pragma nounroll
      for (unsigned spins = 0; spins < (SPIN_CAP << 4); ++spins) {
        if ((lds_word(L + LDS_RDY) & need) == need) break;
        __builtin_amdgcn_s_sleep(2);
      }
      CFENCE();
    }
  }
  STAMP(8);
  if (w >= 4) __builtin_amdgcn_s_setprio(1);
  glds<B_BASE + B_SLOT, 0>(mk_rs((const void*)pX, S_ * D_ * 2), w * 1024, ((lane >> 5) * 16 + ((lane >> 3) & 3)) * 1024 + (lane & 7) * 16,
                             (64 * R + 32 * (w & 1) + 4 * (w >> 1)) * 1024 + N * 128);
  const int tok = 64 * R + 32 * (w & 1) + 4 * (w >> 1) + (lane >> 4);
  const v2u dv = __builtin_amdgcn_raw_buffer_load_b64(mk_rsrc(kp->Dsk, D_ * 2), (lane & 15) * 8, N * 128, 0);
  const float ge = u2f(__builtin_amdgcn_raw_buffer_load_b32(mk_rsrc(kp->ws + WS_G, S_ * 4), tok * 4 + (lane & 1) * 64, 0, AUX_SC1));
  SCHED();
  const v4u ra = mk_rs(kp->ws + WS_A2 + (R * NK + 2 * pc.g0) * 4096, pc.nd * 8192);
  const int va = ((lane >> 4) & 1) * 1024 + (lane >> 5) * 256 + (lane & 15) * 16;
  v4u af[8];
  {
    const int o = p2_ds(pc, 0) * 8192;
    p2_first<0>(af, ra, va, o, o + 4096);
  }
  SCHED();
#define CPRIO 0
  if (w < 4) __builtin_amdgcn_s_setprio(CPRIO);
  f32x16 acc[2][2];
#pragma unroll
  for (int e = 0; e < 2; ++e)
#pragma unroll
    for (int tt = 0; tt < 2; ++tt) { acc[e][tt] = f32x16{}; asm volatile("" : "+v"(acc[e][tt])); }
  asm volatile("s_nop 1");
  auto step = [&](auto Dc) {
    constexpr int d = decltype(Dc)::value;
    const int o = p2_ds(pc, d + 1 - d0) * 8192;
    p2_step<d, 0>(acc, b2[d & 3], af, ra, va, o, o + 4096, w);
    if (d < 2) b2_read(false, L + w * 16384 + d * 8192, lane, b2[d]);
    if (d == PK6 && w >= 4) {
      lds_wait_eq(L + LDS_CO + 4 * (w - 4), 2u);
      b2_read(false, L + (w - 4) * 16384, lane, b2[2]);
      CFENCE();
      *(volatile LAS unsigned*)(L + LDS_PF + 4 * (w - 4)) = 3u;
      CFENCE();
    }
    SCHED();
  };
  if (w >= 4) { step(IC<0>{}); step(IC<1>{}); }
  step(IC<2>{}); step(IC<3>{}); step(IC<4>{}); step(IC<5>{});
  if (w >= 4) step(IC<6>{});
  asm volatile("s_nop 15\n\ts_nop 7" : "+v"(acc[0][0]), "+v"(acc[1][0]), "+v"(acc[0][1]), "+v"(acc[1][1]));
  SCHED();
  STAMP(9);
  kp = kargs();
  if (w < 4) lds_wait_eq(L + LDS_PF + 4 * w, 3u);
  {
    const int t3 = lane & 3, g3 = ((t3 & 1) * 3) ^ (t3 >> 1), hi = lane >> 5;
#pragma unroll
    for (int T = 0; T < 2; ++T)
#pragma unroll
      for (int E = 0; E < 2; ++E)
#pragma unroll
        for (int g = 0; g < 4; ++g) {
          const int e = 2 * E + (g >> 1), q = 2 * (g & 1) + hi;
          *(LAS f32x4*)(L + w * 16384 + ((2 * T + ((lane >> 4) & 1)) * 4 + e) * 1024 + ((16 * q + (((((lane >> 2) & 3) ^ q) << 2) | (e ^ g3))) << 4)) =
              f32x4{acc[E][T][4 * g], acc[E][T][4 * g + 1], acc[E][T][4 * g + 2], acc[E][T][4 * g + 3]};
        }
  }
  if (b == 255 && w == 0) {
    rsrc_t rf = mk_rsrc(kp->ws + WS_FA2 + 32768, 256 * 8);
#pragma nounroll
    for (unsigned spins = 0; spins < SPIN_CAP; ++spins) {
      CFENCE();
      const v4u f0 = ld_sc1(rf, lane * 32, 0), f1 = ld_sc1(rf, lane * 32, 16);
      if (wall(((f0[0] ^ tag) | (f0[1] ^ ~tag) | (f0[2] ^ tag) | (f0[3] ^ ~tag) | (f1[0] ^ tag) | (f1[1] ^ ~tag) | (f1[2] ^ tag) | (f1[3] ^ ~tag)) == 0u)) break;
      __builtin_amdgcn_s_sleep(8);
    }
    st_flag(kp->ws, WS_EPOCH, tag, lane == 0 ? 0 : OOB);
  }
  WAIT_LDS();
  BARRIER();
  STAMP(10);
  {
    const int q = lane & 3, e = (lane >> 2) & 3, t3 = lane >> 4;
    const int base = ((w >> 1) * 4 + e) * 1024 + q * 256 + ((e ^ ((t3 & 1) * 3) ^ (t3 >> 1)) << 4);
    rsrc_t ro = mk_rsrc(kp->out, S_ * D_ * 4);
    const float go = dpp<0xB1>(ge);
    f32x4 res[2];
#pragma unroll
    for (int hf = 0; hf < 2; ++hf) {
      f32x4 sg = {0.f, 0.f, 0.f, 0.f}, ss = {0.f, 0.f, 0.f, 0.f};
      const int a = base + (((2 * (w & 1) + hf) ^ q) << 6);
#pragma unroll
      for (int ww = 0; ww < 8; ++ww) {
        const f32x4 v = *(LAS f32x4*)(L + ww * 16384 + a);
        if (ww < 4) ss += v; else sg += v;
      }
      const float g = (lane & 1) == hf ? ge : go;
      f32x4 m = ss * (2.f * g) + sg * (1.f - g);
      const v2u xv = *(LAS v2u*)(L + B_BASE + B_SLOT + w * 1024 + hf * 512 + lane * 8);
      m[0] += g * (bf2f(xv[0] & 0xFFFFu) * bf2f(dv[0] & 0xFFFFu)); m[1] += g * (bfhi(xv[0]) * bfhi(dv[0]));
      m[2] += g * (bf2f(xv[1] & 0xFFFFu) * bf2f(dv[1] & 0xFFFFu)); m[3] += g * (bfhi(xv[1]) * bfhi(dv[1]));
      res[hf] = m;
    }
    asm volatile("" : "+v"(res[0]), "+v"(res[1]));
#pragma unroll
    for (int hf = 0; hf < 2; ++hf)
      __builtin_amdgcn_raw_buffer_store_b128(__builtin_bit_cast(v4u, res[hf]), ro, tok * 2048 + (lane & 15) * 16 + hf * 32768 + N * 256, 0, OUT_AUX);
  }
  STAMP(11);
}

#if defined(__HIP_DEVICE_COMPILE__)
#pragma clang attribute pop
#endif

extern "C" void kernel_launch(void* const* d_in, const int* in_sizes, int n_in, void* d_out, int out_size, void* d_ws, size_t ws_size,
                              hipStream_t stream) {
  Params p;
  p.x = (const u16*)d_in[0]; p.Lre = (const u16*)d_in[1]; p.Lim = (const u16*)d_in[2]; p.Bre = (const u16*)d_in[3];
  p.Bim = (const u16*)d_in[4]; p.Cre = (const u16*)d_in[5]; p.Cim = (const u16*)d_in[6]; p.ldt = (const u16*)d_in[7];
  p.Dsk = (const u16*)d_in[8]; p.Wg = (const u16*)d_in[10]; p.Wu = (const u16*)d_in[11]; p.Wd = (const u16*)d_in[12];
  p.gw = (const u16*)d_in[13]; p.out = (float*)d_out; p.ws = (unsigned char*)d_ws;
  (void)hipFuncSetAttribute((const void*)k_fused, hipFuncAttributeMaxDynamicSharedMemorySize, LDS_BYTES);
  k_fused<<<256, 512, LDS_BYTES, stream>>>(p);
}
```

```cpp
#define RAWN 3
#define WARM0 4
#define SP_FIRST 8
#define WARMM
#define PRIO2
#include <hip/hip_runtime.h>
#include <stdint.h>

typedef unsigned short u16;
typedef unsigned long long u64;
typedef unsigned v4u __attribute__((ext_vector_type(4)));
typedef unsigned v2u __attribute__((ext_vector_type(2)));
typedef short s16x4 __attribute__((ext_vector_type(4)));
typedef __bf16 bf16x8 __attribute__((ext_vector_type(8)));
typedef __bf16 bf16x2 __attribute__((ext_vector_type(2)));
typedef float f32x2 __attribute__((ext_vector_type(2)));
typedef float f32x4 __attribute__((ext_vector_type(4)));
typedef float f32x16 __attribute__((ext_vector_type(16)));
#define LAS __attribute__((address_space(3)))
#define GAS __attribute__((address_space(1)))
#define RLX_AGENT __ATOMIC_RELAXED, __HIP_MEMORY_SCOPE_AGENT
typedef __amdgpu_buffer_rsrc_t rsrc_t;

constexpr int S_ = 2048, D_ = 512, P_ = 512, F_ = 1792;
constexpr int NK = 88;
constexpr int AUX_SC1 = 16;
#define OUT_AUX 2
constexpr int WS_EPOCH = 0;
constexpr int WS_G = 8192;
constexpr int WS_FA2 = 65536;
constexpr int CTRL_BYTES = 131072;
constexpr int WS_CARRY = 131072;
constexpr int WS_A2 = 524288;
constexpr int A2_BYTES = 32 * NK * 4096;
constexpr int WS_STAMP = 32 << 20;
constexpr int LDS_BYTES = 160 * 1024;
constexpr int X_SLOT = 16384, W_BASE = 65536, W_SLOT = 16384, B_BASE = 131072, B_SLOT = 8192;
constexpr int LDS_DONE = 155648, LDS_PF = LDS_DONE + 32, LDS_MD = LDS_DONE + 48, LDS_CO = LDS_DONE + 64, LDS_TAG = LDS_DONE + 80;
constexpr int LDS_RDY = LDS_DONE + 96;
constexpr int LDS_DUMMY = LDS_DONE + 256, LDS_CONST = LDS_DONE + 512;
constexpr int OOB = 1 << 28;
constexpr int CODE_BYTES = 30 * 1024;
constexpr unsigned SPIN_CAP = 1u << 18;

struct Params {
  const u16 *x, *Wg, *Wu, *Bre, *Bim, *Cre, *Cim, *Wd;
  unsigned char* ws;
  const u16 *Lre, *Lim, *ldt, *Dsk, *gw;
  float* out;
};

#if defined(__HIP_DEVICE_COMPILE__)
#pragma clang attribute push(__attribute__((target("no-packed-fp32-ops"))), apply_to = function)
#endif
__device__ __forceinline__ float u2f(unsigned v) { return __builtin_bit_cast(float, v); }
__device__ __forceinline__ unsigned f2u(float v) { return __builtin_bit_cast(unsigned, v); }
__device__ __forceinline__ bool wall(bool p) { return __builtin_amdgcn_ballot_w64(!p) == 0; }
__device__ __forceinline__ bool wany(bool p) { return __builtin_amdgcn_ballot_w64(p) != 0; }
__device__ __forceinline__ int imin(int a, int b) { return a < b ? a : b; }
__device__ __forceinline__ int imax(int a, int b) { return a > b ? a : b; }
template <int CTRL> __device__ __forceinline__ float dpp(float v) { return u2f(__builtin_amdgcn_mov_dpp(f2u(v), CTRL, 0xF, 0xF, true)); }
__device__ __forceinline__ rsrc_t mk_rsrc(const void* p, int bytes) {
  return __builtin_amdgcn_make_buffer_rsrc((void*)p, 0, bytes, 0x00020000);
}
__device__ __forceinline__ float bf2f(unsigned v) { return u2f(v << 16); }
__device__ __forceinline__ float bfhi(unsigned v) { return u2f(v & 0xFFFF0000u); }
__device__ __forceinline__ unsigned pk_bf16(float lo, float hi) {
  f32x2 t = {lo, hi};
  return __builtin_bit_cast(unsigned, __builtin_convertvector(t, bf16x2));
}
__device__ __forceinline__ void mfma16t(f32x4& c, v4u a, v4u b) {
  asm volatile("v_mfma_f32_16x16x32_bf16 %0, %1, %2, %0" : "+v"(c) : "v"(a), "v"(b));
}
__device__ __forceinline__ void mfma32t(f32x16& c, v4u a, v4u b) {
  asm volatile("v_mfma_f32_32x32x16_bf16 %0, %1, %2, %0" : "+v"(c) : "v"(a), "v"(b));
}
__device__ __forceinline__ v4u lds_rd128(LAS unsigned char* p) { return *(LAS v4u*)p; }
__device__ __forceinline__ v2u lds_rdtr(LAS unsigned char* p) {
  return __builtin_bit_cast(v2u, __builtin_amdgcn_ds_read_tr16_b64_v4i16((LAS s16x4*)p));
}
__device__ __forceinline__ v4u mk_rs(const void* p, unsigned bytes) {
  const u64 a = (u64)p;
  unsigned lo = (unsigned)a, hi = (unsigned)(a >> 32) & 0xFFFFu;
  asm volatile("" : "+s"(lo), "+s"(hi));
  return v4u{lo, hi, bytes, 0x00020000u};
}
template <int LI, int SI> __device__ __forceinline__ void glds(v4u rs, int lds, int voff, int soff) {
  int t;
  asm volatile("s_add_u32 m0, %1, %5\n\ts_add_u32 %0, %4, %6\n\tbuffer_load_dwordx4 %2, %3, %0 offen lds"
               : "=&s"(t) : "s"(lds), "v"(voff), "s"(rs), "s"(soff), "i"(LI), "i"(SI) : "memory", "scc");
}
typedef const __attribute__((address_space(4))) Params* KP;
__device__ __forceinline__ KP kargs() {
  KP k = (KP)__builtin_amdgcn_kernarg_segment_ptr();
  asm volatile("" : "+s"(k));
  return k;
}
__device__ __forceinline__ void warm(v4u rs, int voff, int soff) {
  asm volatile("s_mov_b32 m0, %3\n\ts_nop 0\n\tbuffer_load_dword %0, %1, %2 offen lds" : : "v"(voff), "s"(rs), "s"(soff), "i"(LDS_DUMMY) : "memory");
}
__device__ __forceinline__ v4u ld_sc1(rsrc_t rs, int voff, int soff) { return __builtin_amdgcn_raw_buffer_load_b128(rs, voff, soff, AUX_SC1); }
__device__ __forceinline__ void st_sc1(v4u v, rsrc_t rs, int voff, int soff) { __builtin_amdgcn_raw_buffer_store_b128(v, rs, voff + soff, 0, AUX_SC1); }
__device__ __forceinline__ u64 tag64(unsigned tag) { return ((u64)(~tag) << 32) | tag; }
__device__ __forceinline__ void st_flag(unsigned char* ws, int off, unsigned tag, int voff) {
  __builtin_amdgcn_raw_buffer_store_b64(v2u{tag, ~tag}, mk_rsrc(ws, CTRL_BYTES), voff, off, AUX_SC1);
}
__device__ __forceinline__ unsigned lds_word(LAS unsigned char* p) { return __builtin_amdgcn_readfirstlane(*(volatile LAS unsigned*)p); }
#define BARRIER() asm volatile("s_barrier" ::: "memory")
#define SCHED() __builtin_amdgcn_sched_barrier(0)
#define CFENCE() asm volatile("" ::: "memory")
#define WAIT_LDS() asm volatile("s_waitcnt lgkmcnt(0)" ::: "memory")
template <int N> __device__ __forceinline__ void wait_vm() { asm volatile("s_waitcnt vmcnt(%c0)" ::"i"(N) : "memory"); }

#define STAMP(i) do {} while (0)
#define PICK 0
#define SP_NEXT 8
#define PK6 3
#define POLL_SLEEP 4
#define POLL_IDLE 16

__device__ __forceinline__ void poll_flags(GAS u64* f, u64 want, bool care, bool late) {
#pragma nounroll
  for (unsigned spins = 0; spins < SPIN_CAP; ++spins) {
    CFENCE();
    const u64 v = __hip_atomic_load(f, RLX_AGENT);
    if (wall(!care || v == want)) break;
    if (wany(late && v == want)) __builtin_amdgcn_s_sleep(POLL_SLEEP); else __builtin_amdgcn_s_sleep(POLL_IDLE);
  }
  CFENCE();
}
__device__ __forceinline__ void csq(float& r, float& i, int n) {
#pragma unroll
  for (int z = 0; z < n; ++z) { const float t = r * r - i * i; i = 2.f * r * i; r = t; }
}

#define NROT 102
#define CONST_AT 2
constexpr int LASTB = 13;
#define NRAWD 2
constexpr int raw_lo(int b) { const int n = (b - (LASTB + 1)) * RAWN + 8 * NRAWD; return n < 0 ? 0 : n; }
struct Sched {
  int n;
  int type[40], idx[40];
  int lo[LASTB + 1], hi[LASTB + 1];
  int wait[LASTB + 1];
  bool ok;
};
constexpr Sched make_sched(int kind) {
  Sched s{};
  int cnt[40] = {}, need[40] = {}, avail[40] = {};
  int n = 0;
  auto add = [&](int ty, int ix, int nd, int av) {
    if (kind && ty == 2) return;
    s.type[n] = ty; s.idx[n] = ix; cnt[n] = ty == 2 ? 4 : 3; need[n] = nd < LASTB ? nd : LASTB; avail[n] = av; ++n;
  };
  add(0, 0, 0, -1); add(1, 0, 0, -1); add(2, 0, 0, -1); add(2, 1, 0, -1);
  add(0, 1, 1, -1); add(1, 1, 1, -1); add(2, 2, 1, -1); add(2, 3, 2, -1);
  add(0, 2, 3, -1); add(1, 2, 3, -1);
  add(2, 4, 3, 1); add(2, 5, 4, 1);
  for (int b = 2; b < 12; ++b) {
    const int T = b / 2 + 2;
    add(b & 1, T, 2 * T - 1, kind ? (b & ~1) : b);
    add(2, b + 4, b + 3, b);
  }
  s.n = n;
  s.ok = true;
  const int LIM = 8;
  int endpos[40] = {};
  int next = 0, total = 0;
  for (int b = 0; b <= LASTB; ++b) {
    s.lo[b] = next;
    const int start = total;
    for (;;) {
      if (next >= n || avail[next] > b - 1) break;
      int lastneed = 0;
      for (int i = 0; i < next; ++i) if (need[i] <= b) lastneed = endpos[i];
      if (b < 3 && need[next] > b && total + cnt[next] - lastneed > LIM + 2 * b) break;
      if (b >= 3 && need[next] > b + 1 && total + cnt[next] - start > 11) break;
      total += cnt[next]; endpos[next] = total; ++next;
    }
    s.hi[b] = next;
    if (kind == 0) total += raw_lo(b + 1) - raw_lo(b);
    if (kind == 0 && b >= WARM0 && b < WARM0 + 3) total += 1;
    int lastneed = 0;
    for (int i = 0; i < next; ++i) if (need[i] <= b) lastneed = endpos[i];
    for (int i = next; i < n; ++i) if (need[i] <= b) s.ok = false;
    s.wait[b] = total - lastneed;
  }
  if (next != n) s.ok = false;
  return s;
}
constexpr Sched SCH[2] = {make_sched(0), make_sched(1)};
static_assert(SCH[0].ok && SCH[1].ok, "every ring item must be issued in time");

struct P2Ctx { u64 a0, a1; int so0, sh, g0, nd, rot; unsigned bytes; };
__device__ __forceinline__ P2Ctx p2_ctx(u64 Wd, u64 Cre, u64 Cim, int w, int N) {
  P2Ctx c;
  const int ssm = w < 4;
  const u64 m = 0 - (u64)ssm;
  c.a0 = (Cre & m) | (Wd & ~m); c.a1 = (Cim & m) | (Wd & ~m);
  c.sh = ssm;
  c.so0 = N * 128 + ssm * (N * (65536 - 128) - 14 * 128);
  c.bytes = F_ * D_ * 2u - ssm * (F_ * D_ * 2u - D_ * P_ * 2u);
  c.g0 = 7 * w - 28 + ssm * (56 - 3 * w);
  c.nd = 7 - 3 * ssm;
#define P2ROT 1
  c.rot = P2ROT == 0 ? 0 : (P2ROT == 1 ? (N & 1) * (3 - ssm) : (P2ROT == 2 ? (N & 3) * (2 - ssm) : (N * c.nd) >> 3));
  return c;
}
__device__ __forceinline__ int p2_ds(const P2Ctx& c, int d) { const int t = d + c.rot; return t >= c.nd ? t - c.nd : t; }
__device__ __forceinline__ void b2_src(const P2Ctx& c, int d, v4u& rs, int& so) {
  const int g = c.g0 + p2_ds(c, d);
  const u64 x0 = c.a0, x1 = c.a1;
  rs = mk_rs((const void*)((g & 1) ? x1 : x0), c.bytes);
  so = c.so0 + (c.sh ? (g >> 1) * 128 : g * 65536);
}
template <int LI, int N0, int N1> __device__ __forceinline__ void b2_rows(v4u rs, int lds, int vb, int so) {
  if constexpr (N0 < N1) { glds<LI, N0 * 8192>(rs, lds, (N0 & 1) ? vb ^ 16 : vb, so); b2_rows<LI + 1024, N0 + 1, N1>(rs, lds, vb, so); }
}
template <int LI, int N0, int N1> __device__ __forceinline__ void b2_load(const P2Ctx& c, int vb, int d, int lds) {
  v4u rs; int so;
  b2_src(c, d, rs, so);
  b2_rows<LI, N0, N1>(rs, lds, vb, __builtin_amdgcn_readfirstlane(so));
}
__device__ __forceinline__ void b2_read(bool ssm, LAS unsigned char* img, int lane, v4u (&f)[2][4]) {
  if (ssm) {
    const int i = lane & 31, sw = ((i & 2) << 1) | ((i >> 1) & 2) | ((i >> 3) & 1);
#pragma unroll
    for (int h = 0; h < 2; ++h)
#pragma unroll
      for (int z = 0; z < 4; ++z) f[h][z] = lds_rd128(img + i * 128 + (((4 * h + (z & 2) + (lane >> 5)) ^ sw) << 4) + (z & 1) * 4096);
  } else {
    const int qq = (lane >> 2) & 3, hi = lane >> 5;
    const int c0 = (((lane >> 4) & 1) * 2 + ((lane >> 1) & 1)) ^ (((qq & 2) << 1) | (hi << 1));
#pragma unroll
    for (int h = 0; h < 2; ++h)
#pragma unroll
      for (int z = 0; z < 4; ++z) {
        LAS unsigned char* a = img + (4 * hi + qq) * 128 + ((c0 ^ ((z & 1) << 2) ^ (z >> 1)) << 4) + (lane & 1) * 8 + h * 4096 + (z >> 1) * 1024;
        const v2u lo = lds_rdtr(a), hi2 = lds_rdtr(a + 2048);
        f[h][z] = v4u{lo[0], lo[1], hi2[0], hi2[1]};
      }
  }
}

struct LdCtx { v4u rx, rw, rb; u64 Wd, Cre, Cim; int lw, lb, vx, vw0, vw1, vbb, rot, lane, vb, vwarm, xcd, ch; mutable unsigned sv, lrb, lib, ldb, ep; };
__device__ __forceinline__ void warm_chunk(u64 x0, u64 x1, u64 x2, int xc, int vwarm, int slot) {
  asm volatile("" : "+s"(xc));
  int i = slot < 6 ? slot + xc : slot - 6 + xc;
  if (slot < 6) { i = i >= 12 ? i - 12 : (i >= 6 ? i - 6 : i); } else { i = 6 + (i >= 10 ? i - 10 : (i >= 5 ? i - 5 : i)); }
  const u64 src = i < 7 ? x0 : (i < 9 ? x1 : x2);
  const unsigned bytes = (unsigned)slot > 10u ? 0u : (i < 7 ? F_ * D_ * 2 : D_ * P_ * 2);
  warm(mk_rs((const void*)src, bytes), vwarm, (i < 7 ? i : ((i - 7) & 1)) * 262144);
}

template <int KIND, int I> __device__ __forceinline__ void ld_item(const LdCtx& c) {
  constexpr int ty = SCH[KIND].type[I], t = SCH[KIND].idx[I];
  int rot = c.rot;
  asm volatile("" : "+s"(rot));
  if constexpr (ty < 2) {
    const int k0 = ((t + rot) & 7) * 128;
    glds<(t % 3) * X_SLOT + 2 * ty * 4096, ty * 65536>(c.rx, c.lw * 1024, c.vx, k0);
    glds<(t % 3) * X_SLOT + (2 * ty + 1) * 4096, ty * 65536 + 8192>(c.rx, c.lw * 1024, c.vx, k0);
    glds<B_BASE + (t % 3) * B_SLOT + ty * 2048, ty * 16384>(c.rb, c.lb, c.vbb, k0);
  } else {
    const int k0 = (((t >> 1) + rot) & 7) * (64 * F_ * 2);
    constexpr int LI = W_BASE + (t & 3) * W_SLOT, SI = (t & 1) * 32 * F_ * 2;
    glds<LI, SI>(c.rw, c.lw * 4096, c.vw0, k0);
    glds<LI + 1024, SI + 4 * F_ * 2>(c.rw, c.lw * 4096, c.vw0, k0);
    glds<LI + 2048, SI + 8 * F_ * 2>(c.rw, c.lw * 4096, c.vw1, k0);
    glds<LI + 3072, SI + 12 * F_ * 2>(c.rw, c.lw * 4096, c.vw1, k0);
  }
}
template <int KIND, int LO, int HI> __device__ __forceinline__ void ld_items(const LdCtx& c) {
  if constexpr (LO < HI) { ld_item<KIND, LO>(c); ld_items<KIND, LO + 1, HI>(c); }
}
template <int A, int E> __device__ __forceinline__ void raw_loads(const P2Ctx& pc, int vb, v4u (&raw)[8 * NRAWD]) {
  if constexpr (A < E) {
    v4u rs; int so;
    b2_src(pc, 4 + (A >> 3), rs, so);
    asm volatile("buffer_load_dwordx4 %0, %1, %2, %3 offen" : "=v"(raw[A]) : "v"((A & 1) ? vb ^ 16 : vb), "s"(rs), "s"(so + (A & 7) * 8192) : "memory");
    raw_loads<A + 1, E>(pc, vb, raw);
  }
}
template <int KIND, int B> __device__ __forceinline__ void loader_steps(const LdCtx& c, LAS unsigned char* L, const P2Ctx& pc, v4u (&raw)[8 * NRAWD]) {
  if constexpr (B == 0) {
    constexpr int FIRST = KIND ? 2 : 3;
    ld_items<KIND, 0, FIRST>(c);
    KP kp = kargs();
    const v4u r0 = mk_rs(kp->Lre, P_ * 2), r1 = mk_rs(kp->Lim, P_ * 2), r2 = mk_rs(kp->ldt, P_ * 2), r3 = mk_rs(kp->ws + WS_EPOCH, c.lw == 3 ? 4 : 0);
    asm volatile("buffer_load_ushort %0, %4, %5, 0 offen\n\tbuffer_load_ushort %1, %4, %6, 0 offen\n\tbuffer_load_ushort %2, %4, %7, 0 offen\n\t"
                 "buffer_load_dword %3, off, %8, 0 sc1"
                 : "=&v"(c.lrb), "=&v"(c.lib), "=&v"(c.ldb), "=&v"(c.ep) : "v"(c.ch * 2), "s"(r0), "s"(r1), "s"(r2), "s"(r3) : "memory");
    if constexpr (KIND == 0) {
      ld_items<0, 3, 5>(c);
      wait_vm<7>();
      BARRIER();
      ld_items<0, 5, SCH[0].hi[0]>(c);
    } else {
      ld_items<KIND, FIRST, SCH[KIND].hi[0]>(c);
    }
  } else {
    ld_items<KIND, SCH[KIND].lo[B], SCH[KIND].hi[B]>(c);
  }
  if constexpr (KIND == 0) raw_loads<raw_lo(B), raw_lo(B + 1)>(pc, c.vb, raw);
  if constexpr (KIND == 0 && B == WARM0) warm(mk_rs((const void*)c.Wd, F_ * D_ * 2), c.lane < 16 ? (c.vwarm * 16 + c.lane) * 128 : OOB, 0);
  if constexpr (KIND == 0 && B == WARM0 + 1) warm(mk_rs((const void*)c.Cre, D_ * P_ * 2), c.lane < 5 ? (c.vwarm * 5 + c.lane) * 128 : OOB, 0);
  if constexpr (KIND == 0 && B == WARM0 + 2) warm(mk_rs((const void*)c.Cim, D_ * P_ * 2), c.lane < 5 ? (c.vwarm * 5 + c.lane) * 128 : OOB, 0);
  if constexpr (B == CONST_AT) {
    asm volatile("" : "+v"(c.lrb), "+v"(c.lib), "+v"(c.ldb), "+v"(c.ep) : : "memory");
    const float lr = bf2f(c.lrb), li = bf2f(c.lib);
    float dt = __builtin_amdgcn_exp2f(bf2f(c.ldb) * 1.44269504088896341f);
    dt = bf2f(pk_bf16(dt, 0.f) & 0xFFFFu);
    const float rev = __builtin_amdgcn_fractf(dt * li * 0.15915494309189535f);
    const float ex = __builtin_amdgcn_exp2f(dt * lr * 1.44269504088896341f);
    const float ar = ex * __builtin_amdgcn_cosf(rev), ai = ex * __builtin_amdgcn_sinf(rev);
    const float den = __builtin_amdgcn_rcpf(lr * lr + li * li);
    float p16r = ar, p16i = ai;
    csq(p16r, p16i, 4);
    float p32r = p16r, p32i = p16i;
    csq(p32r, p32i, 1);
    LAS f32x4* d = (LAS f32x4*)(L + LDS_CONST + c.lw * 512 + (c.lane & 15) * 32);
    d[0] = f32x4{ar, ai, ((ar - 1.f) * lr + ai * li) * den, (ai * lr - (ar - 1.f) * li) * den};
    d[1] = f32x4{p16r, p16i, p32r, p32i};
    *(volatile LAS unsigned*)(L + LDS_TAG + 4 * (3 - c.lw)) = c.ep + 1u;
    WAIT_LDS();
  }
  SCHED();
  wait_vm<SCH[KIND].wait[B]>();
  BARRIER();
  if constexpr (B < LASTB) loader_steps<KIND, B + 1>(c, L, pc, raw);
}

__device__ __forceinline__ bool all4(LAS unsigned char* p) {
  const v4u d = *(volatile LAS v4u*)p;
  return __builtin_amdgcn_readfirstlane(d[0] & d[1] & d[2] & d[3]) != 0u;
}
__device__ __forceinline__ void lds_wait4(LAS unsigned char* p) {
#pragma nounroll
  for (unsigned spins = 0; spins < (SPIN_CAP << 4); ++spins) {
    if (all4(p)) break;
    __builtin_amdgcn_s_sleep(2);
  }
  CFENCE();
}
__device__ __forceinline__ void lds_wait(LAS unsigned char* word) {
#pragma nounroll
  for (unsigned spins = 0; spins < (SPIN_CAP << 4); ++spins) {
    if (lds_word(word) != 0u) break;
    __builtin_amdgcn_s_sleep(2);
  }
  CFENCE();
}
__device__ __forceinline__ void lds_wait_eq(LAS unsigned char* word, unsigned want) {
#pragma nounroll
  for (unsigned spins = 0; spins < (SPIN_CAP << 4); ++spins) {
    if (lds_word(word) == want) break;
    __builtin_amdgcn_s_sleep(2);
  }
  CFENCE();
}
__device__ __forceinline__ void cmul(float ar, float ai, float br, float bi, float& cr, float& ci) {
  cr = ar * br - ai * bi; ci = ar * bi + ai * br;
}

template <int V> struct IC { static constexpr int value = V; };
template <int n> __device__ __forceinline__ void p2_first(v4u (&af)[8], v4u ra, int va, int o0, int o1) {
  asm volatile("buffer_load_dwordx4 %0, %1, %2, %3 offen offset:%c4 sc1" : "=v"(af[n]) : "v"(va), "s"(ra), "s"(n < 4 ? o0 : o1), "i"((n & 1) * 2048 + (n & 2) * 256) : "memory");
  if constexpr (n < 7) p2_first<n + 1>(af, ra, va, o0, o1);
}
template <int d, int n>
__device__ __forceinline__ void p2_step(f32x16 (&acc)[2][2], const v4u (&bb)[2][4], v4u (&af)[8], v4u ra, int va, int o0, int o1, int w) {
  constexpr int WG = d == 6 ? 7 - n : 7, WS = d == 5 ? 7 - n : 7;
  if constexpr (d >= 3 && d <= 5 && WS < WG)
    asm volatile("s_waitcnt vmcnt(%c2)\n\ts_cmp_gt_u32 %1, 3\n\ts_cbranch_scc1 1f\n\ts_waitcnt vmcnt(%c3)\n1:" : "+v"(af[n]) : "s"(w), "i"(WG), "i"(WS) : "memory", "scc");
  else
    asm volatile("s_waitcnt vmcnt(%c1)" : "+v"(af[n]) : "i"(WG) : "memory");
  mfma32t(acc[0][n & 1], bb[n >> 2][n & 2], af[n]);
  mfma32t(acc[1][n & 1], bb[n >> 2][(n & 2) + 1], af[n]);
  if constexpr (d < 5)
    asm volatile("buffer_load_dwordx4 %0, %1, %2, %3 offen offset:%c4 sc1" : "+v"(af[n]) : "v"(va), "s"(ra), "s"(n < 4 ? o0 : o1), "i"((n & 1) * 2048 + (n & 2) * 256) : "memory");
  if constexpr (d == 5)
    asm volatile("s_cmp_lt_u32 %5, 4\n\ts_cbranch_scc1 1f\n\tbuffer_load_dwordx4 %0, %1, %2, %3 offen offset:%c4 sc1\n1:"
                 : "+v"(af[n]) : "v"(va), "s"(ra), "s"(n < 4 ? o0 : o1), "i"((n & 1) * 2048 + (n & 2) * 256), "s"(w) : "memory", "scc");
  if constexpr (n < 7) p2_step<d, n + 1>(acc, bb, af, ra, va, o0, o1, w);
}

__global__ __launch_bounds__(512, 2) void k_fused(Params) {
  extern __shared__ __attribute__((aligned(16))) unsigned char lds_[];
  LAS unsigned char* L = (LAS unsigned char*)lds_;
  STAMP(0);
  KP kp = kargs();
  u64 pX = (u64)kp->x, pWg = (u64)kp->Wg, pWu = (u64)kp->Wu, pBre = (u64)kp->Bre, pBim = (u64)kp->Bim, pCre = (u64)kp->Cre, pCim = (u64)kp->Cim, pWd = (u64)kp->Wd;
  const int b = __builtin_amdgcn_workgroup_id_x(), tid = __builtin_amdgcn_workitem_id_x(), lane = tid & 63;
  const int w = __builtin_amdgcn_readfirstlane(tid >> 6);
  const int xcd = b & 7, r = b >> 3;
  warm(mk_rs((const void*)(__builtin_amdgcn_s_getpc() & ~(u64)127), CODE_BYTES), lane < 2 ? lane * 32768 : OOB, (r * 8 + w) * 128);
  const bool light = (r & 7) == 7;
  const int ih = 28 * (xcd & 3) + r - (r >> 3), il = 112 + 4 * (xcd & 3) + (r >> 3);
  const int idx = ih + (il - ih) * (int)light;
  const int pn = 8 * (xcd >> 2) + (idx & 7), cb = idx >> 3;
  const int rot = pn & 6;
  int R = xcd * 4 + (r & 3), N = r >> 2;
  asm volatile("" : "+s"(pX), "+s"(pWg), "+s"(pWu), "+s"(pBre), "+s"(pBim), "+s"(pCre), "+s"(pCim), "+s"(pWd));
  const int vb = (lane >> 3) * 1024 + (((lane & 7) ^ (((lane >> 2) & 4) | ((lane >> 4) & 2))) << 4);
  v4u b2[4][2][4];
  unsigned tag = 0;
  P2Ctx pc;

  if (__builtin_expect(w >= 4, 1)) {
    LdCtx c;
    c.lw = w - 4; c.rot = rot; c.lane = lane; c.vb = vb; c.sv = 0;
    c.Wd = pWd; c.Cre = pCre; c.Cim = pCim;
    c.vwarm = ((r - (r >> 3)) * 8 + xcd) * 4 + c.lw;
    c.xcd = xcd; c.ch = 32 * cb + 16 * (c.lw >> 1) + (lane & 15);
    const int l3 = lane >> 3, kq = lane >> 4;
    c.rx = mk_rs((const void*)pX, S_ * D_ * 2);
    c.vx = (32 * (c.lw & 1) + 16 * (l3 >> 2) + 4 * (c.lw >> 1) + (l3 & 3)) * 1024 + (((lane & 7) ^ ((4 * c.lw + kq) & 7)) << 4) + pn * 128 * 1024;
    c.rb = mk_rs((const void*)((c.lw >> 1) ? pBim : pBre), P_ * D_ * 2);
    c.vbb = (8 * (c.lw & 1) + l3) * 1024 + (((lane & 7) ^ (4 * (c.lw & 1) + kq)) << 4) + cb * 32 * 1024;
    c.lb = (c.lw >> 1) * 4096 + (c.lw & 1) * 1024;
    c.rw = mk_rs((const void*)((c.lw >> 1) ? pWu : pWg), D_ * F_ * 2);
    c.vw0 = ((c.lw & 1) * 16 + kq) * (F_ * 2) + (((lane & 15) ^ (kq << 2)) << 4) + cb * 256;
    c.vw1 = c.vw0 ^ 32;
    STAMP(1);
    pc = p2_ctx(pWd, pCre, pCim, w, N);
    v4u raw[8 * NRAWD];
    if (__builtin_expect(!light, 1)) loader_steps<0, 0>(c, L, pc, raw);
    else { loader_steps<1, 0>(c, L, pc, raw); BARRIER(); raw_loads<0, 8 * NRAWD>(pc, vb, raw); }
    STAMP(2);
    {
      const int wA = c.lw < 2 ? w : c.lw;
      b2_load<0, 0, 8>(pc, vb, 0, wA * 16384);
      b2_load<8192, 0, 8>(pc, vb, 1, wA * 16384);
      const P2Ctx pp = p2_ctx(pWd, pCre, pCim, c.lw, N);
#pragma unroll
      for (int d = 0; d < 4; ++d) {
        wait_vm<8>();
        b2_read(false, L + wA * 16384 + (d & 1) * 8192, lane, b2[d]);
        WAIT_LDS();
        if (d == 2) lds_wait4(L + LDS_MD);
        if (d < 2) b2_load<0, 0, 8>(pc, vb, d + 2, wA * 16384 + (d & 1) * 8192);
        else b2_load<0, 0, 8>(pp, vb, d & 1, c.lw * 16384 + (d & 1) * 8192);
      }
      STAMP(3);
#pragma unroll
      for (int z = 0; z < 16; z += 8)
        asm volatile("" : "+v"(raw[z]), "+v"(raw[z + 1]), "+v"(raw[z + 2]), "+v"(raw[z + 3]), "+v"(raw[z + 4]), "+v"(raw[z + 5]), "+v"(raw[z + 6]), "+v"(raw[z + 7]));
#pragma unroll
      for (int n = 0; n < 16; ++n) *(LAS v4u*)(L + w * 16384 + n * 1024 + lane * 16) = raw[n];
      wait_vm<0>();
      *(volatile LAS unsigned*)(L + LDS_PF + 4 * c.lw) = 1u;
      STAMP(4);
      lds_wait(L + LDS_CO + 4 * c.lw);
      b2_load<0, 0, 8>(pp, vb, 2, c.lw * 16384);
      b2_load<8192, 0, 8>(pp, vb, 3, c.lw * 16384);
      wait_vm<0>();
      *(volatile LAS unsigned*)(L + LDS_PF + 4 * c.lw) = 2u;
      STAMP(5);
    }
    tag = lds_word(L + LDS_TAG);
  } else {
    const int c = w, q = lane >> 4, i15 = lane & 15, G = c & 1;
    const int ch = 32 * cb + 16 * (c >> 1) + i15;
    if (w == 3) {
#pragma unroll
      for (int z = 0; z < 5; ++z) *(volatile LAS v4u*)(L + LDS_DONE + 16 * z) = v4u{0u, 0u, 0u, 0u};
      *(volatile LAS unsigned*)(L + LDS_RDY) = 0u;
      *(volatile LAS v4u*)(L + LDS_RDY + 16) = v4u{0u, 0u, 0u, 0u};
      WAIT_LDS();
    }
    f32x4 k0 = {0.f, 0.f, 0.f, 0.f}, k1 = {0.f, 0.f, 0.f, 0.f};
    unsigned tagv = 0;
    auto rd_const = [&]() {
      k0 = *(LAS f32x4*)(L + LDS_CONST + c * 512 + i15 * 32); k1 = *(LAS f32x4*)(L + LDS_CONST + c * 512 + i15 * 32 + 16);
      tagv = *(volatile LAS unsigned*)(L + LDS_TAG);
    };
    u64 pws = (u64)kp->ws;
    asm volatile("" : "+s"(pws));
    int xa[2];
#pragma unroll
    for (int ks = 0; ks < 2; ++ks) xa[ks] = i15 * 128 + (((4 * ks + q) ^ ((i15 >> 1) & 7)) << 4);
    asm volatile("" : "+v"(xa[0]), "+v"(xa[1]));
    const int bo = B_BASE + (c >> 1) * 2048;
    f32x4 accs[2][4], acc[4][8];
    v4u bfr[2];
    auto zero_accs = [&]() {
#pragma unroll
      for (int m = 0; m < 2; ++m)
#pragma unroll
        for (int t = 0; t < 4; ++t) { accs[m][t] = f32x4{0.f, 0.f, 0.f, 0.f}; asm volatile("" : "+v"(accs[m][t])); }
    };
    float vr[16], vi[16], t0 = 0.f, t1 = 0.f;
    auto scan_half = [&](int n) {
      const int z = n >> 1;
      if (z == 0) { vr[0] = accs[0][0][0]; vi[0] = accs[1][0][0]; }
      else if ((n & 1) == 0) { t0 = __builtin_fmaf(k0[0], vr[z - 1], accs[0][z >> 2][z & 3]); t1 = __builtin_fmaf(k0[0], vi[z - 1], accs[1][z >> 2][z & 3]); }
      else { vr[z] = __builtin_fmaf(-k0[1], vi[z - 1], t0); vi[z] = __builtin_fmaf(k0[1], vr[z - 1], t1); }
    };
    auto sc = [](int v) { asm volatile("" : "+s"(v)); return v; };

    if (!light) {
      int wa[2];
      {
        const int qq = i15 >> 2, pp = lane & 3;
#pragma unroll
        for (int u = 0; u < 2; ++u)
          wa[u] = W_BASE + (8 * q + qq) * 256 + (((2 * (2 * c + u) + (pp >> 1)) ^ ((qq << 2) | ((q & 1) << 1))) << 4) + 8 * (pp & 1);
      }
      asm volatile("" : "+v"(wa[0]), "+v"(wa[1]));
#pragma unroll
      for (int a = 0; a < 4; ++a)
#pragma unroll
        for (int u = 0; u < 8; ++u) { acc[a][u] = f32x4{0.f, 0.f, 0.f, 0.f}; asm volatile("" : "+v"(acc[a][u])); }
      zero_accs();
      v4u xf[4];
      v2u wf[2][8];
      auto rdw = [&](int s, int z) {
        const int m = z >> 2, u = (z >> 1) & 1, t = z & 1;
        wf[s & 1][z] = lds_rdtr(L + wa[u] + (s & 3) * W_SLOT + m * 8192 + t * 1024);
      };
      STAMP(1);
      BARRIER();
      STAMP(2);
#pragma unroll
      for (int z = 0; z < 8; ++z) rdw(0, z);
#pragma unroll
      for (int z = 0; z < 3; ++z) xf[z] = lds_rd128(L + xa[0] + G * 8192 + z * 2048);
#pragma unroll
      for (int m = 0; m < 2; ++m) bfr[m] = lds_rd128(L + xa[0] + bo + m * 4096);
      auto glu4 = [&](int s, int u, const v4u& x, auto&& between) {
#pragma unroll
        for (int a = 0; a < 4; ++a) {
          const v4u wv = {wf[s & 1][2 * a][0], wf[s & 1][2 * a][1], wf[s & 1][2 * a + 1][0], wf[s & 1][2 * a + 1][1]};
          mfma16t(acc[a][u], wv, x);
          SCHED();
          between(a);
          SCHED();
        }
      };
      int xb = 0, bb = 0;
      auto steps = [&](int s0, int n, const int (&xo)[5]) {
#pragma unroll
        for (int j = 0; j < n; ++j)
#pragma unroll
          for (int u = 0; u < 8; ++u) {
            const int s = s0 + j, jn = j + ((u + 3) >> 3), un = (u + 3) & 7, uw = s == 0 ? u - 4 : u;
            SCHED();
            if (u == (s == 0 ? 4 : 0)) { BARRIER(); SCHED(); }
            glu4(s, u, xf[u & 3], [&](int a) {
              if (a == 0) {
                if ((un & 3) == 0 || (j == 0 && u == 0)) xb = xa[(s0 + jn) & 1] + sc(xo[jn] + ((un >> 2) ^ G) * 8192);
                xf[(u + 3) & 3] = lds_rd128(L + xb + (un & 3) * 2048);
              }
              if ((a == 1 || a == 2) && uw >= 0 && uw < 4) rdw(s + 1, 2 * uw + a - 1);
              if (a == 3 && (u == 4 || u == 5)) {
                if (u == 4) bb = xa[(s + 1) & 1] + sc(bo + (xo[j + 1] >> 1));
                bfr[u - 4] = lds_rd128(L + bb + (u - 4) * 4096);
              }
            });
            if (u < 4) { mfma16t(accs[0][u], xf[u & 3], bfr[0]); mfma16t(accs[1][u], xf[u & 3], bfr[1]); }
          }
      };
      {
        const int xo[5] = {0, 0, 0, 0, 0};
        steps(0, 1, xo);
      }
      int xo0 = 0;
#pragma nounroll
      for (int o = 0; o < 3; ++o) {
        const int xo1 = xo0 == 2 * X_SLOT ? 0 : xo0 + X_SLOT, xo2 = xo1 == 2 * X_SLOT ? 0 : xo1 + X_SLOT;
        const int xo[5] = {xo0, xo1, xo1, xo2, xo2};
        steps(1, 4, xo);
        xo0 = xo2;
      }
      {
        const int xo[5] = {0, X_SLOT, 0, 0, 0};
        steps(1, 1, xo);
      }
      v4u bf2[2];
#pragma unroll
      for (int it = 0; it < 16; ++it) {
        const int s = (it >> 2) & 1, u = (it & 3) + 4 * (it >> 3), nx = it + 3;
        SCHED();
        glu4(s, u, xf[it & 3], [&](int a) {
          if (a == 0 && nx < 16) {
            if ((nx & 3) == 0) xb = xa[(nx >> 2) & 1] + sc(X_SLOT + ((nx >> 3) ^ G) * 8192);
            xf[nx & 3] = lds_rd128(L + xb + (nx & 3) * 2048);
          }
          if (a >= 1 && it < 3 && 3 * it + a - 1 < 8) rdw(3, 3 * it + a - 1);
          if (it == 0 && a == 3) { bb = xa[1] + sc(bo + B_SLOT); bf2[0] = lds_rd128(L + bb); bf2[1] = lds_rd128(L + bb + 4096); }
          if (it == 4 && a == 1) rd_const();
          if (it >= 8) scan_half(4 * (it - 8) + a);
        });
        if (it < 4) { mfma16t(accs[0][u], xf[it & 3], bfr[0]); mfma16t(accs[1][u], xf[it & 3], bfr[1]); }
        else if (it < 8) { mfma16t(accs[0][u], xf[it & 3], bf2[0]); mfma16t(accs[1][u], xf[it & 3], bf2[1]); }
      }
      asm volatile("s_nop 15" : "+v"(acc[0][7]), "+v"(acc[1][7]), "+v"(acc[2][7]), "+v"(acc[3][7]), "+v"(acc[0][6]), "+v"(acc[1][6]), "+v"(acc[2][6]), "+v"(acc[3][6]));
    } else {
      {
      kp = kargs();
      rsrc_t rx = mk_rsrc(kp->x, S_ * D_ * 2), rg = mk_rsrc(kp->gw, D_ * 2), rgo = mk_rsrc(kp->ws + WS_G, S_ * 4);
      const int tk0 = 128 * pn + 64 * (cb - 14) + 16 * c;
      v4u gv[8], xv[2][8];
#pragma unroll
      for (int z = 0; z < 8; ++z) gv[z] = __builtin_amdgcn_raw_buffer_load_b128(rg, (lane & 7) * 16, z * 128, 0);
#pragma unroll
      for (int gi = 0; gi < 2; ++gi)
#pragma unroll
        for (int z = 0; z < 8; ++z) xv[gi][z] = __builtin_amdgcn_raw_buffer_load_b128(rx, (lane >> 3) * 1024 + (lane & 7) * 16, (tk0 + gi * 8) * 1024 + z * 128, 0);
#pragma unroll
      for (int gi = 0; gi < 2; ++gi) {
        float s = 0.f;
#pragma unroll
        for (int z = 0; z < 8; ++z)
#pragma unroll
          for (int y = 0; y < 4; ++y) {
            const unsigned xa_ = xv[gi][z][y], ga = gv[z][y];
            s = __builtin_amdgcn_fdot2_f32_bf16(__builtin_bit_cast(bf16x2, xa_), __builtin_bit_cast(bf16x2, ga), s, false);
          }
        s += dpp<0xB1>(s); s += dpp<0x4E>(s); s += dpp<0x141>(s);
        const float g = __builtin_amdgcn_rcpf(1.f + __builtin_amdgcn_exp2f(s * -1.44269504088896341f));
        __builtin_amdgcn_raw_buffer_store_b32(f2u(g), rgo, (lane & 7) ? OOB : (lane >> 3) * 4, (tk0 + gi * 8) * 4, AUX_SC1);
      }
      }
      STAMP(1);
      zero_accs();
      int xo = 0;
#pragma nounroll
      for (int T = 0; T < 8; ++T) {
#pragma unroll
        for (int ks = 0; ks < 2; ++ks) {
          if (T < 7) BARRIER();
          v4u xf[4];
#pragma unroll
          for (int t = 0; t < 4; ++t) xf[t] = lds_rd128(L + (xa[ks] + xo + G * 8192) + t * 2048);
#pragma unroll
          for (int m = 0; m < 2; ++m) bfr[m] = lds_rd128(L + (xa[ks] + bo + (xo >> 1)) + m * 4096);
#pragma unroll
          for (int t = 0; t < 4; ++t) { mfma16t(accs[0][t], xf[t], bfr[0]); mfma16t(accs[1][t], xf[t], bfr[1]); }
          SCHED();
        }
        xo = xo == 2 * X_SLOT ? 0 : xo + X_SLOT;
      }
      asm volatile("s_nop 15" : "+v"(accs[0][0]), "+v"(accs[1][0]), "+v"(accs[0][1]), "+v"(accs[1][1]), "+v"(accs[0][2]), "+v"(accs[1][2]), "+v"(accs[0][3]), "+v"(accs[1][3]));
      rd_const();
      BARRIER();
#pragma unroll
      for (int n = 0; n < 32; ++n) { scan_half(n); SCHED(); }
    }
    *(volatile LAS unsigned*)(L + LDS_MD + 4 * w) = 1u;
    SCHED();
    STAMP(3);
    tag = __builtin_amdgcn_readfirstlane(tagv);
    const float ar = k0[0], ai = k0[1], cr = k0[2], ci = k0[3], p16r = k1[0], p16i = k1[1], p32r = k1[2], p32i = k1[3];
    __builtin_amdgcn_s_setprio(2);
    rsrc_t ra2 = mk_rsrc((const void*)(pws + WS_A2), A2_BYTES), rcar = mk_rsrc((const void*)(pws + WS_CARRY), 32 * P_ * 16);
    const int ck = 2 * pn + G;
    float pr, pi;
    {
      float tr, ti;
      const auto xr = __builtin_amdgcn_permlane16_swap(f2u(vr[15]), f2u(vr[15]), false, false);
      const auto xi = __builtin_amdgcn_permlane16_swap(f2u(vi[15]), f2u(vi[15]), false, false);
      const float e0r = u2f(xr[0]), e1r = u2f(xr[1]), e0i = u2f(xi[0]), e1i = u2f(xi[1]);
      cmul(e0r, e0i, p16r, p16i, tr, ti); tr += e1r; ti += e1i;
      const auto yr = __builtin_amdgcn_permlane32_swap(f2u(tr), f2u(tr), false, false);
      const auto yi = __builtin_amdgcn_permlane32_swap(f2u(ti), f2u(ti), false, false);
      const float f0r = u2f(yr[0]), f1r = u2f(yr[1]), f0i = u2f(yi[0]), f1i = u2f(yi[1]);
      float Er, Ei;
      cmul(f0r, f0i, p32r, p32i, Er, Ei); Er += f1r; Ei += f1i;
      const float br = (q & 2) ? f0r : 0.f, bi = (q & 2) ? f0i : 0.f;
      cmul(br, bi, p16r, p16i, tr, ti);
      pr = (q & 1) ? tr + e0r : br; pi = (q & 1) ? ti + e0i : bi;
      const v4u gq = {f2u(Er), tag, f2u(Ei), ~tag};
      st_sc1(gq, rcar, q ? OOB : ch * 16, (31 - ck) * (P_ * 16));
    }
    asm volatile("" : "+v"(pr), "+v"(pi) : : "memory");
    SCHED();
    STAMP(4);
    v4u cv[8];
    const int cvo = ch * 16 + (32 - ck + 8 * q) * (P_ * 16);
    auto ld_carry = [&]() {
#pragma unroll
      for (int z = 0; z < 8; ++z) cv[z] = ld_sc1(rcar, cvo, z * (P_ * 16));
    };
    auto publish = [&](int part) {
      *(volatile LAS unsigned*)(L + LDS_DONE + 16 * part + 4 * w) = 1u;
      const bool last = all4(L + LDS_DONE + 16 * part);
      st_flag((unsigned char*)pws, WS_FA2 + part * 32768 + pn * 128 + cb * 8, tag, last && lane < 16 ? lane * 2048 : OOB);
      CFENCE();
    };
    if (!light) {
#pragma unroll
      for (int u = 0; u < 8; ++u) {
        float hv[8];
#pragma unroll
        for (int j = 0; j < 8; ++j) hv[j] = acc[j >> 2][u][j & 3] * -1.44269504088896341f;
        SCHED();
#pragma unroll
        for (int j = 0; j < 8; ++j) hv[j] = __builtin_amdgcn_exp2f(hv[j]);
        SCHED();
#pragma unroll
        for (int j = 0; j < 8; ++j) hv[j] += 1.f;
        SCHED();
#pragma unroll
        for (int j = 0; j < 8; ++j) hv[j] = __builtin_amdgcn_rcpf(hv[j]);
        SCHED();
#pragma unroll
        for (int j = 0; j < 8; ++j) hv[j] *= acc[2 + (j >> 2)][u][j & 3];
        SCHED();
        st_sc1(v4u{pk_bf16(hv[0], hv[1]), pk_bf16(hv[2], hv[3]), pk_bf16(hv[4], hv[5]), pk_bf16(hv[6], hv[7])}, ra2, lane * 16,
               ((2 * pn + ((u >> 2) ^ G)) * NK + 4 * cb + c) * 4096 + (u & 3) * 1024);
        if (u + 1 < 8) asm volatile("" : "+v"(acc[0][u + 1]), "+v"(acc[1][u + 1]) : : "memory");
      }
    }
    CFENCE();
    SCHED();
    STAMP(5);
    ld_carry();
    CFENCE();
    SCHED();
#pragma unroll
    for (int z = 0; z < 16; ++z) {
      const float t = cr * vr[z] - ci * vi[z];
      vi[z] = -cr * vi[z] - ci * vr[z]; vr[z] = t;
    }
    float qr[4], qi[4];
    qr[0] = ar; qi[0] = ai; qr[1] = ar; qi[1] = ai; csq(qr[1], qi[1], 1);
    cmul(qr[1], qi[1], ar, ai, qr[2], qi[2]); qr[3] = qr[1]; qi[3] = qi[1]; csq(qr[3], qi[3], 1);
    const float p4r = qr[3], p4i = qi[3];
    float p64r = p32r, p64i = p32i; csq(p64r, p64i, 1);
    float p512r = p64r, p512i = p64i; csq(p512r, p512i, 3);
    float p1kr = p512r, p1ki = p512i; csq(p1kr, p1ki, 1);
    float sgr, sgi, wgr, wgi;
    cmul((q & 1) ? p16r : 1.f, (q & 1) ? p16i : 0.f, (q & 2) ? p32r : 1.f, (q & 2) ? p32i : 0.f, sgr, sgi);
    cmul((q & 1) ? p512r : 1.f, (q & 1) ? p512i : 0.f, (q & 2) ? p1kr : 1.f, (q & 2) ? p1ki : 0.f, wgr, wgi);
#pragma unroll
    for (int z = 0; z < 16; ++z) asm volatile("" : "+v"(vr[z]), "+v"(vi[z]));
    asm volatile("" : "+v"(sgr), "+v"(sgi), "+v"(wgr), "+v"(wgi), "+v"(p64r), "+v"(p64i));
    SCHED();
    const int want = imin(imax(ck - 8 * q, 0), 8);
    auto carry_ok = [&]() {
      int good = 0;
#pragma unroll
      for (int z = 0; z < 8; ++z) good += ((cv[z][1] ^ tag) | (cv[z][3] ^ ~tag)) == 0u;
      return wall(good == want);
    };
    {
      lds_wait(L + LDS_PF + 4 * w);
      b2_read(true, L + w * 16384, lane, b2[2]);
      b2_read(true, L + w * 16384 + 8192, lane, b2[3]);
      CFENCE();
      *(volatile LAS unsigned*)(L + LDS_CO + 4 * w) = 1u;
      CFENCE();
    }
    wait_vm<8>();
    publish(0);
    STAMP(14);
    if (__builtin_expect(!carry_ok(), 0)) {
#pragma nounroll
      for (unsigned spins = 0; spins < SPIN_CAP; ++spins) {
        __builtin_amdgcn_s_sleep(2);
        CFENCE();
        ld_carry();
        if (carry_ok()) break;
      }
    }
    STAMP(6);
    float Gr, Gi;
    {
      float Hr = 0.f, Hi = 0.f, tr, ti;
#pragma unroll
      for (int z = 7; z >= 0; --z) { cmul(Hr, Hi, p64r, p64i, tr, ti); Hr = tr + u2f(cv[z][0]); Hi = ti + u2f(cv[z][2]); }
      cmul(Hr, Hi, wgr, wgi, tr, ti);
      { const auto a = __builtin_amdgcn_permlane16_swap(f2u(tr), f2u(tr), false, false);
        const auto c2 = __builtin_amdgcn_permlane16_swap(f2u(ti), f2u(ti), false, false);
        tr = u2f(a[0]) + u2f(a[1]); ti = u2f(c2[0]) + u2f(c2[1]); }
      { const auto a = __builtin_amdgcn_permlane32_swap(f2u(tr), f2u(tr), false, false);
        const auto c2 = __builtin_amdgcn_permlane32_swap(f2u(ti), f2u(ti), false, false);
        tr = u2f(a[0]) + u2f(a[1]); ti = u2f(c2[0]) + u2f(c2[1]); }
      cmul(tr, ti, sgr, sgi, Hr, Hi);
      cmul(Hr + pr, Hi + pi, cr, ci, Gr, Gi);
    }
    {
      LAS unsigned char* T = L + B_BASE + (c >> 1) * (2 * B_SLOT) + (c & 1) * 4096;
      const int s32 = ((i15 >> 1) & 1) | ((i15 >> 2) & 2), s8 = (i15 & 1) | ((i15 >> 1) & 2);
#pragma unroll
      for (int t = 0; t < 4; ++t) {
        float orr[4], oi[4];
#pragma unroll
        for (int z = 0; z < 4; ++z) {
          orr[z] = __builtin_fmaf(-qi[z], Gi, __builtin_fmaf(qr[z], Gr, vr[4 * t + z]));
          oi[z] = __builtin_fmaf(-qi[z], Gr, __builtin_fmaf(-qr[z], Gi, vi[4 * t + z]));
        }
        if (t < 3) { float tr, ti; cmul(Gr, Gi, p4r, p4i, tr, ti); Gr = tr; Gi = ti; }
        LAS unsigned char* a = T + i15 * 128 + ((t ^ s32) << 5) + ((q ^ s8) << 3);
        *(LAS v2u*)a = v2u{pk_bf16(orr[0], orr[1]), pk_bf16(orr[2], orr[3])};
        *(LAS v2u*)(a + 2048) = v2u{pk_bf16(oi[0], oi[1]), pk_bf16(oi[2], oi[3])};
      }
      const int qq = i15 >> 2, pp = lane & 3;
      const int so = (ck * NK + 56 + 4 * (cb >> 1) + (cb & 1)) * 4096 + (c >> 1) * 512;
#pragma unroll
      for (int t = 0; t < 4; ++t) {
        v2u part[2];
#pragma unroll
        for (int hh = 0; hh < 2; ++hh)
          part[hh] = lds_rdtr(T + (8 * q + 4 * hh + qq) * 128 + ((t ^ ((qq >> 1) | ((q & 1) << 1))) << 5) + ((pp ^ ((qq & 1) | (hh << 1))) << 3));
        st_sc1(v4u{part[0][0], part[0][1], part[1][0], part[1][1]}, ra2, (lane >> 5) * 8192 + (lane & 31) * 16, so + t * 1024);
      }
    }
    SCHED();
    STAMP(7);
    wait_vm<0>();
    publish(1);
    STAMP(12);
  }
  kp = kargs();
  asm volatile("" : "+s"(R), "+s"(N));
  pc = p2_ctx(pWd, pCre, pCim, w, N);
  const int d0 = 2 * pc.sh;
  if (w < 4) {
    lds_wait_eq(L + LDS_PF + 4 * w, 2u);
    b2_read(true, L + w * 16384, lane, b2[0]);
    b2_read(true, L + w * 16384 + 8192, lane, b2[1]);
    WAIT_LDS();
    b2_load<0, 0, 8>(p2_ctx(pWd, pCre, pCim, w + 4, N), vb, 6, w * 16384);
    wait_vm<0>();
    *(volatile LAS unsigned*)(L + LDS_CO + 4 * w) = 2u;
    STAMP(13);
  }

  {
    lds_wait4(L + LDS_DONE);
    const int cl = lane & 15, lo = pc.sh ? (pc.g0 - 28) & ~1 : pc.g0 >> 1, hi = pc.sh ? (pc.g0 + pc.nd - 29) | 1 : (pc.g0 + pc.nd - 1) >> 1;
    GAS u64* fl = (GAS u64*)(kp->ws + WS_FA2) + ((pc.sh * 16 + (R & 1) * 8 + N) * 16 + (R >> 1)) * 16 + cl;
    if (w >= 4) {
      poll_flags(fl, tag64(tag), (cl >= lo && cl <= hi) || cl >= 14, cl < 14);
    } else if (w == 3) {
      lds_wait4(L + LDS_DONE + 16);
      __builtin_amdgcn_s_sleep(SP_FIRST);
#pragma nounroll
      for (unsigned spins = 0; spins < SPIN_CAP; ++spins) {
        CFENCE();
        const u64 v = __hip_atomic_load(fl, RLX_AGENT);
        const unsigned up = (unsigned)__builtin_amdgcn_ballot_w64(v == tag64(tag)) & 0xFFFFu;
        CFENCE();
        *(volatile LAS unsigned*)(L + LDS_RDY) = up;
        if (up == 0xFFFFu) break;
        if (up & 0x3FFFu) __builtin_amdgcn_s_sleep(SP_NEXT); else __builtin_amdgcn_s_sleep(32);
      }
      CFENCE();
    } else {
      const unsigned need = (0xFu << lo) | 0xC000u;
#pragma nounroll
      for (unsigned spins = 0; spins < (SPIN_CAP << 4); ++spins) {
        if ((lds_word(L + LDS_RDY) & need) == need) break;
        __builtin_amdgcn_s_sleep(2);
      }
      CFENCE();
    }
  }
  STAMP(8);
  if (w >= 4) __builtin_amdgcn_s_setprio(1);
  glds<B_BASE + B_SLOT, 0>(mk_rs((const void*)pX, S_ * D_ * 2), w * 1024, ((lane >> 5) * 16 + ((lane >> 3) & 3)) * 1024 + (lane & 7) * 16,
                             (64 * R + 32 * (w & 1) + 4 * (w >> 1)) * 1024 + N * 128);
  const int tok = 64 * R + 32 * (w & 1) + 4 * (w >> 1) + (lane >> 4);
  const v2u dv = __builtin_amdgcn_raw_buffer_load_b64(mk_rsrc(kp->Dsk, D_ * 2), (lane & 15) * 8, N * 128, 0);
  const float ge = u2f(__builtin_amdgcn_raw_buffer_load_b32(mk_rsrc(kp->ws + WS_G, S_ * 4), tok * 4 + (lane & 1) * 64, 0, AUX_SC1));
  SCHED();
  const v4u ra = mk_rs(kp->ws + WS_A2 + (R * NK + 2 * pc.g0) * 4096, pc.nd * 8192);
  const int va = ((lane >> 4) & 1) * 1024 + (lane >> 5) * 256 + (lane & 15) * 16;
  v4u af[8];
  {
    const int o = p2_ds(pc, 0) * 8192;
    p2_first<0>(af, ra, va, o, o + 4096);
  }
  SCHED();
#define CPRIO 0
  if (w < 4) __builtin_amdgcn_s_setprio(CPRIO);
  f32x16 acc[2][2];
#pragma unroll
  for (int e = 0; e < 2; ++e)
#pragma unroll
    for (int tt = 0; tt < 2; ++tt) { acc[e][tt] = f32x16{}; asm volatile("" : "+v"(acc[e][tt])); }
  asm volatile("s_nop 1");
  auto step = [&](auto Dc) {
    constexpr int d = decltype(Dc)::value;
    const int o = p2_ds(pc, d + 1 - d0) * 8192;
    p2_step<d, 0>(acc, b2[d & 3], af, ra, va, o, o + 4096, w);
    if (d < 2) b2_read(false, L + w * 16384 + d * 8192, lane, b2[d]);
    if (d == PK6 && w >= 4) {
      lds_wait_eq(L + LDS_CO + 4 * (w - 4), 2u);
      b2_read(false, L + (w - 4) * 16384, lane, b2[2]);
      CFENCE();
      *(volatile LAS unsigned*)(L + LDS_PF + 4 * (w - 4)) = 3u;
      CFENCE();
    }
    SCHED();
  };
  if (w >= 4) { step(IC<0>{}); step(IC<1>{}); }
  step(IC<2>{}); step(IC<3>{}); step(IC<4>{}); step(IC<5>{});
  if (w >= 4) step(IC<6>{});
  asm volatile("s_nop 15\n\ts_nop 7" : "+v"(acc[0][0]), "+v"(acc[1][0]), "+v"(acc[0][1]), "+v"(acc[1][1]));
  SCHED();
  STAMP(9);
  kp = kargs();
  if (w < 4) lds_wait_eq(L + LDS_PF + 4 * w, 3u);
  {
    const int t3 = lane & 3, g3 = ((t3 & 1) * 3) ^ (t3 >> 1), hi = lane >> 5;
#pragma unroll
    for (int T = 0; T < 2; ++T)
#pragma unroll
      for (int E = 0; E < 2; ++E)
#pragma unroll
        for (int g = 0; g < 4; ++g) {
          const int e = 2 * E + (g >> 1), q = 2 * (g & 1) + hi;
          *(LAS f32x4*)(L + w * 16384 + ((2 * T + ((lane >> 4) & 1)) * 4 + e) * 1024 + ((16 * q + (((((lane >> 2) & 3) ^ q) << 2) | (e ^ g3))) << 4)) =
              f32x4{acc[E][T][4 * g], acc[E][T][4 * g + 1], acc[E][T][4 * g + 2], acc[E][T][4 * g + 3]};
        }
  }
  if (b == 255 && w == 0) {
    rsrc_t rf = mk_rsrc(kp->ws + WS_FA2 + 32768, 256 * 8);
#pragma nounroll
    for (unsigned spins = 0; spins < SPIN_CAP; ++spins) {
      CFENCE();
      const v4u f0 = ld_sc1(rf, lane * 32, 0), f1 = ld_sc1(rf, lane * 32, 16);
      if (wall(((f0[0] ^ tag) | (f0[1] ^ ~tag) | (f0[2] ^ tag) | (f0[3] ^ ~tag) | (f1[0] ^ tag) | (f1[1] ^ ~tag) | (f1[2] ^ tag) | (f1[3] ^ ~tag)) == 0u)) break;
      __builtin_amdgcn_s_sleep(8);
    }
    st_flag(kp->ws, WS_EPOCH, tag, lane == 0 ? 0 : OOB);
  }
  WAIT_LDS();
  BARRIER();
  STAMP(10);
  {
    const int q = lane & 3, e = (lane >> 2) & 3, t3 = lane >> 4;
    const int base = ((w >> 1) * 4 + e) * 1024 + q * 256 + ((e ^ ((t3 & 1) * 3) ^ (t3 >> 1)) << 4);
    rsrc_t ro = mk_rsrc(kp->out, S_ * D_ * 4);
    const float go = dpp<0xB1>(ge);
    f32x4 res[2];
#pragma unroll
    for (int hf = 0; hf < 2; ++hf) {
      f32x4 sg = {0.f, 0.f, 0.f, 0.f}, ss = {0.f, 0.f, 0.f, 0.f};
      const int a = base + (((2 * (w & 1) + hf) ^ q) << 6);
#pragma unroll
      for (int ww = 0; ww < 8; ++ww) {
        const f32x4 v = *(LAS f32x4*)(L + ww * 16384 + a);
        if (ww < 4) ss += v; else sg += v;
      }
      const float g = (lane & 1) == hf ? ge : go;
      f32x4 m = ss * (2.f * g) + sg * (1.f - g);
      const v2u xv = *(LAS v2u*)(L + B_BASE + B_SLOT + w * 1024 + hf * 512 + lane * 8);
      m[0] += g * (bf2f(xv[0] & 0xFFFFu) * bf2f(dv[0] & 0xFFFFu)); m[1] += g * (bfhi(xv[0]) * bfhi(dv[0]));
      m[2] += g * (bf2f(xv[1] & 0xFFFFu) * bf2f(dv[1] & 0xFFFFu)); m[3] += g * (bfhi(xv[1]) * bfhi(dv[1]));
      res[hf] = m;
    }
    asm volatile("" : "+v"(res[0]), "+v"(res[1]));
#pragma unroll
    for (int hf = 0; hf < 2; ++hf)
      __builtin_amdgcn_raw_buffer_store_b128(__builtin_bit_cast(v4u, res[hf]), ro, tok * 2048 + (lane & 15) * 16 + hf * 32768 + N * 256, 0, OUT_AUX);
  }
  STAMP(11);
}

#if defined(__HIP_DEVICE_COMPILE__)
#pragma clang attribute pop
#endif

extern "C" void kernel_launch(void* const* d_in, const int* in_sizes, int n_in, void* d_out, int out_size, void* d_ws, size_t ws_size,
                              hipStream_t stream) {
  Params p;
  p.x = (const u16*)d_in[0]; p.Lre = (const u16*)d_in[1]; p.Lim = (const u16*)d_in[2]; p.Bre = (const u16*)d_in[3];
  p.Bim = (const u16*)d_in[4]; p.Cre = (const u16*)d_in[5]; p.Cim = (const u16*)d_in[6]; p.ldt = (const u16*)d_in[7];
  p.Dsk = (const u16*)d_in[8]; p.Wg = (const u16*)d_in[10]; p.Wu = (const u16*)d_in[11]; p.Wd = (const u16*)d_in[12];
  p.gw = (const u16*)d_in[13]; p.out = (float*)d_out; p.ws = (unsigned char*)d_ws;
  (void)hipFuncSetAttribute((const void*)k_fused, hipFuncAttributeMaxDynamicSharedMemorySize, LDS_BYTES);
  k_fused<<<256, 512, LDS_BYTES, stream>>>(p);
}
```

```cpp
#define GPOLL 4
#define SP_FIRST 8
#define WARMM
#define WARM0 2
#define PRIO2
#include <hip/hip_runtime.h>
#include <stdint.h>

typedef unsigned short u16;
typedef unsigned long long u64;
typedef unsigned v4u __attribute__((ext_vector_type(4)));
typedef unsigned v2u __attribute__((ext_vector_type(2)));
typedef short s16x4 __attribute__((ext_vector_type(4)));
typedef __bf16 bf16x8 __attribute__((ext_vector_type(8)));
typedef __bf16 bf16x2 __attribute__((ext_vector_type(2)));
typedef float f32x2 __attribute__((ext_vector_type(2)));
typedef float f32x4 __attribute__((ext_vector_type(4)));
typedef float f32x16 __attribute__((ext_vector_type(16)));
#define LAS __attribute__((address_space(3)))
#define GAS __attribute__((address_space(1)))
#define RLX_AGENT __ATOMIC_RELAXED, __HIP_MEMORY_SCOPE_AGENT
typedef __amdgpu_buffer_rsrc_t rsrc_t;

constexpr int S_ = 2048, D_ = 512, P_ = 512, F_ = 1792;
constexpr int NK = 88;
constexpr int AUX_SC1 = 16;
#define OUT_AUX 2
constexpr int WS_EPOCH = 0;
constexpr int WS_G = 8192;
constexpr int WS_FA2 = 65536;
constexpr int CTRL_BYTES = 131072;
constexpr int WS_CARRY = 131072;
constexpr int WS_A2 = 524288;
constexpr int A2_BYTES = 32 * NK * 4096;
constexpr int WS_STAMP = 32 << 20;
constexpr int LDS_BYTES = 160 * 1024;
constexpr int X_SLOT = 16384, W_BASE = 65536, W_SLOT = 16384, B_BASE = 131072, B_SLOT = 8192;
constexpr int LDS_DONE = 155648, LDS_PF = LDS_DONE + 32, LDS_MD = LDS_DONE + 48, LDS_CO = LDS_DONE + 64, LDS_TAG = LDS_DONE + 80;
constexpr int LDS_RDY = LDS_DONE + 96;
constexpr int LDS_DUMMY = LDS_DONE + 256, LDS_CONST = LDS_DONE + 512;
constexpr int OOB = 1 << 28;
constexpr int CODE_BYTES = 30 * 1024;
constexpr unsigned SPIN_CAP = 1u << 18;

struct Params {
  const u16 *x, *Wg, *Wu, *Bre, *Bim, *Cre, *Cim, *Wd;
  unsigned char* ws;
  const u16 *Lre, *Lim, *ldt, *Dsk, *gw;
  float* out;
};

#if defined(__HIP_DEVICE_COMPILE__)
#pragma clang attribute push(__attribute__((target("no-packed-fp32-ops"))), apply_to = function)
#endif
__device__ __forceinline__ float u2f(unsigned v) { return __builtin_bit_cast(float, v); }
__device__ __forceinline__ unsigned f2u(float v) { return __builtin_bit_cast(unsigned, v); }
__device__ __forceinline__ bool wall(bool p) { return __builtin_amdgcn_ballot_w64(!p) == 0; }
__device__ __forceinline__ bool wany(bool p) { return __builtin_amdgcn_ballot_w64(p) != 0; }
__device__ __forceinline__ int imin(int a, int b) { return a < b ? a : b; }
__device__ __forceinline__ int imax(int a, int b) { return a > b ? a : b; }
template <int CTRL> __device__ __forceinline__ float dpp(float v) { return u2f(__builtin_amdgcn_mov_dpp(f2u(v), CTRL, 0xF, 0xF, true)); }
__device__ __forceinline__ rsrc_t mk_rsrc(const void* p, int bytes) {
  return __builtin_amdgcn_make_buffer_rsrc((void*)p, 0, bytes, 0x00020000);
}
__device__ __forceinline__ float bf2f(unsigned v) { return u2f(v << 16); }
__device__ __forceinline__ float bfhi(unsigned v) { return u2f(v & 0xFFFF0000u); }
__device__ __forceinline__ unsigned pk_bf16(float lo, float hi) {
  f32x2 t = {lo, hi};
  return __builtin_bit_cast(unsigned, __builtin_convertvector(t, bf16x2));
}
__device__ __forceinline__ void mfma16t(f32x4& c, v4u a, v4u b) {
  asm volatile("v_mfma_f32_16x16x32_bf16 %0, %1, %2, %0" : "+v"(c) : "v"(a), "v"(b));
}
__device__ __forceinline__ void mfma32t(f32x16& c, v4u a, v4u b) {
  asm volatile("v_mfma_f32_32x32x16_bf16 %0, %1, %2, %0" : "+v"(c) : "v"(a), "v"(b));
}
__device__ __forceinline__ v4u lds_rd128(LAS unsigned char* p) { return *(LAS v4u*)p; }
__device__ __forceinline__ v2u lds_rdtr(LAS unsigned char* p) {
  return __builtin_bit_cast(v2u, __builtin_amdgcn_ds_read_tr16_b64_v4i16((LAS s16x4*)p));
}
__device__ __forceinline__ v4u mk_rs(const void* p, unsigned bytes) {
  const u64 a = (u64)p;
  unsigned lo = (unsigned)a, hi = (unsigned)(a >> 32) & 0xFFFFu;
  asm volatile("" : "+s"(lo), "+s"(hi));
  return v4u{lo, hi, bytes, 0x00020000u};
}
template <int LI, int SI> __device__ __forceinline__ void glds(v4u rs, int lds, int voff, int soff) {
  int t;
  asm volatile("s_add_u32 m0, %1, %5\n\ts_add_u32 %0, %4, %6\n\tbuffer_load_dwordx4 %2, %3, %0 offen lds"
               : "=&s"(t) : "s"(lds), "v"(voff), "s"(rs), "s"(soff), "i"(LI), "i"(SI) : "memory", "scc");
}
typedef const __attribute__((address_space(4))) Params* KP;
__device__ __forceinline__ KP kargs() {
  KP k = (KP)__builtin_amdgcn_kernarg_segment_ptr();
  asm volatile("" : "+s"(k));
  return k;
}
__device__ __forceinline__ void warm(v4u rs, int voff, int soff) {
  asm volatile("s_mov_b32 m0, %3\n\ts_nop 0\n\tbuffer_load_dword %0, %1, %2 offen lds" : : "v"(voff), "s"(rs), "s"(soff), "i"(LDS_DUMMY) : "memory");
}
__device__ __forceinline__ v4u ld_sc1(rsrc_t rs, int voff, int soff) { return __builtin_amdgcn_raw_buffer_load_b128(rs, voff, soff, AUX_SC1); }
__device__ __forceinline__ void st_sc1(v4u v, rsrc_t rs, int voff, int soff) { __builtin_amdgcn_raw_buffer_store_b128(v, rs, voff + soff, 0, AUX_SC1); }
__device__ __forceinline__ u64 tag64(unsigned tag) { return ((u64)(~tag) << 32) | tag; }
__device__ __forceinline__ void st_flag(unsigned char* ws, int off, unsigned tag, int voff) {
  __builtin_amdgcn_raw_buffer_store_b64(v2u{tag, ~tag}, mk_rsrc(ws, CTRL_BYTES), voff, off, AUX_SC1);
}
__device__ __forceinline__ unsigned lds_word(LAS unsigned char* p) { return __builtin_amdgcn_readfirstlane(*(volatile LAS unsigned*)p); }
#define BARRIER() asm volatile("s_barrier" ::: "memory")
#define SCHED() __builtin_amdgcn_sched_barrier(0)
#define CFENCE() asm volatile("" ::: "memory")
#define WAIT_LDS() asm volatile("s_waitcnt lgkmcnt(0)" ::: "memory")
template <int N> __device__ __forceinline__ void wait_vm() { asm volatile("s_waitcnt vmcnt(%c0)" ::"i"(N) : "memory"); }

#define STAMP(i) do {} while (0)
#define PICK 0
#define SP_NEXT 8
#define PK6 3
#define POLL_SLEEP 4
#define POLL_IDLE 16

__device__ __forceinline__ void poll_flags(GAS u64* f, u64 want, bool care, bool late) {
#pragma nounroll
  for (unsigned spins = 0; spins < SPIN_CAP; ++spins) {
    CFENCE();
    const u64 v = __hip_atomic_load(f, RLX_AGENT);
    if (wall(!care || v == want)) break;
    if (wany(late && v == want)) __builtin_amdgcn_s_sleep(POLL_SLEEP); else __builtin_amdgcn_s_sleep(POLL_IDLE);
  }
  CFENCE();
}
__device__ __forceinline__ void csq(float& r, float& i, int n) {
#pragma unroll
  for (int z = 0; z < n; ++z) { const float t = r * r - i * i; i = 2.f * r * i; r = t; }
}

#define NROT 102
#define CONST_AT 2
constexpr int LASTB = 13;
#define RAWN 2
#define NRAWD 2
constexpr int raw_lo(int b) { const int n = (b - (LASTB + 1)) * RAWN + 8 * NRAWD; return n < 0 ? 0 : n; }
struct Sched {
  int n;
  int type[40], idx[40];
  int lo[LASTB + 1], hi[LASTB + 1];
  int wait[LASTB + 1];
  bool ok;
};
constexpr Sched make_sched(int kind) {
  Sched s{};
  int cnt[40] = {}, need[40] = {}, avail[40] = {};
  int n = 0;
  auto add = [&](int ty, int ix, int nd, int av) {
    if (kind && ty == 2) return;
    s.type[n] = ty; s.idx[n] = ix; cnt[n] = ty == 2 ? 4 : 3; need[n] = nd < LASTB ? nd : LASTB; avail[n] = av; ++n;
  };
  add(0, 0, 0, -1); add(1, 0, 0, -1); add(2, 0, 0, -1); add(2, 1, 0, -1);
  add(0, 1, 1, -1); add(1, 1, 1, -1); add(2, 2, 1, -1); add(2, 3, 2, -1);
  add(0, 2, 3, -1); add(1, 2, 3, -1);
  add(2, 4, 3, 1); add(2, 5, 4, 1);
  for (int b = 2; b < 12; ++b) {
    const int T = b / 2 + 2;
    add(b & 1, T, 2 * T - 1, kind ? (b & ~1) : b);
    add(2, b + 4, b + 3, b);
  }
  s.n = n;
  s.ok = true;
  const int LIM = 8;
  int endpos[40] = {};
  int next = 0, total = 0;
  for (int b = 0; b <= LASTB; ++b) {
    s.lo[b] = next;
    const int start = total;
    for (;;) {
      if (next >= n || avail[next] > b - 1) break;
      int lastneed = 0;
      for (int i = 0; i < next; ++i) if (need[i] <= b) lastneed = endpos[i];
      if (b < 3 && need[next] > b && total + cnt[next] - lastneed > LIM + 2 * b) break;
      if (b >= 3 && need[next] > b + 1 && total + cnt[next] - start > 11) break;
      total += cnt[next]; endpos[next] = total; ++next;
    }
    s.hi[b] = next;
    if (kind == 0) total += raw_lo(b + 1) - raw_lo(b);
    if (kind == 0 && b >= WARM0 && b < WARM0 + 3) total += 1;
    int lastneed = 0;
    for (int i = 0; i < next; ++i) if (need[i] <= b) lastneed = endpos[i];
    for (int i = next; i < n; ++i) if (need[i] <= b) s.ok = false;
    s.wait[b] = total - lastneed;
  }
  if (next != n) s.ok = false;
  return s;
}
constexpr Sched SCH[2] = {make_sched(0), make_sched(1)};
static_assert(SCH[0].ok && SCH[1].ok, "every ring item must be issued in time");

struct P2Ctx { u64 a0, a1; int so0, sh, g0, nd, rot; unsigned bytes; };
__device__ __forceinline__ P2Ctx p2_ctx(u64 Wd, u64 Cre, u64 Cim, int w, int N) {
  P2Ctx c;
  const int ssm = w < 4;
  const u64 m = 0 - (u64)ssm;
  c.a0 = (Cre & m) | (Wd & ~m); c.a1 = (Cim & m) | (Wd & ~m);
  c.sh = ssm;
  c.so0 = N * 128 + ssm * (N * (65536 - 128) - 14 * 128);
  c.bytes = F_ * D_ * 2u - ssm * (F_ * D_ * 2u - D_ * P_ * 2u);
  c.g0 = 7 * w - 28 + ssm * (56 - 3 * w);
  c.nd = 7 - 3 * ssm;
#define P2ROT 1
  c.rot = P2ROT == 0 ? 0 : (P2ROT == 1 ? (N & 1) * (3 - ssm) : (P2ROT == 2 ? (N & 3) * (2 - ssm) : (N * c.nd) >> 3));
  return c;
}
__device__ __forceinline__ int p2_ds(const P2Ctx& c, int d) { const int t = d + c.rot; return t >= c.nd ? t - c.nd : t; }
__device__ __forceinline__ void b2_src(const P2Ctx& c, int d, v4u& rs, int& so) {
  const int g = c.g0 + p2_ds(c, d);
  const u64 x0 = c.a0, x1 = c.a1;
  rs = mk_rs((const void*)((g & 1) ? x1 : x0), c.bytes);
  so = c.so0 + (c.sh ? (g >> 1) * 128 : g * 65536);
}
template <int LI, int N0, int N1> __device__ __forceinline__ void b2_rows(v4u rs, int lds, int vb, int so) {
  if constexpr (N0 < N1) { glds<LI, N0 * 8192>(rs, lds, (N0 & 1) ? vb ^ 16 : vb, so); b2_rows<LI + 1024, N0 + 1, N1>(rs, lds, vb, so); }
}
template <int LI, int N0, int N1> __device__ __forceinline__ void b2_load(const P2Ctx& c, int vb, int d, int lds) {
  v4u rs; int so;
  b2_src(c, d, rs, so);
  b2_rows<LI, N0, N1>(rs, lds, vb, __builtin_amdgcn_readfirstlane(so));
}
__device__ __forceinline__ void b2_read(bool ssm, LAS unsigned char* img, int lane, v4u (&f)[2][4]) {
  if (ssm) {
    const int i = lane & 31, sw = ((i & 2) << 1) | ((i >> 1) & 2) | ((i >> 3) & 1);
#pragma unroll
    for (int h = 0; h < 2; ++h)
#pragma unroll
      for (int z = 0; z < 4; ++z) f[h][z] = lds_rd128(img + i * 128 + (((4 * h + (z & 2) + (lane >> 5)) ^ sw) << 4) + (z & 1) * 4096);
  } else {
    const int qq = (lane >> 2) & 3, hi = lane >> 5;
    const int c0 = (((lane >> 4) & 1) * 2 + ((lane >> 1) & 1)) ^ (((qq & 2) << 1) | (hi << 1));
#pragma unroll
    for (int h = 0; h < 2; ++h)
#pragma unroll
      for (int z = 0; z < 4; ++z) {
        LAS unsigned char* a = img + (4 * hi + qq) * 128 + ((c0 ^ ((z & 1) << 2) ^ (z >> 1)) << 4) + (lane & 1) * 8 + h * 4096 + (z >> 1) * 1024;
        const v2u lo = lds_rdtr(a), hi2 = lds_rdtr(a + 2048);
        f[h][z] = v4u{lo[0], lo[1], hi2[0], hi2[1]};
      }
  }
}

struct LdCtx { v4u rx, rw, rb; u64 Wd, Cre, Cim; int lw, lb, vx, vw0, vw1, vbb, rot, lane, vb, vwarm, xcd, ch; mutable unsigned sv, lrb, lib, ldb, ep; };
__device__ __forceinline__ void warm_chunk(u64 x0, u64 x1, u64 x2, int xc, int vwarm, int slot) {
  asm volatile("" : "+s"(xc));
  int i = slot < 6 ? slot + xc : slot - 6 + xc;
  if (slot < 6) { i = i >= 12 ? i - 12 : (i >= 6 ? i - 6 : i); } else { i = 6 + (i >= 10 ? i - 10 : (i >= 5 ? i - 5 : i)); }
  const u64 src = i < 7 ? x0 : (i < 9 ? x1 : x2);
  const unsigned bytes = (unsigned)slot > 10u ? 0u : (i < 7 ? F_ * D_ * 2 : D_ * P_ * 2);
  warm(mk_rs((const void*)src, bytes), vwarm, (i < 7 ? i : ((i - 7) & 1)) * 262144);
}

template <int KIND, int I> __device__ __forceinline__ void ld_item(const LdCtx& c) {
  constexpr int ty = SCH[KIND].type[I], t = SCH[KIND].idx[I];
  int rot = c.rot;
  asm volatile("" : "+s"(rot));
  if constexpr (ty < 2) {
    const int k0 = ((t + rot) & 7) * 128;
    glds<(t % 3) * X_SLOT + 2 * ty * 4096, ty * 65536>(c.rx, c.lw * 1024, c.vx, k0);
    glds<(t % 3) * X_SLOT + (2 * ty + 1) * 4096, ty * 65536 + 8192>(c.rx, c.lw * 1024, c.vx, k0);
    glds<B_BASE + (t % 3) * B_SLOT + ty * 2048, ty * 16384>(c.rb, c.lb, c.vbb, k0);
  } else {
    const int k0 = (((t >> 1) + rot) & 7) * (64 * F_ * 2);
    constexpr int LI = W_BASE + (t & 3) * W_SLOT, SI = (t & 1) * 32 * F_ * 2;
    glds<LI, SI>(c.rw, c.lw * 4096, c.vw0, k0);
    glds<LI + 1024, SI + 4 * F_ * 2>(c.rw, c.lw * 4096, c.vw0, k0);
    glds<LI + 2048, SI + 8 * F_ * 2>(c.rw, c.lw * 4096, c.vw1, k0);
    glds<LI + 3072, SI + 12 * F_ * 2>(c.rw, c.lw * 4096, c.vw1, k0);
  }
}
template <int KIND, int LO, int HI> __device__ __forceinline__ void ld_items(const LdCtx& c) {
  if constexpr (LO < HI) { ld_item<KIND, LO>(c); ld_items<KIND, LO + 1, HI>(c); }
}
template <int A, int E> __device__ __forceinline__ void raw_loads(const P2Ctx& pc, int vb, v4u (&raw)[8 * NRAWD]) {
  if constexpr (A < E) {
    v4u rs; int so;
    b2_src(pc, 4 + (A >> 3), rs, so);
    asm volatile("buffer_load_dwordx4 %0, %1, %2, %3 offen" : "=v"(raw[A]) : "v"((A & 1) ? vb ^ 16 : vb), "s"(rs), "s"(so + (A & 7) * 8192) : "memory");
    raw_loads<A + 1, E>(pc, vb, raw);
  }
}
template <int KIND, int B> __device__ __forceinline__ void loader_steps(const LdCtx& c, LAS unsigned char* L, const P2Ctx& pc, v4u (&raw)[8 * NRAWD]) {
  if constexpr (B == 0) {
    constexpr int FIRST = KIND ? 2 : 3;
    ld_items<KIND, 0, FIRST>(c);
    KP kp = kargs();
    const v4u r0 = mk_rs(kp->Lre, P_ * 2), r1 = mk_rs(kp->Lim, P_ * 2), r2 = mk_rs(kp->ldt, P_ * 2), r3 = mk_rs(kp->ws + WS_EPOCH, c.lw == 3 ? 4 : 0);
    asm volatile("buffer_load_ushort %0, %4, %5, 0 offen\n\tbuffer_load_ushort %1, %4, %6, 0 offen\n\tbuffer_load_ushort %2, %4, %7, 0 offen\n\t"
                 "buffer_load_dword %3, off, %8, 0 sc1"
                 : "=&v"(c.lrb), "=&v"(c.lib), "=&v"(c.ldb), "=&v"(c.ep) : "v"(c.ch * 2), "s"(r0), "s"(r1), "s"(r2), "s"(r3) : "memory");
    if constexpr (KIND == 0) {
      ld_items<0, 3, 5>(c);
      wait_vm<7>();
      BARRIER();
      ld_items<0, 5, SCH[0].hi[0]>(c);
    } else {
      ld_items<KIND, FIRST, SCH[KIND].hi[0]>(c);
    }
  } else {
    ld_items<KIND, SCH[KIND].lo[B], SCH[KIND].hi[B]>(c);
  }
  if constexpr (KIND == 0) raw_loads<raw_lo(B), raw_lo(B + 1)>(pc, c.vb, raw);
  if constexpr (KIND == 0 && B == WARM0) warm(mk_rs((const void*)c.Wd, F_ * D_ * 2), c.lane < 16 ? (c.vwarm * 16 + c.lane) * 128 : OOB, 0);
  if constexpr (KIND == 0 && B == WARM0 + 1) warm(mk_rs((const void*)c.Cre, D_ * P_ * 2), c.lane < 5 ? (c.vwarm * 5 + c.lane) * 128 : OOB, 0);
  if constexpr (KIND == 0 && B == WARM0 + 2) warm(mk_rs((const void*)c.Cim, D_ * P_ * 2), c.lane < 5 ? (c.vwarm * 5 + c.lane) * 128 : OOB, 0);
  if constexpr (B == CONST_AT) {
    asm volatile("" : "+v"(c.lrb), "+v"(c.lib), "+v"(c.ldb), "+v"(c.ep) : : "memory");
    const float lr = bf2f(c.lrb), li = bf2f(c.lib);
    float dt = __builtin_amdgcn_exp2f(bf2f(c.ldb) * 1.44269504088896341f);
    dt = bf2f(pk_bf16(dt, 0.f) & 0xFFFFu);
    const float rev = __builtin_amdgcn_fractf(dt * li * 0.15915494309189535f);
    const float ex = __builtin_amdgcn_exp2f(dt * lr * 1.44269504088896341f);
    const float ar = ex * __builtin_amdgcn_cosf(rev), ai = ex * __builtin_amdgcn_sinf(rev);
    const float den = __builtin_amdgcn_rcpf(lr * lr + li * li);
    float p16r = ar, p16i = ai;
    csq(p16r, p16i, 4);
    float p32r = p16r, p32i = p16i;
    csq(p32r, p32i, 1);
    LAS f32x4* d = (LAS f32x4*)(L + LDS_CONST + c.lw * 512 + (c.lane & 15) * 32);
    d[0] = f32x4{ar, ai, ((ar - 1.f) * lr + ai * li) * den, (ai * lr - (ar - 1.f) * li) * den};
    d[1] = f32x4{p16r, p16i, p32r, p32i};
    *(volatile LAS unsigned*)(L + LDS_TAG + 4 * (3 - c.lw)) = c.ep + 1u;
    WAIT_LDS();
  }
  SCHED();
  wait_vm<SCH[KIND].wait[B]>();
  BARRIER();
  if constexpr (B < LASTB) loader_steps<KIND, B + 1>(c, L, pc, raw);
}

__device__ __forceinline__ bool all4(LAS unsigned char* p) {
  const v4u d = *(volatile LAS v4u*)p;
  return __builtin_amdgcn_readfirstlane(d[0] & d[1] & d[2] & d[3]) != 0u;
}
__device__ __forceinline__ void lds_wait4(LAS unsigned char* p) {
#pragma nounroll
  for (unsigned spins = 0; spins < (SPIN_CAP << 4); ++spins) {
    if (all4(p)) break;
    __builtin_amdgcn_s_sleep(2);
  }
  CFENCE();
}
__device__ __forceinline__ void lds_wait(LAS unsigned char* word) {
#pragma nounroll
  for (unsigned spins = 0; spins < (SPIN_CAP << 4); ++spins) {
    if (lds_word(word) != 0u) break;
    __builtin_amdgcn_s_sleep(2);
  }
  CFENCE();
}
__device__ __forceinline__ void lds_wait_eq(LAS unsigned char* word, unsigned want) {
#pragma nounroll
  for (unsigned spins = 0; spins < (SPIN_CAP << 4); ++spins) {
    if (lds_word(word) == want) break;
    __builtin_amdgcn_s_sleep(2);
  }
  CFENCE();
}
__device__ __forceinline__ void cmul(float ar, float ai, float br, float bi, float& cr, float& ci) {
  cr = ar * br - ai * bi; ci = ar * bi + ai * br;
}

template <int V> struct IC { static constexpr int value = V; };
template <int n> __device__ __forceinline__ void p2_first(v4u (&af)[8], v4u ra, int va, int o0, int o1) {
  asm volatile("buffer_load_dwordx4 %0, %1, %2, %3 offen offset:%c4 sc1" : "=v"(af[n]) : "v"(va), "s"(ra), "s"(n < 4 ? o0 : o1), "i"((n & 1) * 2048 + (n & 2) * 256) : "memory");
  if constexpr (n < 7) p2_first<n + 1>(af, ra, va, o0, o1);
}
template <int d, int n>
__device__ __forceinline__ void p2_step(f32x16 (&acc)[2][2], const v4u (&bb)[2][4], v4u (&af)[8], v4u ra, int va, int o0, int o1, int w) {
  constexpr int WG = d == 6 ? 7 - n : 7, WS = d == 5 ? 7 - n : 7;
  if constexpr (d >= 3 && d <= 5 && WS < WG)
    asm volatile("s_waitcnt vmcnt(%c2)\n\ts_cmp_gt_u32 %1, 3\n\ts_cbranch_scc1 1f\n\ts_waitcnt vmcnt(%c3)\n1:" : "+v"(af[n]) : "s"(w), "i"(WG), "i"(WS) : "memory", "scc");
  else
    asm volatile("s_waitcnt vmcnt(%c1)" : "+v"(af[n]) : "i"(WG) : "memory");
  mfma32t(acc[0][n & 1], bb[n >> 2][n & 2], af[n]);
  mfma32t(acc[1][n & 1], bb[n >> 2][(n & 2) + 1], af[n]);
  if constexpr (d < 5)
    asm volatile("buffer_load_dwordx4 %0, %1, %2, %3 offen offset:%c4 sc1" : "+v"(af[n]) : "v"(va), "s"(ra), "s"(n < 4 ? o0 : o1), "i"((n & 1) * 2048 + (n & 2) * 256) : "memory");
  if constexpr (d == 5)
    asm volatile("s_cmp_lt_u32 %5, 4\n\ts_cbranch_scc1 1f\n\tbuffer_load_dwordx4 %0, %1, %2, %3 offen offset:%c4 sc1\n1:"
                 : "+v"(af[n]) : "v"(va), "s"(ra), "s"(n < 4 ? o0 : o1), "i"((n & 1) * 2048 + (n & 2) * 256), "s"(w) : "memory", "scc");
  if constexpr (n < 7) p2_step<d, n + 1>(acc, bb, af, ra, va, o0, o1, w);
}

__global__ __launch_bounds__(512, 2) void k_fused(Params) {
  extern __shared__ __attribute__((aligned(16))) unsigned char lds_[];
  LAS unsigned char* L = (LAS unsigned char*)lds_;
  STAMP(0);
  KP kp = kargs();
  u64 pX = (u64)kp->x, pWg = (u64)kp->Wg, pWu = (u64)kp->Wu, pBre = (u64)kp->Bre, pBim = (u64)kp->Bim, pCre = (u64)kp->Cre, pCim = (u64)kp->Cim, pWd = (u64)kp->Wd;
  const int b = __builtin_amdgcn_workgroup_id_x(), tid = __builtin_amdgcn_workitem_id_x(), lane = tid & 63;
  const int w = __builtin_amdgcn_readfirstlane(tid >> 6);
  const int xcd = b & 7, r = b >> 3;
  warm(mk_rs((const void*)(__builtin_amdgcn_s_getpc() & ~(u64)127), CODE_BYTES), lane < 2 ? lane * 32768 : OOB, (r * 8 + w) * 128);
  const bool light = (r & 7) == 7;
  const int ih = 28 * (xcd & 3) + r - (r >> 3), il = 112 + 4 * (xcd & 3) + (r >> 3);
  const int idx = ih + (il - ih) * (int)light;
  const int pn = 8 * (xcd >> 2) + (idx & 7), cb = idx >> 3;
  const int rot = pn & 6;
  int R = xcd * 4 + (r & 3), N = r >> 2;
  asm volatile("" : "+s"(pX), "+s"(pWg), "+s"(pWu), "+s"(pBre), "+s"(pBim), "+s"(pCre), "+s"(pCim), "+s"(pWd));
  const int vb = (lane >> 3) * 1024 + (((lane & 7) ^ (((lane >> 2) & 4) | ((lane >> 4) & 2))) << 4);
  v4u b2[4][2][4];
  unsigned tag = 0;
  P2Ctx pc;

  if (__builtin_expect(w >= 4, 1)) {
    LdCtx c;
    c.lw = w - 4; c.rot = rot; c.lane = lane; c.vb = vb; c.sv = 0;
    c.Wd = pWd; c.Cre = pCre; c.Cim = pCim;
    c.vwarm = ((r - (r >> 3)) * 8 + xcd) * 4 + c.lw;
    c.xcd = xcd; c.ch = 32 * cb + 16 * (c.lw >> 1) + (lane & 15);
    const int l3 = lane >> 3, kq = lane >> 4;
    c.rx = mk_rs((const void*)pX, S_ * D_ * 2);
    c.vx = (32 * (c.lw & 1) + 16 * (l3 >> 2) + 4 * (c.lw >> 1) + (l3 & 3)) * 1024 + (((lane & 7) ^ ((4 * c.lw + kq) & 7)) << 4) + pn * 128 * 1024;
    c.rb = mk_rs((const void*)((c.lw >> 1) ? pBim : pBre), P_ * D_ * 2);
    c.vbb = (8 * (c.lw & 1) + l3) * 1024 + (((lane & 7) ^ (4 * (c.lw & 1) + kq)) << 4) + cb * 32 * 1024;
    c.lb = (c.lw >> 1) * 4096 + (c.lw & 1) * 1024;
    c.rw = mk_rs((const void*)((c.lw >> 1) ? pWu : pWg), D_ * F_ * 2);
    c.vw0 = ((c.lw & 1) * 16 + kq) * (F_ * 2) + (((lane & 15) ^ (kq << 2)) << 4) + cb * 256;
    c.vw1 = c.vw0 ^ 32;
    STAMP(1);
    pc = p2_ctx(pWd, pCre, pCim, w, N);
    v4u raw[8 * NRAWD];
    if (__builtin_expect(!light, 1)) loader_steps<0, 0>(c, L, pc, raw);
    else { loader_steps<1, 0>(c, L, pc, raw); BARRIER(); raw_loads<0, 8 * NRAWD>(pc, vb, raw); }
    STAMP(2);
    {
      const int wA = c.lw < 2 ? w : c.lw;
      b2_load<0, 0, 8>(pc, vb, 0, wA * 16384);
      b2_load<8192, 0, 8>(pc, vb, 1, wA * 16384);
      const P2Ctx pp = p2_ctx(pWd, pCre, pCim, c.lw, N);
#pragma unroll
      for (int d = 0; d < 4; ++d) {
        wait_vm<8>();
        b2_read(false, L + wA * 16384 + (d & 1) * 8192, lane, b2[d]);
        WAIT_LDS();
        if (d == 2) lds_wait4(L + LDS_MD);
        if (d < 2) b2_load<0, 0, 8>(pc, vb, d + 2, wA * 16384 + (d & 1) * 8192);
        else b2_load<0, 0, 8>(pp, vb, d & 1, c.lw * 16384 + (d & 1) * 8192);
      }
      STAMP(3);
#pragma unroll
      for (int z = 0; z < 16; z += 8)
        asm volatile("" : "+v"(raw[z]), "+v"(raw[z + 1]), "+v"(raw[z + 2]), "+v"(raw[z + 3]), "+v"(raw[z + 4]), "+v"(raw[z + 5]), "+v"(raw[z + 6]), "+v"(raw[z + 7]));
#pragma unroll
      for (int n = 0; n < 16; ++n) *(LAS v4u*)(L + w * 16384 + n * 1024 + lane * 16) = raw[n];
      wait_vm<0>();
      *(volatile LAS unsigned*)(L + LDS_PF + 4 * c.lw) = 1u;
      STAMP(4);
      lds_wait(L + LDS_CO + 4 * c.lw);
      b2_load<0, 0, 8>(pp, vb, 2, c.lw * 16384);
      b2_load<8192, 0, 8>(pp, vb, 3, c.lw * 16384);
      wait_vm<0>();
      *(volatile LAS unsigned*)(L + LDS_PF + 4 * c.lw) = 2u;
      STAMP(5);
    }
    tag = lds_word(L + LDS_TAG);
  } else {
    const int c = w, q = lane >> 4, i15 = lane & 15, G = c & 1;
    const int ch = 32 * cb + 16 * (c >> 1) + i15;
    if (w == 3) {
#pragma unroll
      for (int z = 0; z < 5; ++z) *(volatile LAS v4u*)(L + LDS_DONE + 16 * z) = v4u{0u, 0u, 0u, 0u};
      *(volatile LAS unsigned*)(L + LDS_RDY) = 0u;
      *(volatile LAS v4u*)(L + LDS_RDY + 16) = v4u{0u, 0u, 0u, 0u};
      WAIT_LDS();
    }
    f32x4 k0 = {0.f, 0.f, 0.f, 0.f}, k1 = {0.f, 0.f, 0.f, 0.f};
    unsigned tagv = 0;
    auto rd_const = [&]() {
      k0 = *(LAS f32x4*)(L + LDS_CONST + c * 512 + i15 * 32); k1 = *(LAS f32x4*)(L + LDS_CONST + c * 512 + i15 * 32 + 16);
      tagv = *(volatile LAS unsigned*)(L + LDS_TAG);
    };
    u64 pws = (u64)kp->ws;
    asm volatile("" : "+s"(pws));
    int xa[2];
#pragma unroll
    for (int ks = 0; ks < 2; ++ks) xa[ks] = i15 * 128 + (((4 * ks + q) ^ ((i15 >> 1) & 7)) << 4);
    asm volatile("" : "+v"(xa[0]), "+v"(xa[1]));
    const int bo = B_BASE + (c >> 1) * 2048;
    f32x4 accs[2][4], acc[4][8];
    v4u bfr[2];
    auto zero_accs = [&]() {
#pragma unroll
      for (int m = 0; m < 2; ++m)
#pragma unroll
        for (int t = 0; t < 4; ++t) { accs[m][t] = f32x4{0.f, 0.f, 0.f, 0.f}; asm volatile("" : "+v"(accs[m][t])); }
    };
    float vr[16], vi[16], t0 = 0.f, t1 = 0.f;
    auto scan_half = [&](int n) {
      const int z = n >> 1;
      if (z == 0) { vr[0] = accs[0][0][0]; vi[0] = accs[1][0][0]; }
      else if ((n & 1) == 0) { t0 = __builtin_fmaf(k0[0], vr[z - 1], accs[0][z >> 2][z & 3]); t1 = __builtin_fmaf(k0[0], vi[z - 1], accs[1][z >> 2][z & 3]); }
      else { vr[z] = __builtin_fmaf(-k0[1], vi[z - 1], t0); vi[z] = __builtin_fmaf(k0[1], vr[z - 1], t1); }
    };
    auto sc = [](int v) { asm volatile("" : "+s"(v)); return v; };

    if (!light) {
      int wa[2];
      {
        const int qq = i15 >> 2, pp = lane & 3;
#pragma unroll
        for (int u = 0; u < 2; ++u)
          wa[u] = W_BASE + (8 * q + qq) * 256 + (((2 * (2 * c + u) + (pp >> 1)) ^ ((qq << 2) | ((q & 1) << 1))) << 4) + 8 * (pp & 1);
      }
      asm volatile("" : "+v"(wa[0]), "+v"(wa[1]));
#pragma unroll
      for (int a = 0; a < 4; ++a)
#pragma unroll
        for (int u = 0; u < 8; ++u) { acc[a][u] = f32x4{0.f, 0.f, 0.f, 0.f}; asm volatile("" : "+v"(acc[a][u])); }
      zero_accs();
      v4u xf[4];
      v2u wf[2][8];
      auto rdw = [&](int s, int z) {
        const int m = z >> 2, u = (z >> 1) & 1, t = z & 1;
        wf[s & 1][z] = lds_rdtr(L + wa[u] + (s & 3) * W_SLOT + m * 8192 + t * 1024);
      };
      STAMP(1);
      BARRIER();
      STAMP(2);
#pragma unroll
      for (int z = 0; z < 8; ++z) rdw(0, z);
#pragma unroll
      for (int z = 0; z < 3; ++z) xf[z] = lds_rd128(L + xa[0] + G * 8192 + z * 2048);
#pragma unroll
      for (int m = 0; m < 2; ++m) bfr[m] = lds_rd128(L + xa[0] + bo + m * 4096);
      auto glu4 = [&](int s, int u, const v4u& x, auto&& between) {
#pragma unroll
        for (int a = 0; a < 4; ++a) {
          const v4u wv = {wf[s & 1][2 * a][0], wf[s & 1][2 * a][1], wf[s & 1][2 * a + 1][0], wf[s & 1][2 * a + 1][1]};
          mfma16t(acc[a][u], wv, x);
          SCHED();
          between(a);
          SCHED();
        }
      };
      int xb = 0, bb = 0;
      auto steps = [&](int s0, int n, const int (&xo)[5]) {
#pragma unroll
        for (int j = 0; j < n; ++j)
#pragma unroll
          for (int u = 0; u < 8; ++u) {
            const int s = s0 + j, jn = j + ((u + 3) >> 3), un = (u + 3) & 7, uw = s == 0 ? u - 4 : u;
            SCHED();
            if (u == (s == 0 ? 4 : 0)) { BARRIER(); SCHED(); }
            glu4(s, u, xf[u & 3], [&](int a) {
              if (a == 0) {
                if ((un & 3) == 0 || (j == 0 && u == 0)) xb = xa[(s0 + jn) & 1] + sc(xo[jn] + ((un >> 2) ^ G) * 8192);
                xf[(u + 3) & 3] = lds_rd128(L + xb + (un & 3) * 2048);
              }
              if ((a == 1 || a == 2) && uw >= 0 && uw < 4) rdw(s + 1, 2 * uw + a - 1);
              if (a == 3 && (u == 4 || u == 5)) {
                if (u == 4) bb = xa[(s + 1) & 1] + sc(bo + (xo[j + 1] >> 1));
                bfr[u - 4] = lds_rd128(L + bb + (u - 4) * 4096);
              }
            });
            if (u < 4) { mfma16t(accs[0][u], xf[u & 3], bfr[0]); mfma16t(accs[1][u], xf[u & 3], bfr[1]); }
          }
      };
      {
        const int xo[5] = {0, 0, 0, 0, 0};
        steps(0, 1, xo);
      }
      int xo0 = 0;
#pragma nounroll
      for (int o = 0; o < 3; ++o) {
        const int xo1 = xo0 == 2 * X_SLOT ? 0 : xo0 + X_SLOT, xo2 = xo1 == 2 * X_SLOT ? 0 : xo1 + X_SLOT;
        const int xo[5] = {xo0, xo1, xo1, xo2, xo2};
        steps(1, 4, xo);
        xo0 = xo2;
      }
      {
        const int xo[5] = {0, X_SLOT, 0, 0, 0};
        steps(1, 1, xo);
      }
      v4u bf2[2];
#pragma unroll
      for (int it = 0; it < 16; ++it) {
        const int s = (it >> 2) & 1, u = (it & 3) + 4 * (it >> 3), nx = it + 3;
        SCHED();
        glu4(s, u, xf[it & 3], [&](int a) {
          if (a == 0 && nx < 16) {
            if ((nx & 3) == 0) xb = xa[(nx >> 2) & 1] + sc(X_SLOT + ((nx >> 3) ^ G) * 8192);
            xf[nx & 3] = lds_rd128(L + xb + (nx & 3) * 2048);
          }
          if (a >= 1 && it < 3 && 3 * it + a - 1 < 8) rdw(3, 3 * it + a - 1);
          if (it == 0 && a == 3) { bb = xa[1] + sc(bo + B_SLOT); bf2[0] = lds_rd128(L + bb); bf2[1] = lds_rd128(L + bb + 4096); }
          if (it == 4 && a == 1) rd_const();
          if (it >= 8) scan_half(4 * (it - 8) + a);
        });
        if (it < 4) { mfma16t(accs[0][u], xf[it & 3], bfr[0]); mfma16t(accs[1][u], xf[it & 3], bfr[1]); }
        else if (it < 8) { mfma16t(accs[0][u], xf[it & 3], bf2[0]); mfma16t(accs[1][u], xf[it & 3], bf2[1]); }
      }
      asm volatile("s_nop 15" : "+v"(acc[0][7]), "+v"(acc[1][7]), "+v"(acc[2][7]), "+v"(acc[3][7]), "+v"(acc[0][6]), "+v"(acc[1][6]), "+v"(acc[2][6]), "+v"(acc[3][6]));
    } else {
      {
      kp = kargs();
      rsrc_t rx = mk_rsrc(kp->x, S_ * D_ * 2), rg = mk_rsrc(kp->gw, D_ * 2), rgo = mk_rsrc(kp->ws + WS_G, S_ * 4);
      const int tk0 = 128 * pn + 64 * (cb - 14) + 16 * c;
      v4u gv[8], xv[2][8];
#pragma unroll
      for (int z = 0; z < 8; ++z) gv[z] = __builtin_amdgcn_raw_buffer_load_b128(rg, (lane & 7) * 16, z * 128, 0);
#pragma unroll
      for (int gi = 0; gi < 2; ++gi)
#pragma unroll
        for (int z = 0; z < 8; ++z) xv[gi][z] = __builtin_amdgcn_raw_buffer_load_b128(rx, (lane >> 3) * 1024 + (lane & 7) * 16, (tk0 + gi * 8) * 1024 + z * 128, 0);
#pragma unroll
      for (int gi = 0; gi < 2; ++gi) {
        float s = 0.f;
#pragma unroll
        for (int z = 0; z < 8; ++z)
#pragma unroll
          for (int y = 0; y < 4; ++y) {
            const unsigned xa_ = xv[gi][z][y], ga = gv[z][y];
            s = __builtin_amdgcn_fdot2_f32_bf16(__builtin_bit_cast(bf16x2, xa_), __builtin_bit_cast(bf16x2, ga), s, false);
          }
        s += dpp<0xB1>(s); s += dpp<0x4E>(s); s += dpp<0x141>(s);
        const float g = __builtin_amdgcn_rcpf(1.f + __builtin_amdgcn_exp2f(s * -1.44269504088896341f));
        __builtin_amdgcn_raw_buffer_store_b32(f2u(g), rgo, (lane & 7) ? OOB : (lane >> 3) * 4, (tk0 + gi * 8) * 4, AUX_SC1);
      }
      }
      STAMP(1);
      zero_accs();
      int xo = 0;
#pragma nounroll
      for (int T = 0; T < 8; ++T) {
#pragma unroll
        for (int ks = 0; ks < 2; ++ks) {
          if (T < 7) BARRIER();
          v4u xf[4];
#pragma unroll
          for (int t = 0; t < 4; ++t) xf[t] = lds_rd128(L + (xa[ks] + xo + G * 8192) + t * 2048);
#pragma unroll
          for (int m = 0; m < 2; ++m) bfr[m] = lds_rd128(L + (xa[ks] + bo + (xo >> 1)) + m * 4096);
#pragma unroll
          for (int t = 0; t < 4; ++t) { mfma16t(accs[0][t], xf[t], bfr[0]); mfma16t(accs[1][t], xf[t], bfr[1]); }
          SCHED();
        }
        xo = xo == 2 * X_SLOT ? 0 : xo + X_SLOT;
      }
      asm volatile("s_nop 15" : "+v"(accs[0][0]), "+v"(accs[1][0]), "+v"(accs[0][1]), "+v"(accs[1][1]), "+v"(accs[0][2]), "+v"(accs[1][2]), "+v"(accs[0][3]), "+v"(accs[1][3]));
      rd_const();
      BARRIER();
#pragma unroll
      for (int n = 0; n < 32; ++n) { scan_half(n); SCHED(); }
    }
    *(volatile LAS unsigned*)(L + LDS_MD + 4 * w) = 1u;
    SCHED();
    STAMP(3);
    tag = __builtin_amdgcn_readfirstlane(tagv);
    const float ar = k0[0], ai = k0[1], cr = k0[2], ci = k0[3], p16r = k1[0], p16i = k1[1], p32r = k1[2], p32i = k1[3];
    __builtin_amdgcn_s_setprio(2);
    rsrc_t ra2 = mk_rsrc((const void*)(pws + WS_A2), A2_BYTES), rcar = mk_rsrc((const void*)(pws + WS_CARRY), 32 * P_ * 16);
    const int ck = 2 * pn + G;
    float pr, pi;
    {
      float tr, ti;
      const auto xr = __builtin_amdgcn_permlane16_swap(f2u(vr[15]), f2u(vr[15]), false, false);
      const auto xi = __builtin_amdgcn_permlane16_swap(f2u(vi[15]), f2u(vi[15]), false, false);
      const float e0r = u2f(xr[0]), e1r = u2f(xr[1]), e0i = u2f(xi[0]), e1i = u2f(xi[1]);
      cmul(e0r, e0i, p16r, p16i, tr, ti); tr += e1r; ti += e1i;
      const auto yr = __builtin_amdgcn_permlane32_swap(f2u(tr), f2u(tr), false, false);
      const auto yi = __builtin_amdgcn_permlane32_swap(f2u(ti), f2u(ti), false, false);
      const float f0r = u2f(yr[0]), f1r = u2f(yr[1]), f0i = u2f(yi[0]), f1i = u2f(yi[1]);
      float Er, Ei;
      cmul(f0r, f0i, p32r, p32i, Er, Ei); Er += f1r; Ei += f1i;
      const float br = (q & 2) ? f0r : 0.f, bi = (q & 2) ? f0i : 0.f;
      cmul(br, bi, p16r, p16i, tr, ti);
      pr = (q & 1) ? tr + e0r : br; pi = (q & 1) ? ti + e0i : bi;
      const v4u gq = {f2u(Er), tag, f2u(Ei), ~tag};
      st_sc1(gq, rcar, q ? OOB : ch * 16, (31 - ck) * (P_ * 16));
    }
    asm volatile("" : "+v"(pr), "+v"(pi) : : "memory");
    SCHED();
    STAMP(4);
    v4u cv[8];
    const int cvo = ch * 16 + (32 - ck + 8 * q) * (P_ * 16);
    auto ld_carry = [&]() {
#pragma unroll
      for (int z = 0; z < 8; ++z) cv[z] = ld_sc1(rcar, cvo, z * (P_ * 16));
    };
    auto publish = [&](int part) {
      *(volatile LAS unsigned*)(L + LDS_DONE + 16 * part + 4 * w) = 1u;
      const bool last = all4(L + LDS_DONE + 16 * part);
      st_flag((unsigned char*)pws, WS_FA2 + part * 32768 + pn * 128 + cb * 8, tag, last && lane < 16 ? lane * 2048 : OOB);
      CFENCE();
    };
    if (!light) {
#pragma unroll
      for (int u = 0; u < 8; ++u) {
        float hv[8];
#pragma unroll
        for (int j = 0; j < 8; ++j) hv[j] = acc[j >> 2][u][j & 3] * -1.44269504088896341f;
        SCHED();
#pragma unroll
        for (int j = 0; j < 8; ++j) hv[j] = __builtin_amdgcn_exp2f(hv[j]);
        SCHED();
#pragma unroll
        for (int j = 0; j < 8; ++j) hv[j] += 1.f;
        SCHED();
#pragma unroll
        for (int j = 0; j < 8; ++j) hv[j] = __builtin_amdgcn_rcpf(hv[j]);
        SCHED();
#pragma unroll
        for (int j = 0; j < 8; ++j) hv[j] *= acc[2 + (j >> 2)][u][j & 3];
        SCHED();
        st_sc1(v4u{pk_bf16(hv[0], hv[1]), pk_bf16(hv[2], hv[3]), pk_bf16(hv[4], hv[5]), pk_bf16(hv[6], hv[7])}, ra2, lane * 16,
               ((2 * pn + ((u >> 2) ^ G)) * NK + 4 * cb + c) * 4096 + (u & 3) * 1024);
        if (u + 1 < 8) asm volatile("" : "+v"(acc[0][u + 1]), "+v"(acc[1][u + 1]) : : "memory");
      }
    }
    CFENCE();
    SCHED();
    STAMP(5);
    ld_carry();
    CFENCE();
    SCHED();
#pragma unroll
    for (int z = 0; z < 16; ++z) {
      const float t = cr * vr[z] - ci * vi[z];
      vi[z] = -cr * vi[z] - ci * vr[z]; vr[z] = t;
    }
    float qr[4], qi[4];
    qr[0] = ar; qi[0] = ai; qr[1] = ar; qi[1] = ai; csq(qr[1], qi[1], 1);
    cmul(qr[1], qi[1], ar, ai, qr[2], qi[2]); qr[3] = qr[1]; qi[3] = qi[1]; csq(qr[3], qi[3], 1);
    const float p4r = qr[3], p4i = qi[3];
    float p64r = p32r, p64i = p32i; csq(p64r, p64i, 1);
    float p512r = p64r, p512i = p64i; csq(p512r, p512i, 3);
    float p1kr = p512r, p1ki = p512i; csq(p1kr, p1ki, 1);
    float sgr, sgi, wgr, wgi;
    cmul((q & 1) ? p16r : 1.f, (q & 1) ? p16i : 0.f, (q & 2) ? p32r : 1.f, (q & 2) ? p32i : 0.f, sgr, sgi);
    cmul((q & 1) ? p512r : 1.f, (q & 1) ? p512i : 0.f, (q & 2) ? p1kr : 1.f, (q & 2) ? p1ki : 0.f, wgr, wgi);
#pragma unroll
    for (int z = 0; z < 16; ++z) asm volatile("" : "+v"(vr[z]), "+v"(vi[z]));
    asm volatile("" : "+v"(sgr), "+v"(sgi), "+v"(wgr), "+v"(wgi), "+v"(p64r), "+v"(p64i));
    SCHED();
    const int want = imin(imax(ck - 8 * q, 0), 8);
    auto carry_ok = [&]() {
      int good = 0;
#pragma unroll
      for (int z = 0; z < 8; ++z) good += ((cv[z][1] ^ tag) | (cv[z][3] ^ ~tag)) == 0u;
      return wall(good == want);
    };
    {
      lds_wait(L + LDS_PF + 4 * w);
      b2_read(true, L + w * 16384, lane, b2[2]);
      b2_read(true, L + w * 16384 + 8192, lane, b2[3]);
      CFENCE();
      *(volatile LAS unsigned*)(L + LDS_CO + 4 * w) = 1u;
      CFENCE();
    }
    wait_vm<8>();
    publish(0);
    STAMP(14);
    if (__builtin_expect(!carry_ok(), 0)) {
#pragma nounroll
      for (unsigned spins = 0; spins < SPIN_CAP; ++spins) {
        __builtin_amdgcn_s_sleep(2);
        CFENCE();
        ld_carry();
        if (carry_ok()) break;
      }
    }
    STAMP(6);
    float Gr, Gi;
    {
      float Hr = 0.f, Hi = 0.f, tr, ti;
#pragma unroll
      for (int z = 7; z >= 0; --z) { cmul(Hr, Hi, p64r, p64i, tr, ti); Hr = tr + u2f(cv[z][0]); Hi = ti + u2f(cv[z][2]); }
      cmul(Hr, Hi, wgr, wgi, tr, ti);
      { const auto a = __builtin_amdgcn_permlane16_swap(f2u(tr), f2u(tr), false, false);
        const auto c2 = __builtin_amdgcn_permlane16_swap(f2u(ti), f2u(ti), false, false);
        tr = u2f(a[0]) + u2f(a[1]); ti = u2f(c2[0]) + u2f(c2[1]); }
      { const auto a = __builtin_amdgcn_permlane32_swap(f2u(tr), f2u(tr), false, false);
        const auto c2 = __builtin_amdgcn_permlane32_swap(f2u(ti), f2u(ti), false, false);
        tr = u2f(a[0]) + u2f(a[1]); ti = u2f(c2[0]) + u2f(c2[1]); }
      cmul(tr, ti, sgr, sgi, Hr, Hi);
      cmul(Hr + pr, Hi + pi, cr, ci, Gr, Gi);
    }
    {
      LAS unsigned char* T = L + B_BASE + (c >> 1) * (2 * B_SLOT) + (c & 1) * 4096;
      const int s32 = ((i15 >> 1) & 1) | ((i15 >> 2) & 2), s8 = (i15 & 1) | ((i15 >> 1) & 2);
#pragma unroll
      for (int t = 0; t < 4; ++t) {
        float orr[4], oi[4];
#pragma unroll
        for (int z = 0; z < 4; ++z) {
          orr[z] = __builtin_fmaf(-qi[z], Gi, __builtin_fmaf(qr[z], Gr, vr[4 * t + z]));
          oi[z] = __builtin_fmaf(-qi[z], Gr, __builtin_fmaf(-qr[z], Gi, vi[4 * t + z]));
        }
        if (t < 3) { float tr, ti; cmul(Gr, Gi, p4r, p4i, tr, ti); Gr = tr; Gi = ti; }
        LAS unsigned char* a = T + i15 * 128 + ((t ^ s32) << 5) + ((q ^ s8) << 3);
        *(LAS v2u*)a = v2u{pk_bf16(orr[0], orr[1]), pk_bf16(orr[2], orr[3])};
        *(LAS v2u*)(a + 2048) = v2u{pk_bf16(oi[0], oi[1]), pk_bf16(oi[2], oi[3])};
      }
      const int qq = i15 >> 2, pp = lane & 3;
      const int so = (ck * NK + 56 + 4 * (cb >> 1) + (cb & 1)) * 4096 + (c >> 1) * 512;
#pragma unroll
      for (int t = 0; t < 4; ++t) {
        v2u part[2];
#pragma unroll
        for (int hh = 0; hh < 2; ++hh)
          part[hh] = lds_rdtr(T + (8 * q + 4 * hh + qq) * 128 + ((t ^ ((qq >> 1) | ((q & 1) << 1))) << 5) + ((pp ^ ((qq & 1) | (hh << 1))) << 3));
        st_sc1(v4u{part[0][0], part[0][1], part[1][0], part[1][1]}, ra2, (lane >> 5) * 8192 + (lane & 31) * 16, so + t * 1024);
      }
    }
    SCHED();
    STAMP(7);
    wait_vm<0>();
    publish(1);
    STAMP(12);
  }
  kp = kargs();
  asm volatile("" : "+s"(R), "+s"(N));
  pc = p2_ctx(pWd, pCre, pCim, w, N);
  const int d0 = 2 * pc.sh;
  if (w < 4) {
    lds_wait_eq(L + LDS_PF + 4 * w, 2u);
    b2_read(true, L + w * 16384, lane, b2[0]);
    b2_read(true, L + w * 16384 + 8192, lane, b2[1]);
    WAIT_LDS();
    b2_load<0, 0, 8>(p2_ctx(pWd, pCre, pCim, w + 4, N), vb, 6, w * 16384);
    wait_vm<0>();
    *(volatile LAS unsigned*)(L + LDS_CO + 4 * w) = 2u;
    STAMP(13);
  }

  {
    lds_wait4(L + LDS_DONE);
    const int cl = lane & 15, lo = pc.sh ? (pc.g0 - 28) & ~1 : pc.g0 >> 1, hi = pc.sh ? (pc.g0 + pc.nd - 29) | 1 : (pc.g0 + pc.nd - 1) >> 1;
    GAS u64* fl = (GAS u64*)(kp->ws + WS_FA2) + ((pc.sh * 16 + (R & 1) * 8 + N) * 16 + (R >> 1)) * 16 + cl;
    if (w >= 4) {
      const unsigned need = ((2u << hi) - (1u << lo)) | 0xC000u;
#pragma nounroll
      for (unsigned spins = 0; spins < SPIN_CAP; ++spins) {
        CFENCE();
        const v4u st = *(volatile LAS v4u*)(L + LDS_RDY + 16);
        unsigned up = __builtin_amdgcn_readfirstlane(st[3]);
        if ((up & need) == need) break;
        const unsigned gone = __builtin_amdgcn_readfirstlane(st[0] | st[1] | st[2]), lower = (1u << (w - 4)) - 1u;
        if ((gone & lower) == lower) {
          const u64 v = __hip_atomic_load(fl, RLX_AGENT);
          up = (unsigned)__builtin_amdgcn_ballot_w64(v == tag64(tag)) & 0xFFFFu;
          CFENCE();
          *(volatile LAS unsigned*)(L + LDS_RDY + 28) = up;
          if ((up & need) == need) break;
          if (up & 0x3FFFu) __builtin_amdgcn_s_sleep(GPOLL); else __builtin_amdgcn_s_sleep(16);
        } else {
          __builtin_amdgcn_s_sleep(2);
        }
      }
      CFENCE();
      if (w < 7) *(volatile LAS unsigned*)(L + LDS_RDY + 16 + 4 * (w - 4)) = 1u << (w - 4);
    } else if (w == 3) {
      lds_wait4(L + LDS_DONE + 16);
      __builtin_amdgcn_s_sleep(SP_FIRST);
#pragma nounroll
      for (unsigned spins = 0; spins < SPIN_CAP; ++spins) {
        CFENCE();
        const u64 v = __hip_atomic_load(fl, RLX_AGENT);
        const unsigned up = (unsigned)__builtin_amdgcn_ballot_w64(v == tag64(tag)) & 0xFFFFu;
        CFENCE();
        *(volatile LAS unsigned*)(L + LDS_RDY) = up;
        if (up == 0xFFFFu) break;
        if (up & 0x3FFFu) __builtin_amdgcn_s_sleep(SP_NEXT); else __builtin_amdgcn_s_sleep(32);
      }
      CFENCE();
    } else {
      const unsigned need = (0xFu << lo) | 0xC000u;
#pragma nounroll
      for (unsigned spins = 0; spins < (SPIN_CAP << 4); ++spins) {
        if ((lds_word(L + LDS_RDY) & need) == need) break;
        __builtin_amdgcn_s_sleep(2);
      }
      CFENCE();
    }
  }
  STAMP(8);
  if (w >= 4) __builtin_amdgcn_s_setprio(1);
  glds<B_BASE + B_SLOT, 0>(mk_rs((const void*)pX, S_ * D_ * 2), w * 1024, ((lane >> 5) * 16 + ((lane >> 3) & 3)) * 1024 + (lane & 7) * 16,
                             (64 * R + 32 * (w & 1) + 4 * (w >> 1)) * 1024 + N * 128);
  const int tok = 64 * R + 32 * (w & 1) + 4 * (w >> 1) + (lane >> 4);
  const v2u dv = __builtin_amdgcn_raw_buffer_load_b64(mk_rsrc(kp->Dsk, D_ * 2), (lane & 15) * 8, N * 128, 0);
  const float ge = u2f(__builtin_amdgcn_raw_buffer_load_b32(mk_rsrc(kp->ws + WS_G, S_ * 4), tok * 4 + (lane & 1) * 64, 0, AUX_SC1));
  SCHED();
  const v4u ra = mk_rs(kp->ws + WS_A2 + (R * NK + 2 * pc.g0) * 4096, pc.nd * 8192);
  const int va = ((lane >> 4) & 1) * 1024 + (lane >> 5) * 256 + (lane & 15) * 16;
  v4u af[8];
  {
    const int o = p2_ds(pc, 0) * 8192;
    p2_first<0>(af, ra, va, o, o + 4096);
  }
  SCHED();
#define CPRIO 0
  if (w < 4) __builtin_amdgcn_s_setprio(CPRIO);
  f32x16 acc[2][2];
#pragma unroll
  for (int e = 0; e < 2; ++e)
#pragma unroll
    for (int tt = 0; tt < 2; ++tt) { acc[e][tt] = f32x16{}; asm volatile("" : "+v"(acc[e][tt])); }
  asm volatile("s_nop 1");
  auto step = [&](auto Dc) {
    constexpr int d = decltype(Dc)::value;
    const int o = p2_ds(pc, d + 1 - d0) * 8192;
    p2_step<d, 0>(acc, b2[d & 3], af, ra, va, o, o + 4096, w);
    if (d < 2) b2_read(false, L + w * 16384 + d * 8192, lane, b2[d]);
    if (d == PK6 && w >= 4) {
      lds_wait_eq(L + LDS_CO + 4 * (w - 4), 2u);
      b2_read(false, L + (w - 4) * 16384, lane, b2[2]);
      CFENCE();
      *(volatile LAS unsigned*)(L + LDS_PF + 4 * (w - 4)) = 3u;
      CFENCE();
    }
    SCHED();
  };
  if (w >= 4) { step(IC<0>{}); step(IC<1>{}); }
  step(IC<2>{}); step(IC<3>{}); step(IC<4>{}); step(IC<5>{});
  if (w >= 4) step(IC<6>{});
  asm volatile("s_nop 15\n\ts_nop 7" : "+v"(acc[0][0]), "+v"(acc[1][0]), "+v"(acc[0][1]), "+v"(acc[1][1]));
  SCHED();
  STAMP(9);
  kp = kargs();
  if (w < 4) lds_wait_eq(L + LDS_PF + 4 * w, 3u);
  {
    const int t3 = lane & 3, g3 = ((t3 & 1) * 3) ^ (t3 >> 1), hi = lane >> 5;
#pragma unroll
    for (int T = 0; T < 2; ++T)
#pragma unroll
      for (int E = 0; E < 2; ++E)
#pragma unroll
        for (int g = 0; g < 4; ++g) {
          const int e = 2 * E + (g >> 1), q = 2 * (g & 1) + hi;
          *(LAS f32x4*)(L + w * 16384 + ((2 * T + ((lane >> 4) & 1)) * 4 + e) * 1024 + ((16 * q + (((((lane >> 2) & 3) ^ q) << 2) | (e ^ g3))) << 4)) =
              f32x4{acc[E][T][4 * g], acc[E][T][4 * g + 1], acc[E][T][4 * g + 2], acc[E][T][4 * g + 3]};
        }
  }
  if (b == 255 && w == 0) {
    rsrc_t rf = mk_rsrc(kp->ws + WS_FA2 + 32768, 256 * 8);
#pragma nounroll
    for (unsigned spins = 0; spins < SPIN_CAP; ++spins) {
      CFENCE();
      const v4u f0 = ld_sc1(rf, lane * 32, 0), f1 = ld_sc1(rf, lane * 32, 16);
      if (wall(((f0[0] ^ tag) | (f0[1] ^ ~tag) | (f0[2] ^ tag) | (f0[3] ^ ~tag) | (f1[0] ^ tag) | (f1[1] ^ ~tag) | (f1[2] ^ tag) | (f1[3] ^ ~tag)) == 0u)) break;
      __builtin_amdgcn_s_sleep(8);
    }
    st_flag(kp->ws, WS_EPOCH, tag, lane == 0 ? 0 : OOB);
  }
  WAIT_LDS();
  BARRIER();
  STAMP(10);
  {
    const int q = lane & 3, e = (lane >> 2) & 3, t3 = lane >> 4;
    const int base = ((w >> 1) * 4 + e) * 1024 + q * 256 + ((e ^ ((t3 & 1) * 3) ^ (t3 >> 1)) << 4);
    rsrc_t ro = mk_rsrc(kp->out, S_ * D_ * 4);
    const float go = dpp<0xB1>(ge);
    f32x4 res[2];
#pragma unroll
    for (int hf = 0; hf < 2; ++hf) {
      f32x4 sg = {0.f, 0.f, 0.f, 0.f}, ss = {0.f, 0.f, 0.f, 0.f};
      const int a = base + (((2 * (w & 1) + hf) ^ q) << 6);
#pragma unroll
      for (int ww = 0; ww < 8; ++ww) {
        const f32x4 v = *(LAS f32x4*)(L + ww * 16384 + a);
        if (ww < 4) ss += v; else sg += v;
      }
      const float g = (lane & 1) == hf ? ge : go;
      f32x4 m = ss * (2.f * g) + sg * (1.f - g);
      const v2u xv = *(LAS v2u*)(L + B_BASE + B_SLOT + w * 1024 + hf * 512 + lane * 8);
      m[0] += g * (bf2f(xv[0] & 0xFFFFu) * bf2f(dv[0] & 0xFFFFu)); m[1] += g * (bfhi(xv[0]) * bfhi(dv[0]));
      m[2] += g * (bf2f(xv[1] & 0xFFFFu) * bf2f(dv[1] & 0xFFFFu)); m[3] += g * (bfhi(xv[1]) * bfhi(dv[1]));
      res[hf] = m;
    }
    asm volatile("" : "+v"(res[0]), "+v"(res[1]));
#pragma unroll
    for (int hf = 0; hf < 2; ++hf)
      __builtin_amdgcn_raw_buffer_store_b128(__builtin_bit_cast(v4u, res[hf]), ro, tok * 2048 + (lane & 15) * 16 + hf * 32768 + N * 256, 0, OUT_AUX);
  }
  STAMP(11);
}

#if defined(__HIP_DEVICE_COMPILE__)
#pragma clang attribute pop
#endif

extern "C" void kernel_launch(void* const* d_in, const int* in_sizes, int n_in, void* d_out, int out_size, void* d_ws, size_t ws_size,
                              hipStream_t stream) {
  Params p;
  p.x = (const u16*)d_in[0]; p.Lre = (const u16*)d_in[1]; p.Lim = (const u16*)d_in[2]; p.Bre = (const u16*)d_in[3];
  p.Bim = (const u16*)d_in[4]; p.Cre = (const u16*)d_in[5]; p.Cim = (const u16*)d_in[6]; p.ldt = (const u16*)d_in[7];
  p.Dsk = (const u16*)d_in[8]; p.Wg = (const u16*)d_in[10]; p.Wu = (const u16*)d_in[11]; p.Wd = (const u16*)d_in[12];
  p.gw = (const u16*)d_in[13]; p.out = (float*)d_out; p.ws = (unsigned char*)d_ws;
  (void)hipFuncSetAttribute((const void*)k_fused, hipFuncAttributeMaxDynamicSharedMemorySize, LDS_BYTES);
  k_fused<<<256, 512, LDS_BYTES, stream>>>(p);
}
```

```cpp
#define GPOLL 4
#define SP_FIRST 2
#define SP_NEXT 6
#define WARMM
#define WARM0 2
#define PRIO2
#include <hip/hip_runtime.h>
#include <stdint.h>

typedef unsigned short u16;
typedef unsigned long long u64;
typedef unsigned v4u __attribute__((ext_vector_type(4)));
typedef unsigned v2u __attribute__((ext_vector_type(2)));
typedef short s16x4 __attribute__((ext_vector_type(4)));
typedef __bf16 bf16x8 __attribute__((ext_vector_type(8)));
typedef __bf16 bf16x2 __attribute__((ext_vector_type(2)));
typedef float f32x2 __attribute__((ext_vector_type(2)));
typedef float f32x4 __attribute__((ext_vector_type(4)));
#define LAS __attribute__((address_space(3)))
#define GAS __attribute__((address_space(1)))
#define RLX_AGENT __ATOMIC_RELAXED, __HIP_MEMORY_SCOPE_AGENT
typedef __amdgpu_buffer_rsrc_t rsrc_t;

constexpr int S_ = 2048, D_ = 512, P_ = 512, F_ = 1792;
constexpr int NK = 88;
constexpr int AUX_SC1 = 16;
#define OUT_AUX 2
constexpr int WS_EPOCH = 0;
constexpr int WS_G = 8192;
constexpr int WS_FA2 = 65536;
constexpr int CTRL_BYTES = 131072;
constexpr int WS_CARRY = 131072;
constexpr int WS_A2 = 524288;
constexpr int A2_BYTES = 32 * NK * 4096;
constexpr int WS_STAMP = 32 << 20;
constexpr int LDS_BYTES = 160 * 1024;
constexpr int X_SLOT = 16384, W_BASE = 65536, W_SLOT = 16384, B_BASE = 131072, B_SLOT = 8192;
constexpr int LDS_DONE = 155648, LDS_PF = LDS_DONE + 32, LDS_MD = LDS_DONE + 48, LDS_CO = LDS_DONE + 64, LDS_TAG = LDS_DONE + 80;
constexpr int LDS_RDY = LDS_DONE + 96;
constexpr int LDS_DUMMY = LDS_DONE + 256, LDS_CONST = LDS_DONE + 512;
constexpr int OOB = 1 << 28;
constexpr int CODE_BYTES = 31 * 1024;
constexpr unsigned SPIN_CAP = 1u << 18;

struct Params {
  const u16 *x, *Wg, *Wu, *Bre, *Bim, *Cre, *Cim, *Wd;
  unsigned char* ws;
  const u16 *Lre, *Lim, *ldt, *Dsk, *gw;
  float* out;
};

#if defined(__HIP_DEVICE_COMPILE__)
#pragma clang attribute push(__attribute__((target("no-packed-fp32-ops"))), apply_to = function)
#endif
__device__ __forceinline__ float u2f(unsigned v) { return __builtin_bit_cast(float, v); }
__device__ __forceinline__ unsigned f2u(float v) { return __builtin_bit_cast(unsigned, v); }
__device__ __forceinline__ bool wall(bool p) { return __builtin_amdgcn_ballot_w64(!p) == 0; }
__device__ __forceinline__ bool wany(bool p) { return __builtin_amdgcn_ballot_w64(p) != 0; }
__device__ __forceinline__ int imin(int a, int b) { return a < b ? a : b; }
__device__ __forceinline__ int imax(int a, int b) { return a > b ? a : b; }
template <int CTRL> __device__ __forceinline__ float dpp(float v) { return u2f(__builtin_amdgcn_mov_dpp(f2u(v), CTRL, 0xF, 0xF, true)); }
__device__ __forceinline__ rsrc_t mk_rsrc(const void* p, int bytes) {
  return __builtin_amdgcn_make_buffer_rsrc((void*)p, 0, bytes, 0x00020000);
}
__device__ __forceinline__ float bf2f(unsigned v) { return u2f(v << 16); }
__device__ __forceinline__ float bfhi(unsigned v) { return u2f(v & 0xFFFF0000u); }
__device__ __forceinline__ unsigned pk_bf16(float lo, float hi) {
  f32x2 t = {lo, hi};
  return __builtin_bit_cast(unsigned, __builtin_convertvector(t, bf16x2));
}
__device__ __forceinline__ void mfma16t(f32x4& c, v4u a, v4u b) {
  asm volatile("v_mfma_f32_16x16x32_bf16 %0, %1, %2, %0" : "+v"(c) : "v"(a), "v"(b));
}
__device__ __forceinline__ v4u lds_rd128(LAS unsigned char* p) { return *(LAS v4u*)p; }
__device__ __forceinline__ v2u lds_rdtr(LAS unsigned char* p) {
  return __builtin_bit_cast(v2u, __builtin_amdgcn_ds_read_tr16_b64_v4i16((LAS s16x4*)p));
}
__device__ __forceinline__ v4u mk_rs(const void* p, unsigned bytes) {
  const u64 a = (u64)p;
  unsigned lo = (unsigned)a, hi = (unsigned)(a >> 32) & 0xFFFFu;
  asm volatile("" : "+s"(lo), "+s"(hi));
  return v4u{lo, hi, bytes, 0x00020000u};
}
template <int LI, int SI> __device__ __forceinline__ void glds(v4u rs, int lds, int voff, int soff) {
  int t;
  asm volatile("s_add_u32 m0, %1, %5\n\ts_add_u32 %0, %4, %6\n\tbuffer_load_dwordx4 %2, %3, %0 offen lds"
               : "=&s"(t) : "s"(lds), "v"(voff), "s"(rs), "s"(soff), "i"(LI), "i"(SI) : "memory", "scc");
}
typedef const __attribute__((address_space(4))) Params* KP;
__device__ __forceinline__ KP kargs() {
  KP k = (KP)__builtin_amdgcn_kernarg_segment_ptr();
  asm volatile("" : "+s"(k));
  return k;
}
__device__ __forceinline__ void warm(v4u rs, int voff, int soff) {
  asm volatile("s_mov_b32 m0, %3\n\ts_nop 0\n\tbuffer_load_dword %0, %1, %2 offen lds" : : "v"(voff), "s"(rs), "s"(soff), "i"(LDS_DUMMY) : "memory");
}
__device__ __forceinline__ v4u ld_sc1(rsrc_t rs, int voff, int soff) { return __builtin_amdgcn_raw_buffer_load_b128(rs, voff, soff, AUX_SC1); }
__device__ __forceinline__ void st_sc1(v4u v, rsrc_t rs, int voff, int soff) { __builtin_amdgcn_raw_buffer_store_b128(v, rs, voff + soff, 0, AUX_SC1); }
__device__ __forceinline__ u64 tag64(unsigned tag) { return ((u64)(~tag) << 32) | tag; }
__device__ __forceinline__ void st_flag(unsigned char* ws, int off, unsigned tag, int voff) {
  __builtin_amdgcn_raw_buffer_store_b64(v2u{tag, ~tag}, mk_rsrc(ws, CTRL_BYTES), voff, off, AUX_SC1);
}
__device__ __forceinline__ unsigned lds_word(LAS unsigned char* p) { return __builtin_amdgcn_readfirstlane(*(volatile LAS unsigned*)p); }
#define BARRIER() asm volatile("s_barrier" ::: "memory")
#define SCHED() __builtin_amdgcn_sched_barrier(0)
#define CFENCE() asm volatile("" ::: "memory")
#define WAIT_LDS() asm volatile("s_waitcnt lgkmcnt(0)" ::: "memory")
template <int N> __device__ __forceinline__ void wait_vm() { asm volatile("s_waitcnt vmcnt(%c0)" ::"i"(N) : "memory"); }

#define STAMP(i) do {} while (0)
#define PICK 0
#define PK6 3
#define POLL_SLEEP 4
#define POLL_IDLE 16

__device__ __forceinline__ void poll_flags(GAS u64* f, u64 want, bool care, bool late) {
#pragma nounroll
  for (unsigned spins = 0; spins < SPIN_CAP; ++spins) {
    CFENCE();
    const u64 v = __hip_atomic_load(f, RLX_AGENT);
    if (wall(!care || v == want)) break;
    if (wany(late && v == want)) __builtin_amdgcn_s_sleep(POLL_SLEEP); else __builtin_amdgcn_s_sleep(POLL_IDLE);
  }
  CFENCE();
}
__device__ __forceinline__ void csq(float& r, float& i, int n) {
#pragma unroll
  for (int z = 0; z < n; ++z) { const float t = r * r - i * i; i = 2.f * r * i; r = t; }
}

#define NROT 102
#define CONST_AT 2
constexpr int LASTB = 13;
#define RAWN 2
#define NRAWD 2
constexpr int raw_lo(int b) { const int n = (b - (LASTB + 1)) * RAWN + 8 * NRAWD; return n < 0 ? 0 : n; }
struct Sched {
  int n;
  int type[40], idx[40];
  int lo[LASTB + 1], hi[LASTB + 1];
  int wait[LASTB + 1];
  bool ok;
};
constexpr Sched make_sched(int kind) {
  Sched s{};
  int cnt[40] = {}, need[40] = {}, avail[40] = {};
  int n = 0;
  auto add = [&](int ty, int ix, int nd, int av) {
    if (kind && ty == 2) return;
    s.type[n] = ty; s.idx[n] = ix; cnt[n] = ty == 2 ? 4 : 3; need[n] = nd < LASTB ? nd : LASTB; avail[n] = av; ++n;
  };
  add(0, 0, 0, -1); add(1, 0, 0, -1); add(2, 0, 0, -1); add(2, 1, 0, -1);
  add(0, 1, 1, -1); add(1, 1, 1, -1); add(2, 2, 1, -1); add(2, 3, 2, -1);
  add(0, 2, 3, -1); add(1, 2, 3, -1);
  add(2, 4, 3, 1); add(2, 5, 4, 1);
  for (int b = 2; b < 12; ++b) {
    const int T = b / 2 + 2;
    add(b & 1, T, 2 * T - 1, kind ? (b & ~1) : b);
    add(2, b + 4, b + 3, b);
  }
  s.n = n;
  s.ok = true;
  const int LIM = 8;
  int endpos[40] = {};
  int next = 0, total = 0;
  for (int b = 0; b <= LASTB; ++b) {
    s.lo[b] = next;
    const int start = total;
    for (;;) {
      if (next >= n || avail[next] > b - 1) break;
      int lastneed = 0;
      for (int i = 0; i < next; ++i) if (need[i] <= b) lastneed = endpos[i];
      if (b < 3 && need[next] > b && total + cnt[next] - lastneed > LIM + 2 * b) break;
      if (b >= 3 && need[next] > b + 1 && total + cnt[next] - start > 11) break;
      total += cnt[next]; endpos[next] = total; ++next;
    }
    s.hi[b] = next;
    if (kind == 0) total += raw_lo(b + 1) - raw_lo(b);
    if (kind == 0 && b >= WARM0 && b < WARM0 + 3) total += 1;
    int lastneed = 0;
    for (int i = 0; i < next; ++i) if (need[i] <= b) lastneed = endpos[i];
    for (int i = next; i < n; ++i) if (need[i] <= b) s.ok = false;
    s.wait[b] = total - lastneed;
  }
  if (next != n) s.ok = false;
  return s;
}
constexpr Sched SCH[2] = {make_sched(0), make_sched(1)};
static_assert(SCH[0].ok && SCH[1].ok, "every ring item must be issued in time");

struct P2Ctx { u64 a0, a1; int so0, sh, g0, nd, rot; unsigned bytes; };
__device__ __forceinline__ P2Ctx p2_ctx(u64 Wd, u64 Cre, u64 Cim, int w, int N) {
  P2Ctx c;
  const int ssm = w < 4;
  const u64 m = 0 - (u64)ssm;
  c.a0 = (Cre & m) | (Wd & ~m); c.a1 = (Cim & m) | (Wd & ~m);
  c.sh = ssm;
  c.so0 = N * 128 + ssm * (N * (65536 - 128) - 14 * 128);
  c.bytes = F_ * D_ * 2u - ssm * (F_ * D_ * 2u - D_ * P_ * 2u);
  c.g0 = 7 * w - 28 + ssm * (56 - 3 * w);
  c.nd = 7 - 3 * ssm;
#define P2ROT 1
  c.rot = P2ROT == 0 ? 0 : (P2ROT == 1 ? (N & 1) * (3 - ssm) : (P2ROT == 2 ? (N & 3) * (2 - ssm) : (N * c.nd) >> 3));
  return c;
}
__device__ __forceinline__ int p2_ds(const P2Ctx& c, int d) { const int t = d + c.rot; return t >= c.nd ? t - c.nd : t; }
__device__ __forceinline__ void b2_src(const P2Ctx& c, int d, v4u& rs, int& so) {
  const int g = c.g0 + p2_ds(c, d);
  const u64 x0 = c.a0, x1 = c.a1;
  rs = mk_rs((const void*)((g & 1) ? x1 : x0), c.bytes);
  so = c.so0 + (c.sh ? (g >> 1) * 128 : g * 65536);
}
template <int LI, int N0, int N1> __device__ __forceinline__ void b2_rows(v4u rs, int lds, int vb, int so) {
  if constexpr (N0 < N1) { glds<LI, N0 * 8192>(rs, lds, vb, so); b2_rows<LI + 1024, N0 + 1, N1>(rs, lds, vb, so); }
}
template <int LI, int N0, int N1> __device__ __forceinline__ void b2_load(const P2Ctx& c, int vb, int d, int lds) {
  v4u rs; int so;
  b2_src(c, d, rs, so);
  b2_rows<LI, N0, N1>(rs, lds, vb, __builtin_amdgcn_readfirstlane(so));
}
__device__ __forceinline__ void b2_read(bool ssm, LAS unsigned char* img, int lane, v4u (&f)[2][4]) {
  const int q = lane >> 4;
  if (ssm) {
    const int i = lane & 15;
#pragma unroll
    for (int h = 0; h < 2; ++h)
#pragma unroll
      for (int e = 0; e < 4; ++e) f[h][e] = lds_rd128(img + i * 128 + (((4 * h + q) ^ (((i >> 1) & 3) << 1)) << 4) + e * 2048);
  } else {
    const int qq = (lane >> 2) & 3, s = 2 * (q & 1) + (qq >> 1);
#pragma unroll
    for (int h = 0; h < 2; ++h)
#pragma unroll
      for (int e = 0; e < 4; ++e) {
        LAS unsigned char* a = img + (4 * q + qq) * 128 + ((e ^ s) << 5) + (lane & 3) * 8 + h * 4096;
        const v2u lo = lds_rdtr(a), hi = lds_rdtr(a + 2048);
        f[h][e] = v4u{lo[0], lo[1], hi[0], hi[1]};
      }
  }
}

struct LdCtx { v4u rx, rw, rb; u64 Wd, Cre, Cim; int lw, lb, vx, vw0, vw1, vbb, rot, lane, vb, vwarm, xcd, ch; mutable unsigned sv, lrb, lib, ldb, ep; };
__device__ __forceinline__ void warm_chunk(u64 x0, u64 x1, u64 x2, int xc, int vwarm, int slot) {
  asm volatile("" : "+s"(xc));
  int i = slot < 6 ? slot + xc : slot - 6 + xc;
  if (slot < 6) { i = i >= 12 ? i - 12 : (i >= 6 ? i - 6 : i); } else { i = 6 + (i >= 10 ? i - 10 : (i >= 5 ? i - 5 : i)); }
  const u64 src = i < 7 ? x0 : (i < 9 ? x1 : x2);
  const unsigned bytes = (unsigned)slot > 10u ? 0u : (i < 7 ? F_ * D_ * 2 : D_ * P_ * 2);
  warm(mk_rs((const void*)src, bytes), vwarm, (i < 7 ? i : ((i - 7) & 1)) * 262144);
}

template <int KIND, int I> __device__ __forceinline__ void ld_item(const LdCtx& c) {
  constexpr int ty = SCH[KIND].type[I], t = SCH[KIND].idx[I];
  int rot = c.rot;
  asm volatile("" : "+s"(rot));
  if constexpr (ty < 2) {
    const int k0 = ((t + rot) & 7) * 128;
    glds<(t % 3) * X_SLOT + 2 * ty * 4096, ty * 65536>(c.rx, c.lw * 1024, c.vx, k0);
    glds<(t % 3) * X_SLOT + (2 * ty + 1) * 4096, ty * 65536 + 8192>(c.rx, c.lw * 1024, c.vx, k0);
    glds<B_BASE + (t % 3) * B_SLOT + ty * 2048, ty * 16384>(c.rb, c.lb, c.vbb, k0);
  } else {
    const int k0 = (((t >> 1) + rot) & 7) * (64 * F_ * 2);
    constexpr int LI = W_BASE + (t & 3) * W_SLOT, SI = (t & 1) * 32 * F_ * 2;
    glds<LI, SI>(c.rw, c.lw * 4096, c.vw0, k0);
    glds<LI + 1024, SI + 4 * F_ * 2>(c.rw, c.lw * 4096, c.vw0, k0);
    glds<LI + 2048, SI + 8 * F_ * 2>(c.rw, c.lw * 4096, c.vw1, k0);
    glds<LI + 3072, SI + 12 * F_ * 2>(c.rw, c.lw * 4096, c.vw1, k0);
  }
}
template <int KIND, int LO, int HI> __device__ __forceinline__ void ld_items(const LdCtx& c) {
  if constexpr (LO < HI) { ld_item<KIND, LO>(c); ld_items<KIND, LO + 1, HI>(c); }
}
template <int A, int E> __device__ __forceinline__ void raw_loads(const P2Ctx& pc, int vb, v4u (&raw)[8 * NRAWD]) {
  if constexpr (A < E) {
    v4u rs; int so;
    b2_src(pc, 4 + (A >> 3), rs, so);
    asm volatile("buffer_load_dwordx4 %0, %1, %2, %3 offen" : "=v"(raw[A]) : "v"(vb), "s"(rs), "s"(so + (A & 7) * 8192) : "memory");
    raw_loads<A + 1, E>(pc, vb, raw);
  }
}
template <int KIND, int B> __device__ __forceinline__ void loader_steps(const LdCtx& c, LAS unsigned char* L, const P2Ctx& pc, v4u (&raw)[8 * NRAWD]) {
  if constexpr (B == 0) {
    constexpr int FIRST = KIND ? 2 : 3;
    ld_items<KIND, 0, FIRST>(c);
    KP kp = kargs();
    const v4u r0 = mk_rs(kp->Lre, P_ * 2), r1 = mk_rs(kp->Lim, P_ * 2), r2 = mk_rs(kp->ldt, P_ * 2), r3 = mk_rs(kp->ws + WS_EPOCH, c.lw == 3 ? 4 : 0);
    asm volatile("buffer_load_ushort %0, %4, %5, 0 offen\n\tbuffer_load_ushort %1, %4, %6, 0 offen\n\tbuffer_load_ushort %2, %4, %7, 0 offen\n\t"
                 "buffer_load_dword %3, off, %8, 0 sc1"
                 : "=&v"(c.lrb), "=&v"(c.lib), "=&v"(c.ldb), "=&v"(c.ep) : "v"(c.ch * 2), "s"(r0), "s"(r1), "s"(r2), "s"(r3) : "memory");
    if constexpr (KIND == 0) {
      ld_items<0, 3, 5>(c);
      wait_vm<7>();
      BARRIER();
      ld_items<0, 5, SCH[0].hi[0]>(c);
    } else {
      ld_items<KIND, FIRST, SCH[KIND].hi[0]>(c);
    }
  } else {
    ld_items<KIND, SCH[KIND].lo[B], SCH[KIND].hi[B]>(c);
  }
  if constexpr (KIND == 0) raw_loads<raw_lo(B), raw_lo(B + 1)>(pc, c.vb, raw);
  if constexpr (KIND == 0 && B == WARM0) warm(mk_rs((const void*)c.Wd, F_ * D_ * 2), c.lane < 16 ? (c.vwarm * 16 + c.lane) * 128 : OOB, 0);
  if constexpr (KIND == 0 && B == WARM0 + 1) warm(mk_rs((const void*)c.Cre, D_ * P_ * 2), c.lane < 5 ? (c.vwarm * 5 + c.lane) * 128 : OOB, 0);
  if constexpr (KIND == 0 && B == WARM0 + 2) warm(mk_rs((const void*)c.Cim, D_ * P_ * 2), c.lane < 5 ? (c.vwarm * 5 + c.lane) * 128 : OOB, 0);
  if constexpr (B == CONST_AT) {
    asm volatile("" : "+v"(c.lrb), "+v"(c.lib), "+v"(c.ldb), "+v"(c.ep) : : "memory");
    const float lr = bf2f(c.lrb), li = bf2f(c.lib);
    float dt = __builtin_amdgcn_exp2f(bf2f(c.ldb) * 1.44269504088896341f);
    dt = bf2f(pk_bf16(dt, 0.f) & 0xFFFFu);
    const float th = dt * li, rv = th * 0.15915494f;
    const float rev = (rv - __builtin_rintf(rv)) + __builtin_fmaf(th, 6.42063833e-9f, __builtin_fmaf(th, 0.15915494f, -rv));
    const float ex = __builtin_amdgcn_exp2f(dt * lr * 1.44269504088896341f);
    const float ar = ex * __builtin_amdgcn_cosf(rev), ai = ex * __builtin_amdgcn_sinf(rev);
    const float den = __builtin_amdgcn_rcpf(lr * lr + li * li);
    float p16r = ar, p16i = ai;
    csq(p16r, p16i, 4);
    float p32r = p16r, p32i = p16i;
    csq(p32r, p32i, 1);
    LAS f32x4* d = (LAS f32x4*)(L + LDS_CONST + c.lw * 512 + (c.lane & 15) * 32);
    d[0] = f32x4{ar, ai, ((ar - 1.f) * lr + ai * li) * den, (ai * lr - (ar - 1.f) * li) * den};
    d[1] = f32x4{p16r, p16i, p32r, p32i};
    *(volatile LAS unsigned*)(L + LDS_TAG + 4 * (3 - c.lw)) = c.ep + 1u;
    WAIT_LDS();
  }
  SCHED();
  wait_vm<SCH[KIND].wait[B]>();
  BARRIER();
  if constexpr (B < LASTB) loader_steps<KIND, B + 1>(c, L, pc, raw);
}

__device__ __forceinline__ bool all4(LAS unsigned char* p) {
  const v4u d = *(volatile LAS v4u*)p;
  return __builtin_amdgcn_readfirstlane(d[0] & d[1] & d[2] & d[3]) != 0u;
}
__device__ __forceinline__ void lds_wait4(LAS unsigned char* p) {
#pragma nounroll
  for (unsigned spins = 0; spins < (SPIN_CAP << 4); ++spins) {
    if (all4(p)) break;
    __builtin_amdgcn_s_sleep(2);
  }
  CFENCE();
}
__device__ __forceinline__ void lds_wait(LAS unsigned char* word) {
#pragma nounroll
  for (unsigned spins = 0; spins < (SPIN_CAP << 4); ++spins) {
    if (lds_word(word) != 0u) break;
    __builtin_amdgcn_s_sleep(2);
  }
  CFENCE();
}
__device__ __forceinline__ void lds_wait_eq(LAS unsigned char* word, unsigned want) {
#pragma nounroll
  for (unsigned spins = 0; spins < (SPIN_CAP << 4); ++spins) {
    if (lds_word(word) == want) break;
    __builtin_amdgcn_s_sleep(2);
  }
  CFENCE();
}
__device__ __forceinline__ void cmul(float ar, float ai, float br, float bi, float& cr, float& ci) {
  cr = ar * br - ai * bi; ci = ar * bi + ai * br;
}

template <int V> struct IC { static constexpr int value = V; };
template <int n> __device__ __forceinline__ void p2_first(v4u (&af)[8], v4u ra, int va, int o0, int o1) {
  asm volatile("buffer_load_dwordx4 %0, %1, %2, %3 offen offset:%c4 sc1" : "=v"(af[n]) : "v"(va), "s"(ra), "s"(n < 4 ? o0 : o1), "i"((n & 3) * 1024) : "memory");
  if constexpr (n < 7) p2_first<n + 1>(af, ra, va, o0, o1);
}
template <int d, int n>
__device__ __forceinline__ void p2_step(f32x4 (&acc)[4][4], const v4u (&bb)[2][4], v4u (&af)[8], v4u ra, int va, int o0, int o1, int w) {
  constexpr int WG = d == 6 ? 7 - n : 7, WS = d == 5 ? 7 - n : 7;
  if constexpr (d >= 3 && d <= 5 && WS < WG)
    asm volatile("s_waitcnt vmcnt(%c2)\n\ts_cmp_gt_u32 %1, 3\n\ts_cbranch_scc1 1f\n\ts_waitcnt vmcnt(%c3)\n1:" : "+v"(af[n]) : "s"(w), "i"(WG), "i"(WS) : "memory", "scc");
  else
    asm volatile("s_waitcnt vmcnt(%c1)" : "+v"(af[n]) : "i"(WG) : "memory");
#pragma unroll
  for (int e = 0; e < 4; ++e) mfma16t(acc[e][n & 3], bb[n >> 2][e], af[n]);
  if constexpr (d < 5)
    asm volatile("buffer_load_dwordx4 %0, %1, %2, %3 offen offset:%c4 sc1" : "+v"(af[n]) : "v"(va), "s"(ra), "s"(n < 4 ? o0 : o1), "i"((n & 3) * 1024) : "memory");
  if constexpr (d == 5)
    asm volatile("s_cmp_lt_u32 %5, 4\n\ts_cbranch_scc1 1f\n\tbuffer_load_dwordx4 %0, %1, %2, %3 offen offset:%c4 sc1\n1:"
                 : "+v"(af[n]) : "v"(va), "s"(ra), "s"(n < 4 ? o0 : o1), "i"((n & 3) * 1024), "s"(w) : "memory", "scc");
  if constexpr (n < 7) p2_step<d, n + 1>(acc, bb, af, ra, va, o0, o1, w);
}

__global__ __launch_bounds__(512, 2) void k_fused(Params) {
  extern __shared__ __attribute__((aligned(16))) unsigned char lds_[];
  LAS unsigned char* L = (LAS unsigned char*)lds_;
  STAMP(0);
  KP kp = kargs();
  u64 pX = (u64)kp->x, pWg = (u64)kp->Wg, pWu = (u64)kp->Wu, pBre = (u64)kp->Bre, pBim = (u64)kp->Bim, pCre = (u64)kp->Cre, pCim = (u64)kp->Cim, pWd = (u64)kp->Wd;
  const int b = __builtin_amdgcn_workgroup_id_x(), tid = __builtin_amdgcn_workitem_id_x(), lane = tid & 63;
  const int w = __builtin_amdgcn_readfirstlane(tid >> 6);
  const int xcd = b & 7, r = b >> 3;
  warm(mk_rs((const void*)(__builtin_amdgcn_s_getpc() & ~(u64)127), CODE_BYTES), lane < 2 ? lane * 32768 : OOB, (r * 8 + w) * 128);
  const bool light = (r & 7) == 7;
  const int ih = 28 * (xcd & 3) + r - (r >> 3), il = 112 + 4 * (xcd & 3) + (r >> 3);
  const int idx = ih + (il - ih) * (int)light;
  const int pn = 8 * (xcd >> 2) + (idx & 7), cb = idx >> 3;
  const int rot = pn & 6;
  int R = xcd * 4 + (r & 3), N = r >> 2;
  asm volatile("" : "+s"(pX), "+s"(pWg), "+s"(pWu), "+s"(pBre), "+s"(pBim), "+s"(pCre), "+s"(pCim), "+s"(pWd));
  const int vb = (lane >> 3) * 1024 + (((lane & 7) ^ (((lane >> 4) & 3) << 1)) << 4);
  v4u b2[4][2][4];
  unsigned tag = 0;
  P2Ctx pc;

  if (__builtin_expect(w >= 4, 1)) {
    LdCtx c;
    c.lw = w - 4; c.rot = rot; c.lane = lane; c.vb = vb; c.sv = 0;
    c.Wd = pWd; c.Cre = pCre; c.Cim = pCim;
    c.vwarm = ((r - (r >> 3)) * 8 + xcd) * 4 + c.lw;
    c.xcd = xcd; c.ch = 32 * cb + 16 * (c.lw >> 1) + (lane & 15);
    const int l3 = lane >> 3, kq = lane >> 4;
    c.rx = mk_rs((const void*)pX, S_ * D_ * 2);
    c.vx = (32 * (c.lw & 1) + 16 * (l3 >> 2) + 4 * (c.lw >> 1) + (l3 & 3)) * 1024 + (((lane & 7) ^ ((4 * c.lw + kq) & 7)) << 4) + pn * 128 * 1024;
    c.rb = mk_rs((const void*)((c.lw >> 1) ? pBim : pBre), P_ * D_ * 2);
    c.vbb = (8 * (c.lw & 1) + l3) * 1024 + (((lane & 7) ^ (4 * (c.lw & 1) + kq)) << 4) + cb * 32 * 1024;
    c.lb = (c.lw >> 1) * 4096 + (c.lw & 1) * 1024;
    c.rw = mk_rs((const void*)((c.lw >> 1) ? pWu : pWg), D_ * F_ * 2);
    c.vw0 = ((c.lw & 1) * 16 + kq) * (F_ * 2) + (((lane & 15) ^ (kq << 2)) << 4) + cb * 256;
    c.vw1 = c.vw0 ^ 32;
    STAMP(1);
    pc = p2_ctx(pWd, pCre, pCim, w, N);
    v4u raw[8 * NRAWD];
    if (__builtin_expect(!light, 1)) loader_steps<0, 0>(c, L, pc, raw);
    else { loader_steps<1, 0>(c, L, pc, raw); BARRIER(); raw_loads<0, 8 * NRAWD>(pc, vb, raw); }
    STAMP(2);
    {
      const int wA = c.lw < 2 ? w : c.lw;
      b2_load<0, 0, 8>(pc, vb, 0, wA * 16384);
      b2_load<8192, 0, 8>(pc, vb, 1, wA * 16384);
      const P2Ctx pp = p2_ctx(pWd, pCre, pCim, c.lw, N);
#pragma unroll
      for (int d = 0; d < 4; ++d) {
        wait_vm<8>();
        b2_read(false, L + wA * 16384 + (d & 1) * 8192, lane, b2[d]);
        WAIT_LDS();
        if (d == 2) lds_wait4(L + LDS_MD);
        if (d < 2) b2_load<0, 0, 8>(pc, vb, d + 2, wA * 16384 + (d & 1) * 8192);
        else b2_load<0, 0, 8>(pp, vb, d & 1, c.lw * 16384 + (d & 1) * 8192);
      }
      STAMP(3);
#pragma unroll
      for (int z = 0; z < 16; z += 8)
        asm volatile("" : "+v"(raw[z]), "+v"(raw[z + 1]), "+v"(raw[z + 2]), "+v"(raw[z + 3]), "+v"(raw[z + 4]), "+v"(raw[z + 5]), "+v"(raw[z + 6]), "+v"(raw[z + 7]));
#pragma unroll
      for (int n = 0; n < 16; ++n) *(LAS v4u*)(L + w * 16384 + n * 1024 + lane * 16) = raw[n];
      wait_vm<0>();
      *(volatile LAS unsigned*)(L + LDS_PF + 4 * c.lw) = 1u;
      STAMP(4);
      lds_wait(L + LDS_CO + 4 * c.lw);
      b2_load<0, 0, 8>(pp, vb, 2, c.lw * 16384);
      b2_load<8192, 0, 8>(pp, vb, 3, c.lw * 16384);
      wait_vm<0>();
      *(volatile LAS unsigned*)(L + LDS_PF + 4 * c.lw) = 2u;
      STAMP(5);
    }
    tag = lds_word(L + LDS_TAG);
  } else {
    const int c = w, q = lane >> 4, i15 = lane & 15, G = c & 1;
    const int ch = 32 * cb + 16 * (c >> 1) + i15;
    if (w == 3) {
#pragma unroll
      for (int z = 0; z < 5; ++z) *(volatile LAS v4u*)(L + LDS_DONE + 16 * z) = v4u{0u, 0u, 0u, 0u};
      *(volatile LAS unsigned*)(L + LDS_RDY) = 0u;
      *(volatile LAS v4u*)(L + LDS_RDY + 16) = v4u{0u, 0u, 0u, 0u};
      WAIT_LDS();
    }
    f32x4 k0 = {0.f, 0.f, 0.f, 0.f}, k1 = {0.f, 0.f, 0.f, 0.f};
    unsigned tagv = 0;
    auto rd_const = [&]() {
      k0 = *(LAS f32x4*)(L + LDS_CONST + c * 512 + i15 * 32); k1 = *(LAS f32x4*)(L + LDS_CONST + c * 512 + i15 * 32 + 16);
      tagv = *(volatile LAS unsigned*)(L + LDS_TAG);
    };
    u64 pws = (u64)kp->ws;
    asm volatile("" : "+s"(pws));
    int xa[2];
#pragma unroll
    for (int ks = 0; ks < 2; ++ks) xa[ks] = i15 * 128 + (((4 * ks + q) ^ ((i15 >> 1) & 7)) << 4);
    asm volatile("" : "+v"(xa[0]), "+v"(xa[1]));
    const int bo = B_BASE + (c >> 1) * 2048;
    f32x4 accs[2][4], acc[4][8];
    v4u bfr[2];
    auto zero_accs = [&]() {
#pragma unroll
      for (int m = 0; m < 2; ++m)
#pragma unroll
        for (int t = 0; t < 4; ++t) { accs[m][t] = f32x4{0.f, 0.f, 0.f, 0.f}; asm volatile("" : "+v"(accs[m][t])); }
    };
    float vr[16], vi[16], t0 = 0.f, t1 = 0.f;
    auto scan_half = [&](int n) {
      const int z = n >> 1;
      if (z == 0) { vr[0] = accs[0][0][0]; vi[0] = accs[1][0][0]; }
      else if ((n & 1) == 0) { t0 = __builtin_fmaf(k0[0], vr[z - 1], accs[0][z >> 2][z & 3]); t1 = __builtin_fmaf(k0[0], vi[z - 1], accs[1][z >> 2][z & 3]); }
      else { vr[z] = __builtin_fmaf(-k0[1], vi[z - 1], t0); vi[z] = __builtin_fmaf(k0[1], vr[z - 1], t1); }
    };
    auto sc = [](int v) { asm volatile("" : "+s"(v)); return v; };

    if (!light) {
      int wa[2];
      {
        const int qq = i15 >> 2, pp = lane & 3;
#pragma unroll
        for (int u = 0; u < 2; ++u)
          wa[u] = W_BASE + (8 * q + qq) * 256 + (((2 * (2 * c + u) + (pp >> 1)) ^ ((qq << 2) | ((q & 1) << 1))) << 4) + 8 * (pp & 1);
      }
      asm volatile("" : "+v"(wa[0]), "+v"(wa[1]));
#pragma unroll
      for (int a = 0; a < 4; ++a)
#pragma unroll
        for (int u = 0; u < 8; ++u) { acc[a][u] = f32x4{0.f, 0.f, 0.f, 0.f}; asm volatile("" : "+v"(acc[a][u])); }
      zero_accs();
      v4u xf[4];
      v2u wf[2][8];
      auto rdw = [&](int s, int z) {
        const int m = z >> 2, u = (z >> 1) & 1, t = z & 1;
        wf[s & 1][z] = lds_rdtr(L + wa[u] + (s & 3) * W_SLOT + m * 8192 + t * 1024);
      };
      STAMP(1);
      BARRIER();
      STAMP(2);
#pragma unroll
      for (int z = 0; z < 8; ++z) rdw(0, z);
#pragma unroll
      for (int z = 0; z < 3; ++z) xf[z] = lds_rd128(L + xa[0] + G * 8192 + z * 2048);
#pragma unroll
      for (int m = 0; m < 2; ++m) bfr[m] = lds_rd128(L + xa[0] + bo + m * 4096);
      auto glu4 = [&](int s, int u, const v4u& x, auto&& between) {
#pragma unroll
        for (int a = 0; a < 4; ++a) {
          const v4u wv = {wf[s & 1][2 * a][0], wf[s & 1][2 * a][1], wf[s & 1][2 * a + 1][0], wf[s & 1][2 * a + 1][1]};
          mfma16t(acc[a][u], wv, x);
          SCHED();
          between(a);
          SCHED();
        }
      };
      int xb = 0, bb = 0;
      auto steps = [&](int s0, int n, const int (&xo)[5]) {
#pragma unroll
        for (int j = 0; j < n; ++j)
#pragma unroll
          for (int u = 0; u < 8; ++u) {
            const int s = s0 + j, jn = j + ((u + 3) >> 3), un = (u + 3) & 7, uw = s == 0 ? u - 4 : u;
            SCHED();
            if (u == (s == 0 ? 4 : 0)) { BARRIER(); SCHED(); }
            glu4(s, u, xf[u & 3], [&](int a) {
              if (a == 0) {
                if ((un & 3) == 0 || (j == 0 && u == 0)) xb = xa[(s0 + jn) & 1] + sc(xo[jn] + ((un >> 2) ^ G) * 8192);
                xf[(u + 3) & 3] = lds_rd128(L + xb + (un & 3) * 2048);
              }
              if ((a == 1 || a == 2) && uw >= 0 && uw < 4) rdw(s + 1, 2 * uw + a - 1);
              if (a == 3 && (u == 4 || u == 5)) {
                if (u == 4) bb = xa[(s + 1) & 1] + sc(bo + (xo[j + 1] >> 1));
                bfr[u - 4] = lds_rd128(L + bb + (u - 4) * 4096);
              }
            });
            if (u < 4) { mfma16t(accs[0][u], xf[u & 3], bfr[0]); mfma16t(accs[1][u], xf[u & 3], bfr[1]); }
          }
      };
      {
        const int xo[5] = {0, 0, 0, 0, 0};
        steps(0, 1, xo);
      }
      int xo0 = 0;
#pragma nounroll
      for (int o = 0; o < 3; ++o) {
        const int xo1 = xo0 == 2 * X_SLOT ? 0 : xo0 + X_SLOT, xo2 = xo1 == 2 * X_SLOT ? 0 : xo1 + X_SLOT;
        const int xo[5] = {xo0, xo1, xo1, xo2, xo2};
        steps(1, 4, xo);
        xo0 = xo2;
      }
      {
        const int xo[5] = {0, X_SLOT, 0, 0, 0};
        steps(1, 1, xo);
      }
      v4u bf2[2];
#pragma unroll
      for (int it = 0; it < 16; ++it) {
        const int s = (it >> 2) & 1, u = (it & 3) + 4 * (it >> 3), nx = it + 3;
        SCHED();
        glu4(s, u, xf[it & 3], [&](int a) {
          if (a == 0 && nx < 16) {
            if ((nx & 3) == 0) xb = xa[(nx >> 2) & 1] + sc(X_SLOT + ((nx >> 3) ^ G) * 8192);
            xf[nx & 3] = lds_rd128(L + xb + (nx & 3) * 2048);
          }
          if (a >= 1 && it < 3 && 3 * it + a - 1 < 8) rdw(3, 3 * it + a - 1);
          if (it == 0 && a == 3) { bb = xa[1] + sc(bo + B_SLOT); bf2[0] = lds_rd128(L + bb); bf2[1] = lds_rd128(L + bb + 4096); }
          if (it == 4 && a == 1) rd_const();
          if (it >= 8) scan_half(4 * (it - 8) + a);
        });
        if (it < 4) { mfma16t(accs[0][u], xf[it & 3], bfr[0]); mfma16t(accs[1][u], xf[it & 3], bfr[1]); }
        else if (it < 8) { mfma16t(accs[0][u], xf[it & 3], bf2[0]); mfma16t(accs[1][u], xf[it & 3], bf2[1]); }
      }
      asm volatile("s_nop 15" : "+v"(acc[0][7]), "+v"(acc[1][7]), "+v"(acc[2][7]), "+v"(acc[3][7]), "+v"(acc[0][6]), "+v"(acc[1][6]), "+v"(acc[2][6]), "+v"(acc[3][6]));
    } else {
      {
      kp = kargs();
      rsrc_t rx = mk_rsrc(kp->x, S_ * D_ * 2), rg = mk_rsrc(kp->gw, D_ * 2), rgo = mk_rsrc(kp->ws + WS_G, S_ * 4);
      const int tk0 = 128 * pn + 64 * (cb - 14) + 16 * c;
      v4u gv[8], xv[2][8];
#pragma unroll
      for (int z = 0; z < 8; ++z) gv[z] = __builtin_amdgcn_raw_buffer_load_b128(rg, (lane & 7) * 16, z * 128, 0);
#pragma unroll
      for (int gi = 0; gi < 2; ++gi)
#pragma unroll
        for (int z = 0; z < 8; ++z) xv[gi][z] = __builtin_amdgcn_raw_buffer_load_b128(rx, (lane >> 3) * 1024 + (lane & 7) * 16, (tk0 + gi * 8) * 1024 + z * 128, 0);
#pragma unroll
      for (int gi = 0; gi < 2; ++gi) {
        float s = 0.f;
#pragma unroll
        for (int z = 0; z < 8; ++z)
#pragma unroll
          for (int y = 0; y < 4; ++y) {
            const unsigned xa_ = xv[gi][z][y], ga = gv[z][y];
            s = __builtin_amdgcn_fdot2_f32_bf16(__builtin_bit_cast(bf16x2, xa_), __builtin_bit_cast(bf16x2, ga), s, false);
          }
        s += dpp<0xB1>(s); s += dpp<0x4E>(s); s += dpp<0x141>(s);
        const float g = __builtin_amdgcn_rcpf(1.f + __builtin_amdgcn_exp2f(s * -1.44269504088896341f));
        __builtin_amdgcn_raw_buffer_store_b32(f2u(g), rgo, (lane & 7) ? OOB : (lane >> 3) * 4, (tk0 + gi * 8) * 4, AUX_SC1);
      }
      }
      STAMP(1);
      zero_accs();
      int xo = 0;
#pragma nounroll
      for (int T = 0; T < 8; ++T) {
#pragma unroll
        for (int ks = 0; ks < 2; ++ks) {
          if (T < 7) BARRIER();
          v4u xf[4];
#pragma unroll
          for (int t = 0; t < 4; ++t) xf[t] = lds_rd128(L + (xa[ks] + xo + G * 8192) + t * 2048);
#pragma unroll
          for (int m = 0; m < 2; ++m) bfr[m] = lds_rd128(L + (xa[ks] + bo + (xo >> 1)) + m * 4096);
#pragma unroll
          for (int t = 0; t < 4; ++t) { mfma16t(accs[0][t], xf[t], bfr[0]); mfma16t(accs[1][t], xf[t], bfr[1]); }
          SCHED();
        }
        xo = xo == 2 * X_SLOT ? 0 : xo + X_SLOT;
      }
      asm volatile("s_nop 15" : "+v"(accs[0][0]), "+v"(accs[1][0]), "+v"(accs[0][1]), "+v"(accs[1][1]), "+v"(accs[0][2]), "+v"(accs[1][2]), "+v"(accs[0][3]), "+v"(accs[1][3]));
      rd_const();
      BARRIER();
#pragma unroll
      for (int n = 0; n < 32; ++n) { scan_half(n); SCHED(); }
    }
    *(volatile LAS unsigned*)(L + LDS_MD + 4 * w) = 1u;
    SCHED();
    STAMP(3);
    tag = __builtin_amdgcn_readfirstlane(tagv);
    const float ar = k0[0], ai = k0[1], cr = k0[2], ci = k0[3], p16r = k1[0], p16i = k1[1], p32r = k1[2], p32i = k1[3];
    __builtin_amdgcn_s_setprio(2);
    rsrc_t ra2 = mk_rsrc((const void*)(pws + WS_A2), A2_BYTES), rcar = mk_rsrc((const void*)(pws + WS_CARRY), 32 * P_ * 16);
    const int ck = 2 * pn + G;
    float pr, pi;
    {
      float tr, ti;
      const auto xr = __builtin_amdgcn_permlane16_swap(f2u(vr[15]), f2u(vr[15]), false, false);
      const auto xi = __builtin_amdgcn_permlane16_swap(f2u(vi[15]), f2u(vi[15]), false, false);
      const float e0r = u2f(xr[0]), e1r = u2f(xr[1]), e0i = u2f(xi[0]), e1i = u2f(xi[1]);
      cmul(e0r, e0i, p16r, p16i, tr, ti); tr += e1r; ti += e1i;
      const auto yr = __builtin_amdgcn_permlane32_swap(f2u(tr), f2u(tr), false, false);
      const auto yi = __builtin_amdgcn_permlane32_swap(f2u(ti), f2u(ti), false, false);
      const float f0r = u2f(yr[0]), f1r = u2f(yr[1]), f0i = u2f(yi[0]), f1i = u2f(yi[1]);
      float Er, Ei;
      cmul(f0r, f0i, p32r, p32i, Er, Ei); Er += f1r; Ei += f1i;
      const float br = (q & 2) ? f0r : 0.f, bi = (q & 2) ? f0i : 0.f;
      cmul(br, bi, p16r, p16i, tr, ti);
      pr = (q & 1) ? tr + e0r : br; pi = (q & 1) ? ti + e0i : bi;
      const v4u gq = {f2u(Er), tag, f2u(Ei), ~tag};
      st_sc1(gq, rcar, q ? OOB : ch * 16, (31 - ck) * (P_ * 16));
    }
    asm volatile("" : "+v"(pr), "+v"(pi) : : "memory");
    SCHED();
    STAMP(4);
    v4u cv[8];
    const int cvo = ch * 16 + (32 - ck + 8 * q) * (P_ * 16);
    auto ld_carry = [&]() {
#pragma unroll
      for (int z = 0; z < 8; ++z) cv[z] = ld_sc1(rcar, cvo, z * (P_ * 16));
    };
    auto publish = [&](int part) {
      *(volatile LAS unsigned*)(L + LDS_DONE + 16 * part + 4 * w) = 1u;
      const bool last = all4(L + LDS_DONE + 16 * part);
      st_flag((unsigned char*)pws, WS_FA2 + part * 32768 + pn * 128 + cb * 8, tag, last && lane < 16 ? lane * 2048 : OOB);
      CFENCE();
    };
    if (!light) {
#pragma unroll
      for (int u = 0; u < 8; ++u) {
        float hv[8];
#pragma unroll
        for (int j = 0; j < 8; ++j) hv[j] = acc[j >> 2][u][j & 3] * -1.44269504088896341f;
        SCHED();
#pragma unroll
        for (int j = 0; j < 8; ++j) hv[j] = __builtin_amdgcn_exp2f(hv[j]);
        SCHED();
#pragma unroll
        for (int j = 0; j < 8; ++j) hv[j] += 1.f;
        SCHED();
#pragma unroll
        for (int j = 0; j < 8; ++j) hv[j] = __builtin_amdgcn_rcpf(hv[j]);
        SCHED();
#pragma unroll
        for (int j = 0; j < 8; ++j) hv[j] *= acc[2 + (j >> 2)][u][j & 3];
        SCHED();
        st_sc1(v4u{pk_bf16(hv[0], hv[1]), pk_bf16(hv[2], hv[3]), pk_bf16(hv[4], hv[5]), pk_bf16(hv[6], hv[7])}, ra2, lane * 16,
               ((2 * pn + ((u >> 2) ^ G)) * NK + 4 * cb + c) * 4096 + (u & 3) * 1024);
        if (u + 1 < 8) asm volatile("" : "+v"(acc[0][u + 1]), "+v"(acc[1][u + 1]) : : "memory");
      }
    }
    CFENCE();
    SCHED();
    STAMP(5);
    ld_carry();
    CFENCE();
    SCHED();
#pragma unroll
    for (int z = 0; z < 16; ++z) {
      const float t = cr * vr[z] - ci * vi[z];
      vi[z] = -cr * vi[z] - ci * vr[z]; vr[z] = t;
    }
    float qr[4], qi[4];
    qr[0] = ar; qi[0] = ai; qr[1] = ar; qi[1] = ai; csq(qr[1], qi[1], 1);
    cmul(qr[1], qi[1], ar, ai, qr[2], qi[2]); qr[3] = qr[1]; qi[3] = qi[1]; csq(qr[3], qi[3], 1);
    const float p4r = qr[3], p4i = qi[3];
    float p64r = p32r, p64i = p32i; csq(p64r, p64i, 1);
    float p512r = p64r, p512i = p64i; csq(p512r, p512i, 3);
    float p1kr = p512r, p1ki = p512i; csq(p1kr, p1ki, 1);
    float sgr, sgi, wgr, wgi;
    cmul((q & 1) ? p16r : 1.f, (q & 1) ? p16i : 0.f, (q & 2) ? p32r : 1.f, (q & 2) ? p32i : 0.f, sgr, sgi);
    cmul((q & 1) ? p512r : 1.f, (q & 1) ? p512i : 0.f, (q & 2) ? p1kr : 1.f, (q & 2) ? p1ki : 0.f, wgr, wgi);
#pragma unroll
    for (int z = 0; z < 16; ++z) asm volatile("" : "+v"(vr[z]), "+v"(vi[z]));
    asm volatile("" : "+v"(sgr), "+v"(sgi), "+v"(wgr), "+v"(wgi), "+v"(p64r), "+v"(p64i));
    SCHED();
    const int want = imin(imax(ck - 8 * q, 0), 8);
    auto carry_ok = [&]() {
      int good = 0;
#pragma unroll
      for (int z = 0; z < 8; ++z) good += ((cv[z][1] ^ tag) | (cv[z][3] ^ ~tag)) == 0u;
      return wall(good == want);
    };
    auto pickup = [&]() {
      lds_wait(L + LDS_PF + 4 * w);
      b2_read(true, L + w * 16384, lane, b2[2]);
      b2_read(true, L + w * 16384 + 8192, lane, b2[3]);
      CFENCE();
      *(volatile LAS unsigned*)(L + LDS_CO + 4 * w) = 1u;
      CFENCE();
    };
    if (!light) pickup();
    wait_vm<8>();
    publish(0);
    STAMP(14);
    if (__builtin_expect(!carry_ok(), 0)) {
#pragma nounroll
      for (unsigned spins = 0; spins < SPIN_CAP; ++spins) {
        __builtin_amdgcn_s_sleep(2);
        CFENCE();
        ld_carry();
        if (carry_ok()) break;
      }
    }
    STAMP(6);
    float Gr, Gi;
    {
      float Hr = 0.f, Hi = 0.f, tr, ti;
#pragma unroll
      for (int z = 7; z >= 0; --z) { cmul(Hr, Hi, p64r, p64i, tr, ti); Hr = tr + u2f(cv[z][0]); Hi = ti + u2f(cv[z][2]); }
      cmul(Hr, Hi, wgr, wgi, tr, ti);
      { const auto a = __builtin_amdgcn_permlane16_swap(f2u(tr), f2u(tr), false, false);
        const auto c2 = __builtin_amdgcn_permlane16_swap(f2u(ti), f2u(ti), false, false);
        tr = u2f(a[0]) + u2f(a[1]); ti = u2f(c2[0]) + u2f(c2[1]); }
      { const auto a = __builtin_amdgcn_permlane32_swap(f2u(tr), f2u(tr), false, false);
        const auto c2 = __builtin_amdgcn_permlane32_swap(f2u(ti), f2u(ti), false, false);
        tr = u2f(a[0]) + u2f(a[1]); ti = u2f(c2[0]) + u2f(c2[1]); }
      cmul(tr, ti, sgr, sgi, Hr, Hi);
      cmul(Hr + pr, Hi + pi, cr, ci, Gr, Gi);
    }
    {
      LAS unsigned char* T = L + B_BASE + (c >> 1) * (2 * B_SLOT) + (c & 1) * 4096;
      const int s32 = ((i15 >> 1) & 1) | ((i15 >> 2) & 2), s8 = (i15 & 1) | ((i15 >> 1) & 2);
#pragma unroll
      for (int t = 0; t < 4; ++t) {
        float orr[4], oi[4];
#pragma unroll
        for (int z = 0; z < 4; ++z) {
          orr[z] = __builtin_fmaf(-qi[z], Gi, __builtin_fmaf(qr[z], Gr, vr[4 * t + z]));
          oi[z] = __builtin_fmaf(-qi[z], Gr, __builtin_fmaf(-qr[z], Gi, vi[4 * t + z]));
        }
        if (t < 3) { float tr, ti; cmul(Gr, Gi, p4r, p4i, tr, ti); Gr = tr; Gi = ti; }
        LAS unsigned char* a = T + i15 * 128 + ((t ^ s32) << 5) + ((q ^ s8) << 3);
        *(LAS v2u*)a = v2u{pk_bf16(orr[0], orr[1]), pk_bf16(orr[2], orr[3])};
        *(LAS v2u*)(a + 2048) = v2u{pk_bf16(oi[0], oi[1]), pk_bf16(oi[2], oi[3])};
      }
      const int qq = i15 >> 2, pp = lane & 3;
      const int so = (ck * NK + 56 + 4 * (cb >> 1) + (cb & 1)) * 4096 + (c >> 1) * 512;
#pragma unroll
      for (int t = 0; t < 4; ++t) {
        v2u part[2];
#pragma unroll
        for (int hh = 0; hh < 2; ++hh)
          part[hh] = lds_rdtr(T + (8 * q + 4 * hh + qq) * 128 + ((t ^ ((qq >> 1) | ((q & 1) << 1))) << 5) + ((pp ^ ((qq & 1) | (hh << 1))) << 3));
        st_sc1(v4u{part[0][0], part[0][1], part[1][0], part[1][1]}, ra2, (lane >> 5) * 8192 + (lane & 31) * 16, so + t * 1024);
      }
    }
    SCHED();
    STAMP(7);
    wait_vm<0>();
    publish(1);
    if (light) pickup();
    STAMP(12);
  }
  kp = kargs();
  asm volatile("" : "+s"(R), "+s"(N));
  pc = p2_ctx(pWd, pCre, pCim, w, N);
  const int d0 = 2 * pc.sh;
  if (w < 4) {
    lds_wait_eq(L + LDS_PF + 4 * w, 2u);
    b2_read(true, L + w * 16384, lane, b2[0]);
    b2_read(true, L + w * 16384 + 8192, lane, b2[1]);
    WAIT_LDS();
    b2_load<0, 0, 8>(p2_ctx(pWd, pCre, pCim, w + 4, N), vb, 6, w * 16384);
    wait_vm<0>();
    *(volatile LAS unsigned*)(L + LDS_CO + 4 * w) = 2u;
    STAMP(13);
  }

  {
    lds_wait4(L + LDS_DONE);
    const int cl = lane & 15, lo = pc.sh ? (pc.g0 - 28) & ~1 : pc.g0 >> 1, hi = pc.sh ? (pc.g0 + pc.nd - 29) | 1 : (pc.g0 + pc.nd - 1) >> 1;
    GAS u64* fl = (GAS u64*)(kp->ws + WS_FA2) + ((pc.sh * 16 + (R & 1) * 8 + N) * 16 + (R >> 1)) * 16 + cl;
    if (w >= 4) {
      const unsigned need = ((2u << hi) - (1u << lo)) | 0xC000u;
#pragma nounroll
      for (unsigned spins = 0; spins < (SPIN_CAP << 3); ++spins) {
        CFENCE();
        const v4u st = *(volatile LAS v4u*)(L + LDS_RDY + 16);
        unsigned up = __builtin_amdgcn_readfirstlane(st[3]);
        if ((up & need) == need) break;
        const unsigned gone = __builtin_amdgcn_readfirstlane(st[0] | st[1] | st[2]), lower = (1u << (w - 4)) - 1u;
        if ((gone & lower) == lower) {
          const u64 v = __hip_atomic_load(fl, RLX_AGENT);
          up = (unsigned)__builtin_amdgcn_ballot_w64(v == tag64(tag)) & 0xFFFFu;
          CFENCE();
          *(volatile LAS unsigned*)(L + LDS_RDY + 28) = up;
          if ((up & need) == need) break;
          if (up & 0x3FFFu) __builtin_amdgcn_s_sleep(GPOLL); else __builtin_amdgcn_s_sleep(16);
        } else {
          __builtin_amdgcn_s_sleep(2);
        }
      }
      CFENCE();
      if (w < 7) *(volatile LAS unsigned*)(L + LDS_RDY + 16 + 4 * (w - 4)) = 1u << (w - 4);
    } else if (w == 3) {
      lds_wait4(L + LDS_DONE + 16);
      __builtin_amdgcn_s_sleep(SP_FIRST);
#pragma nounroll
      for (unsigned spins = 0; spins < SPIN_CAP; ++spins) {
        CFENCE();
        const u64 v = __hip_atomic_load(fl, RLX_AGENT);
        const unsigned up = (unsigned)__builtin_amdgcn_ballot_w64(v == tag64(tag)) & 0xFFFFu;
        CFENCE();
        *(volatile LAS unsigned*)(L + LDS_RDY) = up;
        if (up == 0xFFFFu) break;
        if (up & 0x3FFFu) __builtin_amdgcn_s_sleep(SP_NEXT); else __builtin_amdgcn_s_sleep(32);
      }
      CFENCE();
    } else {
      const unsigned need = (0xFu << lo) | 0xC000u;
#pragma nounroll
      for (unsigned spins = 0; spins < (SPIN_CAP << 4); ++spins) {
        if ((lds_word(L + LDS_RDY) & need) == need) break;
        __builtin_amdgcn_s_sleep(2);
      }
      CFENCE();
    }
  }
  STAMP(8);
  if (w >= 4) __builtin_amdgcn_s_setprio(1);
  glds<B_BASE + B_SLOT, 0>(mk_rs((const void*)pX, S_ * D_ * 2), w * 1024, ((lane >> 5) * 16 + ((lane >> 3) & 3)) * 1024 + (lane & 7) * 16,
                             (64 * R + 32 * (w & 1) + 4 * (w >> 1)) * 1024 + N * 128);
  const int tok = 64 * R + 32 * (w & 1) + 4 * (w >> 1) + (lane >> 4);
  const v2u dv = __builtin_amdgcn_raw_buffer_load_b64(mk_rsrc(kp->Dsk, D_ * 2), (lane & 15) * 8, N * 128, 0);
  const float ge = u2f(__builtin_amdgcn_raw_buffer_load_b32(mk_rsrc(kp->ws + WS_G, S_ * 4), tok * 4 + (lane & 1) * 64, 0, AUX_SC1));
  SCHED();
  const v4u ra = mk_rs(kp->ws + WS_A2 + (R * NK + 2 * pc.g0) * 4096, pc.nd * 8192);
  const int va = lane * 16;
  v4u af[8];
  {
    const int o = p2_ds(pc, 0) * 8192;
    p2_first<0>(af, ra, va, o, o + 4096);
  }
  SCHED();
#define CPRIO 0
  if (w < 4) __builtin_amdgcn_s_setprio(CPRIO);
  f32x4 acc[4][4];
#pragma unroll
  for (int e = 0; e < 4; ++e)
#pragma unroll
    for (int tt = 0; tt < 4; ++tt) { acc[e][tt] = f32x4{0.f, 0.f, 0.f, 0.f}; asm volatile("" : "+v"(acc[e][tt])); }
  asm volatile("s_nop 1");
  auto step = [&](auto Dc) {
    constexpr int d = decltype(Dc)::value;
    const int o = p2_ds(pc, d + 1 - d0) * 8192;
    p2_step<d, 0>(acc, b2[d & 3], af, ra, va, o, o + 4096, w);
    if (d < 2) b2_read(false, L + w * 16384 + d * 8192, lane, b2[d & 3]);
    if (d == PK6 && w >= 4) {
      lds_wait_eq(L + LDS_CO + 4 * (w - 4), 2u);
      b2_read(false, L + (w - 4) * 16384, lane, b2[2]);
      CFENCE();
      *(volatile LAS unsigned*)(L + LDS_PF + 4 * (w - 4)) = 3u;
      CFENCE();
    }
    SCHED();
  };
  if (w >= 4) { step(IC<0>{}); step(IC<1>{}); }
  step(IC<2>{}); step(IC<3>{}); step(IC<4>{}); step(IC<5>{});
  if (w >= 4) step(IC<6>{});
  asm volatile("s_nop 15" : "+v"(acc[0][3]), "+v"(acc[1][3]), "+v"(acc[2][3]), "+v"(acc[3][3]), "+v"(acc[0][2]), "+v"(acc[1][2]), "+v"(acc[2][2]), "+v"(acc[3][2]));
  SCHED();
  STAMP(9);
  kp = kargs();
  if (w < 4) lds_wait_eq(L + LDS_PF + 4 * w, 3u);
  {
    const int t3 = lane & 3, g3 = ((t3 & 1) * 3) ^ (t3 >> 1);
#pragma unroll
    for (int tt = 0; tt < 4; ++tt)
#pragma unroll
      for (int e = 0; e < 4; ++e)
        *(LAS f32x4*)(L + w * 16384 + (tt * 4 + e) * 1024 + (((lane & 48) | (((((lane >> 2) & 3) ^ (lane >> 4)) << 2) | (e ^ g3))) << 4)) = acc[e][tt];
  }
  if (b == 255 && w == 0) {
    rsrc_t rf = mk_rsrc(kp->ws + WS_FA2 + 32768, 256 * 8);
#pragma nounroll
    for (unsigned spins = 0; spins < SPIN_CAP; ++spins) {
      CFENCE();
      const v4u f0 = ld_sc1(rf, lane * 32, 0), f1 = ld_sc1(rf, lane * 32, 16);
      if (wall(((f0[0] ^ tag) | (f0[1] ^ ~tag) | (f0[2] ^ tag) | (f0[3] ^ ~tag) | (f1[0] ^ tag) | (f1[1] ^ ~tag) | (f1[2] ^ tag) | (f1[3] ^ ~tag)) == 0u)) break;
      __builtin_amdgcn_s_sleep(8);
    }
    st_flag(kp->ws, WS_EPOCH, tag, lane == 0 ? 0 : OOB);
  }
  {
    const int q = lane & 3, e = (lane >> 2) & 3, t3 = lane >> 4;
    const int base = ((w >> 1) * 4 + e) * 1024 + q * 256 + ((e ^ ((t3 & 1) * 3) ^ (t3 >> 1)) << 4);
    u64 pout = (u64)kp->out;
    const float go = dpp<0xB1>(ge);
    f32x4 res[2];
    float g2[2], g1[2];
    int a[4];
#pragma unroll
    for (int hf = 0; hf < 2; ++hf) {
      const float g = (lane & 1) == hf ? ge : go;
      const v2u xv = *(LAS v2u*)(L + B_BASE + B_SLOT + w * 1024 + hf * 512 + lane * 8);
      res[hf] = f32x4{g * (bf2f(xv[0] & 0xFFFFu) * bf2f(dv[0] & 0xFFFFu)), g * (bfhi(xv[0]) * bfhi(dv[0])),
                      g * (bf2f(xv[1] & 0xFFFFu) * bf2f(dv[1] & 0xFFFFu)), g * (bfhi(xv[1]) * bfhi(dv[1]))};
      g2[hf] = 2.f * g; g1[hf] = 1.f - g;
      a[2 * hf] = base + (((2 * (w & 1) + hf) ^ q) << 6); a[2 * hf + 1] = a[2 * hf] + 65536;
    }
    int oo = tok * 2048 + (lane & 15) * 16 + N * 256, oo2 = oo + 32768;
    asm volatile("s_waitcnt lgkmcnt(0)\n\ts_barrier"
                 : "+v"(res[0]), "+v"(res[1]), "+v"(g2[0]), "+v"(g2[1]), "+v"(g1[0]), "+v"(g1[1]), "+v"(a[0]), "+v"(a[1]), "+v"(a[2]), "+v"(a[3]), "+v"(oo),
                   "+v"(oo2), "+s"(pout) : : "memory");
    STAMP(10);
    f32x4 v[4][4];
#pragma unroll
    for (int z = 0; z < 4; ++z)
#pragma unroll
      for (int ww = 0; ww < 4; ++ww) v[z][ww] = *(LAS f32x4*)(L + a[z] + ww * 16384);
    SCHED();
    rsrc_t ro = mk_rsrc((const void*)pout, S_ * D_ * 4);
#pragma unroll
    for (int hf = 0; hf < 2; ++hf) {
      const f32x4 ss = (v[2 * hf][0] + v[2 * hf][1]) + (v[2 * hf][2] + v[2 * hf][3]);
      const f32x4 sg = (v[2 * hf + 1][0] + v[2 * hf + 1][1]) + (v[2 * hf + 1][2] + v[2 * hf + 1][3]);
      const f32x4 m = res[hf] + ss * g2[hf] + sg * g1[hf];
      __builtin_amdgcn_raw_buffer_store_b128(__builtin_bit_cast(v4u, m), ro, hf ? oo2 : oo, 0, OUT_AUX);
    }
  }
  STAMP(11);
}

#if defined(__HIP_DEVICE_COMPILE__)
#pragma clang attribute pop
#endif

extern "C" void kernel_launch(void* const* d_in, const int* in_sizes, int n_in, void* d_out, int out_size, void* d_ws, size_t ws_size,
                              hipStream_t stream) {
  Params p;
  p.x = (const u16*)d_in[0]; p.Lre = (const u16*)d_in[1]; p.Lim = (const u16*)d_in[2]; p.Bre = (const u16*)d_in[3];
  p.Bim = (const u16*)d_in[4]; p.Cre = (const u16*)d_in[5]; p.Cim = (const u16*)d_in[6]; p.ldt = (const u16*)d_in[7];
  p.Dsk = (const u16*)d_in[8]; p.Wg = (const u16*)d_in[10]; p.Wu = (const u16*)d_in[11]; p.Wd = (const u16*)d_in[12];
  p.gw = (const u16*)d_in[13]; p.out = (float*)d_out; p.ws = (unsigned char*)d_ws;
  (void)hipFuncSetAttribute((const void*)k_fused, hipFuncAttributeMaxDynamicSharedMemorySize, LDS_BYTES);
  k_fused<<<256, 512, LDS_BYTES, stream>>>(p);
}
```

```cpp
#define GPOLL 4
#define SP_FIRST 8
#define WARMM
#define WARM0 2
#define PRIO2
#include <hip/hip_runtime.h>
#include <stdint.h>

typedef unsigned short u16;
typedef unsigned long long u64;
typedef unsigned v4u __attribute__((ext_vector_type(4)));
typedef unsigned v2u __attribute__((ext_vector_type(2)));
typedef short s16x4 __attribute__((ext_vector_type(4)));
typedef __bf16 bf16x8 __attribute__((ext_vector_type(8)));
typedef __bf16 bf16x2 __attribute__((ext_vector_type(2)));
typedef float f32x2 __attribute__((ext_vector_type(2)));
typedef float f32x4 __attribute__((ext_vector_type(4)));
#define LAS __attribute__((address_space(3)))
#define GAS __attribute__((address_space(1)))
#define RLX_AGENT __ATOMIC_RELAXED, __HIP_MEMORY_SCOPE_AGENT
typedef __amdgpu_buffer_rsrc_t rsrc_t;

constexpr int S_ = 2048, D_ = 512, P_ = 512, F_ = 1792;
constexpr int NK = 88;
constexpr int AUX_SC1 = 16;
#define OUT_AUX 2
constexpr int WS_EPOCH = 0;
constexpr int WS_G = 8192;
constexpr int WS_FA2 = 65536;
constexpr int CTRL_BYTES = 131072;
constexpr int WS_CARRY = 131072;
constexpr int WS_A2 = 524288;
constexpr int A2_BYTES = 32 * NK * 4096;
constexpr int WS_STAMP = 32 << 20;
constexpr int LDS_BYTES = 160 * 1024;
constexpr int X_SLOT = 16384, W_BASE = 65536, W_SLOT = 16384, B_BASE = 131072, B_SLOT = 8192;
constexpr int LDS_DONE = 155648, LDS_PF = LDS_DONE + 32, LDS_MD = LDS_DONE + 48, LDS_CO = LDS_DONE + 64, LDS_TAG = LDS_DONE + 80;
constexpr int LDS_RDY = LDS_DONE + 96;
constexpr int LDS_DUMMY = LDS_DONE + 256, LDS_CONST = LDS_DONE + 512;
constexpr int OOB = 1 << 28;
constexpr int CODE_BYTES = 31 * 1024;
constexpr unsigned SPIN_CAP = 1u << 18;

struct Params {
  const u16 *x, *Wg, *Wu, *Bre, *Bim, *Cre, *Cim, *Wd;
  unsigned char* ws;
  const u16 *Lre, *Lim, *ldt, *Dsk, *gw;
  float* out;
};

#if defined(__HIP_DEVICE_COMPILE__)
#pragma clang attribute push(__attribute__((target("no-packed-fp32-ops"))), apply_to = function)
#endif
__device__ __forceinline__ float u2f(unsigned v) { return __builtin_bit_cast(float, v); }
__device__ __forceinline__ unsigned f2u(float v) { return __builtin_bit_cast(unsigned, v); }
__device__ __forceinline__ bool wall(bool p) { return __builtin_amdgcn_ballot_w64(!p) == 0; }
__device__ __forceinline__ bool wany(bool p) { return __builtin_amdgcn_ballot_w64(p) != 0; }
__device__ __forceinline__ int imin(int a, int b) { return a < b ? a : b; }
__device__ __forceinline__ int imax(int a, int b) { return a > b ? a : b; }
template <int CTRL> __device__ __forceinline__ float dpp(float v) { return u2f(__builtin_amdgcn_mov_dpp(f2u(v), CTRL, 0xF, 0xF, true)); }
__device__ __forceinline__ rsrc_t mk_rsrc(const void* p, int bytes) {
  return __builtin_amdgcn_make_buffer_rsrc((void*)p, 0, bytes, 0x00020000);
}
__device__ __forceinline__ float bf2f(unsigned v) { return u2f(v << 16); }
__device__ __forceinline__ float bfhi(unsigned v) { return u2f(v & 0xFFFF0000u); }
__device__ __forceinline__ unsigned pk_bf16(float lo, float hi) {
  f32x2 t = {lo, hi};
  return __builtin_bit_cast(unsigned, __builtin_convertvector(t, bf16x2));
}
__device__ __forceinline__ void mfma16t(f32x4& c, v4u a, v4u b) {
  asm volatile("v_mfma_f32_16x16x32_bf16 %0, %1, %2, %0" : "+v"(c) : "v"(a), "v"(b));
}
__device__ __forceinline__ v4u lds_rd128(LAS unsigned char* p) { return *(LAS v4u*)p; }
__device__ __forceinline__ v2u lds_rdtr(LAS unsigned char* p) {
  return __builtin_bit_cast(v2u, __builtin_amdgcn_ds_read_tr16_b64_v4i16((LAS s16x4*)p));
}
__device__ __forceinline__ v4u mk_rs(const void* p, unsigned bytes) {
  const u64 a = (u64)p;
  unsigned lo = (unsigned)a, hi = (unsigned)(a >> 32) & 0xFFFFu;
  asm volatile("" : "+s"(lo), "+s"(hi));
  return v4u{lo, hi, bytes, 0x00020000u};
}
template <int LI, int SI> __device__ __forceinline__ void glds(v4u rs, int lds, int voff, int soff) {
  int t;
  asm volatile("s_add_u32 m0, %1, %5\n\ts_add_u32 %0, %4, %6\n\tbuffer_load_dwordx4 %2, %3, %0 offen lds"
               : "=&s"(t) : "s"(lds), "v"(voff), "s"(rs), "s"(soff), "i"(LI), "i"(SI) : "memory", "scc");
}
typedef const __attribute__((address_space(4))) Params* KP;
__device__ __forceinline__ KP kargs() {
  KP k = (KP)__builtin_amdgcn_kernarg_segment_ptr();
  asm volatile("" : "+s"(k));
  return k;
}
__device__ __forceinline__ void warm(v4u rs, int voff, int soff) {
  asm volatile("s_mov_b32 m0, %3\n\ts_nop 0\n\tbuffer_load_dword %0, %1, %2 offen lds" : : "v"(voff), "s"(rs), "s"(soff), "i"(LDS_DUMMY) : "memory");
}
__device__ __forceinline__ v4u ld_sc1(rsrc_t rs, int voff, int soff) { return __builtin_amdgcn_raw_buffer_load_b128(rs, voff, soff, AUX_SC1); }
__device__ __forceinline__ void st_sc1(v4u v, rsrc_t rs, int voff, int soff) { __builtin_amdgcn_raw_buffer_store_b128(v, rs, voff + soff, 0, AUX_SC1); }
__device__ __forceinline__ u64 tag64(unsigned tag) { return ((u64)(~tag) << 32) | tag; }
__device__ __forceinline__ void st_flag(unsigned char* ws, int off, unsigned tag, int voff) {
  __builtin_amdgcn_raw_buffer_store_b64(v2u{tag, ~tag}, mk_rsrc(ws, CTRL_BYTES), voff, off, AUX_SC1);
}
__device__ __forceinline__ unsigned lds_word(LAS unsigned char* p) { return __builtin_amdgcn_readfirstlane(*(volatile LAS unsigned*)p); }
#define BARRIER() asm volatile("s_barrier" ::: "memory")
#define SCHED() __builtin_amdgcn_sched_barrier(0)
#define CFENCE() asm volatile("" ::: "memory")
#define WAIT_LDS() asm volatile("s_waitcnt lgkmcnt(0)" ::: "memory")
template <int N> __device__ __forceinline__ void wait_vm() { asm volatile("s_waitcnt vmcnt(%c0)" ::"i"(N) : "memory"); }

#define STAMP(i) do {} while (0)
#define PICK 0
#define SP_NEXT 8
#define PK6 3
#define POLL_SLEEP 4
#define POLL_IDLE 16

__device__ __forceinline__ void poll_flags(GAS u64* f, u64 want, bool care, bool late) {
#pragma nounroll
  for (unsigned spins = 0; spins < SPIN_CAP; ++spins) {
    CFENCE();
    const u64 v = __hip_atomic_load(f, RLX_AGENT);
    if (wall(!care || v == want)) break;
    if (wany(late && v == want)) __builtin_amdgcn_s_sleep(POLL_SLEEP); else __builtin_amdgcn_s_sleep(POLL_IDLE);
  }
  CFENCE();
}
__device__ __forceinline__ void csq(float& r, float& i, int n) {
#pragma unroll
  for (int z = 0; z < n; ++z) { const float t = r * r - i * i; i = 2.f * r * i; r = t; }
}

#define NROT 102
#define CONST_AT 2
constexpr int LASTB = 13;
#define RAWN 2
#define NRAWD 2
constexpr int raw_lo(int b) { const int n = (b - (LASTB + 1)) * RAWN + 8 * NRAWD; return n < 0 ? 0 : n; }
struct Sched {
  int n;
  int type[40], idx[40];
  int lo[LASTB + 1], hi[LASTB + 1];
  int wait[LASTB + 1];
  bool ok;
};
constexpr Sched make_sched(int kind) {
  Sched s{};
  int cnt[40] = {}, need[40] = {}, avail[40] = {};
  int n = 0;
  auto add = [&](int ty, int ix, int nd, int av) {
    if (kind && ty == 2) return;
    s.type[n] = ty; s.idx[n] = ix; cnt[n] = ty == 2 ? 4 : 3; need[n] = nd < LASTB ? nd : LASTB; avail[n] = av; ++n;
  };
  add(0, 0, 0, -1); add(1, 0, 0, -1); add(2, 0, 0, -1); add(2, 1, 0, -1);
  add(0, 1, 1, -1); add(1, 1, 1, -1); add(2, 2, 1, -1); add(2, 3, 2, -1);
  add(0, 2, 3, -1); add(1, 2, 3, -1);
  add(2, 4, 3, 1); add(2, 5, 4, 1);
  for (int b = 2; b < 12; ++b) {
    const int T = b / 2 + 2;
    add(b & 1, T, 2 * T - 1, kind ? (b & ~1) : b);
    add(2, b + 4, b + 3, b);
  }
  s.n = n;
  s.ok = true;
  const int LIM = 8;
  int endpos[40] = {};
  int next = 0, total = 0;
  for (int b = 0; b <= LASTB; ++b) {
    s.lo[b] = next;
    const int start = total;
    for (;;) {
      if (next >= n || avail[next] > b - 1) break;
      int lastneed = 0;
      for (int i = 0; i < next; ++i) if (need[i] <= b) lastneed = endpos[i];
      if (b < 3 && need[next] > b && total + cnt[next] - lastneed > LIM + 2 * b) break;
      if (b >= 3 && need[next] > b + 1 && total + cnt[next] - start > 11) break;
      total += cnt[next]; endpos[next] = total; ++next;
    }
    s.hi[b] = next;
    if (kind == 0) total += raw_lo(b + 1) - raw_lo(b);
    if (kind == 0 && b >= WARM0 && b < WARM0 + 3) total += 1;
    int lastneed = 0;
    for (int i = 0; i < next; ++i) if (need[i] <= b) lastneed = endpos[i];
    for (int i = next; i < n; ++i) if (need[i] <= b) s.ok = false;
    s.wait[b] = total - lastneed;
  }
  if (next != n) s.ok = false;
  return s;
}
constexpr Sched SCH[2] = {make_sched(0), make_sched(1)};
static_assert(SCH[0].ok && SCH[1].ok, "every ring item must be issued in time");

struct P2Ctx { u64 a0, a1; int so0, sh, g0, nd, rot; unsigned bytes; };
__device__ __forceinline__ P2Ctx p2_ctx(u64 Wd, u64 Cre, u64 Cim, int w, int N) {
  P2Ctx c;
  const int ssm = w < 4;
  const u64 m = 0 - (u64)ssm;
  c.a0 = (Cre & m) | (Wd & ~m); c.a1 = (Cim & m) | (Wd & ~m);
  c.sh = ssm;
  c.so0 = N * 128 + ssm * (N * (65536 - 128) - 14 * 128);
  c.bytes = F_ * D_ * 2u - ssm * (F_ * D_ * 2u - D_ * P_ * 2u);
  c.g0 = 7 * w - 28 + ssm * (56 - 3 * w);
  c.nd = 7 - 3 * ssm;
#define P2ROT 1
  c.rot = P2ROT == 0 ? 0 : (P2ROT == 1 ? (N & 1) * (3 - ssm) : (P2ROT == 2 ? (N & 3) * (2 - ssm) : (N * c.nd) >> 3));
  return c;
}
__device__ __forceinline__ int p2_ds(const P2Ctx& c, int d) { const int t = d + c.rot; return t >= c.nd ? t - c.nd : t; }
__device__ __forceinline__ void b2_src(const P2Ctx& c, int d, v4u& rs, int& so) {
  const int g = c.g0 + p2_ds(c, d);
  const u64 x0 = c.a0, x1 = c.a1;
  rs = mk_rs((const void*)((g & 1) ? x1 : x0), c.bytes);
  so = c.so0 + (c.sh ? (g >> 1) * 128 : g * 65536);
}
template <int LI, int N0, int N1> __device__ __forceinline__ void b2_rows(v4u rs, int lds, int vb, int so) {
  if constexpr (N0 < N1) { glds<LI, N0 * 8192>(rs, lds, vb, so); b2_rows<LI + 1024, N0 + 1, N1>(rs, lds, vb, so); }
}
template <int LI, int N0, int N1> __device__ __forceinline__ void b2_load(const P2Ctx& c, int vb, int d, int lds) {
  v4u rs; int so;
  b2_src(c, d, rs, so);
  b2_rows<LI, N0, N1>(rs, lds, vb, __builtin_amdgcn_readfirstlane(so));
}
__device__ __forceinline__ void b2_read(bool ssm, LAS unsigned char* img, int lane, v4u (&f)[2][4]) {
  const int q = lane >> 4;
  if (ssm) {
    const int i = lane & 15;
#pragma unroll
    for (int h = 0; h < 2; ++h)
#pragma unroll
      for (int e = 0; e < 4; ++e) f[h][e] = lds_rd128(img + i * 128 + (((4 * h + q) ^ (((i >> 1) & 3) << 1)) << 4) + e * 2048);
  } else {
    const int qq = (lane >> 2) & 3, s = 2 * (q & 1) + (qq >> 1);
#pragma unroll
    for (int h = 0; h < 2; ++h)
#pragma unroll
      for (int e = 0; e < 4; ++e) {
        LAS unsigned char* a = img + (4 * q + qq) * 128 + ((e ^ s) << 5) + (lane & 3) * 8 + h * 4096;
        const v2u lo = lds_rdtr(a), hi = lds_rdtr(a + 2048);
        f[h][e] = v4u{lo[0], lo[1], hi[0], hi[1]};
      }
  }
}

__device__ __forceinline__ int col_block(int r, u64 Wd) {
  asm volatile("" : "+s"(r));
  const int nslow = (3 - (int)(Wd >> 7)) & 7, n = (r - (r >> 3)) >> 2;
  return (r & 7) == 7 ? nslow : n + (n >= nslow);
}

struct LdCtx { v4u rx, rw, rb; u64 Wd, Cre, Cim; int lw, lb, vx, vw0, vw1, vbb, rot, lane, vb, vwarm, xcd, ch, r; mutable unsigned sv, lrb, lib, ldb, ep; };
__device__ __forceinline__ void warm_chunk(u64 x0, u64 x1, u64 x2, int xc, int vwarm, int slot) {
  asm volatile("" : "+s"(xc));
  int i = slot < 6 ? slot + xc : slot - 6 + xc;
  if (slot < 6) { i = i >= 12 ? i - 12 : (i >= 6 ? i - 6 : i); } else { i = 6 + (i >= 10 ? i - 10 : (i >= 5 ? i - 5 : i)); }
  const u64 src = i < 7 ? x0 : (i < 9 ? x1 : x2);
  const unsigned bytes = (unsigned)slot > 10u ? 0u : (i < 7 ? F_ * D_ * 2 : D_ * P_ * 2);
  warm(mk_rs((const void*)src, bytes), vwarm, (i < 7 ? i : ((i - 7) & 1)) * 262144);
}

template <int KIND, int I> __device__ __forceinline__ void ld_item(const LdCtx& c) {
  constexpr int ty = SCH[KIND].type[I], t = SCH[KIND].idx[I];
  int rot = c.rot;
  asm volatile("" : "+s"(rot));
  if constexpr (ty < 2) {
    const int k0 = ((t + rot) & 7) * 128;
    glds<(t % 3) * X_SLOT + 2 * ty * 4096, ty * 65536>(c.rx, c.lw * 1024, c.vx, k0);
    glds<(t % 3) * X_SLOT + (2 * ty + 1) * 4096, ty * 65536 + 8192>(c.rx, c.lw * 1024, c.vx, k0);
    glds<B_BASE + (t % 3) * B_SLOT + ty * 2048, ty * 16384>(c.rb, c.lb, c.vbb, k0);
  } else {
    const int k0 = (((t >> 1) + rot) & 7) * (64 * F_ * 2);
    constexpr int LI = W_BASE + (t & 3) * W_SLOT, SI = (t & 1) * 32 * F_ * 2;
    glds<LI, SI>(c.rw, c.lw * 4096, c.vw0, k0);
    glds<LI + 1024, SI + 4 * F_ * 2>(c.rw, c.lw * 4096, c.vw0, k0);
    glds<LI + 2048, SI + 8 * F_ * 2>(c.rw, c.lw * 4096, c.vw1, k0);
    glds<LI + 3072, SI + 12 * F_ * 2>(c.rw, c.lw * 4096, c.vw1, k0);
  }
}
template <int KIND, int LO, int HI> __device__ __forceinline__ void ld_items(const LdCtx& c) {
  if constexpr (LO < HI) { ld_item<KIND, LO>(c); ld_items<KIND, LO + 1, HI>(c); }
}
template <int A, int E> __device__ __forceinline__ void raw_loads(const P2Ctx& pc, int vb, v4u (&raw)[8 * NRAWD]) {
  if constexpr (A < E) {
    v4u rs; int so;
    b2_src(pc, 4 + (A >> 3), rs, so);
    asm volatile("buffer_load_dwordx4 %0, %1, %2, %3 offen" : "=v"(raw[A]) : "v"(vb), "s"(rs), "s"(so + (A & 7) * 8192) : "memory");
    raw_loads<A + 1, E>(pc, vb, raw);
  }
}
template <int KIND, int B> __device__ __forceinline__ void loader_steps(const LdCtx& c, LAS unsigned char* L, P2Ctx& pc, v4u (&raw)[8 * NRAWD]) {
  if constexpr (B == 0) {
    constexpr int FIRST = KIND ? 2 : 3;
    ld_items<KIND, 0, FIRST>(c);
    KP kp = kargs();
    const v4u r0 = mk_rs(kp->Lre, P_ * 2), r1 = mk_rs(kp->Lim, P_ * 2), r2 = mk_rs(kp->ldt, P_ * 2), r3 = mk_rs(kp->ws + WS_EPOCH, c.lw == 3 ? 4 : 0);
    asm volatile("buffer_load_ushort %0, %4, %5, 0 offen\n\tbuffer_load_ushort %1, %4, %6, 0 offen\n\tbuffer_load_ushort %2, %4, %7, 0 offen\n\t"
                 "buffer_load_dword %3, off, %8, 0 sc1"
                 : "=&v"(c.lrb), "=&v"(c.lib), "=&v"(c.ldb), "=&v"(c.ep) : "v"(c.ch * 2), "s"(r0), "s"(r1), "s"(r2), "s"(r3) : "memory");
    if constexpr (KIND == 0) {
      ld_items<0, 3, 5>(c);
      wait_vm<7>();
      BARRIER();
      ld_items<0, 5, SCH[0].hi[0]>(c);
    } else {
      ld_items<KIND, FIRST, SCH[KIND].hi[0]>(c);
    }
  } else {
    ld_items<KIND, SCH[KIND].lo[B], SCH[KIND].hi[B]>(c);
  }
  if constexpr (B == 0) pc = p2_ctx(c.Wd, c.Cre, c.Cim, c.lw + 4, col_block(c.r, c.Wd));
  if constexpr (KIND == 0) raw_loads<raw_lo(B), raw_lo(B + 1)>(pc, c.vb, raw);
  if constexpr (KIND == 1 && (B & 1) == 0 && B >= 6) raw_loads<2 * (B - 6), 2 * (B - 6) + 4>(pc, c.vb, raw);
  if constexpr (KIND == 0 && B == WARM0) warm(mk_rs((const void*)c.Wd, F_ * D_ * 2), c.lane < 16 ? (c.vwarm * 16 + c.lane) * 128 : OOB, 0);
  if constexpr (KIND == 0 && B == WARM0 + 1) warm(mk_rs((const void*)c.Cre, D_ * P_ * 2), c.lane < 5 ? (c.vwarm * 5 + c.lane) * 128 : OOB, 0);
  if constexpr (KIND == 0 && B == WARM0 + 2) warm(mk_rs((const void*)c.Cim, D_ * P_ * 2), c.lane < 5 ? (c.vwarm * 5 + c.lane) * 128 : OOB, 0);
  if constexpr (B == CONST_AT) {
    asm volatile("" : "+v"(c.lrb), "+v"(c.lib), "+v"(c.ldb), "+v"(c.ep) : : "memory");
    const float lr = bf2f(c.lrb), li = bf2f(c.lib);
    float dt = __builtin_amdgcn_exp2f(bf2f(c.ldb) * 1.44269504088896341f);
    dt = bf2f(pk_bf16(dt, 0.f) & 0xFFFFu);
    const float th = dt * li, rv = th * 0.15915494f;
    const float rev = (rv - __builtin_rintf(rv)) + __builtin_fmaf(th, 6.42063833e-9f, __builtin_fmaf(th, 0.15915494f, -rv));
    const float ex = __builtin_amdgcn_exp2f(dt * lr * 1.44269504088896341f);
    const float ar = ex * __builtin_amdgcn_cosf(rev), ai = ex * __builtin_amdgcn_sinf(rev);
    const float den = __builtin_amdgcn_rcpf(lr * lr + li * li);
    float p16r = ar, p16i = ai;
    csq(p16r, p16i, 4);
    float p32r = p16r, p32i = p16i;
    csq(p32r, p32i, 1);
    LAS f32x4* d = (LAS f32x4*)(L + LDS_CONST + c.lw * 512 + (c.lane & 15) * 32);
    d[0] = f32x4{ar, ai, ((ar - 1.f) * lr + ai * li) * den, (ai * lr - (ar - 1.f) * li) * den};
    d[1] = f32x4{p16r, p16i, p32r, p32i};
    *(volatile LAS unsigned*)(L + LDS_TAG + 4 * (3 - c.lw)) = c.ep + 1u;
    WAIT_LDS();
  }
  SCHED();
  wait_vm<SCH[KIND].wait[B]>();
  BARRIER();
  if constexpr (B < LASTB) loader_steps<KIND, B + 1>(c, L, pc, raw);
}

__device__ __forceinline__ bool all4(LAS unsigned char* p) {
  const v4u d = *(volatile LAS v4u*)p;
  return __builtin_amdgcn_readfirstlane(d[0] & d[1] & d[2] & d[3]) != 0u;
}
__device__ __forceinline__ void lds_wait4(LAS unsigned char* p) {
#pragma nounroll
  for (unsigned spins = 0; spins < (SPIN_CAP << 4); ++spins) {
    if (all4(p)) break;
    __builtin_amdgcn_s_sleep(2);
  }
  CFENCE();
}
__device__ __forceinline__ void lds_wait(LAS unsigned char* word) {
#pragma nounroll
  for (unsigned spins = 0; spins < (SPIN_CAP << 4); ++spins) {
    if (lds_word(word) != 0u) break;
    __builtin_amdgcn_s_sleep(2);
  }
  CFENCE();
}
__device__ __forceinline__ void lds_wait_eq(LAS unsigned char* word, unsigned want) {
#pragma nounroll
  for (unsigned spins = 0; spins < (SPIN_CAP << 4); ++spins) {
    if (lds_word(word) == want) break;
    __builtin_amdgcn_s_sleep(2);
  }
  CFENCE();
}
__device__ __forceinline__ void cmul(float ar, float ai, float br, float bi, float& cr, float& ci) {
  cr = ar * br - ai * bi; ci = ar * bi + ai * br;
}

template <int V> struct IC { static constexpr int value = V; };
template <int n> __device__ __forceinline__ void p2_first(v4u (&af)[8], v4u ra, int va, int o0, int o1) {
  asm volatile("buffer_load_dwordx4 %0, %1, %2, %3 offen offset:%c4 sc1" : "=v"(af[n]) : "v"(va), "s"(ra), "s"(n < 4 ? o0 : o1), "i"((n & 3) * 1024) : "memory");
  if constexpr (n < 7) p2_first<n + 1>(af, ra, va, o0, o1);
}
template <int d, int n>
__device__ __forceinline__ void p2_step(f32x4 (&acc)[4][4], const v4u (&bb)[2][4], v4u (&af)[8], v4u ra, int va, int o0, int o1, int w) {
  constexpr int WG = d == 6 ? 7 - n : 7, WS = d == 5 ? 7 - n : 7;
  if constexpr (d >= 3 && d <= 5 && WS < WG)
    asm volatile("s_waitcnt vmcnt(%c2)\n\ts_cmp_gt_u32 %1, 3\n\ts_cbranch_scc1 1f\n\ts_waitcnt vmcnt(%c3)\n1:" : "+v"(af[n]) : "s"(w), "i"(WG), "i"(WS) : "memory", "scc");
  else
    asm volatile("s_waitcnt vmcnt(%c1)" : "+v"(af[n]) : "i"(WG) : "memory");
#pragma unroll
  for (int e = 0; e < 4; ++e) mfma16t(acc[e][n & 3], bb[n >> 2][e], af[n]);
  if constexpr (d < 5)
    asm volatile("buffer_load_dwordx4 %0, %1, %2, %3 offen offset:%c4 sc1" : "+v"(af[n]) : "v"(va), "s"(ra), "s"(n < 4 ? o0 : o1), "i"((n & 3) * 1024) : "memory");
  if constexpr (d == 5)
    asm volatile("s_cmp_lt_u32 %5, 4\n\ts_cbranch_scc1 1f\n\tbuffer_load_dwordx4 %0, %1, %2, %3 offen offset:%c4 sc1\n1:"
                 : "+v"(af[n]) : "v"(va), "s"(ra), "s"(n < 4 ? o0 : o1), "i"((n & 3) * 1024), "s"(w) : "memory", "scc");
  if constexpr (n < 7) p2_step<d, n + 1>(acc, bb, af, ra, va, o0, o1, w);
}

__global__ __launch_bounds__(512, 2) void k_fused(Params) {
  extern __shared__ __attribute__((aligned(16))) unsigned char lds_[];
  LAS unsigned char* L = (LAS unsigned char*)lds_;
  STAMP(0);
  KP kp = kargs();
  u64 pX = (u64)kp->x, pWg = (u64)kp->Wg, pWu = (u64)kp->Wu, pBre = (u64)kp->Bre, pBim = (u64)kp->Bim, pCre = (u64)kp->Cre, pCim = (u64)kp->Cim, pWd = (u64)kp->Wd;
  const int b = __builtin_amdgcn_workgroup_id_x(), tid = __builtin_amdgcn_workitem_id_x(), lane = tid & 63;
  const int w = __builtin_amdgcn_readfirstlane(tid >> 6);
  const int xcd = b & 7, r = b >> 3;
  warm(mk_rs((const void*)(__builtin_amdgcn_s_getpc() & ~(u64)127), CODE_BYTES), lane < 2 ? lane * 32768 : OOB, (r * 8 + w) * 128);
  const bool light = (r & 7) == 7;
  const int ih = 28 * (xcd & 3) + r - (r >> 3), il = 112 + 4 * (xcd & 3) + (r >> 3);
  const int idx = ih + (il - ih) * (int)light;
  const int pn = 8 * (xcd >> 2) + (idx & 7), cb = idx >> 3;
  const int rot = pn & 6;
  asm volatile("" : "+s"(pX), "+s"(pWg), "+s"(pWu), "+s"(pBre), "+s"(pBim), "+s"(pCre), "+s"(pCim), "+s"(pWd));
  const int vb = (lane >> 3) * 1024 + (((lane & 7) ^ (((lane >> 4) & 3) << 1)) << 4);
  v4u b2[4][2][4];
  unsigned tag = 0;
  P2Ctx pc;

  if (__builtin_expect(w >= 4, 1)) {
    LdCtx c;
    c.lw = w - 4; c.rot = rot; c.lane = lane; c.vb = vb; c.sv = 0;
    c.Wd = pWd; c.Cre = pCre; c.Cim = pCim;
    c.vwarm = ((r - (r >> 3)) * 8 + xcd) * 4 + c.lw;
    c.xcd = xcd; c.r = r; c.ch = 32 * cb + 16 * (c.lw >> 1) + (lane & 15);
    const int l3 = lane >> 3, kq = lane >> 4;
    c.rx = mk_rs((const void*)pX, S_ * D_ * 2);
    c.vx = (32 * (c.lw & 1) + 16 * (l3 >> 2) + 4 * (c.lw >> 1) + (l3 & 3)) * 1024 + (((lane & 7) ^ ((4 * c.lw + kq) & 7)) << 4) + pn * 128 * 1024;
    c.rb = mk_rs((const void*)((c.lw >> 1) ? pBim : pBre), P_ * D_ * 2);
    c.vbb = (8 * (c.lw & 1) + l3) * 1024 + (((lane & 7) ^ (4 * (c.lw & 1) + kq)) << 4) + cb * 32 * 1024;
    c.lb = (c.lw >> 1) * 4096 + (c.lw & 1) * 1024;
    c.rw = mk_rs((const void*)((c.lw >> 1) ? pWu : pWg), D_ * F_ * 2);
    c.vw0 = ((c.lw & 1) * 16 + kq) * (F_ * 2) + (((lane & 15) ^ (kq << 2)) << 4) + cb * 256;
    c.vw1 = c.vw0 ^ 32;
    STAMP(1);
    v4u raw[8 * NRAWD];
    if (__builtin_expect(!light, 1)) loader_steps<0, 0>(c, L, pc, raw);
    else { loader_steps<1, 0>(c, L, pc, raw); BARRIER(); }
    STAMP(2);
    {
      const int wA = c.lw < 2 ? w : c.lw, N = col_block(r, pWd);
      pc = p2_ctx(pWd, pCre, pCim, w, N);
      b2_load<0, 0, 8>(pc, vb, 0, wA * 16384);
      b2_load<8192, 0, 8>(pc, vb, 1, wA * 16384);
      const P2Ctx pp = p2_ctx(pWd, pCre, pCim, c.lw, N);
#pragma unroll
      for (int d = 0; d < 4; ++d) {
        wait_vm<8>();
        b2_read(false, L + wA * 16384 + (d & 1) * 8192, lane, b2[d]);
        WAIT_LDS();
        if (d == 2) lds_wait4(L + LDS_MD);
        if (d < 2) b2_load<0, 0, 8>(pc, vb, d + 2, wA * 16384 + (d & 1) * 8192);
        else b2_load<0, 0, 8>(pp, vb, d & 1, c.lw * 16384 + (d & 1) * 8192);
      }
      STAMP(3);
#pragma unroll
      for (int z = 0; z < 16; z += 8)
        asm volatile("" : "+v"(raw[z]), "+v"(raw[z + 1]), "+v"(raw[z + 2]), "+v"(raw[z + 3]), "+v"(raw[z + 4]), "+v"(raw[z + 5]), "+v"(raw[z + 6]), "+v"(raw[z + 7]));
#pragma unroll
      for (int n = 0; n < 16; ++n) *(LAS v4u*)(L + w * 16384 + n * 1024 + lane * 16) = raw[n];
      wait_vm<0>();
      *(volatile LAS unsigned*)(L + LDS_PF + 4 * c.lw) = 1u;
      STAMP(4);
      lds_wait(L + LDS_CO + 4 * c.lw);
      b2_load<0, 0, 8>(pp, vb, 2, c.lw * 16384);
      b2_load<8192, 0, 8>(pp, vb, 3, c.lw * 16384);
      wait_vm<0>();
      *(volatile LAS unsigned*)(L + LDS_PF + 4 * c.lw) = 2u;
      STAMP(5);
    }
    tag = lds_word(L + LDS_TAG);
  } else {
    const int c = w, q = lane >> 4, i15 = lane & 15, G = c & 1;
    const int ch = 32 * cb + 16 * (c >> 1) + i15;
    if (w == 3) {
#pragma unroll
      for (int z = 0; z < 5; ++z) *(volatile LAS v4u*)(L + LDS_DONE + 16 * z) = v4u{0u, 0u, 0u, 0u};
      *(volatile LAS unsigned*)(L + LDS_RDY) = 0u;
      *(volatile LAS v4u*)(L + LDS_RDY + 16) = v4u{0u, 0u, 0u, 0u};
      WAIT_LDS();
    }
    f32x4 k0 = {0.f, 0.f, 0.f, 0.f}, k1 = {0.f, 0.f, 0.f, 0.f};
    unsigned tagv = 0;
    auto rd_const = [&]() {
      k0 = *(LAS f32x4*)(L + LDS_CONST + c * 512 + i15 * 32); k1 = *(LAS f32x4*)(L + LDS_CONST + c * 512 + i15 * 32 + 16);
      tagv = *(volatile LAS unsigned*)(L + LDS_TAG);
    };
    u64 pws = (u64)kp->ws;
    asm volatile("" : "+s"(pws));
    int xa[2];
#pragma unroll
    for (int ks = 0; ks < 2; ++ks) xa[ks] = i15 * 128 + (((4 * ks + q) ^ ((i15 >> 1) & 7)) << 4);
    asm volatile("" : "+v"(xa[0]), "+v"(xa[1]));
    const int bo = B_BASE + (c >> 1) * 2048;
    f32x4 accs[2][4], acc[4][8];
    v4u bfr[2];
    auto zero_accs = [&]() {
#pragma unroll
      for (int m = 0; m < 2; ++m)
#pragma unroll
        for (int t = 0; t < 4; ++t) { accs[m][t] = f32x4{0.f, 0.f, 0.f, 0.f}; asm volatile("" : "+v"(accs[m][t])); }
    };
    float vr[16], vi[16], t0 = 0.f, t1 = 0.f;
    auto scan_half = [&](int n) {
      const int z = n >> 1;
      if (z == 0) { vr[0] = accs[0][0][0]; vi[0] = accs[1][0][0]; }
      else if ((n & 1) == 0) { t0 = __builtin_fmaf(k0[0], vr[z - 1], accs[0][z >> 2][z & 3]); t1 = __builtin_fmaf(k0[0], vi[z - 1], accs[1][z >> 2][z & 3]); }
      else { vr[z] = __builtin_fmaf(-k0[1], vi[z - 1], t0); vi[z] = __builtin_fmaf(k0[1], vr[z - 1], t1); }
    };
    auto sc = [](int v) { asm volatile("" : "+s"(v)); return v; };

    if (!light) {
      int wa[2];
      {
        const int qq = i15 >> 2, pp = lane & 3;
#pragma unroll
        for (int u = 0; u < 2; ++u)
          wa[u] = W_BASE + (8 * q + qq) * 256 + (((2 * (2 * c + u) + (pp >> 1)) ^ ((qq << 2) | ((q & 1) << 1))) << 4) + 8 * (pp & 1);
      }
      asm volatile("" : "+v"(wa[0]), "+v"(wa[1]));
#pragma unroll
      for (int a = 0; a < 4; ++a)
#pragma unroll
        for (int u = 0; u < 8; ++u) { acc[a][u] = f32x4{0.f, 0.f, 0.f, 0.f}; asm volatile("" : "+v"(acc[a][u])); }
      zero_accs();
      v4u xf[4];
      v2u wf[2][8];
      auto rdw = [&](int s, int z) {
        const int m = z >> 2, u = (z >> 1) & 1, t = z & 1;
        wf[s & 1][z] = lds_rdtr(L + wa[u] + (s & 3) * W_SLOT + m * 8192 + t * 1024);
      };
      STAMP(1);
      BARRIER();
      STAMP(2);
#pragma unroll
      for (int z = 0; z < 8; ++z) rdw(0, z);
#pragma unroll
      for (int z = 0; z < 3; ++z) xf[z] = lds_rd128(L + xa[0] + G * 8192 + z * 2048);
#pragma unroll
      for (int m = 0; m < 2; ++m) bfr[m] = lds_rd128(L + xa[0] + bo + m * 4096);
      auto glu4 = [&](int s, int u, const v4u& x, auto&& between) {
#pragma unroll
        for (int a = 0; a < 4; ++a) {
          const v4u wv = {wf[s & 1][2 * a][0], wf[s & 1][2 * a][1], wf[s & 1][2 * a + 1][0], wf[s & 1][2 * a + 1][1]};
          mfma16t(acc[a][u], wv, x);
          SCHED();
          between(a);
          SCHED();
        }
      };
      int xb = 0, bb = 0;
      auto steps = [&](int s0, int n, const int (&xo)[5]) {
#pragma unroll
        for (int j = 0; j < n; ++j)
#pragma unroll
          for (int u = 0; u < 8; ++u) {
            const int s = s0 + j, jn = j + ((u + 3) >> 3), un = (u + 3) & 7, uw = s == 0 ? u - 4 : u;
            SCHED();
            if (u == (s == 0 ? 4 : 0)) { BARRIER(); SCHED(); }
            glu4(s, u, xf[u & 3], [&](int a) {
              if (a == 0) {
                if ((un & 3) == 0 || (j == 0 && u == 0)) xb = xa[(s0 + jn) & 1] + sc(xo[jn] + ((un >> 2) ^ G) * 8192);
                xf[(u + 3) & 3] = lds_rd128(L + xb + (un & 3) * 2048);
              }
              if ((a == 1 || a == 2) && uw >= 0 && uw < 4) rdw(s + 1, 2 * uw + a - 1);
              if (a == 3 && (u == 4 || u == 5)) {
                if (u == 4) bb = xa[(s + 1) & 1] + sc(bo + (xo[j + 1] >> 1));
                bfr[u - 4] = lds_rd128(L + bb + (u - 4) * 4096);
              }
            });
            if (u < 4) { mfma16t(accs[0][u], xf[u & 3], bfr[0]); mfma16t(accs[1][u], xf[u & 3], bfr[1]); }
          }
      };
      {
        const int xo[5] = {0, 0, 0, 0, 0};
        steps(0, 1, xo);
      }
      int xo0 = 0;
#pragma nounroll
      for (int o = 0; o < 3; ++o) {
        const int xo1 = xo0 == 2 * X_SLOT ? 0 : xo0 + X_SLOT, xo2 = xo1 == 2 * X_SLOT ? 0 : xo1 + X_SLOT;
        const int xo[5] = {xo0, xo1, xo1, xo2, xo2};
        steps(1, 4, xo);
        xo0 = xo2;
      }
      {
        const int xo[5] = {0, X_SLOT, 0, 0, 0};
        steps(1, 1, xo);
      }
      v4u bf2[2];
#pragma unroll
      for (int it = 0; it < 16; ++it) {
        const int s = (it >> 2) & 1, u = (it & 3) + 4 * (it >> 3), nx = it + 3;
        SCHED();
        glu4(s, u, xf[it & 3], [&](int a) {
          if (a == 0 && nx < 16) {
            if ((nx & 3) == 0) xb = xa[(nx >> 2) & 1] + sc(X_SLOT + ((nx >> 3) ^ G) * 8192);
            xf[nx & 3] = lds_rd128(L + xb + (nx & 3) * 2048);
          }
          if (a >= 1 && it < 3 && 3 * it + a - 1 < 8) rdw(3, 3 * it + a - 1);
          if (it == 0 && a == 3) { bb = xa[1] + sc(bo + B_SLOT); bf2[0] = lds_rd128(L + bb); bf2[1] = lds_rd128(L + bb + 4096); }
          if (it == 4 && a == 1) rd_const();
          if (it >= 8) scan_half(4 * (it - 8) + a);
        });
        if (it < 4) { mfma16t(accs[0][u], xf[it & 3], bfr[0]); mfma16t(accs[1][u], xf[it & 3], bfr[1]); }
        else if (it < 8) { mfma16t(accs[0][u], xf[it & 3], bf2[0]); mfma16t(accs[1][u], xf[it & 3], bf2[1]); }
      }
      asm volatile("s_nop 15" : "+v"(acc[0][7]), "+v"(acc[1][7]), "+v"(acc[2][7]), "+v"(acc[3][7]), "+v"(acc[0][6]), "+v"(acc[1][6]), "+v"(acc[2][6]), "+v"(acc[3][6]));
    } else {
      {
      kp = kargs();
      rsrc_t rx = mk_rsrc(kp->x, S_ * D_ * 2), rg = mk_rsrc(kp->gw, D_ * 2), rgo = mk_rsrc(kp->ws + WS_G, S_ * 4);
      const int tk0 = 128 * pn + 64 * (cb - 14) + 16 * c;
      v4u gv[8], xv[2][8];
#pragma unroll
      for (int z = 0; z < 8; ++z) gv[z] = __builtin_amdgcn_raw_buffer_load_b128(rg, (lane & 7) * 16, z * 128, 0);
#pragma unroll
      for (int gi = 0; gi < 2; ++gi)
#pragma unroll
        for (int z = 0; z < 8; ++z) xv[gi][z] = __builtin_amdgcn_raw_buffer_load_b128(rx, (lane >> 3) * 1024 + (lane & 7) * 16, (tk0 + gi * 8) * 1024 + z * 128, 0);
#pragma unroll
      for (int gi = 0; gi < 2; ++gi) {
        float s = 0.f;
#pragma unroll
        for (int z = 0; z < 8; ++z)
#pragma unroll
          for (int y = 0; y < 4; ++y) {
            const unsigned xa_ = xv[gi][z][y], ga = gv[z][y];
            s = __builtin_amdgcn_fdot2_f32_bf16(__builtin_bit_cast(bf16x2, xa_), __builtin_bit_cast(bf16x2, ga), s, false);
          }
        s += dpp<0xB1>(s); s += dpp<0x4E>(s); s += dpp<0x141>(s);
        const float g = __builtin_amdgcn_rcpf(1.f + __builtin_amdgcn_exp2f(s * -1.44269504088896341f));
        __builtin_amdgcn_raw_buffer_store_b32(f2u(g), rgo, (lane & 7) ? OOB : (lane >> 3) * 4, (tk0 + gi * 8) * 4, AUX_SC1);
      }
      }
      STAMP(1);
      zero_accs();
      int xo = 0;
#pragma nounroll
      for (int T = 0; T < 8; ++T) {
#pragma unroll
        for (int ks = 0; ks < 2; ++ks) {
          if (T < 7) BARRIER();
          v4u xf[4];
#pragma unroll
          for (int t = 0; t < 4; ++t) xf[t] = lds_rd128(L + (xa[ks] + xo + G * 8192) + t * 2048);
#pragma unroll
          for (int m = 0; m < 2; ++m) bfr[m] = lds_rd128(L + (xa[ks] + bo + (xo >> 1)) + m * 4096);
#pragma unroll
          for (int t = 0; t < 4; ++t) { mfma16t(accs[0][t], xf[t], bfr[0]); mfma16t(accs[1][t], xf[t], bfr[1]); }
          SCHED();
        }
        xo = xo == 2 * X_SLOT ? 0 : xo + X_SLOT;
      }
      asm volatile("s_nop 15" : "+v"(accs[0][0]), "+v"(accs[1][0]), "+v"(accs[0][1]), "+v"(accs[1][1]), "+v"(accs[0][2]), "+v"(accs[1][2]), "+v"(accs[0][3]), "+v"(accs[1][3]));
      rd_const();
      BARRIER();
#pragma unroll
      for (int n = 0; n < 32; ++n) { scan_half(n); SCHED(); }
    }
    *(volatile LAS unsigned*)(L + LDS_MD + 4 * w) = 1u;
    SCHED();
    STAMP(3);
    tag = __builtin_amdgcn_readfirstlane(tagv);
    const float ar = k0[0], ai = k0[1], cr = k0[2], ci = k0[3], p16r = k1[0], p16i = k1[1], p32r = k1[2], p32i = k1[3];
    __builtin_amdgcn_s_setprio(2);
    rsrc_t ra2 = mk_rsrc((const void*)(pws + WS_A2), A2_BYTES), rcar = mk_rsrc((const void*)(pws + WS_CARRY), 32 * P_ * 16);
    const int ck = 2 * pn + G;
    float pr, pi;
    {
      float tr, ti;
      const auto xr = __builtin_amdgcn_permlane16_swap(f2u(vr[15]), f2u(vr[15]), false, false);
      const auto xi = __builtin_amdgcn_permlane16_swap(f2u(vi[15]), f2u(vi[15]), false, false);
      const float e0r = u2f(xr[0]), e1r = u2f(xr[1]), e0i = u2f(xi[0]), e1i = u2f(xi[1]);
      cmul(e0r, e0i, p16r, p16i, tr, ti); tr += e1r; ti += e1i;
      const auto yr = __builtin_amdgcn_permlane32_swap(f2u(tr), f2u(tr), false, false);
      const auto yi = __builtin_amdgcn_permlane32_swap(f2u(ti), f2u(ti), false, false);
      const float f0r = u2f(yr[0]), f1r = u2f(yr[1]), f0i = u2f(yi[0]), f1i = u2f(yi[1]);
      float Er, Ei;
      cmul(f0r, f0i, p32r, p32i, Er, Ei); Er += f1r; Ei += f1i;
      const float br = (q & 2) ? f0r : 0.f, bi = (q & 2) ? f0i : 0.f;
      cmul(br, bi, p16r, p16i, tr, ti);
      pr = (q & 1) ? tr + e0r : br; pi = (q & 1) ? ti + e0i : bi;
      const v4u gq = {f2u(Er), tag, f2u(Ei), ~tag};
      st_sc1(gq, rcar, q ? OOB : ch * 16, (31 - ck) * (P_ * 16));
    }
    asm volatile("" : "+v"(pr), "+v"(pi) : : "memory");
    SCHED();
    STAMP(4);
    v4u cv[8];
    const int cvo = ch * 16 + (32 - ck + 8 * q) * (P_ * 16);
    auto ld_carry = [&]() {
#pragma unroll
      for (int z = 0; z < 8; ++z) cv[z] = ld_sc1(rcar, cvo, z * (P_ * 16));
    };
    auto publish = [&](int part) {
      *(volatile LAS unsigned*)(L + LDS_DONE + 16 * part + 4 * w) = 1u;
      const bool last = all4(L + LDS_DONE + 16 * part);
      st_flag((unsigned char*)pws, WS_FA2 + part * 32768 + pn * 128 + cb * 8, tag, last && lane < 16 ? lane * 2048 : OOB);
      CFENCE();
    };
    if (!light) {
#pragma unroll
      for (int u = 0; u < 8; ++u) {
        float hv[8];
#pragma unroll
        for (int j = 0; j < 8; ++j) hv[j] = acc[j >> 2][u][j & 3] * -1.44269504088896341f;
        SCHED();
#pragma unroll
        for (int j = 0; j < 8; ++j) hv[j] = __builtin_amdgcn_exp2f(hv[j]);
        SCHED();
#pragma unroll
        for (int j = 0; j < 8; ++j) hv[j] += 1.f;
        SCHED();
#pragma unroll
        for (int j = 0; j < 8; ++j) hv[j] = __builtin_amdgcn_rcpf(hv[j]);
        SCHED();
#pragma unroll
        for (int j = 0; j < 8; ++j) hv[j] *= acc[2 + (j >> 2)][u][j & 3];
        SCHED();
        st_sc1(v4u{pk_bf16(hv[0], hv[1]), pk_bf16(hv[2], hv[3]), pk_bf16(hv[4], hv[5]), pk_bf16(hv[6], hv[7])}, ra2, lane * 16,
               ((2 * pn + ((u >> 2) ^ G)) * NK + 4 * cb + c) * 4096 + (u & 3) * 1024);
        if (u + 1 < 8) asm volatile("" : "+v"(acc[0][u + 1]), "+v"(acc[1][u + 1]) : : "memory");
      }
    }
    CFENCE();
    SCHED();
    STAMP(5);
    ld_carry();
    CFENCE();
    SCHED();
#pragma unroll
    for (int z = 0; z < 16; ++z) {
      const float t = cr * vr[z] - ci * vi[z];
      vi[z] = -cr * vi[z] - ci * vr[z]; vr[z] = t;
    }
    float qr[4], qi[4];
    qr[0] = ar; qi[0] = ai; qr[1] = ar; qi[1] = ai; csq(qr[1], qi[1], 1);
    cmul(qr[1], qi[1], ar, ai, qr[2], qi[2]); qr[3] = qr[1]; qi[3] = qi[1]; csq(qr[3], qi[3], 1);
    const float p4r = qr[3], p4i = qi[3];
    float p64r = p32r, p64i = p32i; csq(p64r, p64i, 1);
    float p512r = p64r, p512i = p64i; csq(p512r, p512i, 3);
    float p1kr = p512r, p1ki = p512i; csq(p1kr, p1ki, 1);
    float sgr, sgi, wgr, wgi;
    cmul((q & 1) ? p16r : 1.f, (q & 1) ? p16i : 0.f, (q & 2) ? p32r : 1.f, (q & 2) ? p32i : 0.f, sgr, sgi);
    cmul((q & 1) ? p512r : 1.f, (q & 1) ? p512i : 0.f, (q & 2) ? p1kr : 1.f, (q & 2) ? p1ki : 0.f, wgr, wgi);
#pragma unroll
    for (int z = 0; z < 16; ++z) asm volatile("" : "+v"(vr[z]), "+v"(vi[z]));
    asm volatile("" : "+v"(sgr), "+v"(sgi), "+v"(wgr), "+v"(wgi), "+v"(p64r), "+v"(p64i));
    SCHED();
    const int want = imin(imax(ck - 8 * q, 0), 8);
    auto carry_ok = [&]() {
      int good = 0;
#pragma unroll
      for (int z = 0; z < 8; ++z) good += ((cv[z][1] ^ tag) | (cv[z][3] ^ ~tag)) == 0u;
      return wall(good == want);
    };
    auto pickup = [&]() {
      lds_wait(L + LDS_PF + 4 * w);
      b2_read(true, L + w * 16384, lane, b2[2]);
      b2_read(true, L + w * 16384 + 8192, lane, b2[3]);
      CFENCE();
      *(volatile LAS unsigned*)(L + LDS_CO + 4 * w) = 1u;
      CFENCE();
    };
    if (!light) pickup();
    wait_vm<8>();
    publish(0);
    STAMP(14);
    if (__builtin_expect(!carry_ok(), 0)) {
#pragma nounroll
      for (unsigned spins = 0; spins < SPIN_CAP; ++spins) {
        __builtin_amdgcn_s_sleep(2);
        CFENCE();
        ld_carry();
        if (carry_ok()) break;
      }
    }
    STAMP(6);
    float Gr, Gi;
    {
      float Hr = 0.f, Hi = 0.f, tr, ti;
#pragma unroll
      for (int z = 7; z >= 0; --z) { cmul(Hr, Hi, p64r, p64i, tr, ti); Hr = tr + u2f(cv[z][0]); Hi = ti + u2f(cv[z][2]); }
      cmul(Hr, Hi, wgr, wgi, tr, ti);
      { const auto a = __builtin_amdgcn_permlane16_swap(f2u(tr), f2u(tr), false, false);
        const auto c2 = __builtin_amdgcn_permlane16_swap(f2u(ti), f2u(ti), false, false);
        tr = u2f(a[0]) + u2f(a[1]); ti = u2f(c2[0]) + u2f(c2[1]); }
      { const auto a = __builtin_amdgcn_permlane32_swap(f2u(tr), f2u(tr), false, false);
        const auto c2 = __builtin_amdgcn_permlane32_swap(f2u(ti), f2u(ti), false, false);
        tr = u2f(a[0]) + u2f(a[1]); ti = u2f(c2[0]) + u2f(c2[1]); }
      cmul(tr, ti, sgr, sgi, Hr, Hi);
      cmul(Hr + pr, Hi + pi, cr, ci, Gr, Gi);
    }
    {
      LAS unsigned char* T = L + B_BASE + (c >> 1) * (2 * B_SLOT) + (c & 1) * 4096;
      const int s32 = ((i15 >> 1) & 1) | ((i15 >> 2) & 2), s8 = (i15 & 1) | ((i15 >> 1) & 2);
#pragma unroll
      for (int t = 0; t < 4; ++t) {
        float orr[4], oi[4];
#pragma unroll
        for (int z = 0; z < 4; ++z) {
          orr[z] = __builtin_fmaf(-qi[z], Gi, __builtin_fmaf(qr[z], Gr, vr[4 * t + z]));
          oi[z] = __builtin_fmaf(-qi[z], Gr, __builtin_fmaf(-qr[z], Gi, vi[4 * t + z]));
        }
        if (t < 3) { float tr, ti; cmul(Gr, Gi, p4r, p4i, tr, ti); Gr = tr; Gi = ti; }
        LAS unsigned char* a = T + i15 * 128 + ((t ^ s32) << 5) + ((q ^ s8) << 3);
        *(LAS v2u*)a = v2u{pk_bf16(orr[0], orr[1]), pk_bf16(orr[2], orr[3])};
        *(LAS v2u*)(a + 2048) = v2u{pk_bf16(oi[0], oi[1]), pk_bf16(oi[2], oi[3])};
      }
      const int qq = i15 >> 2, pp = lane & 3;
      const int so = (ck * NK + 56 + 4 * (cb >> 1) + (cb & 1)) * 4096 + (c >> 1) * 512;
#pragma unroll
      for (int t = 0; t < 4; ++t) {
        v2u part[2];
#pragma unroll
        for (int hh = 0; hh < 2; ++hh)
          part[hh] = lds_rdtr(T + (8 * q + 4 * hh + qq) * 128 + ((t ^ ((qq >> 1) | ((q & 1) << 1))) << 5) + ((pp ^ ((qq & 1) | (hh << 1))) << 3));
        st_sc1(v4u{part[0][0], part[0][1], part[1][0], part[1][1]}, ra2, (lane >> 5) * 8192 + (lane & 31) * 16, so + t * 1024);
      }
    }
    SCHED();
    STAMP(7);
    wait_vm<0>();
    publish(1);
    if (light) pickup();
    STAMP(12);
  }
  kp = kargs();
  const int R = xcd * 4 + (light ? r >> 3 : (r - (r >> 3)) & 3), N = col_block(r, pWd);
  pc = p2_ctx(pWd, pCre, pCim, w, N);
  const int d0 = 2 * pc.sh;
  if (w < 4) {
    lds_wait_eq(L + LDS_PF + 4 * w, 2u);
    b2_read(true, L + w * 16384, lane, b2[0]);
    b2_read(true, L + w * 16384 + 8192, lane, b2[1]);
    WAIT_LDS();
    b2_load<0, 0, 8>(p2_ctx(pWd, pCre, pCim, w + 4, N), vb, 6, w * 16384);
    wait_vm<0>();
    *(volatile LAS unsigned*)(L + LDS_CO + 4 * w) = 2u;
    STAMP(13);
  }

  {
    lds_wait4(L + LDS_DONE);
    const int cl = lane & 15, lo = pc.sh ? (pc.g0 - 28) & ~1 : pc.g0 >> 1, hi = pc.sh ? (pc.g0 + pc.nd - 29) | 1 : (pc.g0 + pc.nd - 1) >> 1;
    GAS u64* fl = (GAS u64*)(kp->ws + WS_FA2) + ((pc.sh * 16 + (R & 1) * 8 + N) * 16 + (R >> 1)) * 16 + cl;
    if (w >= 4) {
      const unsigned need = ((2u << hi) - (1u << lo)) | 0xC000u;
#pragma nounroll
      for (unsigned spins = 0; spins < (SPIN_CAP << 3); ++spins) {
        CFENCE();
        const v4u st = *(volatile LAS v4u*)(L + LDS_RDY + 16);
        unsigned up = __builtin_amdgcn_readfirstlane(st[3]);
        if ((up & need) == need) break;
        const unsigned gone = __builtin_amdgcn_readfirstlane(st[0] | st[1] | st[2]), lower = (1u << (w - 4)) - 1u;
        if ((gone & lower) == lower) {
          const u64 v = __hip_atomic_load(fl, RLX_AGENT);
          up = (unsigned)__builtin_amdgcn_ballot_w64(v == tag64(tag)) & 0xFFFFu;
          CFENCE();
          *(volatile LAS unsigned*)(L + LDS_RDY + 28) = up;
          if ((up & need) == need) break;
          if (up & 0x3FFFu) __builtin_amdgcn_s_sleep(GPOLL); else __builtin_amdgcn_s_sleep(16);
        } else {
          __builtin_amdgcn_s_sleep(2);
        }
      }
      CFENCE();
      if (w < 7) *(volatile LAS unsigned*)(L + LDS_RDY + 16 + 4 * (w - 4)) = 1u << (w - 4);
    } else if (w == 3) {
      lds_wait4(L + LDS_DONE + 16);
      __builtin_amdgcn_s_sleep(SP_FIRST);
#pragma nounroll
      for (unsigned spins = 0; spins < SPIN_CAP; ++spins) {
        CFENCE();
        const u64 v = __hip_atomic_load(fl, RLX_AGENT);
        const unsigned up = (unsigned)__builtin_amdgcn_ballot_w64(v == tag64(tag)) & 0xFFFFu;
        CFENCE();
        *(volatile LAS unsigned*)(L + LDS_RDY) = up;
        if (up == 0xFFFFu) break;
        if (up & 0x3FFFu) __builtin_amdgcn_s_sleep(SP_NEXT); else __builtin_amdgcn_s_sleep(32);
      }
      CFENCE();
    } else {
      const unsigned need = (0xFu << lo) | 0xC000u;
#pragma nounroll
      for (unsigned spins = 0; spins < (SPIN_CAP << 4); ++spins) {
        if ((lds_word(L + LDS_RDY) & need) == need) break;
        __builtin_amdgcn_s_sleep(2);
      }
      CFENCE();
    }
  }
  STAMP(8);
  if (w >= 4) __builtin_amdgcn_s_setprio(1);
  glds<B_BASE + B_SLOT, 0>(mk_rs((const void*)pX, S_ * D_ * 2), w * 1024, ((lane >> 5) * 16 + ((lane >> 3) & 3)) * 1024 + (lane & 7) * 16,
                             (64 * R + 32 * (w & 1) + 4 * (w >> 1)) * 1024 + N * 128);
  const int tok = 64 * R + 32 * (w & 1) + 4 * (w >> 1) + (lane >> 4);
  const v2u dv = __builtin_amdgcn_raw_buffer_load_b64(mk_rsrc(kp->Dsk, D_ * 2), (lane & 15) * 8, N * 128, 0);
  const float ge = u2f(__builtin_amdgcn_raw_buffer_load_b32(mk_rsrc(kp->ws + WS_G, S_ * 4), tok * 4 + (lane & 1) * 64, 0, AUX_SC1));
  SCHED();
  const v4u ra = mk_rs(kp->ws + WS_A2 + (R * NK + 2 * pc.g0) * 4096, pc.nd * 8192);
  const int va = lane * 16;
  v4u af[8];
  {
    const int o = p2_ds(pc, 0) * 8192;
    p2_first<0>(af, ra, va, o, o + 4096);
  }
  SCHED();
#define CPRIO 0
  if (w < 4) __builtin_amdgcn_s_setprio(CPRIO);
  f32x4 acc[4][4];
#pragma unroll
  for (int e = 0; e < 4; ++e)
#pragma unroll
    for (int tt = 0; tt < 4; ++tt) { acc[e][tt] = f32x4{0.f, 0.f, 0.f, 0.f}; asm volatile("" : "+v"(acc[e][tt])); }
  asm volatile("s_nop 1");
  auto step = [&](auto Dc) {
    constexpr int d = decltype(Dc)::value;
    const int o = p2_ds(pc, d + 1 - d0) * 8192;
    p2_step<d, 0>(acc, b2[d & 3], af, ra, va, o, o + 4096, w);
    if (d < 2) b2_read(false, L + w * 16384 + d * 8192, lane, b2[d & 3]);
    if (d == PK6 && w >= 4) {
      lds_wait_eq(L + LDS_CO + 4 * (w - 4), 2u);
      b2_read(false, L + (w - 4) * 16384, lane, b2[2]);
      CFENCE();
      *(volatile LAS unsigned*)(L + LDS_PF + 4 * (w - 4)) = 3u;
      CFENCE();
    }
    SCHED();
  };
  if (w >= 4) { step(IC<0>{}); step(IC<1>{}); }
  step(IC<2>{}); step(IC<3>{}); step(IC<4>{}); step(IC<5>{});
  if (w >= 4) step(IC<6>{});
  asm volatile("s_nop 15" : "+v"(acc[0][3]), "+v"(acc[1][3]), "+v"(acc[2][3]), "+v"(acc[3][3]), "+v"(acc[0][2]), "+v"(acc[1][2]), "+v"(acc[2][2]), "+v"(acc[3][2]));
  SCHED();
  STAMP(9);
  kp = kargs();
  if (w < 4) lds_wait_eq(L + LDS_PF + 4 * w, 3u);
  {
    const int t3 = lane & 3, g3 = ((t3 & 1) * 3) ^ (t3 >> 1);
#pragma unroll
    for (int tt = 0; tt < 4; ++tt)
#pragma unroll
      for (int e = 0; e < 4; ++e)
        *(LAS f32x4*)(L + w * 16384 + (tt * 4 + e) * 1024 + (((lane & 48) | (((((lane >> 2) & 3) ^ (lane >> 4)) << 2) | (e ^ g3))) << 4)) = acc[e][tt];
  }
  if (b == 255 && w == 0) {
    rsrc_t rf = mk_rsrc(kp->ws + WS_FA2 + 32768, 256 * 8);
#pragma nounroll
    for (unsigned spins = 0; spins < SPIN_CAP; ++spins) {
      CFENCE();
      const v4u f0 = ld_sc1(rf, lane * 32, 0), f1 = ld_sc1(rf, lane * 32, 16);
      if (wall(((f0[0] ^ tag) | (f0[1] ^ ~tag) | (f0[2] ^ tag) | (f0[3] ^ ~tag) | (f1[0] ^ tag) | (f1[1] ^ ~tag) | (f1[2] ^ tag) | (f1[3] ^ ~tag)) == 0u)) break;
      __builtin_amdgcn_s_sleep(8);
    }
    st_flag(kp->ws, WS_EPOCH, tag, lane == 0 ? 0 : OOB);
  }
  {
    const int q = lane & 3, e = (lane >> 2) & 3, t3 = lane >> 4;
    const int base = ((w >> 1) * 4 + e) * 1024 + q * 256 + ((e ^ ((t3 & 1) * 3) ^ (t3 >> 1)) << 4);
    u64 pout = (u64)kp->out;
    const float go = dpp<0xB1>(ge);
    f32x4 res[2];
    float g2[2], g1[2];
    int a[4];
#pragma unroll
    for (int hf = 0; hf < 2; ++hf) {
      const float g = (lane & 1) == hf ? ge : go;
      const v2u xv = *(LAS v2u*)(L + B_BASE + B_SLOT + w * 1024 + hf * 512 + lane * 8);
      res[hf] = f32x4{g * (bf2f(xv[0] & 0xFFFFu) * bf2f(dv[0] & 0xFFFFu)), g * (bfhi(xv[0]) * bfhi(dv[0])),
                      g * (bf2f(xv[1] & 0xFFFFu) * bf2f(dv[1] & 0xFFFFu)), g * (bfhi(xv[1]) * bfhi(dv[1]))};
      g2[hf] = 2.f * g; g1[hf] = 1.f - g;
      a[2 * hf] = base + (((2 * (w & 1) + hf) ^ q) << 6); a[2 * hf + 1] = a[2 * hf] + 65536;
    }
    int oo = tok * 2048 + (lane & 15) * 16 + N * 256, oo2 = oo + 32768;
    asm volatile("s_waitcnt lgkmcnt(0)\n\ts_barrier"
                 : "+v"(res[0]), "+v"(res[1]), "+v"(g2[0]), "+v"(g2[1]), "+v"(g1[0]), "+v"(g1[1]), "+v"(a[0]), "+v"(a[1]), "+v"(a[2]), "+v"(a[3]), "+v"(oo),
                   "+v"(oo2), "+s"(pout) : : "memory");
    STAMP(10);
    f32x4 v[4][4];
#pragma unroll
    for (int z = 0; z < 4; ++z)
#pragma unroll
      for (int ww = 0; ww < 4; ++ww) v[z][ww] = *(LAS f32x4*)(L + a[z] + ww * 16384);
    SCHED();
    rsrc_t ro = mk_rsrc((const void*)pout, S_ * D_ * 4);
#pragma unroll
    for (int hf = 0; hf < 2; ++hf) {
      const f32x4 ss = (v[2 * hf][0] + v[2 * hf][1]) + (v[2 * hf][2] + v[2 * hf][3]);
      const f32x4 sg = (v[2 * hf + 1][0] + v[2 * hf + 1][1]) + (v[2 * hf + 1][2] + v[2 * hf + 1][3]);
      const f32x4 m = res[hf] + ss * g2[hf] + sg * g1[hf];
      __builtin_amdgcn_raw_buffer_store_b128(__builtin_bit_cast(v4u, m), ro, hf ? oo2 : oo, 0, OUT_AUX);
    }
  }
  STAMP(11);
}

#if defined(__HIP_DEVICE_COMPILE__)
#pragma clang attribute pop
#endif

extern "C" void kernel_launch(void* const* d_in, const int* in_sizes, int n_in, void* d_out, int out_size, void* d_ws, size_t ws_size,
                              hipStream_t stream) {
  Params p;
  p.x = (const u16*)d_in[0]; p.Lre = (const u16*)d_in[1]; p.Lim = (const u16*)d_in[2]; p.Bre = (const u16*)d_in[3];
  p.Bim = (const u16*)d_in[4]; p.Cre = (const u16*)d_in[5]; p.Cim = (const u16*)d_in[6]; p.ldt = (const u16*)d_in[7];
  p.Dsk = (const u16*)d_in[8]; p.Wg = (const u16*)d_in[10]; p.Wu = (const u16*)d_in[11]; p.Wd = (const u16*)d_in[12];
  p.gw = (const u16*)d_in[13]; p.out = (float*)d_out; p.ws = (unsigned char*)d_ws;
  (void)hipFuncSetAttribute((const void*)k_fused, hipFuncAttributeMaxDynamicSharedMemorySize, LDS_BYTES);
  k_fused<<<256, 512, LDS_BYTES, stream>>>(p);
}
```

```cpp
#include <hip/hip_runtime.h>
#include <stdint.h>

typedef unsigned short u16;
typedef unsigned long long u64;
typedef unsigned v4u __attribute__((ext_vector_type(4)));
typedef unsigned v2u __attribute__((ext_vector_type(2)));
typedef short s16x4 __attribute__((ext_vector_type(4)));
typedef __bf16 bf16x2 __attribute__((ext_vector_type(2)));
typedef float f32x2 __attribute__((ext_vector_type(2)));
typedef float f32x4 __attribute__((ext_vector_type(4)));
#define LAS __attribute__((address_space(3)))
#define GAS __attribute__((address_space(1)))
#define RLX_AGENT __ATOMIC_RELAXED, __HIP_MEMORY_SCOPE_AGENT
typedef __amdgpu_buffer_rsrc_t rsrc_t;

constexpr int S_ = 2048, D_ = 512, P_ = 512, F_ = 1792;
constexpr int NK = 88;
constexpr int AUX_SC1 = 16, AUX_NT = 2;
constexpr int WS_EPOCH = 0;
constexpr int WS_G = 8192;
constexpr int WS_FA2 = 65536;
constexpr int CTRL_BYTES = 131072;
constexpr int WS_CARRY = 131072;
constexpr int WS_A2 = 524288;
constexpr int A2_BYTES = 32 * NK * 4096;
constexpr int LDS_BYTES = 160 * 1024;
constexpr int X_SLOT = 16384, W_BASE = 65536, W_SLOT = 16384, B_BASE = 131072, B_SLOT = 8192;
constexpr int LDS_DONE = 155648, LDS_PF = LDS_DONE + 32, LDS_MD = LDS_DONE + 48, LDS_CO = LDS_DONE + 64, LDS_TAG = LDS_DONE + 80;
constexpr int LDS_RDY = LDS_DONE + 96;
constexpr int LDS_DUMMY = LDS_DONE + 256, LDS_CONST = LDS_DONE + 512;
constexpr int OOB = 1 << 28;
constexpr int CODE_BYTES = 31 * 1024;
constexpr unsigned SPIN_CAP = 1u << 18;

struct Params {
  const u16 *x, *Wg, *Wu, *Bre, *Bim, *Cre, *Cim, *Wd;
  unsigned char* ws;
  const u16 *Lre, *Lim, *ldt, *Dsk, *gw;
  float* out;
};

#if defined(__HIP_DEVICE_COMPILE__)
#pragma clang attribute push(__attribute__((target("no-packed-fp32-ops"))), apply_to = function)
#endif
__device__ __forceinline__ float u2f(unsigned v) { return __builtin_bit_cast(float, v); }
__device__ __forceinline__ unsigned f2u(float v) { return __builtin_bit_cast(unsigned, v); }
__device__ __forceinline__ bool wall(bool p) { return __builtin_amdgcn_ballot_w64(!p) == 0; }
__device__ __forceinline__ int imin(int a, int b) { return a < b ? a : b; }
__device__ __forceinline__ int imax(int a, int b) { return a > b ? a : b; }
template <int CTRL> __device__ __forceinline__ float dpp(float v) { return u2f(__builtin_amdgcn_mov_dpp(f2u(v), CTRL, 0xF, 0xF, true)); }
__device__ __forceinline__ rsrc_t mk_rsrc(const void* p, int bytes) {
  return __builtin_amdgcn_make_buffer_rsrc((void*)p, 0, bytes, 0x00020000);
}
__device__ __forceinline__ float bf2f(unsigned v) { return u2f(v << 16); }
__device__ __forceinline__ float bfhi(unsigned v) { return u2f(v & 0xFFFF0000u); }
__device__ __forceinline__ unsigned pk_bf16(float lo, float hi) {
  f32x2 t = {lo, hi};
  return __builtin_bit_cast(unsigned, __builtin_convertvector(t, bf16x2));
}
__device__ __forceinline__ void mfma16t(f32x4& c, v4u a, v4u b) {
  asm volatile("v_mfma_f32_16x16x32_bf16 %0, %1, %2, %0" : "+v"(c) : "v"(a), "v"(b));
}
__device__ __forceinline__ v4u lds_rd128(LAS unsigned char* p) { return *(LAS v4u*)p; }
__device__ __forceinline__ v2u lds_rdtr(LAS unsigned char* p) {
  return __builtin_bit_cast(v2u, __builtin_amdgcn_ds_read_tr16_b64_v4i16((LAS s16x4*)p));
}
__device__ __forceinline__ v4u mk_rs(const void* p, unsigned bytes) {
  const u64 a = (u64)p;
  unsigned lo = (unsigned)a, hi = (unsigned)(a >> 32) & 0xFFFFu;
  asm volatile("" : "+s"(lo), "+s"(hi));
  return v4u{lo, hi, bytes, 0x00020000u};
}
template <int LI, int SI> __device__ __forceinline__ void glds(v4u rs, int lds, int voff, int soff) {
  int t;
  asm volatile("s_add_u32 m0, %1, %5\n\ts_add_u32 %0, %4, %6\n\tbuffer_load_dwordx4 %2, %3, %0 offen lds"
               : "=&s"(t) : "s"(lds), "v"(voff), "s"(rs), "s"(soff), "i"(LI), "i"(SI) : "memory", "scc");
}
typedef const __attribute__((address_space(4))) Params* KP;
__device__ __forceinline__ KP kargs() {
  KP k = (KP)__builtin_amdgcn_kernarg_segment_ptr();
  asm volatile("" : "+s"(k));
  return k;
}
__device__ __forceinline__ void warm(v4u rs, int voff, int soff) {
  asm volatile("s_mov_b32 m0, %3\n\ts_nop 0\n\tbuffer_load_dword %0, %1, %2 offen lds" : : "v"(voff), "s"(rs), "s"(soff), "i"(LDS_DUMMY) : "memory");
}
__device__ __forceinline__ v4u ld_sc1(rsrc_t rs, int voff, int soff) { return __builtin_amdgcn_raw_buffer_load_b128(rs, voff, soff, AUX_SC1); }
__device__ __forceinline__ void st_sc1(v4u v, rsrc_t rs, int voff, int soff) { __builtin_amdgcn_raw_buffer_store_b128(v, rs, voff + soff, 0, AUX_SC1); }
__device__ __forceinline__ u64 tag64(unsigned tag) { return ((u64)(~tag) << 32) | tag; }
__device__ __forceinline__ void st_flag(unsigned char* ws, int off, unsigned tag, int voff) {
  __builtin_amdgcn_raw_buffer_store_b64(v2u{tag, ~tag}, mk_rsrc(ws, CTRL_BYTES), voff, off, AUX_SC1);
}
__device__ __forceinline__ unsigned lds_word(LAS unsigned char* p) { return __builtin_amdgcn_readfirstlane(*(volatile LAS unsigned*)p); }
#define BARRIER() asm volatile("s_barrier" ::: "memory")
#define SCHED() __builtin_amdgcn_sched_barrier(0)
#define CFENCE() asm volatile("" ::: "memory")
#define WAIT_LDS() asm volatile("s_waitcnt lgkmcnt(0)" ::: "memory")
template <int N> __device__ __forceinline__ void wait_vm() { asm volatile("s_waitcnt vmcnt(%c0)" ::"i"(N) : "memory"); }

__device__ __forceinline__ void csq(float& r, float& i, int n) {
#pragma unroll
  for (int z = 0; z < n; ++z) { const float t = r * r - i * i; i = 2.f * r * i; r = t; }
}

constexpr int WARM0 = 2, CONST_AT = 2;
constexpr int RAWN = 2, NRAWD = 2;
constexpr int LASTB = 13;
constexpr int raw_lo(int b) { const int n = (b - (LASTB + 1)) * RAWN + 8 * NRAWD; return n < 0 ? 0 : n; }
struct Sched {
  int n;
  int type[40], idx[40];
  int lo[LASTB + 1], hi[LASTB + 1];
  int wait[LASTB + 1];
  bool ok;
};
constexpr Sched make_sched(int kind) {
  Sched s{};
  int cnt[40] = {}, need[40] = {}, avail[40] = {};
  int n = 0;
  auto add = [&](int ty, int ix, int nd, int av) {
    if (kind && ty == 2) return;
    s.type[n] = ty; s.idx[n] = ix; cnt[n] = ty == 2 ? 4 : 3; need[n] = nd < LASTB ? nd : LASTB; avail[n] = av; ++n;
  };
  add(0, 0, 0, -1); add(1, 0, 0, -1); add(2, 0, 0, -1); add(2, 1, 0, -1);
  add(0, 1, 1, -1); add(1, 1, 1, -1); add(2, 2, 1, -1); add(2, 3, 2, -1);
  add(0, 2, 3, -1); add(1, 2, 3, -1);
  add(2, 4, 3, 1); add(2, 5, 4, 1);
  for (int b = 2; b < 12; ++b) {
    const int T = b / 2 + 2;
    add(b & 1, T, 2 * T - 1, kind ? (b & ~1) : b);
    add(2, b + 4, b + 3, b);
  }
  s.n = n;
  s.ok = true;
  const int LIM = 8;
  int endpos[40] = {};
  int next = 0, total = 0;
  for (int b = 0; b <= LASTB; ++b) {
    s.lo[b] = next;
    const int start = total;
    for (;;) {
      if (next >= n || avail[next] > b - 1) break;
      int lastneed = 0;
      for (int i = 0; i < next; ++i) if (need[i] <= b) lastneed = endpos[i];
      if (b < 3 && need[next] > b && total + cnt[next] - lastneed > LIM + 2 * b) break;
      if (b >= 3 && need[next] > b + 1 && total + cnt[next] - start > 11) break;
      total += cnt[next]; endpos[next] = total; ++next;
    }
    s.hi[b] = next;
    if (kind == 0) total += raw_lo(b + 1) - raw_lo(b);
    if (kind == 0 && b >= WARM0 && b < WARM0 + 3) total += 1;
    int lastneed = 0;
    for (int i = 0; i < next; ++i) if (need[i] <= b) lastneed = endpos[i];
    for (int i = next; i < n; ++i) if (need[i] <= b) s.ok = false;
    s.wait[b] = total - lastneed;
  }
  if (next != n) s.ok = false;
  return s;
}
constexpr Sched SCH[2] = {make_sched(0), make_sched(1)};
static_assert(SCH[0].ok && SCH[1].ok, "every ring item must be issued in time");

struct P2Ctx { u64 a0, a1; int so0, sh, g0, nd, rot; unsigned bytes; };
__device__ __forceinline__ P2Ctx p2_ctx(u64 Wd, u64 Cre, u64 Cim, int w, int N) {
  P2Ctx c;
  const int ssm = w < 4;
  const u64 m = 0 - (u64)ssm;
  c.a0 = (Cre & m) | (Wd & ~m); c.a1 = (Cim & m) | (Wd & ~m);
  c.sh = ssm;
  c.so0 = N * 128 + ssm * (N * (65536 - 128) - 14 * 128);
  c.bytes = F_ * D_ * 2u - ssm * (F_ * D_ * 2u - D_ * P_ * 2u);
  c.g0 = 7 * w - 28 + ssm * (56 - 3 * w);
  c.nd = 7 - 3 * ssm;
  c.rot = (N & 1) * (3 - ssm);
  return c;
}
__device__ __forceinline__ int p2_ds(const P2Ctx& c, int d) { const int t = d + c.rot; return t >= c.nd ? t - c.nd : t; }
__device__ __forceinline__ void b2_src(const P2Ctx& c, int d, v4u& rs, int& so) {
  const int g = c.g0 + p2_ds(c, d);
  const u64 x0 = c.a0, x1 = c.a1;
  rs = mk_rs((const void*)((g & 1) ? x1 : x0), c.bytes);
  so = c.so0 + (c.sh ? (g >> 1) * 128 : g * 65536);
}
template <int LI, int N0, int N1> __device__ __forceinline__ void b2_rows(v4u rs, int lds, int vb, int so) {
  if constexpr (N0 < N1) { glds<LI, N0 * 8192>(rs, lds, vb, so); b2_rows<LI + 1024, N0 + 1, N1>(rs, lds, vb, so); }
}
template <int LI, int N0, int N1> __device__ __forceinline__ void b2_load(const P2Ctx& c, int vb, int d, int lds) {
  v4u rs; int so;
  b2_src(c, d, rs, so);
  b2_rows<LI, N0, N1>(rs, lds, vb, __builtin_amdgcn_readfirstlane(so));
}
__device__ __forceinline__ void b2_read(bool ssm, LAS unsigned char* img, int lane, v4u (&f)[2][4]) {
  const int q = lane >> 4;
  if (ssm) {
    const int i = lane & 15;
#pragma unroll
    for (int h = 0; h < 2; ++h)
#pragma unroll
      for (int e = 0; e < 4; ++e) f[h][e] = lds_rd128(img + i * 128 + (((4 * h + q) ^ (((i >> 1) & 3) << 1)) << 4) + e * 2048);
  } else {
    const int qq = (lane >> 2) & 3, s = 2 * (q & 1) + (qq >> 1);
#pragma unroll
    for (int h = 0; h < 2; ++h)
#pragma unroll
      for (int e = 0; e < 4; ++e) {
        LAS unsigned char* a = img + (4 * q + qq) * 128 + ((e ^ s) << 5) + (lane & 3) * 8 + h * 4096;
        const v2u lo = lds_rdtr(a), hi = lds_rdtr(a + 2048);
        f[h][e] = v4u{lo[0], lo[1], hi[0], hi[1]};
      }
  }
}

struct LdCtx { v4u rx, rw, rb; u64 Wd, Cre, Cim; int lw, lb, vx, vw0, vw1, vbb, rot, lane, vb, vwarm, ch; mutable unsigned lrb, lib, ldb, ep; };

template <int KIND, int I> __device__ __forceinline__ void ld_item(const LdCtx& c) {
  constexpr int ty = SCH[KIND].type[I], t = SCH[KIND].idx[I];
  int rot = c.rot;
  asm volatile("" : "+s"(rot));
  if constexpr (ty < 2) {
    const int k0 = ((t + rot) & 7) * 128;
    glds<(t % 3) * X_SLOT + 2 * ty * 4096, ty * 65536>(c.rx, c.lw * 1024, c.vx, k0);
    glds<(t % 3) * X_SLOT + (2 * ty + 1) * 4096, ty * 65536 + 8192>(c.rx, c.lw * 1024, c.vx, k0);
    glds<B_BASE + (t % 3) * B_SLOT + ty * 2048, ty * 16384>(c.rb, c.lb, c.vbb, k0);
  } else {
    const int k0 = (((t >> 1) + rot) & 7) * (64 * F_ * 2);
    constexpr int LI = W_BASE + (t & 3) * W_SLOT, SI = (t & 1) * 32 * F_ * 2;
    glds<LI, SI>(c.rw, c.lw * 4096, c.vw0, k0);
    glds<LI + 1024, SI + 4 * F_ * 2>(c.rw, c.lw * 4096, c.vw0, k0);
    glds<LI + 2048, SI + 8 * F_ * 2>(c.rw, c.lw * 4096, c.vw1, k0);
    glds<LI + 3072, SI + 12 * F_ * 2>(c.rw, c.lw * 4096, c.vw1, k0);
  }
}
template <int KIND, int LO, int HI> __device__ __forceinline__ void ld_items(const LdCtx& c) {
  if constexpr (LO < HI) { ld_item<KIND, LO>(c); ld_items<KIND, LO + 1, HI>(c); }
}
template <int A, int E> __device__ __forceinline__ void raw_loads(const P2Ctx& pc, int vb, v4u (&raw)[8 * NRAWD]) {
  if constexpr (A < E) {
    v4u rs; int so;
    b2_src(pc, 4 + (A >> 3), rs, so);
    asm volatile("buffer_load_dwordx4 %0, %1, %2, %3 offen" : "=v"(raw[A]) : "v"(vb), "s"(rs), "s"(so + (A & 7) * 8192) : "memory");
    raw_loads<A + 1, E>(pc, vb, raw);
  }
}
template <int KIND, int B> __device__ __forceinline__ void loader_steps(const LdCtx& c, LAS unsigned char* L, const P2Ctx& pc, v4u (&raw)[8 * NRAWD]) {
  if constexpr (B == 0) {
    constexpr int FIRST = KIND ? 2 : 3;
    ld_items<KIND, 0, FIRST>(c);
    KP kp = kargs();
    const v4u r0 = mk_rs(kp->Lre, P_ * 2), r1 = mk_rs(kp->Lim, P_ * 2), r2 = mk_rs(kp->ldt, P_ * 2), r3 = mk_rs(kp->ws + WS_EPOCH, c.lw == 3 ? 4 : 0);
    asm volatile("buffer_load_ushort %0, %4, %5, 0 offen\n\tbuffer_load_ushort %1, %4, %6, 0 offen\n\tbuffer_load_ushort %2, %4, %7, 0 offen\n\t"
                 "buffer_load_dword %3, off, %8, 0 sc1"
                 : "=&v"(c.lrb), "=&v"(c.lib), "=&v"(c.ldb), "=&v"(c.ep) : "v"(c.ch * 2), "s"(r0), "s"(r1), "s"(r2), "s"(r3) : "memory");
    if constexpr (KIND == 0) {
      ld_items<0, 3, 5>(c);
      wait_vm<7>();
      BARRIER();
      ld_items<0, 5, SCH[0].hi[0]>(c);
    } else {
      ld_items<KIND, FIRST, SCH[KIND].hi[0]>(c);
    }
  } else {
    ld_items<KIND, SCH[KIND].lo[B], SCH[KIND].hi[B]>(c);
  }
  if constexpr (KIND == 0) raw_loads<raw_lo(B), raw_lo(B + 1)>(pc, c.vb, raw);
  if constexpr (KIND == 0 && B == WARM0) warm(mk_rs((const void*)c.Wd, F_ * D_ * 2), c.lane < 16 ? (c.vwarm * 16 + c.lane) * 128 : OOB, 0);
  if constexpr (KIND == 0 && B == WARM0 + 1) warm(mk_rs((const void*)c.Cre, D_ * P_ * 2), c.lane < 5 ? (c.vwarm * 5 + c.lane) * 128 : OOB, 0);
  if constexpr (KIND == 0 && B == WARM0 + 2) warm(mk_rs((const void*)c.Cim, D_ * P_ * 2), c.lane < 5 ? (c.vwarm * 5 + c.lane) * 128 : OOB, 0);
  if constexpr (B == CONST_AT) {
    asm volatile("" : "+v"(c.lrb), "+v"(c.lib), "+v"(c.ldb), "+v"(c.ep) : : "memory");
    const float lr = bf2f(c.lrb), li = bf2f(c.lib);
    float dt = __builtin_amdgcn_exp2f(bf2f(c.ldb) * 1.44269504088896341f);
    dt = bf2f(pk_bf16(dt, 0.f) & 0xFFFFu);
    const float th = dt * li, rv = th * 0.15915494f;
    const float rev = (rv - __builtin_rintf(rv)) + __builtin_fmaf(th, 6.42063833e-9f, __builtin_fmaf(th, 0.15915494f, -rv));
    const float ex = __builtin_amdgcn_exp2f(dt * lr * 1.44269504088896341f);
    const float ar = ex * __builtin_amdgcn_cosf(rev), ai = ex * __builtin_amdgcn_sinf(rev);
    const float den = __builtin_amdgcn_rcpf(lr * lr + li * li);
    float p16r = ar, p16i = ai;
    csq(p16r, p16i, 4);
    float p32r = p16r, p32i = p16i;
    csq(p32r, p32i, 1);
    LAS f32x4* d = (LAS f32x4*)(L + LDS_CONST + c.lw * 512 + (c.lane & 15) * 32);
    d[0] = f32x4{ar, ai, ((ar - 1.f) * lr + ai * li) * den, (ai * lr - (ar - 1.f) * li) * den};
    d[1] = f32x4{p16r, p16i, p32r, p32i};
    *(volatile LAS unsigned*)(L + LDS_TAG + 4 * (3 - c.lw)) = c.ep + 1u;
    WAIT_LDS();
  }
  SCHED();
  wait_vm<SCH[KIND].wait[B]>();
  BARRIER();
  if constexpr (B < LASTB) loader_steps<KIND, B + 1>(c, L, pc, raw);
}

__device__ __forceinline__ bool all4(LAS unsigned char* p) {
  const v4u d = *(volatile LAS v4u*)p;
  return __builtin_amdgcn_readfirstlane(d[0] & d[1] & d[2] & d[3]) != 0u;
}
__device__ __forceinline__ void lds_wait4(LAS unsigned char* p) {
#pragma nounroll
  for (unsigned spins = 0; spins < (SPIN_CAP << 4); ++spins) {
    if (all4(p)) break;
    __builtin_amdgcn_s_sleep(2);
  }
  CFENCE();
}
__device__ __forceinline__ void lds_wait(LAS unsigned char* word) {
#pragma nounroll
  for (unsigned spins = 0; spins < (SPIN_CAP << 4); ++spins) {
    if (lds_word(word) != 0u) break;
    __builtin_amdgcn_s_sleep(2);
  }
  CFENCE();
}
__device__ __forceinline__ void lds_wait_eq(LAS unsigned char* word, unsigned want) {
#pragma nounroll
  for (unsigned spins = 0; spins < (SPIN_CAP << 4); ++spins) {
    if (lds_word(word) == want) break;
    __builtin_amdgcn_s_sleep(2);
  }
  CFENCE();
}
__device__ __forceinline__ void cmul(float ar, float ai, float br, float bi, float& cr, float& ci) {
  cr = ar * br - ai * bi; ci = ar * bi + ai * br;
}

template <int V> struct IC { static constexpr int value = V; };
template <int n> __device__ __forceinline__ void p2_first(v4u (&af)[8], v4u ra, int va, int o0, int o1) {
  asm volatile("buffer_load_dwordx4 %0, %1, %2, %3 offen offset:%c4 sc1" : "=v"(af[n]) : "v"(va), "s"(ra), "s"(n < 4 ? o0 : o1), "i"((n & 3) * 1024) : "memory");
  if constexpr (n < 7) p2_first<n + 1>(af, ra, va, o0, o1);
}
template <int d, int n>
__device__ __forceinline__ void p2_step(f32x4 (&acc)[4][4], const v4u (&bb)[2][4], v4u (&af)[8], v4u ra, int va, int o0, int o1, int w) {
  constexpr int WG = d == 6 ? 7 - n : 7, WS = d == 5 ? 7 - n : 7;
  if constexpr (d >= 3 && d <= 5 && WS < WG)
    asm volatile("s_waitcnt vmcnt(%c2)\n\ts_cmp_gt_u32 %1, 3\n\ts_cbranch_scc1 1f\n\ts_waitcnt vmcnt(%c3)\n1:" : "+v"(af[n]) : "s"(w), "i"(WG), "i"(WS) : "memory", "scc");
  else
    asm volatile("s_waitcnt vmcnt(%c1)" : "+v"(af[n]) : "i"(WG) : "memory");
#pragma unroll
  for (int e = 0; e < 4; ++e) mfma16t(acc[e][n & 3], bb[n >> 2][e], af[n]);
  if constexpr (d < 5)
    asm volatile("buffer_load_dwordx4 %0, %1, %2, %3 offen offset:%c4 sc1" : "+v"(af[n]) : "v"(va), "s"(ra), "s"(n < 4 ? o0 : o1), "i"((n & 3) * 1024) : "memory");
  if constexpr (d == 5)
    asm volatile("s_cmp_lt_u32 %5, 4\n\ts_cbranch_scc1 1f\n\tbuffer_load_dwordx4 %0, %1, %2, %3 offen offset:%c4 sc1\n1:"
                 : "+v"(af[n]) : "v"(va), "s"(ra), "s"(n < 4 ? o0 : o1), "i"((n & 3) * 1024), "s"(w) : "memory", "scc");
  if constexpr (n < 7) p2_step<d, n + 1>(acc, bb, af, ra, va, o0, o1, w);
}

__global__ __launch_bounds__(512, 2) void k_fused(Params) {
  extern __shared__ __attribute__((aligned(16))) unsigned char lds_[];
  LAS unsigned char* L = (LAS unsigned char*)lds_;
  KP kp = kargs();
  u64 pX = (u64)kp->x, pWg = (u64)kp->Wg, pWu = (u64)kp->Wu, pBre = (u64)kp->Bre, pBim = (u64)kp->Bim, pCre = (u64)kp->Cre, pCim = (u64)kp->Cim, pWd = (u64)kp->Wd;
  const int b = __builtin_amdgcn_workgroup_id_x(), tid = __builtin_amdgcn_workitem_id_x(), lane = tid & 63;
  const int w = __builtin_amdgcn_readfirstlane(tid >> 6);
  const int xcd = b & 7, r = b >> 3;
  warm(mk_rs((const void*)(__builtin_amdgcn_s_getpc() & ~(u64)127), CODE_BYTES), lane < 2 ? lane * 32768 : OOB, (r * 8 + w) * 128);
  const bool light = (r & 7) == 7;
  const int ih = 28 * (xcd & 3) + r - (r >> 3), il = 112 + 4 * (xcd & 3) + (r >> 3);
  const int idx = ih + (il - ih) * (int)light;
  const int pn = 8 * (xcd >> 2) + (idx & 7), cb = idx >> 3;
  const int rot = pn & 6;
  int R = xcd * 4 + (r & 3), N = r >> 2;
  asm volatile("" : "+s"(pX), "+s"(pWg), "+s"(pWu), "+s"(pBre), "+s"(pBim), "+s"(pCre), "+s"(pCim), "+s"(pWd));
  const int vb = (lane >> 3) * 1024 + (((lane & 7) ^ (((lane >> 4) & 3) << 1)) << 4);
  v4u b2[4][2][4];
  unsigned tag = 0;
  P2Ctx pc;

  if (__builtin_expect(w >= 4, 1)) {
    LdCtx c;
    c.lw = w - 4; c.rot = rot; c.lane = lane; c.vb = vb;
    c.Wd = pWd; c.Cre = pCre; c.Cim = pCim;
    c.vwarm = ((r - (r >> 3)) * 8 + xcd) * 4 + c.lw;
    c.ch = 32 * cb + 16 * (c.lw >> 1) + (lane & 15);
    const int l3 = lane >> 3, kq = lane >> 4;
    c.rx = mk_rs((const void*)pX, S_ * D_ * 2);
    c.vx = (32 * (c.lw & 1) + 16 * (l3 >> 2) + 4 * (c.lw >> 1) + (l3 & 3)) * 1024 + (((lane & 7) ^ ((4 * c.lw + kq) & 7)) << 4) + pn * 128 * 1024;
    c.rb = mk_rs((const void*)((c.lw >> 1) ? pBim : pBre), P_ * D_ * 2);
    c.vbb = (8 * (c.lw & 1) + l3) * 1024 + (((lane & 7) ^ (4 * (c.lw & 1) + kq)) << 4) + cb * 32 * 1024;
    c.lb = (c.lw >> 1) * 4096 + (c.lw & 1) * 1024;
    c.rw = mk_rs((const void*)((c.lw >> 1) ? pWu : pWg), D_ * F_ * 2);
    c.vw0 = ((c.lw & 1) * 16 + kq) * (F_ * 2) + (((lane & 15) ^ (kq << 2)) << 4) + cb * 256;
    c.vw1 = c.vw0 ^ 32;
    pc = p2_ctx(pWd, pCre, pCim, w, N);
    v4u raw[8 * NRAWD];
    if (__builtin_expect(!light, 1)) loader_steps<0, 0>(c, L, pc, raw);
    else { loader_steps<1, 0>(c, L, pc, raw); BARRIER(); raw_loads<0, 8 * NRAWD>(pc, vb, raw); }
    {
      const int wA = c.lw < 2 ? w : c.lw;
      b2_load<0, 0, 8>(pc, vb, 0, wA * 16384);
      b2_load<8192, 0, 8>(pc, vb, 1, wA * 16384);
      const P2Ctx pp = p2_ctx(pWd, pCre, pCim, c.lw, N);
#pragma unroll
      for (int d = 0; d < 4; ++d) {
        wait_vm<8>();
        b2_read(false, L + wA * 16384 + (d & 1) * 8192, lane, b2[d]);
        WAIT_LDS();
        if (d == 2) lds_wait4(L + LDS_MD);
        if (d < 2) b2_load<0, 0, 8>(pc, vb, d + 2, wA * 16384 + (d & 1) * 8192);
        else b2_load<0, 0, 8>(pp, vb, d & 1, c.lw * 16384 + (d & 1) * 8192);
      }
#pragma unroll
      for (int z = 0; z < 16; z += 8)
        asm volatile("" : "+v"(raw[z]), "+v"(raw[z + 1]), "+v"(raw[z + 2]), "+v"(raw[z + 3]), "+v"(raw[z + 4]), "+v"(raw[z + 5]), "+v"(raw[z + 6]), "+v"(raw[z + 7]));
#pragma unroll
      for (int n = 0; n < 16; ++n) *(LAS v4u*)(L + w * 16384 + n * 1024 + lane * 16) = raw[n];
      wait_vm<0>();
      *(volatile LAS unsigned*)(L + LDS_PF + 4 * c.lw) = 1u;
      lds_wait(L + LDS_CO + 4 * c.lw);
      b2_load<0, 0, 8>(pp, vb, 2, c.lw * 16384);
      b2_load<8192, 0, 8>(pp, vb, 3, c.lw * 16384);
      wait_vm<0>();
      *(volatile LAS unsigned*)(L + LDS_PF + 4 * c.lw) = 2u;
    }
    tag = lds_word(L + LDS_TAG);
  } else {
    const int c = w, q = lane >> 4, i15 = lane & 15, G = c & 1;
    const int ch = 32 * cb + 16 * (c >> 1) + i15;
    if (w == 3) {
#pragma unroll
      for (int z = 0; z < 5; ++z) *(volatile LAS v4u*)(L + LDS_DONE + 16 * z) = v4u{0u, 0u, 0u, 0u};
      *(volatile LAS unsigned*)(L + LDS_RDY) = 0u;
      *(volatile LAS v4u*)(L + LDS_RDY + 16) = v4u{0u, 0u, 0u, 0u};
      WAIT_LDS();
    }
    f32x4 k0 = {0.f, 0.f, 0.f, 0.f}, k1 = {0.f, 0.f, 0.f, 0.f};
    unsigned tagv = 0;
    auto rd_const = [&]() {
      k0 = *(LAS f32x4*)(L + LDS_CONST + c * 512 + i15 * 32); k1 = *(LAS f32x4*)(L + LDS_CONST + c * 512 + i15 * 32 + 16);
      tagv = *(volatile LAS unsigned*)(L + LDS_TAG);
    };
    u64 pws = (u64)kp->ws;
    asm volatile("" : "+s"(pws));
    int xa[2];
#pragma unroll
    for (int ks = 0; ks < 2; ++ks) xa[ks] = i15 * 128 + (((4 * ks + q) ^ ((i15 >> 1) & 7)) << 4);
    asm volatile("" : "+v"(xa[0]), "+v"(xa[1]));
    const int bo = B_BASE + (c >> 1) * 2048;
    f32x4 accs[2][4], acc[4][8];
    v4u bfr[2];
    auto zero_accs = [&]() {
#pragma unroll
      for (int m = 0; m < 2; ++m)
#pragma unroll
        for (int t = 0; t < 4; ++t) { accs[m][t] = f32x4{0.f, 0.f, 0.f, 0.f}; asm volatile("" : "+v"(accs[m][t])); }
    };
    float vr[16], vi[16], t0 = 0.f, t1 = 0.f;
    auto scan_half = [&](int n) {
      const int z = n >> 1;
      if (z == 0) { vr[0] = accs[0][0][0]; vi[0] = accs[1][0][0]; }
      else if ((n & 1) == 0) { t0 = __builtin_fmaf(k0[0], vr[z - 1], accs[0][z >> 2][z & 3]); t1 = __builtin_fmaf(k0[0], vi[z - 1], accs[1][z >> 2][z & 3]); }
      else { vr[z] = __builtin_fmaf(-k0[1], vi[z - 1], t0); vi[z] = __builtin_fmaf(k0[1], vr[z - 1], t1); }
    };
    auto sc = [](int v) { asm volatile("" : "+s"(v)); return v; };

    if (!light) {
      int wa[2];
      {
        const int qq = i15 >> 2, pp = lane & 3;
#pragma unroll
        for (int u = 0; u < 2; ++u)
          wa[u] = W_BASE + (8 * q + qq) * 256 + (((2 * (2 * c + u) + (pp >> 1)) ^ ((qq << 2) | ((q & 1) << 1))) << 4) + 8 * (pp & 1);
      }
      asm volatile("" : "+v"(wa[0]), "+v"(wa[1]));
#pragma unroll
      for (int a = 0; a < 4; ++a)
#pragma unroll
        for (int u = 0; u < 8; ++u) { acc[a][u] = f32x4{0.f, 0.f, 0.f, 0.f}; asm volatile("" : "+v"(acc[a][u])); }
      zero_accs();
      v4u xf[4];
      v2u wf[2][8];
      auto rdw = [&](int s, int z) {
        const int m = z >> 2, u = (z >> 1) & 1, t = z & 1;
        wf[s & 1][z] = lds_rdtr(L + wa[u] + (s & 3) * W_SLOT + m * 8192 + t * 1024);
      };
      BARRIER();
#pragma unroll
      for (int z = 0; z < 8; ++z) rdw(0, z);
#pragma unroll
      for (int z = 0; z < 3; ++z) xf[z] = lds_rd128(L + xa[0] + G * 8192 + z * 2048);
#pragma unroll
      for (int m = 0; m < 2; ++m) bfr[m] = lds_rd128(L + xa[0] + bo + m * 4096);
      auto glu4 = [&](int s, int u, const v4u& x, auto&& between) {
#pragma unroll
        for (int a = 0; a < 4; ++a) {
          const v4u wv = {wf[s & 1][2 * a][0], wf[s & 1][2 * a][1], wf[s & 1][2 * a + 1][0], wf[s & 1][2 * a + 1][1]};
          mfma16t(acc[a][u], wv, x);
          SCHED();
          between(a);
          SCHED();
        }
      };
      int xb = 0, bb = 0;
      auto steps = [&](int s0, int n, const int (&xo)[5]) {
#pragma unroll
        for (int j = 0; j < n; ++j)
#pragma unroll
          for (int u = 0; u < 8; ++u) {
            const int s = s0 + j, jn = j + ((u + 3) >> 3), un = (u + 3) & 7, uw = s == 0 ? u - 4 : u;
            SCHED();
            if (u == (s == 0 ? 4 : 0)) { BARRIER(); SCHED(); }
            glu4(s, u, xf[u & 3], [&](int a) {
              if (a == 0) {
                if ((un & 3) == 0 || (j == 0 && u == 0)) xb = xa[(s0 + jn) & 1] + sc(xo[jn] + ((un >> 2) ^ G) * 8192);
                xf[(u + 3) & 3] = lds_rd128(L + xb + (un & 3) * 2048);
              }
              if ((a == 1 || a == 2) && uw >= 0 && uw < 4) rdw(s + 1, 2 * uw + a - 1);
              if (a == 3 && (u == 4 || u == 5)) {
                if (u == 4) bb = xa[(s + 1) & 1] + sc(bo + (xo[j + 1] >> 1));
                bfr[u - 4] = lds_rd128(L + bb + (u - 4) * 4096);
              }
            });
            if (u < 4) { mfma16t(accs[0][u], xf[u & 3], bfr[0]); mfma16t(accs[1][u], xf[u & 3], bfr[1]); }
          }
      };
      {
        const int xo[5] = {0, 0, 0, 0, 0};
        steps(0, 1, xo);
      }
      int xo0 = 0;
#pragma nounroll
      for (int o = 0; o < 3; ++o) {
        const int xo1 = xo0 == 2 * X_SLOT ? 0 : xo0 + X_SLOT, xo2 = xo1 == 2 * X_SLOT ? 0 : xo1 + X_SLOT;
        const int xo[5] = {xo0, xo1, xo1, xo2, xo2};
        steps(1, 4, xo);
        xo0 = xo2;
      }
      {
        const int xo[5] = {0, X_SLOT, 0, 0, 0};
        steps(1, 1, xo);
      }
      v4u bf2[2];
#pragma unroll
      for (int it = 0; it < 16; ++it) {
        const int s = (it >> 2) & 1, u = (it & 3) + 4 * (it >> 3), nx = it + 3;
        SCHED();
        glu4(s, u, xf[it & 3], [&](int a) {
          if (a == 0 && nx < 16) {
            if ((nx & 3) == 0) xb = xa[(nx >> 2) & 1] + sc(X_SLOT + ((nx >> 3) ^ G) * 8192);
            xf[nx & 3] = lds_rd128(L + xb + (nx & 3) * 2048);
          }
          if (a >= 1 && it < 3 && 3 * it + a - 1 < 8) rdw(3, 3 * it + a - 1);
          if (it == 0 && a == 3) { bb = xa[1] + sc(bo + B_SLOT); bf2[0] = lds_rd128(L + bb); bf2[1] = lds_rd128(L + bb + 4096); }
          if (it == 4 && a == 1) rd_const();
          if (it >= 8) scan_half(4 * (it - 8) + a);
        });
        if (it < 4) { mfma16t(accs[0][u], xf[it & 3], bfr[0]); mfma16t(accs[1][u], xf[it & 3], bfr[1]); }
        else if (it < 8) { mfma16t(accs[0][u], xf[it & 3], bf2[0]); mfma16t(accs[1][u], xf[it & 3], bf2[1]); }
      }
      asm volatile("s_nop 15" : "+v"(acc[0][7]), "+v"(acc[1][7]), "+v"(acc[2][7]), "+v"(acc[3][7]), "+v"(acc[0][6]), "+v"(acc[1][6]), "+v"(acc[2][6]), "+v"(acc[3][6]));
    } else {
      {
      kp = kargs();
      rsrc_t rx = mk_rsrc(kp->x, S_ * D_ * 2), rg = mk_rsrc(kp->gw, D_ * 2), rgo = mk_rsrc(kp->ws + WS_G, S_ * 4);
      const int tk0 = 128 * pn + 64 * (cb - 14) + 16 * c;
      v4u gv[8], xv[2][8];
#pragma unroll
      for (int z = 0; z < 8; ++z) gv[z] = __builtin_amdgcn_raw_buffer_load_b128(rg, (lane & 7) * 16, z * 128, 0);
#pragma unroll
      for (int gi = 0; gi < 2; ++gi)
#pragma unroll
        for (int z = 0; z < 8; ++z) xv[gi][z] = __builtin_amdgcn_raw_buffer_load_b128(rx, (lane >> 3) * 1024 + (lane & 7) * 16, (tk0 + gi * 8) * 1024 + z * 128, 0);
#pragma unroll
      for (int gi = 0; gi < 2; ++gi) {
        float s = 0.f;
#pragma unroll
        for (int z = 0; z < 8; ++z)
#pragma unroll
          for (int y = 0; y < 4; ++y) {
            const unsigned xa_ = xv[gi][z][y], ga = gv[z][y];
            s = __builtin_amdgcn_fdot2_f32_bf16(__builtin_bit_cast(bf16x2, xa_), __builtin_bit_cast(bf16x2, ga), s, false);
          }
        s += dpp<0xB1>(s); s += dpp<0x4E>(s); s += dpp<0x141>(s);
        const float g = __builtin_amdgcn_rcpf(1.f + __builtin_amdgcn_exp2f(s * -1.44269504088896341f));
        __builtin_amdgcn_raw_buffer_store_b32(f2u(g), rgo, (lane & 7) ? OOB : (lane >> 3) * 4, (tk0 + gi * 8) * 4, AUX_SC1);
      }
      }
      zero_accs();
      int xo = 0;
#pragma nounroll
      for (int T = 0; T < 8; ++T) {
#pragma unroll
        for (int ks = 0; ks < 2; ++ks) {
          if (T < 7) BARRIER();
          v4u xf[4];
#pragma unroll
          for (int t = 0; t < 4; ++t) xf[t] = lds_rd128(L + (xa[ks] + xo + G * 8192) + t * 2048);
#pragma unroll
          for (int m = 0; m < 2; ++m) bfr[m] = lds_rd128(L + (xa[ks] + bo + (xo >> 1)) + m * 4096);
#pragma unroll
          for (int t = 0; t < 4; ++t) { mfma16t(accs[0][t], xf[t], bfr[0]); mfma16t(accs[1][t], xf[t], bfr[1]); }
          SCHED();
        }
        xo = xo == 2 * X_SLOT ? 0 : xo + X_SLOT;
      }
      asm volatile("s_nop 15" : "+v"(accs[0][0]), "+v"(accs[1][0]), "+v"(accs[0][1]), "+v"(accs[1][1]), "+v"(accs[0][2]), "+v"(accs[1][2]), "+v"(accs[0][3]), "+v"(accs[1][3]));
      rd_const();
      BARRIER();
#pragma unroll
      for (int n = 0; n < 32; ++n) { scan_half(n); SCHED(); }
    }
    *(volatile LAS unsigned*)(L + LDS_MD + 4 * w) = 1u;
    SCHED();
    tag = __builtin_amdgcn_readfirstlane(tagv);
    const float ar = k0[0], ai = k0[1], cr = k0[2], ci = k0[3], p16r = k1[0], p16i = k1[1], p32r = k1[2], p32i = k1[3];
    __builtin_amdgcn_s_setprio(2);
    rsrc_t ra2 = mk_rsrc((const void*)(pws + WS_A2), A2_BYTES), rcar = mk_rsrc((const void*)(pws + WS_CARRY), 32 * P_ * 16);
    const int ck = 2 * pn + G;
    float pr, pi;
    {
      float tr, ti;
      const auto xr = __builtin_amdgcn_permlane16_swap(f2u(vr[15]), f2u(vr[15]), false, false);
      const auto xi = __builtin_amdgcn_permlane16_swap(f2u(vi[15]), f2u(vi[15]), false, false);
      const float e0r = u2f(xr[0]), e1r = u2f(xr[1]), e0i = u2f(xi[0]), e1i = u2f(xi[1]);
      cmul(e0r, e0i, p16r, p16i, tr, ti); tr += e1r; ti += e1i;
      const auto yr = __builtin_amdgcn_permlane32_swap(f2u(tr), f2u(tr), false, false);
      const auto yi = __builtin_amdgcn_permlane32_swap(f2u(ti), f2u(ti), false, false);
      const float f0r = u2f(yr[0]), f1r = u2f(yr[1]), f0i = u2f(yi[0]), f1i = u2f(yi[1]);
      float Er, Ei;
      cmul(f0r, f0i, p32r, p32i, Er, Ei); Er += f1r; Ei += f1i;
      const float br = (q & 2) ? f0r : 0.f, bi = (q & 2) ? f0i : 0.f;
      cmul(br, bi, p16r, p16i, tr, ti);
      pr = (q & 1) ? tr + e0r : br; pi = (q & 1) ? ti + e0i : bi;
      const v4u gq = {f2u(Er), tag, f2u(Ei), ~tag};
      st_sc1(gq, rcar, q ? OOB : ch * 16, (31 - ck) * (P_ * 16));
    }
    asm volatile("" : "+v"(pr), "+v"(pi) : : "memory");
    SCHED();
    v4u cv[8];
    const int cvo = ch * 16 + (32 - ck + 8 * q) * (P_ * 16);
    auto ld_carry = [&]() {
#pragma unroll
      for (int z = 0; z < 8; ++z) cv[z] = ld_sc1(rcar, cvo, z * (P_ * 16));
    };
    auto publish = [&](int part) {
      *(volatile LAS unsigned*)(L + LDS_DONE + 16 * part + 4 * w) = 1u;
      const bool last = all4(L + LDS_DONE + 16 * part);
      st_flag((unsigned char*)pws, WS_FA2 + part * 32768 + pn * 128 + cb * 8, tag, last && lane < 16 ? lane * 2048 : OOB);
      CFENCE();
    };
    if (!light) {
#pragma unroll
      for (int u = 0; u < 8; ++u) {
        float hv[8];
#pragma unroll
        for (int j = 0; j < 8; ++j) hv[j] = acc[j >> 2][u][j & 3] * -1.44269504088896341f;
        SCHED();
#pragma unroll
        for (int j = 0; j < 8; ++j) hv[j] = __builtin_amdgcn_exp2f(hv[j]);
        SCHED();
#pragma unroll
        for (int j = 0; j < 8; ++j) hv[j] += 1.f;
        SCHED();
#pragma unroll
        for (int j = 0; j < 8; ++j) hv[j] = __builtin_amdgcn_rcpf(hv[j]);
        SCHED();
#pragma unroll
        for (int j = 0; j < 8; ++j) hv[j] *= acc[2 + (j >> 2)][u][j & 3];
        SCHED();
        st_sc1(v4u{pk_bf16(hv[0], hv[1]), pk_bf16(hv[2], hv[3]), pk_bf16(hv[4], hv[5]), pk_bf16(hv[6], hv[7])}, ra2, lane * 16,
               ((2 * pn + ((u >> 2) ^ G)) * NK + 4 * cb + c) * 4096 + (u & 3) * 1024);
        if (u + 1 < 8) asm volatile("" : "+v"(acc[0][u + 1]), "+v"(acc[1][u + 1]) : : "memory");
      }
    }
    CFENCE();
    SCHED();
    ld_carry();
    CFENCE();
    SCHED();
#pragma unroll
    for (int z = 0; z < 16; ++z) {
      const float t = cr * vr[z] - ci * vi[z];
      vi[z] = -cr * vi[z] - ci * vr[z]; vr[z] = t;
    }
    float qr[4], qi[4];
    qr[0] = ar; qi[0] = ai; qr[1] = ar; qi[1] = ai; csq(qr[1], qi[1], 1);
    cmul(qr[1], qi[1], ar, ai, qr[2], qi[2]); qr[3] = qr[1]; qi[3] = qi[1]; csq(qr[3], qi[3], 1);
    const float p4r = qr[3], p4i = qi[3];
    float p64r = p32r, p64i = p32i; csq(p64r, p64i, 1);
    float p512r = p64r, p512i = p64i; csq(p512r, p512i, 3);
    float p1kr = p512r, p1ki = p512i; csq(p1kr, p1ki, 1);
    float sgr, sgi, wgr, wgi;
    cmul((q & 1) ? p16r : 1.f, (q & 1) ? p16i : 0.f, (q & 2) ? p32r : 1.f, (q & 2) ? p32i : 0.f, sgr, sgi);
    cmul((q & 1) ? p512r : 1.f, (q & 1) ? p512i : 0.f, (q & 2) ? p1kr : 1.f, (q & 2) ? p1ki : 0.f, wgr, wgi);
#pragma unroll
    for (int z = 0; z < 16; ++z) asm volatile("" : "+v"(vr[z]), "+v"(vi[z]));
    asm volatile("" : "+v"(sgr), "+v"(sgi), "+v"(wgr), "+v"(wgi), "+v"(p64r), "+v"(p64i));
    SCHED();
    const int want = imin(imax(ck - 8 * q, 0), 8);
    auto carry_ok = [&]() {
      int good = 0;
#pragma unroll
      for (int z = 0; z < 8; ++z) good += ((cv[z][1] ^ tag) | (cv[z][3] ^ ~tag)) == 0u;
      return wall(good == want);
    };
    auto pickup = [&]() {
      lds_wait(L + LDS_PF + 4 * w);
      b2_read(true, L + w * 16384, lane, b2[2]);
      b2_read(true, L + w * 16384 + 8192, lane, b2[3]);
      CFENCE();
      *(volatile LAS unsigned*)(L + LDS_CO + 4 * w) = 1u;
      CFENCE();
    };
    if (!light) pickup();
    wait_vm<8>();
    publish(0);
    if (__builtin_expect(!carry_ok(), 0)) {
#pragma nounroll
      for (unsigned spins = 0; spins < SPIN_CAP; ++spins) {
        __builtin_amdgcn_s_sleep(2);
        CFENCE();
        ld_carry();
        if (carry_ok()) break;
      }
    }
    float Gr, Gi;
    {
      float Hr = 0.f, Hi = 0.f, tr, ti;
#pragma unroll
      for (int z = 7; z >= 0; --z) { cmul(Hr, Hi, p64r, p64i, tr, ti); Hr = tr + u2f(cv[z][0]); Hi = ti + u2f(cv[z][2]); }
      cmul(Hr, Hi, wgr, wgi, tr, ti);
      { const auto a = __builtin_amdgcn_permlane16_swap(f2u(tr), f2u(tr), false, false);
        const auto c2 = __builtin_amdgcn_permlane16_swap(f2u(ti), f2u(ti), false, false);
        tr = u2f(a[0]) + u2f(a[1]); ti = u2f(c2[0]) + u2f(c2[1]); }
      { const auto a = __builtin_amdgcn_permlane32_swap(f2u(tr), f2u(tr), false, false);
        const auto c2 = __builtin_amdgcn_permlane32_swap(f2u(ti), f2u(ti), false, false);
        tr = u2f(a[0]) + u2f(a[1]); ti = u2f(c2[0]) + u2f(c2[1]); }
      cmul(tr, ti, sgr, sgi, Hr, Hi);
      cmul(Hr + pr, Hi + pi, cr, ci, Gr, Gi);
    }
    {
      LAS unsigned char* T = L + B_BASE + (c >> 1) * (2 * B_SLOT) + (c & 1) * 4096;
      const int s32 = ((i15 >> 1) & 1) | ((i15 >> 2) & 2), s8 = (i15 & 1) | ((i15 >> 1) & 2);
#pragma unroll
      for (int t = 0; t < 4; ++t) {
        float orr[4], oi[4];
#pragma unroll
        for (int z = 0; z < 4; ++z) {
          orr[z] = __builtin_fmaf(-qi[z], Gi, __builtin_fmaf(qr[z], Gr, vr[4 * t + z]));
          oi[z] = __builtin_fmaf(-qi[z], Gr, __builtin_fmaf(-qr[z], Gi, vi[4 * t + z]));
        }
        if (t < 3) { float tr, ti; cmul(Gr, Gi, p4r, p4i, tr, ti); Gr = tr; Gi = ti; }
        LAS unsigned char* a = T + i15 * 128 + ((t ^ s32) << 5) + ((q ^ s8) << 3);
        *(LAS v2u*)a = v2u{pk_bf16(orr[0], orr[1]), pk_bf16(orr[2], orr[3])};
        *(LAS v2u*)(a + 2048) = v2u{pk_bf16(oi[0], oi[1]), pk_bf16(oi[2], oi[3])};
      }
      const int qq = i15 >> 2, pp = lane & 3;
      const int so = (ck * NK + 56 + 4 * (cb >> 1) + (cb & 1)) * 4096 + (c >> 1) * 512;
#pragma unroll
      for (int t = 0; t < 4; ++t) {
        v2u part[2];
#pragma unroll
        for (int hh = 0; hh < 2; ++hh)
          part[hh] = lds_rdtr(T + (8 * q + 4 * hh + qq) * 128 + ((t ^ ((qq >> 1) | ((q & 1) << 1))) << 5) + ((pp ^ ((qq & 1) | (hh << 1))) << 3));
        st_sc1(v4u{part[0][0], part[0][1], part[1][0], part[1][1]}, ra2, (lane >> 5) * 8192 + (lane & 31) * 16, so + t * 1024);
      }
    }
    SCHED();
    wait_vm<0>();
    publish(1);
    if (light) pickup();
  }
  kp = kargs();
  asm volatile("" : "+s"(R), "+s"(N));
  pc = p2_ctx(pWd, pCre, pCim, w, N);
  const int d0 = 2 * pc.sh;
  if (w < 4) {
    lds_wait_eq(L + LDS_PF + 4 * w, 2u);
    b2_read(true, L + w * 16384, lane, b2[0]);
    b2_read(true, L + w * 16384 + 8192, lane, b2[1]);
    WAIT_LDS();
    b2_load<0, 0, 8>(p2_ctx(pWd, pCre, pCim, w + 4, N), vb, 6, w * 16384);
    wait_vm<0>();
    *(volatile LAS unsigned*)(L + LDS_CO + 4 * w) = 2u;
  }

  {
    lds_wait4(L + LDS_DONE);
    const int cl = lane & 15, lo = pc.sh ? (pc.g0 - 28) & ~1 : pc.g0 >> 1, hi = pc.sh ? (pc.g0 + pc.nd - 29) | 1 : (pc.g0 + pc.nd - 1) >> 1;
    GAS u64* fl = (GAS u64*)(kp->ws + WS_FA2) + ((pc.sh * 16 + (R & 1) * 8 + N) * 16 + (R >> 1)) * 16 + cl;
    if (w >= 4) {
      const unsigned need = ((2u << hi) - (1u << lo)) | 0xC000u;
#pragma nounroll
      for (unsigned spins = 0; spins < (SPIN_CAP << 3); ++spins) {
        CFENCE();
        const v4u st = *(volatile LAS v4u*)(L + LDS_RDY + 16);
        unsigned up = __builtin_amdgcn_readfirstlane(st[3]);
        if ((up & need) == need) break;
        const unsigned gone = __builtin_amdgcn_readfirstlane(st[0] | st[1] | st[2]), lower = (1u << (w - 4)) - 1u;
        if ((gone & lower) == lower) {
          const u64 v = __hip_atomic_load(fl, RLX_AGENT);
          up = (unsigned)__builtin_amdgcn_ballot_w64(v == tag64(tag)) & 0xFFFFu;
          CFENCE();
          *(volatile LAS unsigned*)(L + LDS_RDY + 28) = up;
          if ((up & need) == need) break;
          if (up & 0x3FFFu) __builtin_amdgcn_s_sleep(4); else __builtin_amdgcn_s_sleep(16);
        } else {
          __builtin_amdgcn_s_sleep(2);
        }
      }
      CFENCE();
      if (w < 7) *(volatile LAS unsigned*)(L + LDS_RDY + 16 + 4 * (w - 4)) = 1u << (w - 4);
    } else if (w == 3) {
      lds_wait4(L + LDS_DONE + 16);
      __builtin_amdgcn_s_sleep(8);
#pragma nounroll
      for (unsigned spins = 0; spins < SPIN_CAP; ++spins) {
        CFENCE();
        const u64 v = __hip_atomic_load(fl, RLX_AGENT);
        const unsigned up = (unsigned)__builtin_amdgcn_ballot_w64(v == tag64(tag)) & 0xFFFFu;
        CFENCE();
        *(volatile LAS unsigned*)(L + LDS_RDY) = up;
        if (up == 0xFFFFu) break;
        if (up & 0x3FFFu) __builtin_amdgcn_s_sleep(8); else __builtin_amdgcn_s_sleep(32);
      }
      CFENCE();
    } else {
      const unsigned need = (0xFu << lo) | 0xC000u;
#pragma nounroll
      for (unsigned spins = 0; spins < (SPIN_CAP << 4); ++spins) {
        if ((lds_word(L + LDS_RDY) & need) == need) break;
        __builtin_amdgcn_s_sleep(2);
      }
      CFENCE();
    }
  }
  if (w >= 4) __builtin_amdgcn_s_setprio(1);
  glds<B_BASE + B_SLOT, 0>(mk_rs((const void*)pX, S_ * D_ * 2), w * 1024, ((lane >> 5) * 16 + ((lane >> 3) & 3)) * 1024 + (lane & 7) * 16,
                             (64 * R + 32 * (w & 1) + 4 * (w >> 1)) * 1024 + N * 128);
  const int tok = 64 * R + 32 * (w & 1) + 4 * (w >> 1) + (lane >> 4);
  const v2u dv = __builtin_amdgcn_raw_buffer_load_b64(mk_rsrc(kp->Dsk, D_ * 2), (lane & 15) * 8, N * 128, 0);
  const float ge = u2f(__builtin_amdgcn_raw_buffer_load_b32(mk_rsrc(kp->ws + WS_G, S_ * 4), tok * 4 + (lane & 1) * 64, 0, AUX_SC1));
  SCHED();
  const v4u ra = mk_rs(kp->ws + WS_A2 + (R * NK + 2 * pc.g0) * 4096, pc.nd * 8192);
  const int va = lane * 16;
  v4u af[8];
  {
    const int o = p2_ds(pc, 0) * 8192;
    p2_first<0>(af, ra, va, o, o + 4096);
  }
  SCHED();
  if (w < 4) __builtin_amdgcn_s_setprio(0);
  f32x4 acc[4][4];
#pragma unroll
  for (int e = 0; e < 4; ++e)
#pragma unroll
    for (int tt = 0; tt < 4; ++tt) { acc[e][tt] = f32x4{0.f, 0.f, 0.f, 0.f}; asm volatile("" : "+v"(acc[e][tt])); }
  asm volatile("s_nop 1");
  auto step = [&](auto Dc) {
    constexpr int d = decltype(Dc)::value;
    const int o = p2_ds(pc, d + 1 - d0) * 8192;
    p2_step<d, 0>(acc, b2[d & 3], af, ra, va, o, o + 4096, w);
    if (d < 2) b2_read(false, L + w * 16384 + d * 8192, lane, b2[d & 3]);
    if (d == 3 && w >= 4) {
      lds_wait_eq(L + LDS_CO + 4 * (w - 4), 2u);
      b2_read(false, L + (w - 4) * 16384, lane, b2[2]);
      CFENCE();
      *(volatile LAS unsigned*)(L + LDS_PF + 4 * (w - 4)) = 3u;
      CFENCE();
    }
    SCHED();
  };
  if (w >= 4) { step(IC<0>{}); step(IC<1>{}); }
  step(IC<2>{}); step(IC<3>{}); step(IC<4>{}); step(IC<5>{});
  if (w >= 4) step(IC<6>{});
  asm volatile("s_nop 15" : "+v"(acc[0][3]), "+v"(acc[1][3]), "+v"(acc[2][3]), "+v"(acc[3][3]), "+v"(acc[0][2]), "+v"(acc[1][2]), "+v"(acc[2][2]), "+v"(acc[3][2]));
  SCHED();
  kp = kargs();
  if (w < 4) lds_wait_eq(L + LDS_PF + 4 * w, 3u);
  {
    const int t3 = lane & 3, g3 = ((t3 & 1) * 3) ^ (t3 >> 1);
#pragma unroll
    for (int tt = 0; tt < 4; ++tt)
#pragma unroll
      for (int e = 0; e < 4; ++e)
        *(LAS f32x4*)(L + w * 16384 + (tt * 4 + e) * 1024 + (((lane & 48) | (((((lane >> 2) & 3) ^ (lane >> 4)) << 2) | (e ^ g3))) << 4)) = acc[e][tt];
  }
  if (b == 255 && w == 0) {
    rsrc_t rf = mk_rsrc(kp->ws + WS_FA2 + 32768, 256 * 8);
#pragma nounroll
    for (unsigned spins = 0; spins < SPIN_CAP; ++spins) {
      CFENCE();
      const v4u f0 = ld_sc1(rf, lane * 32, 0), f1 = ld_sc1(rf, lane * 32, 16);
      if (wall(((f0[0] ^ tag) | (f0[1] ^ ~tag) | (f0[2] ^ tag) | (f0[3] ^ ~tag) | (f1[0] ^ tag) | (f1[1] ^ ~tag) | (f1[2] ^ tag) | (f1[3] ^ ~tag)) == 0u)) break;
      __builtin_amdgcn_s_sleep(8);
    }
    st_flag(kp->ws, WS_EPOCH, tag, lane == 0 ? 0 : OOB);
  }
  {
    const int q = lane & 3, e = (lane >> 2) & 3, t3 = lane >> 4;
    const int base = ((w >> 1) * 4 + e) * 1024 + q * 256 + ((e ^ ((t3 & 1) * 3) ^ (t3 >> 1)) << 4);
    u64 pout = (u64)kp->out;
    const float go = dpp<0xB1>(ge);
    f32x4 res[2];
    float g2[2], g1[2];
    int a[4];
#pragma unroll
    for (int hf = 0; hf < 2; ++hf) {
      const float g = (lane & 1) == hf ? ge : go;
      const v2u xv = *(LAS v2u*)(L + B_BASE + B_SLOT + w * 1024 + hf * 512 + lane * 8);
      res[hf] = f32x4{g * (bf2f(xv[0] & 0xFFFFu) * bf2f(dv[0] & 0xFFFFu)), g * (bfhi(xv[0]) * bfhi(dv[0])),
                      g * (bf2f(xv[1] & 0xFFFFu) * bf2f(dv[1] & 0xFFFFu)), g * (bfhi(xv[1]) * bfhi(dv[1]))};
      g2[hf] = 2.f * g; g1[hf] = 1.f - g;
      a[2 * hf] = base + (((2 * (w & 1) + hf) ^ q) << 6); a[2 * hf + 1] = a[2 * hf] + 65536;
    }
    int oo = tok * 2048 + (lane & 15) * 16 + N * 256, oo2 = oo + 32768;
    asm volatile("s_waitcnt lgkmcnt(0)\n\ts_barrier"
                 : "+v"(res[0]), "+v"(res[1]), "+v"(g2[0]), "+v"(g2[1]), "+v"(g1[0]), "+v"(g1[1]), "+v"(a[0]), "+v"(a[1]), "+v"(a[2]), "+v"(a[3]), "+v"(oo),
                   "+v"(oo2), "+s"(pout) : : "memory");
    f32x4 v[4][4];
#pragma unroll
    for (int z = 0; z < 4; ++z)
#pragma unroll
      for (int ww = 0; ww < 4; ++ww) v[z][ww] = *(LAS f32x4*)(L + a[z] + ww * 16384);
    SCHED();
    rsrc_t ro = mk_rsrc((const void*)pout, S_ * D_ * 4);
#pragma unroll
    for (int hf = 0; hf < 2; ++hf) {
      const f32x4 ss = (v[2 * hf][0] + v[2 * hf][1]) + (v[2 * hf][2] + v[2 * hf][3]);
      const f32x4 sg = (v[2 * hf + 1][0] + v[2 * hf + 1][1]) + (v[2 * hf + 1][2] + v[2 * hf + 1][3]);
      const f32x4 m = res[hf] + ss * g2[hf] + sg * g1[hf];
      __builtin_amdgcn_raw_buffer_store_b128(__builtin_bit_cast(v4u, m), ro, hf ? oo2 : oo, 0, AUX_NT);
    }
  }
}

#if defined(__HIP_DEVICE_COMPILE__)
#pragma clang attribute pop
#endif

extern "C" void kernel_launch(void* const* d_in, const int* in_sizes, int n_in, void* d_out, int out_size, void* d_ws, size_t ws_size,
                              hipStream_t stream) {
  Params p;
  p.x = (const u16*)d_in[0]; p.Lre = (const u16*)d_in[1]; p.Lim = (const u16*)d_in[2]; p.Bre = (const u16*)d_in[3];
  p.Bim = (const u16*)d_in[4]; p.Cre = (const u16*)d_in[5]; p.Cim = (const u16*)d_in[6]; p.ldt = (const u16*)d_in[7];
  p.Dsk = (const u16*)d_in[8]; p.Wg = (const u16*)d_in[10]; p.Wu = (const u16*)d_in[11]; p.Wd = (const u16*)d_in[12];
  p.gw = (const u16*)d_in[13]; p.out = (float*)d_out; p.ws = (unsigned char*)d_ws;
  (void)hipFuncSetAttribute((const void*)k_fused, hipFuncAttributeMaxDynamicSharedMemorySize, LDS_BYTES);
  k_fused<<<256, 512, LDS_BYTES, stream>>>(p);
}
```
